```python
import torch

def _softmax(x, axis=-1):
    return torch.softmax(x, dim=axis)
def _gelu(x):
    return torch.nn.functional.gelu(x, approximate="tanh")
def _silu(x):
    return torch.nn.functional.silu(x)
def _sigmoid(x):
    return torch.sigmoid(x)
def _layernorm(x, axis=-1, eps=1e-5):
    m = x.mean(dim=axis, keepdim=True)
    v = x.var(dim=axis, keepdim=True, unbiased=False)
    return (x - m) / torch.sqrt(v + eps)
def _scatter_add(tgt, idx, src, axis=0):
    return tgt.clone().index_add_(axis, idx, src)
def _segment_sum(src, seg, n):
    out = torch.zeros((n,) + src.shape[1:], dtype=src.dtype)
    return out.index_add_(0, seg, src)
def _pad(x, pw):
    flat = tuple(v for pair in reversed(pw) for v in pair)
    return torch.nn.functional.pad(x, flat)

kernel_name = 'sequential_preLN__softmax_full_mha__geglu_ffn__prompt'

def setup_inputs():

    _w = (lambda a: (lambda t: t.to(torch.bfloat16) if t.is_floating_point() else t)(torch.as_tensor(a)))
    def _A_setup():
        B, S, D = 1, 2048, 512
        return {
            "x": _w((lambda *s: torch.randn(*s, dtype=torch.bfloat16) * 0.05)(B, S, D)),
            "wqkv": _w((lambda *s: torch.randn(*s, dtype=torch.bfloat16) * 0.05)(D, 3 * D)),
            "wo": _w((lambda *s: torch.randn(*s, dtype=torch.bfloat16) * 0.05)(D, D)),
        }
    _A_in = _A_setup()

    def _B_setup():
        B, S, D, F = 1, 2048, 512, 1376
        return {
            "x": _w((lambda *s: torch.randn(*s, dtype=torch.bfloat16) * 0.05)(B, S, D)),
            "w_gate": _w((lambda *s: torch.randn(*s, dtype=torch.bfloat16) * 0.05)(D, F)),
            "w_up": _w((lambda *s: torch.randn(*s, dtype=torch.bfloat16) * 0.05)(D, F)),
            "w_down": _w((lambda *s: torch.randn(*s, dtype=torch.bfloat16) * 0.05)(F, D)),
        }
    _B_in = _B_setup()

    return {
        "A_x": _A_in["x"],
        "A_wqkv": _A_in["wqkv"],
        "A_wo": _A_in["wo"],
        "B_x": _B_in["x"],
        "B_w_gate": _B_in["w_gate"],
        "B_w_up": _B_in["w_up"],
        "B_w_down": _B_in["w_down"],
    }

def reference(A_x, A_wqkv, A_wo, B_x, B_w_gate, B_w_up, B_w_down):
    def _A_ref(x, wqkv, wo):
        B, S, D = x.shape
        H = 32; Dh = D // H
        qkv = (lambda x, *d: x.permute(*d))((x @ wqkv).reshape(B, S, 3, H, Dh), 2, 0, 3, 1, 4)
        q, k, v = qkv[0], qkv[1], qkv[2]
        scale = Dh ** -0.5
        scores = torch.einsum("bhqd,bhkd->bhqk", q, k) * scale
        p = _softmax(scores, axis=-1)
        o = torch.einsum("bhqk,bhkd->bhqd", p, v)
        return (lambda x, *d: x.permute(*d))(o, 0, 2, 1, 3).reshape(B, S, D) @ wo

    def _B_ref(x, w_gate, w_up, w_down):
        return (_gelu(x @ w_gate) * (x @ w_up)) @ w_down

    x = A_x
    h = x + _A_ref(_layernorm(x, axis=-1), A_wqkv, A_wo)
    h2 = h + _B_ref(_layernorm(h, axis=-1), B_w_gate, B_w_up, B_w_down)
    return h2
```

```cpp
#include <hip/hip_runtime.h>
#include <stdint.h>

typedef short bf16x8 __attribute__((ext_vector_type(8)));
typedef short s16x4 __attribute__((ext_vector_type(4)));
typedef float f32x4 __attribute__((ext_vector_type(4)));
typedef float f32x2 __attribute__((ext_vector_type(2)));
typedef float f32x16 __attribute__((ext_vector_type(16)));
typedef unsigned u32x4 __attribute__((ext_vector_type(4)));
typedef unsigned u32x2 __attribute__((ext_vector_type(2)));
typedef __bf16 bf16x2_t __attribute__((ext_vector_type(2)));
#define LAS __attribute__((address_space(3)))
typedef const LAS char* lds_cptr;
typedef LAS char* lds_ptr;

constexpr int SEQ = 2048, DM = 512, NH = 32, FF = 1376;
constexpr float EPS = 1e-5f;
constexpr float QSCALE = 0.25f * 1.4426950408889634f;

constexpr size_t WS_Q = 0, WS_K = 2u << 20, WS_V = 4u << 20, WS_O = 6u << 20, WS_H = 8u << 20, WS_ACT = 10u << 20;
constexpr size_t WS_KMAX = 16u << 20, WS_HST = (16u << 20) + (64u << 10);

struct Params {
  const char* x; const char* wqkv; const char* wo; const char* wg; const char* wu; const char* wd;
  char* out; char* ws;
};

#define MFMA16(a, b, c) __builtin_amdgcn_mfma_f32_16x16x32_bf16(a, b, c, 0, 0, 0)
#define MFMA32(a, b, c) __builtin_amdgcn_mfma_f32_32x32x16_bf16(a, b, c, 0, 0, 0)
#define WAIT_BAR(N) asm volatile("s_waitcnt vmcnt(" #N ") lgkmcnt(0)\n\ts_barrier" ::: "memory")
#define PIN(x) asm volatile("" : "+v"(x))

template <int N> __device__ __forceinline__ void wait_bar() {
  asm volatile("s_waitcnt vmcnt(%0) lgkmcnt(0)\n\ts_barrier" ::"n"(N) : "memory");
}
__device__ __forceinline__ void lds_bar() { asm volatile("s_waitcnt lgkmcnt(0)\n\ts_barrier" ::: "memory"); }

__device__ __forceinline__ void glds16(const void* sbase, unsigned voff, unsigned lds_addr) {
  unsigned keep;
  asm volatile("s_mov_b32 %0, m0\n\ts_mov_b32 m0, %3\n\ts_nop 2\n\tglobal_load_lds_dwordx4 %1, %2\n\ts_mov_b32 m0, %0"
               : "=&s"(keep) : "v"(voff), "s"(sbase), "s"(lds_addr) : "memory");
}
__device__ __forceinline__ void glds4(const void* sbase, unsigned voff, unsigned lds_addr) {
  unsigned keep;
  asm volatile("s_mov_b32 %0, m0\n\ts_mov_b32 m0, %3\n\ts_nop 2\n\tglobal_load_lds_dword %1, %2\n\ts_mov_b32 m0, %0"
               : "=&s"(keep) : "v"(voff), "s"(sbase), "s"(lds_addr) : "memory");
}
__device__ __forceinline__ bf16x8 ld128(lds_cptr p) { return *(const LAS bf16x8*)p; }
typedef short v4i16_t __attribute__((ext_vector_type(4)));
__device__ __forceinline__ s16x4 ldtr(lds_cptr p) {
  return __builtin_bit_cast(s16x4, __builtin_amdgcn_ds_read_tr16_b64_v4i16((LAS v4i16_t*)p));
}
__device__ __forceinline__ bf16x8 ldtr2(lds_cptr p, int second) {
  const s16x4 a = ldtr(p), b = ldtr(p + second);
  return (bf16x8){a[0], a[1], a[2], a[3], b[0], b[1], b[2], b[3]};
}
__device__ __forceinline__ unsigned cvtpk(float lo, float hi) {
  f32x2 v = {lo, hi}; bf16x2_t b = __builtin_convertvector(v, bf16x2_t); return __builtin_bit_cast(unsigned, b);
}
__device__ __forceinline__ float bflo(unsigned w) { return __uint_as_float(w << 16); }
__device__ __forceinline__ float bfhi(unsigned w) { return __uint_as_float(w & 0xffff0000u); }
__device__ __forceinline__ int rfl(int v) { return __builtin_amdgcn_readfirstlane(v); }
__device__ __forceinline__ bf16x8 ones8() { return (bf16x8){0x3F80, 0x3F80, 0x3F80, 0x3F80, 0x3F80, 0x3F80, 0x3F80, 0x3F80}; }

__device__ __forceinline__ void p1_qkv(const Params& p, int rb, int cb, char* smem) {
  const int tid = threadIdx.x, lane = tid & 63, wid = rfl(tid >> 6), wm = wid >> 2, wn = wid & 3;
  const int r16 = lane & 15, g = lane >> 4;
  const unsigned lds0 = (unsigned)(size_t)(lds_ptr)smem;
  lds_cptr L = (lds_cptr)smem;
  constexpr int STG = 32768, ND = 4, NS = 8;
  const unsigned voffA = (unsigned)((8 * wid + (lane >> 3)) * DM + (((lane & 7) ^ (lane >> 3)) * 8)) * 2u;
  unsigned voffB;
  { const int r = lane >> 3, c = lane & 7, f = ((r >> 1) & 1) | ((wid & 1) << 1), j = (c >> 1) ^ f;
    voffB = (unsigned)((8 * wid + r) * (3 * DM) + j * 16 + (c & 1) * 8) * 2u; }
  const char* xa = p.x + (size_t)rb * 64 * DM * 2;
  const char* wb = p.wqkv + (size_t)cb * 64 * 2;
  auto issue = [&](int s) {
    const unsigned d = lds0 + (s % ND) * STG;
    glds16(xa + s * 128, voffA, d + wid * 1024);
#pragma unroll
    for (int seg = 0; seg < 3; ++seg) glds16(wb + (size_t)s * 64 * (3 * DM) * 2 + seg * DM * 2, voffB, d + 8192 + (wid * 3 + seg) * 1024);
  };
  issue(0); issue(1); issue(2);
  int aoff[2];
#pragma unroll
  for (int ks = 0; ks < 2; ++ks) aoff[ks] = wm * 4096 + (r16 >> 3) * 1024 + (r16 & 7) * 128 + (((4 * ks + g) ^ (r16 & 7)) << 4);
  int boff;
  { const int q = (lane >> 2) & 3, pp = lane & 3, f = ((q >> 1) & 1) | ((g & 1) << 1);
    boff = 8192 + g * 3072 + q * 128 + ((wn ^ f) << 5) + pp * 8; }
  f32x4 acc[3][2], cs[3], sx[2], sq[2];
#pragma unroll
  for (int i = 0; i < 3; ++i) { acc[i][0] = (f32x4){0, 0, 0, 0}; acc[i][1] = (f32x4){0, 0, 0, 0}; cs[i] = (f32x4){0, 0, 0, 0}; }
  sx[0] = sx[1] = sq[0] = sq[1] = (f32x4){0, 0, 0, 0};
  const bf16x8 one = ones8();
#pragma unroll
  for (int s = 0; s < NS; ++s) {
    if (s <= NS - 3) wait_bar<8>(); else if (s == NS - 2) wait_bar<4>(); else wait_bar<0>();
    if (s + 3 < NS) issue(s + 3);
    lds_cptr B = L + (s % ND) * STG;
#pragma unroll
    for (int ks = 0; ks < 2; ++ks) {
      const int kk = 2 * s + ks;
      bf16x8 a[2], w[3];
      a[0] = ld128(B + aoff[ks]); a[1] = ld128(B + aoff[ks] + 2048);
#pragma unroll
      for (int seg = 0; seg < 3; ++seg) w[seg] = ldtr2(B + boff + ks * 12288 + seg * 1024, 512);
#pragma unroll
      for (int mt = 0; mt < 2; ++mt) {
        acc[0][mt] = MFMA16(w[0], a[mt], acc[0][mt]);
        acc[1][mt] = MFMA16(w[1], a[mt], acc[1][mt]);
        acc[2][mt] = MFMA16(a[mt], w[2], acc[2][mt]);
      }
      if ((kk & 1) == wm) { cs[0] = MFMA16(w[0], one, cs[0]); cs[1] = MFMA16(w[1], one, cs[1]); cs[2] = MFMA16(one, w[2], cs[2]); }
      if ((kk & 3) == wn) {
#pragma unroll
        for (int mt = 0; mt < 2; ++mt) { sx[mt] = MFMA16(one, a[mt], sx[mt]); sq[mt] = MFMA16(a[mt], a[mt], sq[mt]); }
      }
    }
  }
  lds_bar();
  LAS float* st = (LAS float*)smem;
  LAS float* csl = (LAS float*)(smem + 4096);
#pragma unroll
  for (int mt = 0; mt < 2; ++mt) {
    const int row = wm * 32 + mt * 16 + r16;
    if (g == 0) st[(wn * 64 + row) * 2] = sx[mt][0];
    if ((r16 >> 2) == g) { const int e = r16 & 3; const float v = e == 0 ? sq[mt][0] : e == 1 ? sq[mt][1] : e == 2 ? sq[mt][2] : sq[mt][3]; st[(wn * 64 + row) * 2 + 1] = v; }
  }
  if (r16 == 0) { *(LAS f32x4*)(csl + ((wm * 4 + wn) * 3 + 0) * 16 + 4 * g) = cs[0]; *(LAS f32x4*)(csl + ((wm * 4 + wn) * 3 + 1) * 16 + 4 * g) = cs[1]; }
  if (g == 0) csl[((wm * 4 + wn) * 3 + 2) * 16 + r16] = cs[2][0];
  lds_bar();
  const int h = cb * 4 + wn;
  const f32x4 cq = *(LAS f32x4*)(csl + (wn * 3 + 0) * 16 + 4 * g) + *(LAS f32x4*)(csl + ((4 + wn) * 3 + 0) * 16 + 4 * g);
  const f32x4 ck = *(LAS f32x4*)(csl + (wn * 3 + 1) * 16 + 4 * g) + *(LAS f32x4*)(csl + ((4 + wn) * 3 + 1) * 16 + 4 * g);
  const float cv = csl[(wn * 3 + 2) * 16 + r16] + csl[((4 + wn) * 3 + 2) * 16 + r16];
  auto rowstat = [&](int row, float& mean, float& rstd) {
    float a = 0.f, b = 0.f;
#pragma unroll
    for (int w4 = 0; w4 < 4; ++w4) { const f32x2 v = *(LAS f32x2*)(st + (w4 * 64 + row) * 2); a += v.x; b += v.y; }
    mean = a * (1.f / DM); const float var = fmaxf(b * (1.f / DM) - mean * mean, 0.f); rstd = rsqrtf(var + EPS);
  };
  float kmx = 0.f;
#pragma unroll
  for (int mt = 0; mt < 2; ++mt) {
    const int lrow = wm * 32 + mt * 16, grow = rb * 64 + lrow;
    float mean, rstd; rowstat(lrow + r16, mean, rstd);
    const size_t t32 = (size_t)h * 64 + (grow >> 5);
    const size_t qk_off = (t32 * 2 + (g >> 1)) * 512 + (((grow & 31) + r16) * 16) + (g & 1) * 8;
    { const f32x4 v = (acc[0][mt] - mean * cq) * (rstd * QSCALE);
      *(u32x2*)(p.ws + WS_Q + qk_off) = (u32x2){cvtpk(v[0], v[1]), cvtpk(v[2], v[3])}; }
    { const f32x4 v = (acc[1][mt] - mean * ck) * rstd;
      *(u32x2*)(p.ws + WS_K + qk_off) = (u32x2){cvtpk(v[0], v[1]), cvtpk(v[2], v[3])};
      float n2 = v[0] * v[0] + v[1] * v[1] + v[2] * v[2] + v[3] * v[3];
      n2 += __shfl_xor(n2, 16); n2 += __shfl_xor(n2, 32); kmx = fmaxf(kmx, n2); }
    { float v[4];
#pragma unroll
      for (int e = 0; e < 4; ++e) { float m2, r2; rowstat(lrow + 4 * g + e, m2, r2); v[e] = (acc[2][mt][e] - m2 * cv) * r2; }
      const size_t v_off = ((t32 * 2 + (mt)) * 2 + (g & 1)) * 256 + r16 * 16 + (g >> 1) * 8;
      *(u32x2*)(p.ws + WS_V + v_off) = (u32x2){cvtpk(v[0], v[1]), cvtpk(v[2], v[3])}; }
  }
#pragma unroll
  for (int o = 1; o < 16; o <<= 1) kmx = fmaxf(kmx, __shfl_xor(kmx, o));
  if (lane == 0) ((float*)(p.ws + WS_KMAX))[h * 64 + rb * 2 + wm] = kmx;
}

constexpr int A_K = 0, A_V = 65536, A_ONE = 131072, A_Q = A_ONE + 4096, A_KM = A_Q + 8192, A_BYTES = A_KM + 256;
constexpr float SAFE_BOUND = 48.f;

__device__ __forceinline__ void p2_attn(const Params& p, int h, int qr, char* smem) {
  const int tid = threadIdx.x, lane = tid & 63, wid = rfl(tid >> 6);
  const int r32 = lane & 31, hh = lane >> 5;
  const unsigned lds0 = (unsigned)(size_t)(lds_ptr)smem;
  lds_cptr L = (lds_cptr)smem;
  const char* kg = p.ws + WS_K + (size_t)h * 65536;
  const char* vg = p.ws + WS_V + (size_t)h * 65536;
  const unsigned vo = lane * 16;
  glds16(p.ws + WS_Q + ((size_t)h * 64 + qr * 8 + wid) * 1024, vo, lds0 + A_Q + wid * 1024);
  glds4(p.ws + WS_KMAX + h * 256, lane * 4, lds0 + A_KM);
#pragma unroll
  for (int c = 0; c < 4; ++c)
#pragma unroll
    for (int i = 0; i < 2; ++i) {
      const int t = c * 16 + wid * 2 + i;
      glds16(kg + t * 1024, vo, lds0 + A_K + t * 1024);
      glds16(vg + t * 1024, vo, lds0 + A_V + t * 1024);
    }
  *(LAS u32x2*)(smem + A_ONE + tid * 8) = (u32x2){0x3F803F80u, 0x3F803F80u};
  wait_bar<12>();
  const bf16x8 qf = ld128(L + A_Q + wid * 1024 + lane * 16);
  float c;
  { float km = *(const LAS float*)(L + A_KM + lane * 4);
#pragma unroll
    for (int o = 1; o < 64; o <<= 1) km = fmaxf(km, __shfl_xor(km, o));
    float q2 = 0.f;
#pragma unroll
    for (int j = 0; j < 8; ++j) { const float v = __uint_as_float((unsigned)(unsigned short)qf[j] << 16); q2 += v * v; }
    q2 += __shfl_xor(q2, 32);
    c = sqrtf(q2 * km) * 1.02f + 0.01f; }
  const bool fast = __all(c <= SAFE_BOUND);
  const bool vlane = r32 < 16;
  int vaddr = (vlane ? A_V : A_ONE) + hh * 256 + (r32 & 15) * 16;
  const int vstep = vlane ? 4096 : 0;
  const int kaddr = A_K + lane * 16;
  f32x16 o;
#pragma unroll
  for (int i = 0; i < 16; ++i) o[i] = 0.f;
  if (fast) {
    f32x16 negc;
#pragma unroll
    for (int i = 0; i < 16; ++i) negc[i] = -c;
    PIN(negc);
#pragma unroll
    for (int ch = 0; ch < 4; ++ch) {
      if (ch == 1) wait_bar<8>(); else if (ch == 2) wait_bar<4>(); else if (ch == 3) wait_bar<0>();
      f32x16 sc = MFMA32(ld128(L + kaddr + ch * 16384), qf, negc);
#pragma unroll 1
      for (int t4 = 0; t4 < 4; ++t4) {
#pragma unroll
        for (int u = 0; u < 4; ++u) {
          const int t = ch * 16 + t4 * 4 + u;
          const bf16x8 v0 = ld128(L + vaddr + u * 1024), v1 = ld128(L + vaddr + u * 1024 + 512);
          const int tn = (t4 * 4 + u == 15) ? t : t + 1;
          const f32x16 sn = MFMA32(ld128(L + kaddr + tn * 1024), qf, negc);
          u32x4 p0, p1;
#pragma unroll
          for (int i = 0; i < 4; ++i) {
            p0[i] = cvtpk(__builtin_amdgcn_exp2f(sc[2 * i]), __builtin_amdgcn_exp2f(sc[2 * i + 1]));
            p1[i] = cvtpk(__builtin_amdgcn_exp2f(sc[8 + 2 * i]), __builtin_amdgcn_exp2f(sc[8 + 2 * i + 1]));
          }
          o = MFMA32(v0, __builtin_bit_cast(bf16x8, p0), o);
          o = MFMA32(v1, __builtin_bit_cast(bf16x8, p1), o);
          sc = sn;
        }
        vaddr += vstep;
      }
    }
  } else {
    float m = -INFINITY;
#pragma unroll 1
    for (int ch = 0; ch < 4; ++ch) {
      if (ch == 1) wait_bar<8>(); else if (ch == 2) wait_bar<4>(); else if (ch == 3) wait_bar<0>();
#pragma unroll 1
      for (int t4 = 0; t4 < 4; ++t4) {
#pragma unroll 1
        for (int u = 0; u < 4; ++u) {
          const int t = ch * 16 + t4 * 4 + u;
          f32x16 z;
#pragma unroll
          for (int i = 0; i < 16; ++i) z[i] = 0.f;
          const f32x16 s = MFMA32(ld128(L + kaddr + t * 1024), qf, z);
          float mx = s[0];
#pragma unroll
          for (int i = 1; i < 16; ++i) mx = fmaxf(mx, s[i]);
          mx = fmaxf(mx, __shfl_xor(mx, 32));
          const float mn = fmaxf(m, mx);
          const float al = __builtin_amdgcn_exp2f(m - mn);
          m = mn;
#pragma unroll
          for (int i = 0; i < 16; ++i) o[i] *= al;
          u32x4 p0, p1;
#pragma unroll
          for (int i = 0; i < 4; ++i) {
            p0[i] = cvtpk(__builtin_amdgcn_exp2f(s[2 * i] - mn), __builtin_amdgcn_exp2f(s[2 * i + 1] - mn));
            p1[i] = cvtpk(__builtin_amdgcn_exp2f(s[8 + 2 * i] - mn), __builtin_amdgcn_exp2f(s[8 + 2 * i + 1] - mn));
          }
          const bf16x8 v0 = ld128(L + vaddr + u * 1024), v1 = ld128(L + vaddr + u * 1024 + 512);
          o = MFMA32(v0, __builtin_bit_cast(bf16x8, p0), o);
          o = MFMA32(v1, __builtin_bit_cast(bf16x8, p1), o);
        }
        vaddr += vstep;
      }
    }
  }
  const float inv = 1.f / o[8];
  unsigned a0 = cvtpk(o[0] * inv, o[1] * inv), a1 = cvtpk(o[2] * inv, o[3] * inv);
  unsigned b0 = cvtpk(o[4] * inv, o[5] * inv), b1 = cvtpk(o[6] * inv, o[7] * inv);
  { auto r = __builtin_amdgcn_permlane32_swap(a0, b0, false, false); a0 = r[0]; b0 = r[1]; }
  { auto r = __builtin_amdgcn_permlane32_swap(a1, b1, false, false); a1 = r[0]; b1 = r[1]; }
  const int row = qr * 256 + wid * 32 + r32;
  const size_t off = ((((size_t)(row >> 4) * 16 + (h >> 1)) * 4 + (h & 1) * 2 + hh) * 16 + (row & 15)) * 16;
  *(u32x4*)(p.ws + WS_O + off) = (u32x4){a0, a1, b0, b1};
}

__device__ __forceinline__ unsigned voff_w128(int lane, int par) {
  const int r = lane >> 3, c = lane & 7, f = ((r >> 1) & 1) | (par << 1), j = (c >> 1) ^ f;
  return (unsigned)(r * DM + j * 16 + (c & 1) * 8) * 2u;
}
__device__ __forceinline__ void p3_oproj(const Params& p, int rb, int cb, char* smem) {
  const int tid = threadIdx.x, lane = tid & 63, wid = rfl(tid >> 6), wm = wid >> 1, wn = wid & 1;
  const int r16 = lane & 15, g = lane >> 4;
  const unsigned lds0 = (unsigned)(size_t)(lds_ptr)smem;
  lds_cptr L = (lds_cptr)smem;
  const char* ag = p.ws + WS_O + (size_t)rb * 4 * 16384;
  const char* bg = p.wo + cb * 128;
  const unsigned vb0 = voff_w128(lane, 0), vb1 = voff_w128(lane, 1);
#pragma unroll
  for (int c = 0; c < 4; ++c) {
#pragma unroll
    for (int i = 0; i < 2; ++i) { const int rt = wid >> 1, ks = c * 4 + (wid & 1) * 2 + i; glds16(ag + (rt * 16 + ks) * 1024, lane * 16, lds0 + (rt * 16 + ks) * 1024); }
#pragma unroll
    for (int i = 0; i < 2; ++i) { const int kg = c * 16 + wid * 2 + i; glds16(bg + (size_t)kg * 8 * DM * 2, i ? vb1 : vb0, lds0 + 65536 + kg * 1024); }
  }
  int boff[2];
  { const int q = (lane >> 2) & 3, pp = lane & 3, f = ((q >> 1) & 1) | ((g & 1) << 1);
    boff[0] = 65536 + g * 1024 + q * 128 + (((2 * wn) ^ f) << 5) + pp * 8; boff[1] = 65536 + g * 1024 + q * 128 + (((2 * wn + 1) ^ f) << 5) + pp * 8; }
  const int aoff = wm * 16384 + lane * 16;
  const int row = rb * 64 + wm * 16 + r16;
  f32x4 acc[2] = {(f32x4){0, 0, 0, 0}, (f32x4){0, 0, 0, 0}};
#pragma unroll
  for (int c = 0; c < 4; ++c) {
    if (c == 0) wait_bar<12>(); else if (c == 1) wait_bar<8>(); else if (c == 2) wait_bar<4>(); else wait_bar<0>();
#pragma unroll
    for (int k4 = 0; k4 < 4; ++k4) {
      const int ks = c * 4 + k4;
      const bf16x8 a = ld128(L + aoff + ks * 1024);
#pragma unroll
      for (int jj = 0; jj < 2; ++jj) acc[jj] = MFMA16(ldtr2(L + boff[jj] + ks * 4096, 512), a, acc[jj]);
    }
  }
  float s1 = 0.f, s2 = 0.f;
#pragma unroll
  for (int jj = 0; jj < 2; ++jj) {
    const int col = cb * 64 + (2 * wn + jj) * 16 + 4 * g;
    const u32x2 xv = *(const u32x2*)(p.x + ((size_t)row * DM + col) * 2);
    const unsigned w0 = cvtpk(bflo(xv[0]) + acc[jj][0], bfhi(xv[0]) + acc[jj][1]), w1 = cvtpk(bflo(xv[1]) + acc[jj][2], bfhi(xv[1]) + acc[jj][3]);
    const size_t off = ((((size_t)(row >> 4) * 16 + cb * 2 + wn) * 4 + jj * 2 + (g >> 1)) * 16 + r16) * 16 + (g & 1) * 8;
    *(u32x2*)(p.ws + WS_H + off) = (u32x2){w0, w1};
    const float a = bflo(w0), b = bfhi(w0), c = bflo(w1), d = bfhi(w1);
    s1 += (a + b) + (c + d); s2 += (a * a + b * b) + (c * c + d * d);
  }
  s1 += __shfl_xor(s1, 16); s1 += __shfl_xor(s1, 32); s2 += __shfl_xor(s2, 16); s2 += __shfl_xor(s2, 32);
  if (g == 0) *(f32x2*)(p.ws + WS_HST + ((size_t)row * 16 + cb * 2 + wn) * 8) = (f32x2){s1, s2};
}

__device__ __forceinline__ float gelu_tanh(float x) {
  const float u = x * (0.7978845608028654f + 0.035677408136300125f * x * x);
  const float e = __builtin_amdgcn_exp2f(u * -2.885390081777927f);
  return x * __builtin_amdgcn_rcpf(1.f + e);
}
__device__ __forceinline__ void p4_gateup(const Params& p, int mb, int fb, char* smem) {
  const int tid = threadIdx.x, lane = tid & 63, wid = rfl(tid >> 6), wm = wid >> 1, wn = wid & 1;
  const int r16 = lane & 15, g = lane >> 4;
  const unsigned lds0 = (unsigned)(size_t)(lds_ptr)smem;
  lds_cptr L = (lds_cptr)smem;
  constexpr int STG = 40960, ND = 3, NS = 8, ST_OFF = ND * STG;
  const int u0 = fb < 11 ? 3 * fb : 33 + 2 * (fb - 11), nu = fb < 11 ? 3 : 2;
  const char* ag = p.ws + WS_H + (size_t)(mb * 8 + wid) * 16384;
  const char* bg = ((wid >> 2) ? p.wu : p.wg) + (size_t)(wid & 3) * 16 * FF * 2;
  unsigned vb;
  { const int r = lane >> 2, c = lane & 3, j = (c >> 1) ^ ((r >> 3) & 1); vb = (unsigned)(r * FF + j * 16 + (c & 1) * 8) * 2u; }
#pragma unroll
  for (int i = 0; i < 2; ++i) glds16(p.ws + WS_HST + (size_t)mb * 16384 + (wid * 2 + i) * 1024, lane * 16, lds0 + ST_OFF + (wid * 2 + i) * 1024);
  auto issue = [&](int s) {
    const unsigned d = lds0 + (s % ND) * STG;
#pragma unroll
    for (int i = 0; i < 2; ++i) glds16(ag + (2 * s + i) * 1024, lane * 16, d + (wid * 2 + i) * 1024);
#pragma unroll
    for (int u = 0; u < 3; ++u) { const int uu = u0 + (u < nu ? u : 0); glds16(bg + (size_t)s * 64 * FF * 2 + uu * 64, vb, d + 16384 + (wid * 3 + u) * 1024); }
  };
  issue(0); issue(1);
  const int aoff = wm * 4096 + lane * 16;
  int boff[3];
#pragma unroll
  for (int i = 0; i < 3; ++i) {
    const int jt = 3 * wn + i, u = jt >> 1, j = jt & 1, q = (lane >> 2) & 3, pp = lane & 3;
    boff[i] = 16384 + (g >> 1) * 3072 + u * 1024 + (8 * (g & 1) + q) * 64 + ((j ^ (g & 1)) << 5) + pp * 8;
  }
  f32x4 acc[2][3][2], cs[2][3];
#pragma unroll
  for (int a = 0; a < 2; ++a)
#pragma unroll
    for (int i = 0; i < 3; ++i) { acc[a][i][0] = (f32x4){0, 0, 0, 0}; acc[a][i][1] = (f32x4){0, 0, 0, 0}; cs[a][i] = (f32x4){0, 0, 0, 0}; }
  const bf16x8 one = ones8();
#pragma unroll
  for (int s = 0; s < NS; ++s) {
    if (s < NS - 1) wait_bar<5>(); else wait_bar<0>();
    if (s + 2 < NS) issue(s + 2);
    lds_cptr B = L + (s % ND) * STG;
#pragma unroll
    for (int ks = 0; ks < 2; ++ks) {
      const int kk = 2 * s + ks;
      bf16x8 a[2];
      a[0] = ld128(B + aoff + ks * 1024); a[1] = ld128(B + aoff + 2048 + ks * 1024);
#pragma unroll
      for (int mat = 0; mat < 2; ++mat)
#pragma unroll
        for (int i = 0; i < 3; ++i) {
          const bf16x8 w = ldtr2(B + boff[i] + mat * 12288 + ks * 6144, 256);
          acc[mat][i][0] = MFMA16(w, a[0], acc[mat][i][0]);
          acc[mat][i][1] = MFMA16(w, a[1], acc[mat][i][1]);
          if ((kk & 3) == wm) cs[mat][i] = MFMA16(w, one, cs[mat][i]);
        }
    }
  }
  lds_bar();
  LAS float* csl = (LAS float*)smem;
  if (r16 == 0) {
#pragma unroll
    for (int mat = 0; mat < 2; ++mat)
#pragma unroll
      for (int i = 0; i < 3; ++i) *(LAS f32x4*)(csl + ((((wm * 2 + wn) * 2 + mat) * 3 + i) * 16 + 4 * g)) = cs[mat][i];
  }
  LAS float* ms = (LAS float*)(smem + 8192);
  { const int row = tid >> 2, part = tid & 3;
    const f32x4 v0 = *(const LAS f32x4*)(L + ST_OFF + row * 128 + part * 32), v1 = *(const LAS f32x4*)(L + ST_OFF + row * 128 + part * 32 + 16);
    float a = (v0[0] + v0[2]) + (v1[0] + v1[2]), b = (v0[1] + v0[3]) + (v1[1] + v1[3]);
    a += __shfl_xor(a, 1); a += __shfl_xor(a, 2); b += __shfl_xor(b, 1); b += __shfl_xor(b, 2);
    const float mean = a * (1.f / DM), var = fmaxf(b * (1.f / DM) - mean * mean, 0.f);
    if (part == 0) *(LAS f32x2*)(ms + row * 2) = (f32x2){mean, rsqrtf(var + EPS)}; }
  lds_bar();
#pragma unroll
  for (int i = 0; i < 3; ++i) {
    const int jt = 3 * wn + i, u = jt >> 1, j = jt & 1;
    f32x4 cg = (f32x4){0, 0, 0, 0}, cu = (f32x4){0, 0, 0, 0};
#pragma unroll
    for (int w4 = 0; w4 < 4; ++w4) {
      cg += *(LAS f32x4*)(csl + ((((w4 * 2 + wn) * 2 + 0) * 3 + i) * 16 + 4 * g));
      cu += *(LAS f32x4*)(csl + ((((w4 * 2 + wn) * 2 + 1) * 3 + i) * 16 + 4 * g));
    }
    if (u < nu) {
#pragma unroll
      for (int mt = 0; mt < 2; ++mt) {
        const int lrow = wm * 32 + mt * 16 + r16;
        const f32x2 st = *(LAS f32x2*)(ms + lrow * 2);
        const f32x4 ga = (acc[0][i][mt] - st.x * cg) * st.y, up = (acc[1][i][mt] - st.x * cu) * st.y;
        const float a0 = gelu_tanh(ga[0]) * up[0], a1 = gelu_tanh(ga[1]) * up[1], a2 = gelu_tanh(ga[2]) * up[2], a3 = gelu_tanh(ga[3]) * up[3];
        const size_t rt = (size_t)mb * 8 + wm * 2 + mt;
        const size_t off = (((rt * 43 + (u0 + u)) * 4 + j * 2 + (g >> 1)) * 16 + r16) * 16 + (g & 1) * 8;
        *(u32x2*)(p.ws + WS_ACT + off) = (u32x2){cvtpk(a0, a1), cvtpk(a2, a3)};
      }
    }
  }
}

__device__ __forceinline__ void p5_down(const Params& p, int rb, int cb, char* smem) {
  const int tid = threadIdx.x, lane = tid & 63, wid = rfl(tid >> 6), wm = wid >> 1, wn = wid & 1;
  const int r16 = lane & 15, g = lane >> 4;
  const unsigned lds0 = (unsigned)(size_t)(lds_ptr)smem;
  lds_cptr L = (lds_cptr)smem;
  constexpr int STG = 32768, ND = 4, NS = 11;
  const char* ag = p.ws + WS_ACT + (size_t)(rb * 4 + (wid >> 1)) * 43 * 1024;
  const char* bg = p.wd + cb * 128;
  const unsigned vb0 = voff_w128(lane, 0), vb1 = voff_w128(lane, 1);
  auto issue = [&](int s) {
    const unsigned d = lds0 + (s % ND) * STG;
#pragma unroll
    for (int i = 0; i < 2; ++i) { const int kl = (wid & 1) * 2 + i; int ks = s * 4 + kl; if (s == NS - 1) ks = ks > 42 ? 42 : ks; glds16(ag + ks * 1024, lane * 16, d + ((wid >> 1) * 4 + kl) * 1024); }
#pragma unroll
    for (int i = 0; i < 2; ++i) { int kg = s * 16 + wid * 2 + i; if (s == NS - 1) kg = kg > 171 ? 170 + i : kg; glds16(bg + (size_t)kg * 8 * DM * 2, i ? vb1 : vb0, d + 16384 + (wid * 2 + i) * 1024); }
  };
  issue(0); issue(1); issue(2);
  int boff[2];
  { const int q = (lane >> 2) & 3, pp = lane & 3, f = ((q >> 1) & 1) | ((g & 1) << 1);
    boff[0] = 16384 + g * 1024 + q * 128 + (((2 * wn) ^ f) << 5) + pp * 8; boff[1] = 16384 + g * 1024 + q * 128 + (((2 * wn + 1) ^ f) << 5) + pp * 8; }
  const int aoff = wm * 4096 + lane * 16;
  f32x4 acc[2] = {(f32x4){0, 0, 0, 0}, (f32x4){0, 0, 0, 0}};
#pragma unroll
  for (int s = 0; s < NS; ++s) {
    if (s <= NS - 3) wait_bar<8>(); else if (s == NS - 2) wait_bar<4>(); else wait_bar<0>();
    if (s + 3 < NS) issue(s + 3);
    lds_cptr B = L + (s % ND) * STG;
#pragma unroll
    for (int k4 = 0; k4 < 4; ++k4) {
      if (s * 4 + k4 < 43) {
        const bf16x8 a = ld128(B + aoff + k4 * 1024);
#pragma unroll
        for (int jj = 0; jj < 2; ++jj) acc[jj] = MFMA16(ldtr2(B + boff[jj] + k4 * 4096, 512), a, acc[jj]);
      }
    }
  }
  const int row = rb * 64 + wm * 16 + r16;
#pragma unroll
  for (int jj = 0; jj < 2; ++jj) {
    const int col = cb * 64 + (2 * wn + jj) * 16 + 4 * g;
    const size_t hoff = ((((size_t)(row >> 4) * 16 + cb * 2 + wn) * 4 + jj * 2 + (g >> 1)) * 16 + r16) * 16 + (g & 1) * 8;
    const u32x2 hv = *(const u32x2*)(p.ws + WS_H + hoff);
    *(u32x2*)(p.out + ((size_t)row * DM + col) * 2) =
        (u32x2){cvtpk(bflo(hv[0]) + acc[jj][0], bfhi(hv[0]) + acc[jj][1]), cvtpk(bflo(hv[1]) + acc[jj][2], bfhi(hv[1]) + acc[jj][3])};
  }
}

extern __shared__ __attribute__((aligned(16))) char smem_dyn[];

__global__ __launch_bounds__(512, 2) void k1(Params p) { const int b = blockIdx.x, x = b & 7, i = b >> 3; p1_qkv(p, (x >> 1) * 8 + (i & 7), (x & 1) * 4 + (i >> 3), smem_dyn); }
__global__ __launch_bounds__(512, 2) void k2(Params p) { const int b = blockIdx.x, x = b & 7, i = b >> 3; p2_attn(p, x * 4 + (i & 3), i >> 2, smem_dyn); }
__global__ __launch_bounds__(512, 2) void k3(Params p) { const int b = blockIdx.x, x = b & 7, i = b >> 3; p3_oproj(p, x * 4 + (i & 3), i >> 2, smem_dyn); }
__global__ __launch_bounds__(512, 2) void k4(Params p) { const int b = blockIdx.x, x = b & 7, i = b >> 3; p4_gateup(p, x * 2 + (i & 1), i >> 1, smem_dyn); }
__global__ __launch_bounds__(512, 2) void k5(Params p) { const int b = blockIdx.x, x = b & 7, i = b >> 3; p5_down(p, x * 4 + (i & 3), i >> 2, smem_dyn); }

extern "C" void kernel_launch(void* const* d_in, const int* in_sizes, int n_in, void* d_out, int out_size, void* d_ws, size_t ws_size, hipStream_t stream) {
  Params p;
  p.x = (const char*)d_in[0]; p.wqkv = (const char*)d_in[1]; p.wo = (const char*)d_in[2];
  p.wg = (const char*)d_in[4]; p.wu = (const char*)d_in[5]; p.wd = (const char*)d_in[6];
  p.out = (char*)d_out; p.ws = (char*)d_ws;
  constexpr int LDS = 160 * 1024;
  (void)hipFuncSetAttribute((const void*)k1, hipFuncAttributeMaxDynamicSharedMemorySize, LDS);
  (void)hipFuncSetAttribute((const void*)k2, hipFuncAttributeMaxDynamicSharedMemorySize, LDS);
  (void)hipFuncSetAttribute((const void*)k3, hipFuncAttributeMaxDynamicSharedMemorySize, LDS);
  (void)hipFuncSetAttribute((const void*)k4, hipFuncAttributeMaxDynamicSharedMemorySize, LDS);
  (void)hipFuncSetAttribute((const void*)k5, hipFuncAttributeMaxDynamicSharedMemorySize, LDS);
  k1<<<256, 512, 131072, stream>>>(p);
  k2<<<256, 512, A_BYTES, stream>>>(p);
  k3<<<256, 512, 131072, stream>>>(p);
  k4<<<256, 512, 3 * 40960 + 16384, stream>>>(p);
  k5<<<256, 512, 131072, stream>>>(p);
}
```

```cpp
#include <hip/hip_runtime.h>
#include <stdint.h>
#define ATTN_FAST_ASM \
  "ds_read_b128 v[168:171], %[ka]\n\t" \
  "ds_read_b128 v[172:175], %[ka] offset:1024\n\t" \
  "ds_read_b128 v[176:179], %[va]\n\t" \
  "ds_read_b128 v[184:187], %[va] offset:512\n\t" \
  "v_mov_b32 v164, 0\n\t" \
  "v_mov_b32 v165, 0\n\t" \
  "v_mov_b32 v166, 0\n\t" \
  "v_mov_b32 v167, 0\n\t" \
  "v_mov_b32 v188, 0\n\t" \
  "v_mov_b32 v189, 0\n\t" \
  "v_mov_b32 v190, 0\n\t" \
  "v_mov_b32 v191, 0\n\t" \
  "s_mov_b32 %[cnt], 0\n\t" \
  "s_waitcnt lgkmcnt(3)\n\t" \
  "v_mfma_f32_32x32x16_bf16 v[128:143], v[168:171], %[q], %[nc]\n\t" \
  "s_nop 7\n\t" \
  "L_attn_loop_%=:\n\t" \
  "s_cmp_lg_u32 %[cnt], 3\n\t" \
  "s_cbranch_scc1 L_attn_nobar_%=\n\t" \
  "s_waitcnt vmcnt(0)\n\t" \
  "s_barrier\n\t" \
  "L_attn_nobar_%=:\n\t" \
  "s_cmp_lg_u32 %[cnt], %[sw]\n\t" \
  "s_cbranch_scc1 L_attn_nosw_%=\n\t" \
  "s_setprio 0\n\t" \
  "L_attn_nosw_%=:\n\t" \
  "s_waitcnt lgkmcnt(2)\n\t" \
  "v_mfma_f32_32x32x16_bf16 v[144:159], v[172:175], %[q], %[nc]\n\t" \
  "ds_read_b128 v[168:171], %[ka] offset:2048\n\t" \
  "v_exp_f32 v128, v128\n\t" \
  "v_exp_f32 v129, v129\n\t" \
  "v_exp_f32 v130, v130\n\t" \
  "v_exp_f32 v131, v131\n\t" \
  "v_cvt_pk_bf16_f32 v160, v128, v129\n\t" \
  "v_exp_f32 v132, v132\n\t" \
  "v_exp_f32 v133, v133\n\t" \
  "v_cvt_pk_bf16_f32 v161, v130, v131\n\t" \
  "v_mfma_f32_32x32x16_bf16 %[o], v[188:191], v[164:167], %[o]\n\t" \
  "ds_read_b128 v[180:183], %[va] offset:1024\n\t" \
  "ds_read_b128 v[188:191], %[va] offset:1536\n\t" \
  "v_exp_f32 v134, v134\n\t" \
  "v_exp_f32 v135, v135\n\t" \
  "v_cvt_pk_bf16_f32 v162, v132, v133\n\t" \
  "v_exp_f32 v136, v136\n\t" \
  "v_exp_f32 v137, v137\n\t" \
  "v_cvt_pk_bf16_f32 v163, v134, v135\n\t" \
  "v_exp_f32 v138, v138\n\t" \
  "v_exp_f32 v139, v139\n\t" \
  "s_waitcnt lgkmcnt(4)\n\t" \
  "v_mfma_f32_32x32x16_bf16 %[o], v[176:179], v[160:163], %[o]\n\t" \
  "v_cvt_pk_bf16_f32 v164, v136, v137\n\t" \
  "v_exp_f32 v140, v140\n\t" \
  "v_exp_f32 v141, v141\n\t" \
  "v_cvt_pk_bf16_f32 v165, v138, v139\n\t" \
  "v_exp_f32 v142, v142\n\t" \
  "v_exp_f32 v143, v143\n\t" \
  "v_cvt_pk_bf16_f32 v166, v140, v141\n\t" \
  "v_cvt_pk_bf16_f32 v167, v142, v143\n\t" \
  "s_waitcnt lgkmcnt(2)\n\t" \
  "v_mfma_f32_32x32x16_bf16 v[128:143], v[168:171], %[q], %[nc]\n\t" \
  "ds_read_b128 v[172:175], %[ka] offset:3072\n\t" \
  "v_exp_f32 v144, v144\n\t" \
  "v_exp_f32 v145, v145\n\t" \
  "v_exp_f32 v146, v146\n\t" \
  "v_exp_f32 v147, v147\n\t" \
  "v_cvt_pk_bf16_f32 v160, v144, v145\n\t" \
  "v_exp_f32 v148, v148\n\t" \
  "v_exp_f32 v149, v149\n\t" \
  "v_cvt_pk_bf16_f32 v161, v146, v147\n\t" \
  "v_mfma_f32_32x32x16_bf16 %[o], v[184:187], v[164:167], %[o]\n\t" \
  "ds_read_b128 v[176:179], %[va] offset:2048\n\t" \
  "ds_read_b128 v[184:187], %[va] offset:2560\n\t" \
  "v_exp_f32 v150, v150\n\t" \
  "v_exp_f32 v151, v151\n\t" \
  "v_cvt_pk_bf16_f32 v162, v148, v149\n\t" \
  "v_exp_f32 v152, v152\n\t" \
  "v_exp_f32 v153, v153\n\t" \
  "v_cvt_pk_bf16_f32 v163, v150, v151\n\t" \
  "v_exp_f32 v154, v154\n\t" \
  "v_exp_f32 v155, v155\n\t" \
  "s_waitcnt lgkmcnt(4)\n\t" \
  "v_mfma_f32_32x32x16_bf16 %[o], v[180:183], v[160:163], %[o]\n\t" \
  "v_cvt_pk_bf16_f32 v164, v152, v153\n\t" \
  "v_exp_f32 v156, v156\n\t" \
  "v_exp_f32 v157, v157\n\t" \
  "v_cvt_pk_bf16_f32 v165, v154, v155\n\t" \
  "v_exp_f32 v158, v158\n\t" \
  "v_exp_f32 v159, v159\n\t" \
  "v_cvt_pk_bf16_f32 v166, v156, v157\n\t" \
  "v_cvt_pk_bf16_f32 v167, v158, v159\n\t" \
  "s_waitcnt lgkmcnt(2)\n\t" \
  "v_mfma_f32_32x32x16_bf16 v[144:159], v[172:175], %[q], %[nc]\n\t" \
  "ds_read_b128 v[168:171], %[ka] offset:4096\n\t" \
  "v_exp_f32 v128, v128\n\t" \
  "v_exp_f32 v129, v129\n\t" \
  "v_exp_f32 v130, v130\n\t" \
  "v_exp_f32 v131, v131\n\t" \
  "v_cvt_pk_bf16_f32 v160, v128, v129\n\t" \
  "v_exp_f32 v132, v132\n\t" \
  "v_exp_f32 v133, v133\n\t" \
  "v_cvt_pk_bf16_f32 v161, v130, v131\n\t" \
  "v_mfma_f32_32x32x16_bf16 %[o], v[188:191], v[164:167], %[o]\n\t" \
  "ds_read_b128 v[180:183], %[va] offset:3072\n\t" \
  "ds_read_b128 v[188:191], %[va] offset:3584\n\t" \
  "v_exp_f32 v134, v134\n\t" \
  "v_exp_f32 v135, v135\n\t" \
  "v_cvt_pk_bf16_f32 v162, v132, v133\n\t" \
  "v_exp_f32 v136, v136\n\t" \
  "v_exp_f32 v137, v137\n\t" \
  "v_cvt_pk_bf16_f32 v163, v134, v135\n\t" \
  "v_exp_f32 v138, v138\n\t" \
  "v_exp_f32 v139, v139\n\t" \
  "s_waitcnt lgkmcnt(4)\n\t" \
  "v_mfma_f32_32x32x16_bf16 %[o], v[176:179], v[160:163], %[o]\n\t" \
  "v_cvt_pk_bf16_f32 v164, v136, v137\n\t" \
  "v_exp_f32 v140, v140\n\t" \
  "v_exp_f32 v141, v141\n\t" \
  "v_cvt_pk_bf16_f32 v165, v138, v139\n\t" \
  "v_exp_f32 v142, v142\n\t" \
  "v_exp_f32 v143, v143\n\t" \
  "v_cvt_pk_bf16_f32 v166, v140, v141\n\t" \
  "v_cvt_pk_bf16_f32 v167, v142, v143\n\t" \
  "s_waitcnt lgkmcnt(2)\n\t" \
  "v_mfma_f32_32x32x16_bf16 v[128:143], v[168:171], %[q], %[nc]\n\t" \
  "ds_read_b128 v[172:175], %[ka] offset:5120\n\t" \
  "v_exp_f32 v144, v144\n\t" \
  "v_exp_f32 v145, v145\n\t" \
  "v_exp_f32 v146, v146\n\t" \
  "v_exp_f32 v147, v147\n\t" \
  "v_cvt_pk_bf16_f32 v160, v144, v145\n\t" \
  "v_exp_f32 v148, v148\n\t" \
  "v_exp_f32 v149, v149\n\t" \
  "v_cvt_pk_bf16_f32 v161, v146, v147\n\t" \
  "v_mfma_f32_32x32x16_bf16 %[o], v[184:187], v[164:167], %[o]\n\t" \
  "ds_read_b128 v[176:179], %[va] offset:4096\n\t" \
  "ds_read_b128 v[184:187], %[va] offset:4608\n\t" \
  "v_exp_f32 v150, v150\n\t" \
  "v_exp_f32 v151, v151\n\t" \
  "v_cvt_pk_bf16_f32 v162, v148, v149\n\t" \
  "v_exp_f32 v152, v152\n\t" \
  "v_exp_f32 v153, v153\n\t" \
  "v_cvt_pk_bf16_f32 v163, v150, v151\n\t" \
  "v_exp_f32 v154, v154\n\t" \
  "v_exp_f32 v155, v155\n\t" \
  "s_waitcnt lgkmcnt(4)\n\t" \
  "v_mfma_f32_32x32x16_bf16 %[o], v[180:183], v[160:163], %[o]\n\t" \
  "v_cvt_pk_bf16_f32 v164, v152, v153\n\t" \
  "v_exp_f32 v156, v156\n\t" \
  "v_exp_f32 v157, v157\n\t" \
  "v_cvt_pk_bf16_f32 v165, v154, v155\n\t" \
  "v_exp_f32 v158, v158\n\t" \
  "v_exp_f32 v159, v159\n\t" \
  "v_cvt_pk_bf16_f32 v166, v156, v157\n\t" \
  "v_cvt_pk_bf16_f32 v167, v158, v159\n\t" \
  "v_add_u32 %[ka], 0x1000, %[ka]\n\t" \
  "v_add_u32 %[va], %[vs], %[va]\n\t" \
  "s_add_u32 %[cnt], %[cnt], 1\n\t" \
  "s_cmp_lt_u32 %[cnt], 16\n\t" \
  "s_cbranch_scc1 L_attn_loop_%=\n\t" \
  "s_waitcnt lgkmcnt(0)\n\t" \
  "v_mfma_f32_32x32x16_bf16 %[o], v[188:191], v[164:167], %[o]\n\t" \
  "s_setprio 0\n\t" \
  "s_nop 15\n\t" \
  ""
#define ATTN_FAST_CLOBBERS "v128", "v129", "v130", "v131", "v132", "v133", "v134", "v135", "v136", "v137", "v138", "v139", "v140", "v141", "v142", "v143", "v144", "v145", "v146", "v147", "v148", "v149", "v150", "v151", "v152", "v153", "v154", "v155", "v156", "v157", "v158", "v159", "v160", "v161", "v162", "v163", "v164", "v165", "v166", "v167", "v168", "v169", "v170", "v171", "v172", "v173", "v174", "v175", "v176", "v177", "v178", "v179", "v180", "v181", "v182", "v183", "v184", "v185", "v186", "v187", "v188", "v189", "v190", "v191"

typedef short bf16x8 __attribute__((ext_vector_type(8)));
typedef short s16x4 __attribute__((ext_vector_type(4)));
typedef float f32x4 __attribute__((ext_vector_type(4)));
typedef float f32x2 __attribute__((ext_vector_type(2)));
typedef float f32x16 __attribute__((ext_vector_type(16)));
typedef unsigned u32x4 __attribute__((ext_vector_type(4)));
typedef unsigned u32x2 __attribute__((ext_vector_type(2)));
typedef __bf16 bf16x2_t __attribute__((ext_vector_type(2)));
#define LAS __attribute__((address_space(3)))
#define GAS __attribute__((address_space(1)))
typedef const LAS char* lds_cptr;
typedef LAS char* lds_ptr;
typedef GAS unsigned gu32;
typedef GAS unsigned long long gu64;
typedef __amdgpu_buffer_rsrc_t rsrc_t;

constexpr int SEQ = 2048, DM = 512, NH = 32, FF = 1376;
constexpr float EPS = 1e-5f;
constexpr float QSCALE = 0.25f * 1.4426950408889634f;

constexpr unsigned WS_Q = 0, WS_K = 2u << 20, WS_V = 4u << 20, WS_O = 6u << 20, WS_H = 8u << 20, WS_ACT = 10u << 20;
constexpr unsigned WS_KMAX = 16u << 20, WS_HST = (16u << 20) + (64u << 10);
constexpr unsigned WS_WOF = 17u << 20, WS_WDF = WS_WOF + 512 * 1024;
constexpr unsigned WS_FLAG = 20u << 20;
constexpr unsigned FL_EPOCH = 0, FL_TMO = 256, FL_1 = 1024, FL_2 = 4096, FL_3 = 8192, FL_4 = 12288;
constexpr unsigned WS_USED = 24u << 20;
constexpr size_t WS_DBG = 64u << 20;

#define TS(i) do {} while (0)
#define TS_BEGIN() do {} while (0)
#define TS_END() do {} while (0)
#define TSARG
#define TSPASS

struct Params {
  const char* x; const char* wqkv; const char* wo; const char* wg; const char* wu; const char* wd;
  char* out; char* ws;
};

#define MFMA16(a, b, c) __builtin_amdgcn_mfma_f32_16x16x32_bf16(a, b, c, 0, 0, 0)
#define MFMA32(a, b, c) __builtin_amdgcn_mfma_f32_32x32x16_bf16(a, b, c, 0, 0, 0)
#define PIN(x) asm volatile("" : "+v"(x))
#define SB() __builtin_amdgcn_sched_barrier(0)
#define RLX_AGENT __ATOMIC_RELAXED, __HIP_MEMORY_SCOPE_AGENT

template <int N> __device__ __forceinline__ void wait_bar() {
  asm volatile("s_waitcnt vmcnt(%0) lgkmcnt(0)\n\ts_barrier" ::"n"(N) : "memory");
}
template <int N> __device__ __forceinline__ void wait_vm() { asm volatile("s_waitcnt vmcnt(%0)" ::"n"(N) : "memory"); }
__device__ __forceinline__ void lds_bar() { asm volatile("s_waitcnt lgkmcnt(0)\n\ts_barrier" ::: "memory"); }
__device__ __forceinline__ void raw_bar() { asm volatile("s_barrier" ::: "memory"); }

template <bool SC1> __device__ __forceinline__ void glds16(const void* sbase, unsigned voff, unsigned lds_addr) {
  unsigned keep;
  if (SC1) asm volatile("s_mov_b32 %0, m0\n\ts_mov_b32 m0, %3\n\ts_nop 2\n\tglobal_load_lds_dwordx4 %1, %2 sc1\n\ts_mov_b32 m0, %0"
                        : "=&s"(keep) : "v"(voff), "s"(sbase), "s"(lds_addr) : "memory");
  else     asm volatile("s_mov_b32 %0, m0\n\ts_mov_b32 m0, %3\n\ts_nop 2\n\tglobal_load_lds_dwordx4 %1, %2\n\ts_mov_b32 m0, %0"
                        : "=&s"(keep) : "v"(voff), "s"(sbase), "s"(lds_addr) : "memory");
}
__device__ __forceinline__ void glds4_sc1(const void* sbase, unsigned voff, unsigned lds_addr) {
  unsigned keep;
  asm volatile("s_mov_b32 %0, m0\n\ts_mov_b32 m0, %3\n\ts_nop 2\n\tglobal_load_lds_dword %1, %2 sc1\n\ts_mov_b32 m0, %0"
               : "=&s"(keep) : "v"(voff), "s"(sbase), "s"(lds_addr) : "memory");
}
__device__ __forceinline__ bf16x8 ld128(lds_cptr p) { return *(const LAS bf16x8*)p; }
typedef short v4i16_t __attribute__((ext_vector_type(4)));
__device__ __forceinline__ s16x4 ldtr(lds_cptr p) {
  return __builtin_bit_cast(s16x4, __builtin_amdgcn_ds_read_tr16_b64_v4i16((LAS v4i16_t*)p));
}
__device__ __forceinline__ bf16x8 ldtr2(lds_cptr p, int second) {
  const s16x4 a = ldtr(p), b = ldtr(p + second);
  return (bf16x8){a[0], a[1], a[2], a[3], b[0], b[1], b[2], b[3]};
}
__device__ __forceinline__ unsigned cvtpk(float lo, float hi) {
  f32x2 v = {lo, hi}; bf16x2_t b = __builtin_convertvector(v, bf16x2_t); return __builtin_bit_cast(unsigned, b);
}
__device__ __forceinline__ float bflo(unsigned w) { return __uint_as_float(w << 16); }
__device__ __forceinline__ float bfhi(unsigned w) { return __uint_as_float(w & 0xffff0000u); }
__device__ __forceinline__ int rfl(int v) { return __builtin_amdgcn_readfirstlane(v); }
__device__ __forceinline__ bf16x8 ones8() { return (bf16x8){0x3F80, 0x3F80, 0x3F80, 0x3F80, 0x3F80, 0x3F80, 0x3F80, 0x3F80}; }
#define Z4 ((f32x4){0.f, 0.f, 0.f, 0.f})
__device__ __forceinline__ void st16(rsrc_t ws, unsigned off, u32x4 v) { __builtin_amdgcn_raw_buffer_store_b128(v, ws, off, 0, 16); }
__device__ __forceinline__ bf16x8 ldw16(rsrc_t ws, unsigned off) { return __builtin_bit_cast(bf16x8, __builtin_amdgcn_raw_buffer_load_b128(ws, off, 0, 16)); }
__device__ __forceinline__ u32x4 pair16(u32x2 X, u32x2 Y) {
  auto a = __builtin_amdgcn_permlane16_swap(X[0], Y[0], false, false);
  auto b = __builtin_amdgcn_permlane16_swap(X[1], Y[1], false, false);
  return (u32x4){a[0], b[0], a[1], b[1]};
}
__device__ __forceinline__ u32x4 pair32(u32x2 X, u32x2 Y) {
  auto a = __builtin_amdgcn_permlane32_swap(X[0], Y[0], false, false);
  auto b = __builtin_amdgcn_permlane32_swap(X[1], Y[1], false, false);
  return (u32x4){a[0], b[0], a[1], b[1]};
}

__device__ __forceinline__ void publish(char* ws, unsigned flag_off, unsigned tag) {
  if (threadIdx.x == 0) __hip_atomic_store((gu32*)(ws + WS_FLAG + flag_off), tag, RLX_AGENT);
}
template <int NW>
__device__ __forceinline__ void await(char* ws, unsigned flag_off, unsigned tag) {
  if (rfl(threadIdx.x >> 6) == 0) {
    const int lane = threadIdx.x & 63;
    gu32* f = (gu32*)(ws + WS_FLAG + flag_off);
    const unsigned long long t0 = __builtin_amdgcn_s_memrealtime();
    for (unsigned spins = 1;; ++spins) {
      bool ok = true;
      if (NW == 256) {
#pragma unroll
        for (int i = 0; i < 4; ++i) ok &= __hip_atomic_load(f + i * 64 + lane, RLX_AGENT) == tag;
      } else ok = __hip_atomic_load(f + (lane < NW ? lane : 0), RLX_AGENT) == tag;
      if (__all(ok)) break;
      if ((spins & 63u) == 0u && __builtin_amdgcn_s_memrealtime() - t0 > 20000000ull) {
        if (lane == 0) __hip_atomic_store((gu32*)(ws + WS_FLAG + FL_TMO), tag, RLX_AGENT);
        break;
      }
      __builtin_amdgcn_s_sleep(1);
    }
  }
  raw_bar();
}

struct F1 { bf16x8 a[4], w[3]; };
__device__ __forceinline__ void p1_qkv(const Params& p, rsrc_t ws, int b, int rb, int cb, char* smem TSARG) {
  const int tid = threadIdx.x, lane = tid & 63, wid = rfl(tid >> 6);
  const int r16 = lane & 15, g = lane >> 4;
  const unsigned lds0 = (unsigned)(size_t)(lds_ptr)smem;
  lds_cptr L = (lds_cptr)smem;
  constexpr int STG = 32768, ND = 4, NS = 8, RL = ND * STG;
  const int f = b * 8 + wid;
  const bool rl = f < 512 + 32 * 43;
  auto rl_issue = [&]() {
    const int fw = rl ? f : 0;
    const bool isd = fw >= 512; const int f2 = isd ? fw - 512 : fw, ct = isd ? f2 / 43 : f2 >> 4, ks = isd ? f2 - ct * 43 : f2 & 15;
    const char* src = (isd ? p.wd : p.wo) + ((size_t)ks * 32 * DM + ct * 16) * 2;
    const int t = lane >> 5, gg = (lane >> 3) & 3, q = (lane >> 1) & 3;
    glds16<false>(src, (unsigned)((8 * gg + 4 * t + q) * DM + (lane & 1) * 8) * 2u, lds0 + RL + wid * 1024);
  };
  f32x4 acc[3][4], cs[3], sx = Z4, sq = Z4;
  if (wid >= 4) {
    const int lw = wid - 4;
    const unsigned voffA = (unsigned)((16 * lw + (lane >> 3)) * DM + (((lane & 7) ^ (lane >> 3)) * 8)) * 2u;
    unsigned voffB[2];
#pragma unroll
    for (int i = 0; i < 2; ++i) { const int r = lane >> 3, c = lane & 7, ff = ((r >> 1) & 1) | (i << 1), j = (c >> 1) ^ ff;
      voffB[i] = (unsigned)((16 * lw + 8 * i + r) * (3 * DM) + j * 16 + (c & 1) * 8) * 2u; }
    const char* xa = p.x + (size_t)rb * 64 * DM * 2;
    const char* wb = p.wqkv + (size_t)cb * 64 * 2;
    auto issue = [&](int s) {
      const unsigned d = lds0 + (s % ND) * STG;
#pragma unroll
      for (int i = 0; i < 2; ++i) glds16<false>(xa + s * 128 + i * 8 * DM * 2, voffA, d + (2 * lw + i) * 1024);
#pragma unroll
      for (int i = 0; i < 2; ++i)
#pragma unroll
        for (int seg = 0; seg < 3; ++seg) glds16<false>(wb + (size_t)s * 64 * (3 * DM) * 2 + seg * DM * 2, voffB[i], d + 8192 + ((2 * lw + i) * 3 + seg) * 1024);
    };
    issue(0); raw_bar(); issue(1); raw_bar(); issue(2); raw_bar(); issue(3); rl_issue();
    wait_bar<25>();
#pragma unroll
    for (int j = 1; j < NS; ++j) {
      if (j == 1) wait_bar<17>(); else if (j == 2) wait_bar<17>(); else if (j == 3) wait_bar<17>(); else if (j <= 5) wait_bar<16>(); else if (j == 6) wait_bar<8>(); else wait_bar<0>();
      if (j - 1 + ND < NS) issue(j - 1 + ND);
    }
  } else {
    raw_bar(); raw_bar(); raw_bar(); rl_issue();
    int aoff[2][4];
#pragma unroll
    for (int ks = 0; ks < 2; ++ks)
#pragma unroll
      for (int i = 0; i < 4; ++i) aoff[ks][i] = ((wid + i) & 3) * 2048 + (r16 >> 3) * 1024 + (r16 & 7) * 128 + (((4 * ks + g) ^ (r16 & 7)) << 4);
    int boff;
    { const int q = (lane >> 2) & 3, pp = lane & 3, ff = ((q >> 1) & 1) | ((g & 1) << 1);
      boff = 8192 + g * 3072 + q * 128 + ((wid ^ ff) << 5) + pp * 8; }
#pragma unroll
    for (int i = 0; i < 3; ++i) { cs[i] = Z4;
#pragma unroll
      for (int j = 0; j < 4; ++j) acc[i][j] = Z4; }
    const bf16x8 one = ones8();
    F1 fa, fb;
    auto rd = [&](F1& fr, int s, int ks) {
      lds_cptr B = L + (s % ND) * STG;
#pragma unroll
      for (int seg = 0; seg < 3; ++seg) fr.w[seg] = ldtr2(B + boff + ks * 12288 + seg * 1024, 512);
#pragma unroll
      for (int i = 0; i < 4; ++i) fr.a[i] = ld128(B + aoff[ks][i]);
    };
    auto mm = [&](const F1& fr) {
#pragma unroll
      for (int i = 0; i < 4; ++i) {
        acc[0][i] = MFMA16(fr.w[0], fr.a[i], acc[0][i]);
        acc[1][i] = MFMA16(fr.w[1], fr.a[i], acc[1][i]);
        acc[2][i] = MFMA16(fr.a[i], fr.w[2], acc[2][i]);
      }
      cs[0] = MFMA16(fr.w[0], one, cs[0]); cs[1] = MFMA16(fr.w[1], one, cs[1]); cs[2] = MFMA16(one, fr.w[2], cs[2]);
      sx = MFMA16(one, fr.a[0], sx); sq = MFMA16(fr.a[0], fr.a[0], sq);
    };
    lds_bar();
    TS(1);
    rd(fa, 0, 0);
#pragma unroll
    for (int s = 0; s < NS; ++s) {
      rd(fb, s, 1); SB();
      mm(fa); SB();
      if (s + 1 < NS) { lds_bar(); rd(fa, s + 1, 0); SB(); }
      mm(fb); SB();
    }
  }
  TS(2);
  wait_bar<0>();
  if (rl) st16(ws, WS_WOF + (unsigned)f * 1024 + lane * 16, __builtin_bit_cast(u32x4, ldtr2(L + RL + wid * 1024 + lane * 8, 512)));
  LAS f32x2* ms = (LAS f32x2*)smem;
  if (wid < 4 && (r16 >> 2) == g) {
    const int e = r16 & 3; const float s2 = e == 0 ? sq[0] : e == 1 ? sq[1] : e == 2 ? sq[2] : sq[3];
    const float mean = sx[0] * (1.f / DM), var = fmaxf(s2 * (1.f / DM) - mean * mean, 0.f);
    ms[wid * 16 + r16] = (f32x2){mean, rsqrtf(var + EPS)};
  }
  lds_bar();
  if (wid < 4) {
    const int h = cb * 4 + wid;
    u32x2 qv[4], kv[4], vv[4];
    float kmx = 0.f;
#pragma unroll
    for (int i = 0; i < 4; ++i) {
      const int rt = (wid + i) & 3;
      const f32x2 st = ms[rt * 16 + r16];
      { const f32x4 v = (acc[0][i] - st.x * cs[0]) * (st.y * QSCALE); qv[i] = (u32x2){cvtpk(v[0], v[1]), cvtpk(v[2], v[3])}; }
      { const f32x4 v = (acc[1][i] - st.x * cs[1]) * st.y; kv[i] = (u32x2){cvtpk(v[0], v[1]), cvtpk(v[2], v[3])};
        float n2 = (v[0] * v[0] + v[1] * v[1]) + (v[2] * v[2] + v[3] * v[3]);
        n2 += __shfl_xor(n2, 16); n2 += __shfl_xor(n2, 32); kmx = fmaxf(kmx, n2); }
      { const f32x4 m01 = *(LAS f32x4*)(ms + rt * 16 + 4 * g), m23 = *(LAS f32x4*)(ms + rt * 16 + 4 * g + 2); const float cv = cs[2][0];
        vv[i] = (u32x2){cvtpk((acc[2][i][0] - m01[0] * cv) * m01[1], (acc[2][i][1] - m01[2] * cv) * m01[3]),
                        cvtpk((acc[2][i][2] - m23[0] * cv) * m23[1], (acc[2][i][3] - m23[2] * cv) * m23[3])}; }
    }
#pragma unroll
    for (int i = 0; i < 4; i += 2) {
      { const int rt = (wid + i + (g & 1)) & 3, grow = rb * 64 + rt * 16;
        const unsigned off = (((unsigned)h * 64 + (grow >> 5)) * 2 + (g >> 1)) * 512 + ((grow & 31) + r16) * 16;
        st16(ws, WS_Q + off, pair16(qv[i], qv[i + 1])); st16(ws, WS_K + off, pair16(kv[i], kv[i + 1])); }
      { const int rt = (wid + i + (g >> 1)) & 3, grow = rb * 64 + rt * 16;
        const unsigned off = ((((unsigned)h * 64 + (grow >> 5)) * 2 + (rt & 1)) * 2 + (g & 1)) * 256 + r16 * 16;
        st16(ws, WS_V + off, pair32(vv[i], vv[i + 1])); }
    }
#pragma unroll
    for (int o = 1; o < 16; o <<= 1) kmx = fmaxf(kmx, __shfl_xor(kmx, o));
    if (lane == 0) __hip_atomic_store((gu32*)(p.ws + WS_KMAX) + h * 32 + rb, __float_as_uint(kmx), RLX_AGENT);
  }
}

constexpr int A_K = 0, A_V = 65536, A_ONE = 131072, A_Q = A_ONE + 5120, A_KM = A_Q + 8192, A_BYTES = A_KM + 256;
constexpr float SAFE_BOUND = 48.f;
constexpr int PRIO_SWITCH = 12;

__device__ __forceinline__ void p2_attn(const Params& p, rsrc_t ws, int h, int qr, char* smem, int wct  , bf16x8 (&wof)[8] TSARG) {
  const int tid = threadIdx.x, lane = tid & 63, wid = rfl(tid >> 6);
  const int r32 = lane & 31, hh = lane >> 5;
  const unsigned lds0 = (unsigned)(size_t)(lds_ptr)smem;
  lds_cptr L = (lds_cptr)smem;
  const char* kg = p.ws + WS_K + (size_t)h * 65536;
  const char* vg = p.ws + WS_V + (size_t)h * 65536;
  const unsigned vo = lane * 16;
  glds16<true>(p.ws + WS_Q + ((size_t)h * 64 + qr * 8 + wid) * 1024, vo, lds0 + A_Q + wid * 1024);
  glds4_sc1(p.ws + WS_KMAX + h * 128, (lane & 31) * 4, lds0 + A_KM);
#pragma unroll
  for (int c = 0; c < 4; ++c) {
#pragma unroll
    for (int i = 0; i < 2; ++i) {
      const int t = c * 16 + wid * 2 + i;
      glds16<true>(kg + t * 1024, vo, lds0 + A_K + t * 1024);
      glds16<true>(vg + t * 1024, vo, lds0 + A_V + t * 1024);
    }
    if (c == 0) raw_bar();
  }
#pragma unroll
  for (int i = 0; i < 8; ++i) wof[i] = ldw16(ws, WS_WOF + ((unsigned)wct * 16 + 2 * i + (wid >> 2)) * 1024 + lane * 16);
  { const unsigned o1 = (tid & 31) < 2 ? 0x3F803F80u : 0u;
    *(LAS u32x2*)(smem + A_ONE + tid * 8) = (u32x2){o1, o1};
    if (tid < 128) *(LAS u32x2*)(smem + A_ONE + 4096 + tid * 8) = (u32x2){o1, o1}; }
  wait_bar<20>();
  TS(4);
  const bf16x8 qf = ld128(L + A_Q + wid * 1024 + lane * 16);
  float c;
  { float km = *(const LAS float*)(L + A_KM + lane * 4);
#pragma unroll
    for (int o = 1; o < 32; o <<= 1) km = fmaxf(km, __shfl_xor(km, o));
    float q2 = 0.f;
#pragma unroll
    for (int j = 0; j < 8; ++j) { const float v = __uint_as_float((unsigned)(unsigned short)qf[j] << 16); q2 += v * v; }
    q2 += __shfl_xor(q2, 32);
    c = sqrtf(q2 * km) * 1.02f + 0.01f; }
  const bool fast = __all(c <= SAFE_BOUND);
  const bool vlane = r32 < 16;
  int vaddr = (int)lds0 + (vlane ? A_V : A_ONE) + hh * 256 + (r32 & 15) * 16;
  const int vstep = vlane ? 4096 : 0;
  int kaddr = (int)lds0 + A_K + lane * 16;
  f32x16 o;
#pragma unroll
  for (int i = 0; i < 16; ++i) o[i] = 0.f;
  if (fast) {
    f32x16 negc;
#pragma unroll
    for (int i = 0; i < 16; ++i) negc[i] = -c;
    int cnt;
    int sw = 99;
    if (wid < 4) { __builtin_amdgcn_s_setprio(2); sw = PRIO_SWITCH; } else __builtin_amdgcn_s_setprio(1);
    asm volatile(ATTN_FAST_ASM
                 : [o] "+v"(o), [ka] "+v"(kaddr), [va] "+v"(vaddr), [cnt] "=&s"(cnt)
                 : [q] "v"(qf), [nc] "v"(negc), [vs] "v"(vstep), [sw] "s"(sw)
                 : "memory", "scc", ATTN_FAST_CLOBBERS);
  } else {
    float m = -INFINITY;
    vaddr -= (int)lds0; kaddr -= (int)lds0;
#pragma unroll 1
    for (int t4 = 0; t4 < 16; ++t4) {
      if (t4 == 3) wait_bar<0>();
#pragma unroll 1
      for (int u = 0; u < 4; ++u) {
        const int t = t4 * 4 + u;
        f32x16 z;
#pragma unroll
        for (int i = 0; i < 16; ++i) z[i] = 0.f;
        const f32x16 s = MFMA32(ld128(L + kaddr + t * 1024), qf, z);
        float mx = s[0];
#pragma unroll
        for (int i = 1; i < 16; ++i) mx = fmaxf(mx, s[i]);
        mx = fmaxf(mx, __shfl_xor(mx, 32));
        const float mn = fmaxf(m, mx);
        const float al = __builtin_amdgcn_exp2f(m - mn);
        m = mn;
#pragma unroll
        for (int i = 0; i < 16; ++i) o[i] *= al;
        u32x4 p0, p1;
#pragma unroll
        for (int i = 0; i < 4; ++i) {
          p0[i] = cvtpk(__builtin_amdgcn_exp2f(s[2 * i] - mn), __builtin_amdgcn_exp2f(s[2 * i + 1] - mn));
          p1[i] = cvtpk(__builtin_amdgcn_exp2f(s[8 + 2 * i] - mn), __builtin_amdgcn_exp2f(s[8 + 2 * i + 1] - mn));
        }
        const bf16x8 v0 = ld128(L + vaddr + u * 1024), v1 = ld128(L + vaddr + u * 1024 + 512);
        o = MFMA32(v0, __builtin_bit_cast(bf16x8, p0), o);
        o = MFMA32(v1, __builtin_bit_cast(bf16x8, p1), o);
      }
      vaddr += vstep;
    }
  }
#pragma unroll
  for (int i = 0; i < 8; ++i) PIN(wof[i]);
  TS(5);
  const float l = __shfl(o[8], r32);
  const float inv = 1.f / l;
  const u32x4 ov = pair32((u32x2){cvtpk(o[0] * inv, o[1] * inv), cvtpk(o[2] * inv, o[3] * inv)}, (u32x2){cvtpk(o[4] * inv, o[5] * inv), cvtpk(o[6] * inv, o[7] * inv)});
  const int row = qr * 256 + wid * 32 + r32;
  st16(ws, WS_O + ((((unsigned)(row >> 4) * 16 + (h >> 1)) * 4 + (h & 1) * 2 + hh) * 16 + (row & 15)) * 16, ov);
}

constexpr int g64_wait(int j, int NS, int D) { return j == 0 ? (((D < NS ? D : NS) - 1) * 4) : ((((j - 2 + D) < (NS - 1) ? (j - 2 + D) : (NS - 1)) - j) * 4); }
template <int NKS, int NF, int ND, int TS0>
__device__ __forceinline__ void gemm64(const char* ag  , const bf16x8 (&wf)[NF], char* smem, f32x4 (&res)[2] TSARG) {
  const int tid = threadIdx.x, lane = tid & 63, wid = rfl(tid >> 6), wn = wid & 3, kh = wid >> 2;
  const unsigned lds0 = (unsigned)(size_t)(lds_ptr)smem;
  lds_cptr L = (lds_cptr)smem;
  constexpr int STG = 32768, NS = (NKS + 7) / 8;
  auto issue = [&](int s) {
    const unsigned d = lds0 + (s % ND) * STG + ((wid >> 1) * 8 + (wid & 1) * 4) * 1024;
#pragma unroll
    for (int i = 0; i < 4; ++i) { int ks = s * 8 + (wid & 1) * 4 + i; if (s == NS - 1 && NKS % 8) ks = ks > NKS - 1 ? NKS - 1 : ks; glds16<true>(ag + ks * 1024, lane * 16, d + i * 1024); }
  };
#pragma unroll
  for (int s = 0; s < ND; ++s) { issue(s); if (s + 1 < ND) raw_bar(); }
  const int aoff = kh * 1024 + lane * 16;
  f32x4 acc[4] = {Z4, Z4, Z4, Z4};
  bf16x8 fa[2][4], fb[2][4];
  auto rd = [&](bf16x8 (&f)[2][4], int s, int hf) {
    lds_cptr B = L + (s % ND) * STG + aoff + hf * 4096;
#pragma unroll
    for (int i = 0; i < 2; ++i)
#pragma unroll
      for (int rt = 0; rt < 4; ++rt) f[i][rt] = ld128(B + rt * 8192 + i * 2048);
  };
  auto mm = [&](const bf16x8 (&f)[2][4], int f0) {
#pragma unroll
    for (int i = 0; i < 2; ++i)
      if (f0 + i < NF) {
#pragma unroll
        for (int rt = 0; rt < 4; ++rt) acc[rt] = MFMA16(wf[f0 + i], f[i][rt], acc[rt]);
      }
  };
  wait_bar<g64_wait(0, NS, ND)>();
  TS(TS0);
  rd(fa, 0, 0);
#pragma unroll
  for (int s = 0; s < NS; ++s) {
    const bool h1 = 4 * s + 2 < NF;
    if (h1) rd(fb, s, 1);
    SB();
    mm(fa, 4 * s); SB();
    if (s + 1 < NS) {
      switch (s + 1) {
        case 1: wait_bar<g64_wait(1, NS, ND)>(); break; case 2: wait_bar<g64_wait(2, NS, ND)>(); break;
        case 3: wait_bar<g64_wait(3, NS, ND)>(); break; case 4: wait_bar<g64_wait(4, NS, ND)>(); break;
        default: wait_bar<g64_wait(5, NS, ND)>(); break;
      }
      if (s + ND < NS) issue(s + ND);
      rd(fa, s + 1, 0); SB();
    }
    if (h1) mm(fb, 4 * s + 2);
    SB();
  }
  TS(TS0 + 1);
  lds_bar();
  LAS f32x4* X = (LAS f32x4*)smem;
  if (kh == 0) { X[((wn * 2 + 1) * 2 + 0) * 64 + lane] = acc[2]; X[((wn * 2 + 1) * 2 + 1) * 64 + lane] = acc[3]; }
  else         { X[((wn * 2 + 0) * 2 + 0) * 64 + lane] = acc[0]; X[((wn * 2 + 0) * 2 + 1) * 64 + lane] = acc[1]; }
  lds_bar();
  const f32x4 o0 = X[((wn * 2 + kh) * 2 + 0) * 64 + lane], o1 = X[((wn * 2 + kh) * 2 + 1) * 64 + lane];
  if (kh == 0) { res[0] = acc[0] + o0; res[1] = acc[1] + o1; } else { res[0] = acc[2] + o0; res[1] = acc[3] + o1; }
}

constexpr int G_B = 0, G_BS = 24576, G_A = 4 * G_BS, G_AS = 16384, G_ST = G_A + 3 * G_AS;
__device__ __forceinline__ float gelu_tanh(float x) {
  const float u = x * (0.7978845608028654f + 0.035677408136300125f * x * x);
  const float e = __builtin_amdgcn_exp2f(u * -2.885390081777927f);
  return x * __builtin_amdgcn_rcpf(1.f + e);
}
struct WIssue {
  const char* base; unsigned voff[3];
  __device__ __forceinline__ void init(const Params& p, int mat, int kg, int u0, int nu, int lane) {
    base = (mat ? p.wu : p.wg) + ((size_t)kg * 16 * FF + u0 * 32) * 2;
#pragma unroll
    for (int i = 0; i < 3; ++i) {
      const int ch = i * 64 + lane, r = ch / 12, cp = ch - r * 12;
      int c = cp ^ (((r >> 3) & 1) << 1);
      if (c >= nu * 4) c -= 4 * (3 - nu) + 4 * (nu == 2 ? 0 : 0), c = c < 0 ? 0 : c;
      voff[i] = (unsigned)(r * FF + c * 8) * 2u;
    }
  }
  __device__ __forceinline__ void issue(int s, unsigned dst) const {
#pragma unroll
    for (int i = 0; i < 3; ++i) glds16<false>(base + (size_t)s * 64 * FF * 2, voff[i], dst + i * 1024);
  }
};
struct F4 { bf16x8 a[4], w[2][3]; };
__device__ __forceinline__ void p4_gateup(const Params& p, rsrc_t ws, int mb, int fb, char* smem, int wct, bf16x8 (&wdf)[22] TSARG) {
  const int tid = threadIdx.x, lane = tid & 63, wid = rfl(tid >> 6);
  const int r16 = lane & 15, g = lane >> 4, kh = wid >> 2;
  const unsigned lds0 = (unsigned)(size_t)(lds_ptr)smem;
  lds_cptr L = (lds_cptr)smem;
  constexpr int NS = 8;
  const int u0 = fb < 11 ? 3 * fb : 33 + 2 * (fb - 11), nu = fb < 11 ? 3 : 2;
  auto wd_load = [&](int i) { const int ks = 2 * i + kh; wdf[i] = ldw16(ws, WS_WDF + ((unsigned)wct * 43 + (ks > 42 ? 42 : ks)) * 1024 + lane * 16); };
  if (wid >= 4) {
    const int lw = wid - 4;
    WIssue wg, wu; wg.init(p, 0, lw, u0, nu, lane); wu.init(p, 1, lw, u0, nu, lane);
    const char* ag = p.ws + WS_H + (size_t)(mb * 8 + 2 * lw) * 16384;
    auto issueA = [&](int s) {
      const unsigned d = lds0 + G_A + (s % 3) * G_AS + lw * 4096;
#pragma unroll
      for (int i = 0; i < 4; ++i) glds16<true>(ag + (i >> 1) * 16384 + (2 * s + (i & 1)) * 1024, lane * 16, d + i * 1024);
    };
    auto issueB = [&](int s) { const unsigned d = lds0 + G_B + (s & 3) * G_BS + lw * 3072; wg.issue(s, d); wu.issue(s, d + 12288); };
    issueA(0); issueA(1); issueA(2);
#pragma unroll
    for (int i = 0; i < 2; ++i) glds16<true>(p.ws + WS_HST + (size_t)mb * 8192 + (lw * 2 + i) * 1024, lane * 16, lds0 + G_ST + (lw * 2 + i) * 1024);
    wait_bar<10>();
    wait_bar<6>();  issueA(3); issueB(4);
    wait_bar<10>(); issueA(4); issueB(5);
    wait_bar<16>(); issueA(5); issueB(6);
    wait_bar<16>(); issueA(6); issueB(7);
    wait_bar<16>(); issueA(7);
    wait_bar<10>();
    wait_bar<0>();
#pragma unroll
    for (int i = 0; i < 22; ++i) wd_load(i);
    { const int row = tid - 256;
      if (row < 128) {
        const f32x4 v0 = *(const LAS f32x4*)(L + G_ST + row * 64), v1 = *(const LAS f32x4*)(L + G_ST + row * 64 + 16), v2 = *(const LAS f32x4*)(L + G_ST + row * 64 + 32), v3 = *(const LAS f32x4*)(L + G_ST + row * 64 + 48);
        const float a = ((v0[0] + v0[2]) + (v1[0] + v1[2])) + ((v2[0] + v2[2]) + (v3[0] + v3[2])), b = ((v0[1] + v0[3]) + (v1[1] + v1[3])) + ((v2[1] + v2[3]) + (v3[1] + v3[3]));
        const float mean = a * (1.f / DM), var = fmaxf(b * (1.f / DM) - mean * mean, 0.f);
        *(LAS f32x2*)(smem + G_ST + 8192 + row * 8) = (f32x2){mean, rsqrtf(var + EPS)};
      } }
    lds_bar();
    lds_bar();
  } else {
    const int wm = wid >> 1, wn = wid & 1;
    const int aoff = G_A + wm * 8192 + lane * 16;
    int boff[3];
#pragma unroll
    for (int i = 0; i < 3; ++i) {
      const int jt = 3 * wn + i, q = (lane >> 2) & 3, pp = lane & 3;
      boff[i] = G_B + (g >> 1) * 3072 + (8 * (g & 1) + q) * 192 + ((jt ^ (g & 1)) << 5) + pp * 8;
    }
    const int m0 = wm * 12288, m1 = (wm ^ 1) * 12288;
    f32x4 acc[2][3][4], cs[3];
#pragma unroll
    for (int i = 0; i < 3; ++i) { cs[i] = Z4;
#pragma unroll
      for (int j = 0; j < 4; ++j) { acc[0][i][j] = Z4; acc[1][i][j] = Z4; } }
    const bf16x8 one = ones8();
    F4 fa, fb2;
    auto rd = [&](F4& f, int s, int ks) {
      lds_cptr B = L + (s & 3) * G_BS, A = L + (s % 3) * G_AS;
#pragma unroll
      for (int i = 0; i < 4; ++i) f.a[i] = ld128(A + aoff + i * 2048 + ks * 1024);
#pragma unroll
      for (int i = 0; i < 3; ++i) { f.w[0][i] = ldtr2(B + boff[i] + m0 + ks * 6144, 768); f.w[1][i] = ldtr2(B + boff[i] + m1 + ks * 6144, 768); }
    };
    auto mm = [&](const F4& f) {
#pragma unroll
      for (int m = 0; m < 2; ++m)
#pragma unroll
        for (int i = 0; i < 3; ++i)
#pragma unroll
          for (int j = 0; j < 4; ++j) acc[m][i][j] = MFMA16(f.w[m][i], f.a[j], acc[m][i][j]);
#pragma unroll
      for (int i = 0; i < 3; ++i) cs[i] = MFMA16(f.w[0][i], one, cs[i]);
    };
    lds_bar();
    TS(11);
    rd(fa, 0, 0);
#pragma unroll
    for (int s = 0; s < NS; ++s) {
      rd(fb2, s, 1); SB();
      mm(fa); SB();
      if (s + 1 < NS) { lds_bar(); rd(fa, s + 1, 0); SB(); }
      mm(fb2); SB();
    }
    TS(12);
#pragma unroll
    for (int i = 0; i < 11; ++i) wd_load(i);
    LAS float* csl = (LAS float*)smem;
    lds_bar();
    if (r16 == 0) {
#pragma unroll
      for (int i = 0; i < 3; ++i) *(LAS f32x4*)(csl + (((wm * 2 + wn) * 3 + i) * 16 + 4 * g)) = cs[i];
    }
    lds_bar();
    const LAS f32x2* ms = (const LAS f32x2*)(smem + G_ST + 8192);
    f32x2 st[4];
#pragma unroll
    for (int j = 0; j < 4; ++j) st[j] = ms[wm * 64 + j * 16 + r16];
#pragma unroll
    for (int i = 0; i < 3; ++i) {
      const int jt = 3 * wn + i, u = jt >> 1, jj = jt & 1;
      const f32x4 cg = *(LAS f32x4*)(csl + ((0 * 2 + wn) * 3 + i) * 16 + 4 * g), cu = *(LAS f32x4*)(csl + ((1 * 2 + wn) * 3 + i) * 16 + 4 * g);
      u32x2 av[4];
#pragma unroll
      for (int j = 0; j < 4; ++j) {
        const f32x4 ag2 = wm ? acc[1][i][j] : acc[0][i][j], au = wm ? acc[0][i][j] : acc[1][i][j];
        const f32x4 ga = (ag2 - st[j].x * cg) * st[j].y, up = (au - st[j].x * cu) * st[j].y;
        av[j] = (u32x2){cvtpk(gelu_tanh(ga[0]) * up[0], gelu_tanh(ga[1]) * up[1]), cvtpk(gelu_tanh(ga[2]) * up[2], gelu_tanh(ga[3]) * up[3])};
      }
      if (u < nu) {
#pragma unroll
        for (int j = 0; j < 4; j += 2) {
          const unsigned rt = (unsigned)mb * 8 + wm * 4 + j + (g & 1);
          st16(ws, WS_ACT + (((rt * 43 + (u0 + u)) * 4 + jj * 2 + (g >> 1)) * 16 + r16) * 16, pair16(av[j], av[j + 1]));
        }
      }
    }
    SB();
#pragma unroll
    for (int i = 11; i < 22; ++i) wd_load(i);
  }
}

extern __shared__ __attribute__((aligned(16))) char smem_dyn[];

__global__ __launch_bounds__(512, 2) void mega(Params p) {
  char* smem = smem_dyn;
  const int b = blockIdx.x, x = b & 7, i5 = b >> 3;
  const int tid = threadIdx.x, lane = tid & 63, wid = rfl(tid >> 6);
  const int r16 = lane & 15, g = lane >> 4, wn = wid & 3, kh = wid >> 2;
  TS_BEGIN();
  const rsrc_t ws = __builtin_amdgcn_make_buffer_rsrc(p.ws, 0, WS_USED, 0x00020000);
  const unsigned epoch = __hip_atomic_load((gu32*)(p.ws + WS_FLAG + FL_EPOCH), RLX_AGENT);

  p1_qkv(p, ws, b, (x >> 1) * 8 + (i5 & 7), (x & 1) * 4 + (i5 >> 3), smem TSPASS);
  const unsigned tag = (unsigned)rfl((int)(epoch * 0x9E3779B1u + 0x7F4A7C15u));
  wait_bar<0>();
  publish(p.ws, FL_1 + b * 4, tag);
  TS(3);
  await<256>(p.ws, FL_1, tag);
  if (b == 0 && tid == 0) __hip_atomic_store((gu32*)(p.ws + WS_FLAG + FL_EPOCH), tag, RLX_AGENT);

  const int rb = x * 4 + (i5 & 3), cb = i5 >> 2;
  const int wct = cb * 4 + wn;
  bf16x8 wof[8];
  const int h = x * 4 + (i5 & 3), qr = i5 >> 2;
  p2_attn(p, ws, h, qr, smem, wct, wof TSPASS);
  const int mb = x * 2 + (i5 & 1), fb = i5 >> 1;
  u32x2 xv[2];
  const int col = cb * 64 + wn * 16 + 4 * g;
#pragma unroll
  for (int i = 0; i < 2; ++i) xv[i] = *(const u32x2*)(p.x + ((size_t)(rb * 64 + (2 * kh + i) * 16 + r16) * DM + col) * 2);
  lds_bar();
  { const int u0 = fb < 11 ? 3 * fb : 33 + 2 * (fb - 11), nu = fb < 11 ? 3 : 2;
    WIssue w; w.init(p, wid >> 2, wid & 3, u0, nu, lane);
    const unsigned lds0 = (unsigned)(size_t)(lds_ptr)smem;
#pragma unroll
    for (int s = 0; s < 4; ++s) w.issue(s, lds0 + G_B + s * G_BS + (wid >> 2) * 12288 + (wid & 3) * 3072); }
  wait_bar<12>();
  publish(p.ws, FL_2 + (qr * 32 + h) * 4, tag);
  TS(6);

  await<32>(p.ws, FL_2 + (rb >> 2) * 128, tag);
  TS(7);
  PIN(xv[0]); PIN(xv[1]);
  u32x2 hv[2];
  {
    f32x4 res[2];
    gemm64<16, 8, 2, 8>(p.ws + WS_O + (size_t)(rb * 4 + (wid >> 1)) * 16384, wof, smem + G_A, res TSPASS);
    LAS f32x2* S = (LAS f32x2*)(smem + G_A + 16384);
#pragma unroll
    for (int i = 0; i < 2; ++i) {
      hv[i] = (u32x2){cvtpk(bflo(xv[i][0]) + res[i][0], bfhi(xv[i][0]) + res[i][1]), cvtpk(bflo(xv[i][1]) + res[i][2], bfhi(xv[i][1]) + res[i][3])};
      const float a = bflo(hv[i][0]), bb = bfhi(hv[i][0]), c = bflo(hv[i][1]), d = bfhi(hv[i][1]);
      float s1 = (a + bb) + (c + d), s2 = (a * a + bb * bb) + (c * c + d * d);
      s1 += __shfl_xor(s1, 16); s1 += __shfl_xor(s1, 32); s2 += __shfl_xor(s2, 16); s2 += __shfl_xor(s2, 32);
      if (g == 0) S[((2 * kh + i) * 16 + r16) * 4 + wn] = (f32x2){s1, s2};
    }
    { const unsigned rt = (unsigned)rb * 4 + 2 * kh + (g & 1);
      st16(ws, WS_H + (((rt * 16 + cb * 2 + (wn >> 1)) * 4 + (wn & 1) * 2 + (g >> 1)) * 16 + r16) * 16, pair16(hv[0], hv[1])); }
    lds_bar();
    if (tid < 64) {
      const f32x2 a = S[tid * 4], bb = S[tid * 4 + 1], c = S[tid * 4 + 2], d = S[tid * 4 + 3];
      const float s1 = (a.x + bb.x) + (c.x + d.x), s2 = (a.y + bb.y) + (c.y + d.y);
      __hip_atomic_store((gu64*)(p.ws + WS_HST + ((size_t)(rb * 64 + tid) * 8 + cb) * 8), ((unsigned long long)__float_as_uint(s2) << 32) | __float_as_uint(s1), RLX_AGENT);
    }
  }
  wait_bar<0>();
  publish(p.ws, FL_3 + (rb >> 1) * 128 + ((rb & 1) * 8 + cb) * 4, tag);
  TS(9);

  await<16>(p.ws, FL_3 + mb * 128, tag);
  TS(10);
  bf16x8 wdf[22];
  p4_gateup(p, ws, mb, fb, smem, wct, wdf TSPASS);
  if (wid >= 4) wait_bar<22>(); else wait_bar<11>();
  publish(p.ws, FL_4 + mb * 128 + fb * 4, tag);
  TS(13);

  await<16>(p.ws, FL_4 + (rb >> 1) * 128, tag);
  TS(14);
  if (kh) wdf[21] = (bf16x8){0, 0, 0, 0, 0, 0, 0, 0};
#pragma unroll
  for (int i = 0; i < 22; ++i) PIN(wdf[i]);
  {
    f32x4 res[2];
    gemm64<43, 22, 5, 15>(p.ws + WS_ACT + (size_t)(rb * 4 + (wid >> 1)) * 43 * 1024, wdf, smem, res TSPASS);
#pragma unroll
    for (int i = 0; i < 2; ++i) {
      const int row = rb * 64 + (2 * kh + i) * 16 + r16;
      *(u32x2*)(p.out + ((size_t)row * DM + col) * 2) =
          (u32x2){cvtpk(bflo(hv[i][0]) + res[i][0], bfhi(hv[i][0]) + res[i][1]), cvtpk(bflo(hv[i][1]) + res[i][2], bfhi(hv[i][1]) + res[i][3])};
    }
  }
  TS_END();
}

extern "C" void kernel_launch(void* const* d_in, const int* in_sizes, int n_in, void* d_out, int out_size, void* d_ws, size_t ws_size, hipStream_t stream) {
  Params p;
  p.x = (const char*)d_in[0]; p.wqkv = (const char*)d_in[1]; p.wo = (const char*)d_in[2];
  p.wg = (const char*)d_in[4]; p.wu = (const char*)d_in[5]; p.wd = (const char*)d_in[6];
  p.out = (char*)d_out; p.ws = (char*)d_ws;
  constexpr int LDS = 160 * 1024;
  (void)hipFuncSetAttribute((const void*)mega, hipFuncAttributeMaxDynamicSharedMemorySize, LDS);
  mega<<<256, 512, LDS, stream>>>(p);
}
```

```cpp
#include <hip/hip_runtime.h>
#include <stdint.h>
#define ATTN_FAST_ASM \
  "s_mov_b32 %[keep], m0\n\t" \
  "ds_read_b128 v[168:171], %[ka]\n\t" \
  "ds_read_b128 v[172:175], %[ka] offset:1024\n\t" \
  "ds_read_b128 v[176:179], %[va]\n\t" \
  "ds_read_b128 v[184:187], %[va] offset:512\n\t" \
  "v_mov_b32 v164, 0\n\t" \
  "v_mov_b32 v165, 0\n\t" \
  "v_mov_b32 v166, 0\n\t" \
  "v_mov_b32 v167, 0\n\t" \
  "v_mov_b32 v188, 0\n\t" \
  "v_mov_b32 v189, 0\n\t" \
  "v_mov_b32 v190, 0\n\t" \
  "v_mov_b32 v191, 0\n\t" \
  "s_waitcnt lgkmcnt(3)\n\t" \
  "v_mfma_f32_32x32x16_bf16 v[128:143], v[168:171], %[q], %[nc]\n\t" \
  "s_nop 10\n\t" \
  "s_waitcnt lgkmcnt(2)\n\t" \
  "v_mfma_f32_32x32x16_bf16 v[144:159], v[172:175], %[q], %[nc]\n\t" \
  "ds_read_b128 v[168:171], %[ka] offset:2048\n\t" \
  "s_mov_b32 m0, %[ld]\n\t" \
  "v_exp_f32 v128, v128\n\t" \
  "v_exp_f32 v129, v129\n\t" \
  "global_load_lds_dwordx4 %[go], %[kg] sc1\n\t" \
  "v_exp_f32 v130, v130\n\t" \
  "v_exp_f32 v131, v131\n\t" \
  "v_cvt_pk_bf16_f32 v160, v128, v129\n\t" \
  "v_exp_f32 v132, v132\n\t" \
  "v_exp_f32 v133, v133\n\t" \
  "v_cvt_pk_bf16_f32 v161, v130, v131\n\t" \
  "v_mfma_f32_32x32x16_bf16 %[o], v[188:191], v[164:167], %[o]\n\t" \
  "ds_read_b128 v[180:183], %[va] offset:1024\n\t" \
  "ds_read_b128 v[188:191], %[va] offset:1536\n\t" \
  "s_add_u32 m0, %[ld], 0x10000\n\t" \
  "v_exp_f32 v134, v134\n\t" \
  "v_exp_f32 v135, v135\n\t" \
  "global_load_lds_dwordx4 %[go], %[vg] sc1\n\t" \
  "s_add_u32 %[ld], %[ld], 0x2000\n\t" \
  "v_cvt_pk_bf16_f32 v162, v132, v133\n\t" \
  "v_exp_f32 v136, v136\n\t" \
  "v_exp_f32 v137, v137\n\t" \
  "v_cvt_pk_bf16_f32 v163, v134, v135\n\t" \
  "v_add_u32 %[go], 0x2000, %[go]\n\t" \
  "v_exp_f32 v138, v138\n\t" \
  "v_exp_f32 v139, v139\n\t" \
  "s_waitcnt lgkmcnt(4)\n\t" \
  "v_mfma_f32_32x32x16_bf16 %[o], v[176:179], v[160:163], %[o]\n\t" \
  "v_cvt_pk_bf16_f32 v164, v136, v137\n\t" \
  "v_exp_f32 v140, v140\n\t" \
  "v_exp_f32 v141, v141\n\t" \
  "v_cvt_pk_bf16_f32 v165, v138, v139\n\t" \
  "v_exp_f32 v142, v142\n\t" \
  "v_exp_f32 v143, v143\n\t" \
  "v_cvt_pk_bf16_f32 v166, v140, v141\n\t" \
  "v_cvt_pk_bf16_f32 v167, v142, v143\n\t" \
  "s_waitcnt lgkmcnt(2)\n\t" \
  "v_mfma_f32_32x32x16_bf16 v[128:143], v[168:171], %[q], %[nc]\n\t" \
  "ds_read_b128 v[172:175], %[ka] offset:3072\n\t" \
  "s_mov_b32 m0, %[ld]\n\t" \
  "v_exp_f32 v144, v144\n\t" \
  "v_exp_f32 v145, v145\n\t" \
  "global_load_lds_dwordx4 %[go], %[kg] sc1\n\t" \
  "v_exp_f32 v146, v146\n\t" \
  "v_exp_f32 v147, v147\n\t" \
  "v_cvt_pk_bf16_f32 v160, v144, v145\n\t" \
  "v_exp_f32 v148, v148\n\t" \
  "v_exp_f32 v149, v149\n\t" \
  "v_cvt_pk_bf16_f32 v161, v146, v147\n\t" \
  "v_mfma_f32_32x32x16_bf16 %[o], v[184:187], v[164:167], %[o]\n\t" \
  "ds_read_b128 v[176:179], %[va] offset:2048\n\t" \
  "ds_read_b128 v[184:187], %[va] offset:2560\n\t" \
  "s_add_u32 m0, %[ld], 0x10000\n\t" \
  "v_exp_f32 v150, v150\n\t" \
  "v_exp_f32 v151, v151\n\t" \
  "global_load_lds_dwordx4 %[go], %[vg] sc1\n\t" \
  "s_add_u32 %[ld], %[ld], 0x2000\n\t" \
  "v_cvt_pk_bf16_f32 v162, v148, v149\n\t" \
  "v_exp_f32 v152, v152\n\t" \
  "v_exp_f32 v153, v153\n\t" \
  "v_cvt_pk_bf16_f32 v163, v150, v151\n\t" \
  "v_add_u32 %[go], 0x2000, %[go]\n\t" \
  "v_exp_f32 v154, v154\n\t" \
  "v_exp_f32 v155, v155\n\t" \
  "s_waitcnt lgkmcnt(4)\n\t" \
  "v_mfma_f32_32x32x16_bf16 %[o], v[180:183], v[160:163], %[o]\n\t" \
  "v_cvt_pk_bf16_f32 v164, v152, v153\n\t" \
  "v_exp_f32 v156, v156\n\t" \
  "v_exp_f32 v157, v157\n\t" \
  "v_cvt_pk_bf16_f32 v165, v154, v155\n\t" \
  "v_exp_f32 v158, v158\n\t" \
  "v_exp_f32 v159, v159\n\t" \
  "v_cvt_pk_bf16_f32 v166, v156, v157\n\t" \
  "v_cvt_pk_bf16_f32 v167, v158, v159\n\t" \
  "s_waitcnt lgkmcnt(2)\n\t" \
  "v_mfma_f32_32x32x16_bf16 v[144:159], v[172:175], %[q], %[nc]\n\t" \
  "ds_read_b128 v[168:171], %[ka] offset:4096\n\t" \
  "s_mov_b32 m0, %[ld]\n\t" \
  "v_exp_f32 v128, v128\n\t" \
  "v_exp_f32 v129, v129\n\t" \
  "global_load_lds_dwordx4 %[go], %[kg] sc1\n\t" \
  "v_exp_f32 v130, v130\n\t" \
  "v_exp_f32 v131, v131\n\t" \
  "v_cvt_pk_bf16_f32 v160, v128, v129\n\t" \
  "v_exp_f32 v132, v132\n\t" \
  "v_exp_f32 v133, v133\n\t" \
  "v_cvt_pk_bf16_f32 v161, v130, v131\n\t" \
  "v_mfma_f32_32x32x16_bf16 %[o], v[188:191], v[164:167], %[o]\n\t" \
  "ds_read_b128 v[180:183], %[va] offset:3072\n\t" \
  "ds_read_b128 v[188:191], %[va] offset:3584\n\t" \
  "s_add_u32 m0, %[ld], 0x10000\n\t" \
  "v_exp_f32 v134, v134\n\t" \
  "v_exp_f32 v135, v135\n\t" \
  "global_load_lds_dwordx4 %[go], %[vg] sc1\n\t" \
  "s_add_u32 %[ld], %[ld], 0x2000\n\t" \
  "v_cvt_pk_bf16_f32 v162, v132, v133\n\t" \
  "v_exp_f32 v136, v136\n\t" \
  "v_exp_f32 v137, v137\n\t" \
  "v_cvt_pk_bf16_f32 v163, v134, v135\n\t" \
  "v_add_u32 %[go], 0x2000, %[go]\n\t" \
  "v_exp_f32 v138, v138\n\t" \
  "v_exp_f32 v139, v139\n\t" \
  "s_waitcnt lgkmcnt(4)\n\t" \
  "v_mfma_f32_32x32x16_bf16 %[o], v[176:179], v[160:163], %[o]\n\t" \
  "v_cvt_pk_bf16_f32 v164, v136, v137\n\t" \
  "v_exp_f32 v140, v140\n\t" \
  "v_exp_f32 v141, v141\n\t" \
  "v_cvt_pk_bf16_f32 v165, v138, v139\n\t" \
  "v_exp_f32 v142, v142\n\t" \
  "v_exp_f32 v143, v143\n\t" \
  "v_cvt_pk_bf16_f32 v166, v140, v141\n\t" \
  "v_cvt_pk_bf16_f32 v167, v142, v143\n\t" \
  "s_waitcnt lgkmcnt(2)\n\t" \
  "v_mfma_f32_32x32x16_bf16 v[128:143], v[168:171], %[q], %[nc]\n\t" \
  "ds_read_b128 v[172:175], %[ka] offset:5120\n\t" \
  "s_mov_b32 m0, %[ld]\n\t" \
  "v_exp_f32 v144, v144\n\t" \
  "v_exp_f32 v145, v145\n\t" \
  "global_load_lds_dwordx4 %[go], %[kg] sc1\n\t" \
  "v_exp_f32 v146, v146\n\t" \
  "v_exp_f32 v147, v147\n\t" \
  "v_cvt_pk_bf16_f32 v160, v144, v145\n\t" \
  "v_exp_f32 v148, v148\n\t" \
  "v_exp_f32 v149, v149\n\t" \
  "v_cvt_pk_bf16_f32 v161, v146, v147\n\t" \
  "v_mfma_f32_32x32x16_bf16 %[o], v[184:187], v[164:167], %[o]\n\t" \
  "ds_read_b128 v[176:179], %[va] offset:4096\n\t" \
  "ds_read_b128 v[184:187], %[va] offset:4608\n\t" \
  "s_add_u32 m0, %[ld], 0x10000\n\t" \
  "v_exp_f32 v150, v150\n\t" \
  "v_exp_f32 v151, v151\n\t" \
  "global_load_lds_dwordx4 %[go], %[vg] sc1\n\t" \
  "s_add_u32 %[ld], %[ld], 0x2000\n\t" \
  "v_cvt_pk_bf16_f32 v162, v148, v149\n\t" \
  "v_exp_f32 v152, v152\n\t" \
  "v_exp_f32 v153, v153\n\t" \
  "v_cvt_pk_bf16_f32 v163, v150, v151\n\t" \
  "v_add_u32 %[go], 0x2000, %[go]\n\t" \
  "v_exp_f32 v154, v154\n\t" \
  "v_exp_f32 v155, v155\n\t" \
  "s_waitcnt lgkmcnt(4)\n\t" \
  "v_mfma_f32_32x32x16_bf16 %[o], v[180:183], v[160:163], %[o]\n\t" \
  "v_cvt_pk_bf16_f32 v164, v152, v153\n\t" \
  "v_exp_f32 v156, v156\n\t" \
  "v_exp_f32 v157, v157\n\t" \
  "v_cvt_pk_bf16_f32 v165, v154, v155\n\t" \
  "v_exp_f32 v158, v158\n\t" \
  "v_exp_f32 v159, v159\n\t" \
  "v_cvt_pk_bf16_f32 v166, v156, v157\n\t" \
  "v_cvt_pk_bf16_f32 v167, v158, v159\n\t" \
  "v_add_u32 %[ka], 0x1000, %[ka]\n\t" \
  "v_add_u32 %[va], %[vs], %[va]\n\t" \
  "s_waitcnt lgkmcnt(2)\n\t" \
  "v_mfma_f32_32x32x16_bf16 v[144:159], v[172:175], %[q], %[nc]\n\t" \
  "ds_read_b128 v[168:171], %[ka] offset:2048\n\t" \
  "s_mov_b32 m0, %[ld]\n\t" \
  "v_exp_f32 v128, v128\n\t" \
  "v_exp_f32 v129, v129\n\t" \
  "global_load_lds_dwordx4 %[go], %[kg] sc1\n\t" \
  "v_exp_f32 v130, v130\n\t" \
  "v_exp_f32 v131, v131\n\t" \
  "v_cvt_pk_bf16_f32 v160, v128, v129\n\t" \
  "v_exp_f32 v132, v132\n\t" \
  "v_exp_f32 v133, v133\n\t" \
  "v_cvt_pk_bf16_f32 v161, v130, v131\n\t" \
  "v_mfma_f32_32x32x16_bf16 %[o], v[188:191], v[164:167], %[o]\n\t" \
  "ds_read_b128 v[180:183], %[va] offset:1024\n\t" \
  "ds_read_b128 v[188:191], %[va] offset:1536\n\t" \
  "s_add_u32 m0, %[ld], 0x10000\n\t" \
  "v_exp_f32 v134, v134\n\t" \
  "v_exp_f32 v135, v135\n\t" \
  "global_load_lds_dwordx4 %[go], %[vg] sc1\n\t" \
  "s_add_u32 %[ld], %[ld], 0x2000\n\t" \
  "v_cvt_pk_bf16_f32 v162, v132, v133\n\t" \
  "v_exp_f32 v136, v136\n\t" \
  "v_exp_f32 v137, v137\n\t" \
  "v_cvt_pk_bf16_f32 v163, v134, v135\n\t" \
  "v_add_u32 %[go], 0x2000, %[go]\n\t" \
  "v_exp_f32 v138, v138\n\t" \
  "v_exp_f32 v139, v139\n\t" \
  "s_waitcnt lgkmcnt(4)\n\t" \
  "v_mfma_f32_32x32x16_bf16 %[o], v[176:179], v[160:163], %[o]\n\t" \
  "v_cvt_pk_bf16_f32 v164, v136, v137\n\t" \
  "v_exp_f32 v140, v140\n\t" \
  "v_exp_f32 v141, v141\n\t" \
  "v_cvt_pk_bf16_f32 v165, v138, v139\n\t" \
  "v_exp_f32 v142, v142\n\t" \
  "v_exp_f32 v143, v143\n\t" \
  "v_cvt_pk_bf16_f32 v166, v140, v141\n\t" \
  "v_cvt_pk_bf16_f32 v167, v142, v143\n\t" \
  "s_waitcnt lgkmcnt(2)\n\t" \
  "v_mfma_f32_32x32x16_bf16 v[128:143], v[168:171], %[q], %[nc]\n\t" \
  "ds_read_b128 v[172:175], %[ka] offset:3072\n\t" \
  "s_mov_b32 m0, %[ld]\n\t" \
  "v_exp_f32 v144, v144\n\t" \
  "v_exp_f32 v145, v145\n\t" \
  "global_load_lds_dwordx4 %[go], %[kg] sc1\n\t" \
  "v_exp_f32 v146, v146\n\t" \
  "v_exp_f32 v147, v147\n\t" \
  "v_cvt_pk_bf16_f32 v160, v144, v145\n\t" \
  "v_exp_f32 v148, v148\n\t" \
  "v_exp_f32 v149, v149\n\t" \
  "v_cvt_pk_bf16_f32 v161, v146, v147\n\t" \
  "v_mfma_f32_32x32x16_bf16 %[o], v[184:187], v[164:167], %[o]\n\t" \
  "ds_read_b128 v[176:179], %[va] offset:2048\n\t" \
  "ds_read_b128 v[184:187], %[va] offset:2560\n\t" \
  "s_add_u32 m0, %[ld], 0x10000\n\t" \
  "v_exp_f32 v150, v150\n\t" \
  "v_exp_f32 v151, v151\n\t" \
  "global_load_lds_dwordx4 %[go], %[vg] sc1\n\t" \
  "s_add_u32 %[ld], %[ld], 0x2000\n\t" \
  "v_cvt_pk_bf16_f32 v162, v148, v149\n\t" \
  "v_exp_f32 v152, v152\n\t" \
  "v_exp_f32 v153, v153\n\t" \
  "v_cvt_pk_bf16_f32 v163, v150, v151\n\t" \
  "v_add_u32 %[go], 0x2000, %[go]\n\t" \
  "v_exp_f32 v154, v154\n\t" \
  "v_exp_f32 v155, v155\n\t" \
  "s_waitcnt lgkmcnt(4)\n\t" \
  "v_mfma_f32_32x32x16_bf16 %[o], v[180:183], v[160:163], %[o]\n\t" \
  "v_cvt_pk_bf16_f32 v164, v152, v153\n\t" \
  "v_exp_f32 v156, v156\n\t" \
  "v_exp_f32 v157, v157\n\t" \
  "v_cvt_pk_bf16_f32 v165, v154, v155\n\t" \
  "v_exp_f32 v158, v158\n\t" \
  "v_exp_f32 v159, v159\n\t" \
  "v_cvt_pk_bf16_f32 v166, v156, v157\n\t" \
  "v_cvt_pk_bf16_f32 v167, v158, v159\n\t" \
  "s_waitcnt lgkmcnt(2)\n\t" \
  "v_mfma_f32_32x32x16_bf16 v[144:159], v[172:175], %[q], %[nc]\n\t" \
  "ds_read_b128 v[168:171], %[ka] offset:4096\n\t" \
  "v_exp_f32 v128, v128\n\t" \
  "v_exp_f32 v129, v129\n\t" \
  "v_exp_f32 v130, v130\n\t" \
  "v_exp_f32 v131, v131\n\t" \
  "v_cvt_pk_bf16_f32 v160, v128, v129\n\t" \
  "v_exp_f32 v132, v132\n\t" \
  "v_exp_f32 v133, v133\n\t" \
  "v_cvt_pk_bf16_f32 v161, v130, v131\n\t" \
  "v_mfma_f32_32x32x16_bf16 %[o], v[188:191], v[164:167], %[o]\n\t" \
  "ds_read_b128 v[180:183], %[va] offset:3072\n\t" \
  "ds_read_b128 v[188:191], %[va] offset:3584\n\t" \
  "v_exp_f32 v134, v134\n\t" \
  "v_exp_f32 v135, v135\n\t" \
  "v_cvt_pk_bf16_f32 v162, v132, v133\n\t" \
  "v_exp_f32 v136, v136\n\t" \
  "v_exp_f32 v137, v137\n\t" \
  "v_cvt_pk_bf16_f32 v163, v134, v135\n\t" \
  "v_exp_f32 v138, v138\n\t" \
  "v_exp_f32 v139, v139\n\t" \
  "s_waitcnt lgkmcnt(4)\n\t" \
  "v_mfma_f32_32x32x16_bf16 %[o], v[176:179], v[160:163], %[o]\n\t" \
  "v_cvt_pk_bf16_f32 v164, v136, v137\n\t" \
  "v_exp_f32 v140, v140\n\t" \
  "v_exp_f32 v141, v141\n\t" \
  "v_cvt_pk_bf16_f32 v165, v138, v139\n\t" \
  "v_exp_f32 v142, v142\n\t" \
  "v_exp_f32 v143, v143\n\t" \
  "v_cvt_pk_bf16_f32 v166, v140, v141\n\t" \
  "v_cvt_pk_bf16_f32 v167, v142, v143\n\t" \
  "s_waitcnt lgkmcnt(2)\n\t" \
  "v_mfma_f32_32x32x16_bf16 v[128:143], v[168:171], %[q], %[nc]\n\t" \
  "ds_read_b128 v[172:175], %[ka] offset:5120\n\t" \
  "v_exp_f32 v144, v144\n\t" \
  "v_exp_f32 v145, v145\n\t" \
  "v_exp_f32 v146, v146\n\t" \
  "v_exp_f32 v147, v147\n\t" \
  "v_cvt_pk_bf16_f32 v160, v144, v145\n\t" \
  "v_exp_f32 v148, v148\n\t" \
  "v_exp_f32 v149, v149\n\t" \
  "v_cvt_pk_bf16_f32 v161, v146, v147\n\t" \
  "v_mfma_f32_32x32x16_bf16 %[o], v[184:187], v[164:167], %[o]\n\t" \
  "ds_read_b128 v[176:179], %[va] offset:4096\n\t" \
  "ds_read_b128 v[184:187], %[va] offset:4608\n\t" \
  "v_exp_f32 v150, v150\n\t" \
  "v_exp_f32 v151, v151\n\t" \
  "v_cvt_pk_bf16_f32 v162, v148, v149\n\t" \
  "v_exp_f32 v152, v152\n\t" \
  "v_exp_f32 v153, v153\n\t" \
  "v_cvt_pk_bf16_f32 v163, v150, v151\n\t" \
  "v_exp_f32 v154, v154\n\t" \
  "v_exp_f32 v155, v155\n\t" \
  "s_waitcnt lgkmcnt(4)\n\t" \
  "v_mfma_f32_32x32x16_bf16 %[o], v[180:183], v[160:163], %[o]\n\t" \
  "v_cvt_pk_bf16_f32 v164, v152, v153\n\t" \
  "v_exp_f32 v156, v156\n\t" \
  "v_exp_f32 v157, v157\n\t" \
  "v_cvt_pk_bf16_f32 v165, v154, v155\n\t" \
  "v_exp_f32 v158, v158\n\t" \
  "v_exp_f32 v159, v159\n\t" \
  "v_cvt_pk_bf16_f32 v166, v156, v157\n\t" \
  "v_cvt_pk_bf16_f32 v167, v158, v159\n\t" \
  "v_add_u32 %[ka], 0x1000, %[ka]\n\t" \
  "v_add_u32 %[va], %[vs], %[va]\n\t" \
  "s_mov_b32 m0, %[keep]\n\t" \
  "s_mov_b32 %[cnt], 2\n\t" \
  "s_waitcnt vmcnt(0)\n\t" \
  "s_barrier\n\t" \
  "s_cmp_eq_u32 %[sw], 99\n\t" \
  "s_cbranch_scc1 L_attn_y_%=\n\t" \
  "s_setprio 2\n\t" \
  "s_branch L_attn_loop_%=\n\t" \
  "L_attn_y_%=:\n\t" \
  "s_setprio 1\n\t" \
  "L_attn_loop_%=:\n\t" \
  "s_cmp_lg_u32 %[cnt], %[sw]\n\t" \
  "s_cbranch_scc1 L_attn_nosw_%=\n\t" \
  "s_setprio 0\n\t" \
  "L_attn_nosw_%=:\n\t" \
  "s_waitcnt lgkmcnt(2)\n\t" \
  "v_mfma_f32_32x32x16_bf16 v[144:159], v[172:175], %[q], %[nc]\n\t" \
  "ds_read_b128 v[168:171], %[ka] offset:2048\n\t" \
  "v_exp_f32 v128, v128\n\t" \
  "v_exp_f32 v129, v129\n\t" \
  "v_exp_f32 v130, v130\n\t" \
  "v_exp_f32 v131, v131\n\t" \
  "v_cvt_pk_bf16_f32 v160, v128, v129\n\t" \
  "v_exp_f32 v132, v132\n\t" \
  "v_exp_f32 v133, v133\n\t" \
  "v_cvt_pk_bf16_f32 v161, v130, v131\n\t" \
  "v_mfma_f32_32x32x16_bf16 %[o], v[188:191], v[164:167], %[o]\n\t" \
  "ds_read_b128 v[180:183], %[va] offset:1024\n\t" \
  "ds_read_b128 v[188:191], %[va] offset:1536\n\t" \
  "v_exp_f32 v134, v134\n\t" \
  "v_exp_f32 v135, v135\n\t" \
  "v_cvt_pk_bf16_f32 v162, v132, v133\n\t" \
  "v_exp_f32 v136, v136\n\t" \
  "v_exp_f32 v137, v137\n\t" \
  "v_cvt_pk_bf16_f32 v163, v134, v135\n\t" \
  "v_exp_f32 v138, v138\n\t" \
  "v_exp_f32 v139, v139\n\t" \
  "s_waitcnt lgkmcnt(4)\n\t" \
  "v_mfma_f32_32x32x16_bf16 %[o], v[176:179], v[160:163], %[o]\n\t" \
  "v_cvt_pk_bf16_f32 v164, v136, v137\n\t" \
  "v_exp_f32 v140, v140\n\t" \
  "v_exp_f32 v141, v141\n\t" \
  "v_cvt_pk_bf16_f32 v165, v138, v139\n\t" \
  "v_exp_f32 v142, v142\n\t" \
  "v_exp_f32 v143, v143\n\t" \
  "v_cvt_pk_bf16_f32 v166, v140, v141\n\t" \
  "v_cvt_pk_bf16_f32 v167, v142, v143\n\t" \
  "s_waitcnt lgkmcnt(2)\n\t" \
  "v_mfma_f32_32x32x16_bf16 v[128:143], v[168:171], %[q], %[nc]\n\t" \
  "ds_read_b128 v[172:175], %[ka] offset:3072\n\t" \
  "v_exp_f32 v144, v144\n\t" \
  "v_exp_f32 v145, v145\n\t" \
  "v_exp_f32 v146, v146\n\t" \
  "v_exp_f32 v147, v147\n\t" \
  "v_cvt_pk_bf16_f32 v160, v144, v145\n\t" \
  "v_exp_f32 v148, v148\n\t" \
  "v_exp_f32 v149, v149\n\t" \
  "v_cvt_pk_bf16_f32 v161, v146, v147\n\t" \
  "v_mfma_f32_32x32x16_bf16 %[o], v[184:187], v[164:167], %[o]\n\t" \
  "ds_read_b128 v[176:179], %[va] offset:2048\n\t" \
  "ds_read_b128 v[184:187], %[va] offset:2560\n\t" \
  "v_exp_f32 v150, v150\n\t" \
  "v_exp_f32 v151, v151\n\t" \
  "v_cvt_pk_bf16_f32 v162, v148, v149\n\t" \
  "v_exp_f32 v152, v152\n\t" \
  "v_exp_f32 v153, v153\n\t" \
  "v_cvt_pk_bf16_f32 v163, v150, v151\n\t" \
  "v_exp_f32 v154, v154\n\t" \
  "v_exp_f32 v155, v155\n\t" \
  "s_waitcnt lgkmcnt(4)\n\t" \
  "v_mfma_f32_32x32x16_bf16 %[o], v[180:183], v[160:163], %[o]\n\t" \
  "v_cvt_pk_bf16_f32 v164, v152, v153\n\t" \
  "v_exp_f32 v156, v156\n\t" \
  "v_exp_f32 v157, v157\n\t" \
  "v_cvt_pk_bf16_f32 v165, v154, v155\n\t" \
  "v_exp_f32 v158, v158\n\t" \
  "v_exp_f32 v159, v159\n\t" \
  "v_cvt_pk_bf16_f32 v166, v156, v157\n\t" \
  "v_cvt_pk_bf16_f32 v167, v158, v159\n\t" \
  "s_waitcnt lgkmcnt(2)\n\t" \
  "v_mfma_f32_32x32x16_bf16 v[144:159], v[172:175], %[q], %[nc]\n\t" \
  "ds_read_b128 v[168:171], %[ka] offset:4096\n\t" \
  "v_exp_f32 v128, v128\n\t" \
  "v_exp_f32 v129, v129\n\t" \
  "v_exp_f32 v130, v130\n\t" \
  "v_exp_f32 v131, v131\n\t" \
  "v_cvt_pk_bf16_f32 v160, v128, v129\n\t" \
  "v_exp_f32 v132, v132\n\t" \
  "v_exp_f32 v133, v133\n\t" \
  "v_cvt_pk_bf16_f32 v161, v130, v131\n\t" \
  "v_mfma_f32_32x32x16_bf16 %[o], v[188:191], v[164:167], %[o]\n\t" \
  "ds_read_b128 v[180:183], %[va] offset:3072\n\t" \
  "ds_read_b128 v[188:191], %[va] offset:3584\n\t" \
  "v_exp_f32 v134, v134\n\t" \
  "v_exp_f32 v135, v135\n\t" \
  "v_cvt_pk_bf16_f32 v162, v132, v133\n\t" \
  "v_exp_f32 v136, v136\n\t" \
  "v_exp_f32 v137, v137\n\t" \
  "v_cvt_pk_bf16_f32 v163, v134, v135\n\t" \
  "v_exp_f32 v138, v138\n\t" \
  "v_exp_f32 v139, v139\n\t" \
  "s_waitcnt lgkmcnt(4)\n\t" \
  "v_mfma_f32_32x32x16_bf16 %[o], v[176:179], v[160:163], %[o]\n\t" \
  "v_cvt_pk_bf16_f32 v164, v136, v137\n\t" \
  "v_exp_f32 v140, v140\n\t" \
  "v_exp_f32 v141, v141\n\t" \
  "v_cvt_pk_bf16_f32 v165, v138, v139\n\t" \
  "v_exp_f32 v142, v142\n\t" \
  "v_exp_f32 v143, v143\n\t" \
  "v_cvt_pk_bf16_f32 v166, v140, v141\n\t" \
  "v_cvt_pk_bf16_f32 v167, v142, v143\n\t" \
  "s_waitcnt lgkmcnt(2)\n\t" \
  "v_mfma_f32_32x32x16_bf16 v[128:143], v[168:171], %[q], %[nc]\n\t" \
  "ds_read_b128 v[172:175], %[ka] offset:5120\n\t" \
  "v_exp_f32 v144, v144\n\t" \
  "v_exp_f32 v145, v145\n\t" \
  "v_exp_f32 v146, v146\n\t" \
  "v_exp_f32 v147, v147\n\t" \
  "v_cvt_pk_bf16_f32 v160, v144, v145\n\t" \
  "v_exp_f32 v148, v148\n\t" \
  "v_exp_f32 v149, v149\n\t" \
  "v_cvt_pk_bf16_f32 v161, v146, v147\n\t" \
  "v_mfma_f32_32x32x16_bf16 %[o], v[184:187], v[164:167], %[o]\n\t" \
  "ds_read_b128 v[176:179], %[va] offset:4096\n\t" \
  "ds_read_b128 v[184:187], %[va] offset:4608\n\t" \
  "v_exp_f32 v150, v150\n\t" \
  "v_exp_f32 v151, v151\n\t" \
  "v_cvt_pk_bf16_f32 v162, v148, v149\n\t" \
  "v_exp_f32 v152, v152\n\t" \
  "v_exp_f32 v153, v153\n\t" \
  "v_cvt_pk_bf16_f32 v163, v150, v151\n\t" \
  "v_exp_f32 v154, v154\n\t" \
  "v_exp_f32 v155, v155\n\t" \
  "s_waitcnt lgkmcnt(4)\n\t" \
  "v_mfma_f32_32x32x16_bf16 %[o], v[180:183], v[160:163], %[o]\n\t" \
  "v_cvt_pk_bf16_f32 v164, v152, v153\n\t" \
  "v_exp_f32 v156, v156\n\t" \
  "v_exp_f32 v157, v157\n\t" \
  "v_cvt_pk_bf16_f32 v165, v154, v155\n\t" \
  "v_exp_f32 v158, v158\n\t" \
  "v_exp_f32 v159, v159\n\t" \
  "v_cvt_pk_bf16_f32 v166, v156, v157\n\t" \
  "v_cvt_pk_bf16_f32 v167, v158, v159\n\t" \
  "v_add_u32 %[ka], 0x1000, %[ka]\n\t" \
  "v_add_u32 %[va], %[vs], %[va]\n\t" \
  "s_add_u32 %[cnt], %[cnt], 1\n\t" \
  "s_cmp_lt_u32 %[cnt], 16\n\t" \
  "s_cbranch_scc1 L_attn_loop_%=\n\t" \
  "s_waitcnt lgkmcnt(0)\n\t" \
  "v_mfma_f32_32x32x16_bf16 %[o], v[188:191], v[164:167], %[o]\n\t" \
  "s_setprio 0\n\t" \
  "s_nop 15\n\t" \
  ""
#define ATTN_FAST_CLOBBERS "v128", "v129", "v130", "v131", "v132", "v133", "v134", "v135", "v136", "v137", "v138", "v139", "v140", "v141", "v142", "v143", "v144", "v145", "v146", "v147", "v148", "v149", "v150", "v151", "v152", "v153", "v154", "v155", "v156", "v157", "v158", "v159", "v160", "v161", "v162", "v163", "v164", "v165", "v166", "v167", "v168", "v169", "v170", "v171", "v172", "v173", "v174", "v175", "v176", "v177", "v178", "v179", "v180", "v181", "v182", "v183", "v184", "v185", "v186", "v187", "v188", "v189", "v190", "v191"

typedef short bf16x8 __attribute__((ext_vector_type(8)));
typedef short s16x4 __attribute__((ext_vector_type(4)));
typedef float f32x4 __attribute__((ext_vector_type(4)));
typedef float f32x2 __attribute__((ext_vector_type(2)));
typedef float f32x16 __attribute__((ext_vector_type(16)));
typedef unsigned u32x4 __attribute__((ext_vector_type(4)));
typedef unsigned u32x2 __attribute__((ext_vector_type(2)));
typedef __bf16 bf16x2_t __attribute__((ext_vector_type(2)));
#define LAS __attribute__((address_space(3)))
#define GAS __attribute__((address_space(1)))
typedef const LAS char* lds_cptr;
typedef LAS char* lds_ptr;
typedef GAS unsigned gu32;
typedef GAS unsigned long long gu64;
typedef __amdgpu_buffer_rsrc_t rsrc_t;

constexpr int SEQ = 2048, DM = 512, NH = 32, FF = 1376;
constexpr float EPS = 1e-5f;
constexpr float QSCALE = 0.25f * 1.4426950408889634f;

constexpr unsigned WS_Q = 0, WS_K = 2u << 20, WS_V = 4u << 20, WS_O = 6u << 20, WS_H = 8u << 20, WS_ACT = 10u << 20;
constexpr unsigned WS_KMAX = 16u << 20, WS_HST = (16u << 20) + (64u << 10);
constexpr unsigned WS_WOF = 17u << 20, WS_WDF = WS_WOF + 512 * 1024;
constexpr unsigned WS_FLAG = 20u << 20;
constexpr unsigned FL_EPOCH = 0, FL_TMO = 256, FL_1 = 1024, FL_2 = 4096, FL_3 = 8192, FL_4 = 12288;
constexpr unsigned WS_USED = 24u << 20;
constexpr size_t WS_DBG = 64u << 20;

#define TS(i) do {} while (0)
#define TS_BEGIN() do {} while (0)
#define TS_END() do {} while (0)
#define TSARG
#define TSPASS

struct Params {
  const char* x; const char* wqkv; const char* wo; const char* wg; const char* wu; const char* wd;
  char* out; char* ws;
};

#define MFMA16(a, b, c) __builtin_amdgcn_mfma_f32_16x16x32_bf16(a, b, c, 0, 0, 0)
#define MFMA32(a, b, c) __builtin_amdgcn_mfma_f32_32x32x16_bf16(a, b, c, 0, 0, 0)
#define PIN(x) asm volatile("" : "+v"(x))
#define SB() __builtin_amdgcn_sched_barrier(0)
#define RLX_AGENT __ATOMIC_RELAXED, __HIP_MEMORY_SCOPE_AGENT

template <int N> __device__ __forceinline__ void wait_bar() {
  asm volatile("s_waitcnt vmcnt(%0) lgkmcnt(0)\n\ts_barrier" ::"n"(N) : "memory");
}
template <int N> __device__ __forceinline__ void wait_vm() { asm volatile("s_waitcnt vmcnt(%0)" ::"n"(N) : "memory"); }
__device__ __forceinline__ void lds_bar() { asm volatile("s_waitcnt lgkmcnt(0)\n\ts_barrier" ::: "memory"); }
__device__ __forceinline__ void raw_bar() { asm volatile("s_barrier" ::: "memory"); }

template <bool SC1> __device__ __forceinline__ void glds16(const void* sbase, unsigned voff, unsigned lds_addr) {
  unsigned keep;
  if (SC1) asm volatile("s_mov_b32 %0, m0\n\ts_mov_b32 m0, %3\n\ts_nop 2\n\tglobal_load_lds_dwordx4 %1, %2 sc1\n\ts_mov_b32 m0, %0"
                        : "=&s"(keep) : "v"(voff), "s"(sbase), "s"(lds_addr) : "memory");
  else     asm volatile("s_mov_b32 %0, m0\n\ts_mov_b32 m0, %3\n\ts_nop 2\n\tglobal_load_lds_dwordx4 %1, %2\n\ts_mov_b32 m0, %0"
                        : "=&s"(keep) : "v"(voff), "s"(sbase), "s"(lds_addr) : "memory");
}
__device__ __forceinline__ void glds4_sc1(const void* sbase, unsigned voff, unsigned lds_addr) {
  unsigned keep;
  asm volatile("s_mov_b32 %0, m0\n\ts_mov_b32 m0, %3\n\ts_nop 2\n\tglobal_load_lds_dword %1, %2 sc1\n\ts_mov_b32 m0, %0"
               : "=&s"(keep) : "v"(voff), "s"(sbase), "s"(lds_addr) : "memory");
}
__device__ __forceinline__ bf16x8 ld128(lds_cptr p) { return *(const LAS bf16x8*)p; }
typedef short v4i16_t __attribute__((ext_vector_type(4)));
__device__ __forceinline__ s16x4 ldtr(lds_cptr p) {
  return __builtin_bit_cast(s16x4, __builtin_amdgcn_ds_read_tr16_b64_v4i16((LAS v4i16_t*)p));
}
__device__ __forceinline__ bf16x8 ldtr2(lds_cptr p, int second) {
  const s16x4 a = ldtr(p), b = ldtr(p + second);
  return (bf16x8){a[0], a[1], a[2], a[3], b[0], b[1], b[2], b[3]};
}
__device__ __forceinline__ unsigned cvtpk(float lo, float hi) {
  f32x2 v = {lo, hi}; bf16x2_t b = __builtin_convertvector(v, bf16x2_t); return __builtin_bit_cast(unsigned, b);
}
__device__ __forceinline__ u32x2 cvt4(f32x4 v) { return (u32x2){cvtpk(v[0], v[1]), cvtpk(v[2], v[3])}; }
__device__ __forceinline__ float bflo(unsigned w) { return __uint_as_float(w << 16); }
__device__ __forceinline__ float bfhi(unsigned w) { return __uint_as_float(w & 0xffff0000u); }
__device__ __forceinline__ f32x4 unpack4(u32x2 w) { return (f32x4){bflo(w[0]), bfhi(w[0]), bflo(w[1]), bfhi(w[1])}; }
__device__ __forceinline__ int rfl(int v) { return __builtin_amdgcn_readfirstlane(v); }
__device__ __forceinline__ bf16x8 ones8() { return (bf16x8){0x3F80, 0x3F80, 0x3F80, 0x3F80, 0x3F80, 0x3F80, 0x3F80, 0x3F80}; }
#define Z4 ((f32x4){0.f, 0.f, 0.f, 0.f})
__device__ __forceinline__ void st16(rsrc_t ws, unsigned off, u32x4 v) { __builtin_amdgcn_raw_buffer_store_b128(v, ws, off, 0, 16); }
__device__ __forceinline__ bf16x8 ldw16(rsrc_t ws, unsigned off) { return __builtin_bit_cast(bf16x8, __builtin_amdgcn_raw_buffer_load_b128(ws, off, 0, 16)); }
__device__ __forceinline__ u32x4 pair16(u32x2 X, u32x2 Y) {
  auto a = __builtin_amdgcn_permlane16_swap(X[0], Y[0], false, false);
  auto b = __builtin_amdgcn_permlane16_swap(X[1], Y[1], false, false);
  return (u32x4){a[0], b[0], a[1], b[1]};
}
__device__ __forceinline__ u32x4 pair32(u32x2 X, u32x2 Y) {
  auto a = __builtin_amdgcn_permlane32_swap(X[0], Y[0], false, false);
  auto b = __builtin_amdgcn_permlane32_swap(X[1], Y[1], false, false);
  return (u32x4){a[0], b[0], a[1], b[1]};
}
__device__ __forceinline__ float sum_x16(float v) { auto r = __builtin_amdgcn_permlane16_swap(__float_as_uint(v), __float_as_uint(v), false, false); return __uint_as_float(r[0]) + __uint_as_float(r[1]); }
__device__ __forceinline__ float sum_x32(float v) { auto r = __builtin_amdgcn_permlane32_swap(__float_as_uint(v), __float_as_uint(v), false, false); return __uint_as_float(r[0]) + __uint_as_float(r[1]); }
__device__ __forceinline__ float max_x16(float v) { auto r = __builtin_amdgcn_permlane16_swap(__float_as_uint(v), __float_as_uint(v), false, false); return fmaxf(__uint_as_float(r[0]), __uint_as_float(r[1])); }
__device__ __forceinline__ float lo_x32(float v) { auto r = __builtin_amdgcn_permlane32_swap(__float_as_uint(v), __float_as_uint(v), false, false); return __uint_as_float(r[0]); }
template <int N> __device__ __forceinline__ float ror16(float v) { return __uint_as_float(__builtin_amdgcn_update_dpp(0u, __float_as_uint(v), 0x120 + N, 0xf, 0xf, false)); }
__device__ __forceinline__ float max_row16(float v) { v = fmaxf(v, ror16<8>(v)); v = fmaxf(v, ror16<4>(v)); v = fmaxf(v, ror16<2>(v)); return fmaxf(v, ror16<1>(v)); }
template <int ND, int NM> __device__ __forceinline__ void weave() {
#pragma unroll
  for (int i = 0; i < ND; ++i) { __builtin_amdgcn_sched_group_barrier(0x008, 1, 0); __builtin_amdgcn_sched_group_barrier(0x100, 1, 0); }
  if (NM > ND) __builtin_amdgcn_sched_group_barrier(0x008, NM - ND, 0);
}

__device__ __forceinline__ void publish(char* ws, unsigned flag_off, unsigned tag) {
  if (threadIdx.x == 0) __hip_atomic_store((gu32*)(ws + WS_FLAG + flag_off), tag, RLX_AGENT);
}
template <int NW>
__device__ __forceinline__ void poll(char* ws, unsigned flag_off, unsigned tag) {
  const int lane = threadIdx.x & 63;
  gu32* f = (gu32*)(ws + WS_FLAG + flag_off);
  const unsigned long long t0 = __builtin_amdgcn_s_memrealtime();
  for (unsigned spins = 1;; ++spins) {
    bool ok = true;
    if (NW == 256) {
#pragma unroll
      for (int i = 0; i < 4; ++i) ok &= __hip_atomic_load(f + i * 64 + lane, RLX_AGENT) == tag;
    } else ok = __hip_atomic_load(f + (lane < NW ? lane : 0), RLX_AGENT) == tag;
    if (__all(ok)) break;
    if ((spins & 63u) == 0u && __builtin_amdgcn_s_memrealtime() - t0 > 20000000ull) {
      if (lane == 0) __hip_atomic_store((gu32*)(ws + WS_FLAG + FL_TMO), tag, RLX_AGENT);
      break;
    }
    __builtin_amdgcn_s_sleep(1);
  }
}
template <int NW> __device__ __forceinline__ void await(char* ws, unsigned flag_off, unsigned tag) {
  if (rfl(threadIdx.x >> 6) == 0) poll<NW>(ws, flag_off, tag);
  raw_bar();
}

struct F1 { bf16x8 a[4], w[3]; };
__device__ __forceinline__ void p1_qkv(const Params& p, rsrc_t ws, int b, int rb, int cb, char* smem TSARG) {
  const int tid = threadIdx.x, lane = tid & 63, wid = rfl(tid >> 6);
  const int r16 = lane & 15, g = lane >> 4;
  const unsigned lds0 = (unsigned)(size_t)(lds_ptr)smem;
  lds_cptr L = (lds_cptr)smem;
  constexpr int STG = 32768, ND = 3, NS = 8, RL = ND * STG, MS = RL + 8192;
  LAS f32x2* ms = (LAS f32x2*)(smem + MS);
  if (wid >= 4) {
    const int lw = wid - 4;
    const unsigned voffA = (unsigned)((16 * lw + (lane >> 3)) * DM + (((lane & 7) ^ (lane >> 3)) * 8)) * 2u;
    unsigned voffB[2];
#pragma unroll
    for (int i = 0; i < 2; ++i) { const int r = lane >> 3, c = lane & 7, ff = ((r >> 1) & 1) | (i << 1), j = (c >> 1) ^ ff;
      voffB[i] = (unsigned)((16 * lw + 8 * i + r) * (3 * DM) + j * 16 + (c & 1) * 8) * 2u; }
    const char* xa = p.x + (size_t)rb * 64 * DM * 2;
    const char* wb = p.wqkv + (size_t)cb * 64 * 2;
    auto issue = [&](int s) {
      const unsigned d = lds0 + (s % ND) * STG;
#pragma unroll
      for (int i = 0; i < 2; ++i) glds16<false>(xa + s * 128 + i * 8 * DM * 2, voffA, d + (2 * lw + i) * 1024);
#pragma unroll
      for (int i = 0; i < 2; ++i)
#pragma unroll
        for (int seg = 0; seg < 3; ++seg) glds16<false>(wb + (size_t)s * 64 * (3 * DM) * 2 + seg * DM * 2, voffB[i], d + 8192 + ((2 * lw + i) * 3 + seg) * 1024);
    };
    TS(20);
    issue(0); issue(1);
    TS(21);
    wait_bar<8>();
    TS(23);
    const int f0 = b * 8 + 2 * lw;
    auto rl_issue = [&](int i) {
      const int f = f0 + i, fw = f < 512 + 32 * 43 ? f : 0;
      const bool isd = fw >= 512; const int f2 = isd ? fw - 512 : fw, ct = isd ? f2 / 43 : f2 >> 4, ks = isd ? f2 - ct * 43 : f2 & 15;
      const char* src = (isd ? p.wd : p.wo) + ((size_t)ks * 32 * DM + ct * 16) * 2;
      const int t = lane >> 5, gg = (lane >> 3) & 3, q = (lane >> 1) & 3;
      glds16<false>(src, (unsigned)((8 * gg + 4 * t + q) * DM + (lane & 1) * 8) * 2u, lds0 + RL + (2 * lw + i) * 1024);
    };
#pragma unroll
    for (int j = 1; j < NS; ++j) {
      if (j + 1 < NS) issue(j + 1);
      if (j + 1 == NS - 1) { rl_issue(0); rl_issue(1); }
      if (j + 1 < NS - 1) wait_bar<8>(); else if (j + 1 == NS - 1) wait_bar<10>(); else wait_bar<2>();
    }
    wait_vm<0>();
#pragma unroll
    for (int i = 0; i < 2; ++i)
      if (f0 + i < 512 + 32 * 43) st16(ws, WS_WOF + (unsigned)(f0 + i) * 1024 + lane * 16, __builtin_bit_cast(u32x4, ldtr2(L + RL + (2 * lw + i) * 1024 + lane * 8, 512)));
    lds_bar();
  } else {
    f32x4 acc[3][4], cs[3], sx = Z4, sq = Z4;
    int aoff[2][4];
#pragma unroll
    for (int ks = 0; ks < 2; ++ks)
#pragma unroll
      for (int i = 0; i < 4; ++i) aoff[ks][i] = ((wid + i) & 3) * 2048 + (r16 >> 3) * 1024 + (r16 & 7) * 128 + (((4 * ks + g) ^ (r16 & 7)) << 4);
    int boff;
    { const int q = (lane >> 2) & 3, pp = lane & 3, ff = ((q >> 1) & 1) | ((g & 1) << 1);
      boff = 8192 + g * 3072 + q * 128 + ((wid ^ ff) << 5) + pp * 8; }
#pragma unroll
    for (int i = 0; i < 3; ++i) { cs[i] = Z4;
#pragma unroll
      for (int j = 0; j < 4; ++j) acc[i][j] = Z4; }
    const bf16x8 one = ones8();
    F1 fa, fb;
    auto rd = [&](F1& fr, int s, int ks) {
      lds_cptr B = L + (s % ND) * STG;
#pragma unroll
      for (int seg = 0; seg < 3; ++seg) fr.w[seg] = ldtr2(B + boff + ks * 12288 + seg * 1024, 512);
#pragma unroll
      for (int i = 0; i < 4; ++i) fr.a[i] = ld128(B + aoff[ks][i]);
    };
    auto mm = [&](const F1& fr) {
#pragma unroll
      for (int i = 0; i < 4; ++i) {
        acc[0][i] = MFMA16(fr.w[0], fr.a[i], acc[0][i]);
        acc[1][i] = MFMA16(fr.w[1], fr.a[i], acc[1][i]);
        acc[2][i] = MFMA16(fr.a[i], fr.w[2], acc[2][i]);
      }
      cs[0] = MFMA16(fr.w[0], one, cs[0]); cs[1] = MFMA16(fr.w[1], one, cs[1]); cs[2] = MFMA16(one, fr.w[2], cs[2]);
      sx = MFMA16(one, fr.a[0], sx); sq = MFMA16(fr.a[0], fr.a[0], sq);
    };
    lds_bar();
    TS(1);
    rd(fa, 0, 0); SB();
#pragma unroll
    for (int s = 0; s < NS; ++s) {
      rd(fb, s, 1); mm(fa); weave<10, 17>(); SB();
      if (s + 1 < NS) { lds_bar(); rd(fa, s + 1, 0); mm(fb); weave<10, 17>(); SB(); }
      else { mm(fb); SB(); }
    }
    TS(2);
    if ((r16 >> 2) == g) {
      const int e = r16 & 3; const float s2 = e == 0 ? sq[0] : e == 1 ? sq[1] : e == 2 ? sq[2] : sq[3];
      const float mean = sx[0] * (1.f / DM), var = fmaxf(s2 * (1.f / DM) - mean * mean, 0.f);
      ms[wid * 16 + r16] = (f32x2){mean, rsqrtf(var + EPS)};
    }
    lds_bar();
    TS(25);
    const int h = cb * 4 + wid;
    u32x2 qv[4], kv[4], vv[4];
    float kmx = 0.f;
#pragma unroll
    for (int i = 0; i < 4; ++i) {
      const int rt = (wid + i) & 3;
      const f32x2 st = ms[rt * 16 + r16];
      qv[i] = cvt4((acc[0][i] - st.x * cs[0]) * (st.y * QSCALE));
      { const f32x4 v = (acc[1][i] - st.x * cs[1]) * st.y; kv[i] = cvt4(v);
        kmx = fmaxf(kmx, sum_x32(sum_x16((v[0] * v[0] + v[1] * v[1]) + (v[2] * v[2] + v[3] * v[3])))); }
      { const f32x4 m01 = *(LAS f32x4*)(ms + rt * 16 + 4 * g), m23 = *(LAS f32x4*)(ms + rt * 16 + 4 * g + 2); const float cv = cs[2][0];
        vv[i] = (u32x2){cvtpk((acc[2][i][0] - m01[0] * cv) * m01[1], (acc[2][i][1] - m01[2] * cv) * m01[3]),
                        cvtpk((acc[2][i][2] - m23[0] * cv) * m23[1], (acc[2][i][3] - m23[2] * cv) * m23[3])}; }
    }
#pragma unroll
    for (int i = 0; i < 4; i += 2) {
      { const int rt = (wid + i + (g & 1)) & 3, grow = rb * 64 + rt * 16;
        const unsigned off = (((unsigned)h * 64 + (grow >> 5)) * 2 + (g >> 1)) * 512 + ((grow & 31) + r16) * 16;
        st16(ws, WS_K + off, pair16(kv[i], kv[i + 1])); st16(ws, WS_Q + off, pair16(qv[i], qv[i + 1])); }
      { const int rt = (wid + i + (g >> 1)) & 3, grow = rb * 64 + rt * 16;
        const unsigned off = ((((unsigned)h * 64 + (grow >> 5)) * 2 + (rt & 1)) * 2 + (g & 1)) * 256 + r16 * 16;
        st16(ws, WS_V + off, pair32(vv[i], vv[i + 1])); }
    }
    kmx = max_row16(kmx);
    if (lane == 0) __hip_atomic_store((gu32*)(p.ws + WS_KMAX) + h * 32 + rb, __float_as_uint(kmx), RLX_AGENT);
    TS(26);
  }
}

constexpr int A_K = 0, A_V = 65536, A_ONE = 131072, A_Q = A_ONE + 5120, A_KM = A_Q + 8192, A_BYTES = A_KM + 256;
constexpr float SAFE_BOUND = 48.f;
constexpr int PRIO_SWITCH = 13;

__device__ __forceinline__ void p2_attn(const Params& p, rsrc_t ws, int h, int qr, char* smem, unsigned tag TSARG) {
  const int tid = threadIdx.x, lane = tid & 63, wid = rfl(tid >> 6);
  const int r32 = lane & 31, hh = lane >> 5;
  const unsigned lds0 = (unsigned)(size_t)(lds_ptr)smem;
  lds_cptr L = (lds_cptr)smem;
  const char* kg = p.ws + WS_K + (size_t)h * 65536;
  const char* vg = p.ws + WS_V + (size_t)h * 65536;
  const unsigned vo = lane * 16;
  glds16<true>(p.ws + WS_Q + ((size_t)h * 64 + qr * 8 + wid) * 1024, vo, lds0 + A_Q + wid * 1024);
  glds4_sc1(p.ws + WS_KMAX + h * 128, (lane & 31) * 4, lds0 + A_KM);
#pragma unroll
  for (int i = 0; i < 2; ++i) {
    const int t = wid + 8 * i;
    glds16<true>(kg + t * 1024, vo, lds0 + A_K + t * 1024);
    glds16<true>(vg + t * 1024, vo, lds0 + A_V + t * 1024);
  }
  { const unsigned o1 = (tid & 31) < 2 ? 0x3F803F80u : 0u;
    *(LAS u32x2*)(smem + A_ONE + tid * 8) = (u32x2){o1, o1};
    if (tid < 128) *(LAS u32x2*)(smem + A_ONE + 4096 + tid * 8) = (u32x2){o1, o1}; }
  if (wid == 0) {
    poll<256>(p.ws, FL_1, tag);
    if (blockIdx.x == 0 && lane == 0) __hip_atomic_store((gu32*)(p.ws + WS_FLAG + FL_EPOCH), tag, RLX_AGENT);
  }
  wait_bar<0>();
  TS(4);
  const bf16x8 qf = ld128(L + A_Q + wid * 1024 + lane * 16);
  float c;
  { float km = *(const LAS float*)(L + A_KM + lane * 4);
    km = max_x16(max_row16(km));
    float q2 = 0.f;
#pragma unroll
    for (int j = 0; j < 8; ++j) { const float v = __uint_as_float((unsigned)(unsigned short)qf[j] << 16); q2 += v * v; }
    q2 = sum_x32(q2);
    c = sqrtf(q2 * km) * 1.02f + 0.01f; }
  const bool fast = __all(c <= SAFE_BOUND);
  const bool vlane = r32 < 16;
  int vaddr = (int)lds0 + (vlane ? A_V : A_ONE) + hh * 256 + (r32 & 15) * 16;
  const int vstep = vlane ? 4096 : 0;
  int kaddr = (int)lds0 + A_K + lane * 16;
  f32x16 o;
#pragma unroll
  for (int i = 0; i < 16; ++i) o[i] = 0.f;
  unsigned go = vo + (16 + wid) * 1024;
  unsigned ld = lds0 + A_K + (16 + wid) * 1024;
  if (fast) {
    f32x16 negc;
#pragma unroll
    for (int i = 0; i < 16; ++i) negc[i] = -c;
    int cnt, keep;
    const int sw = wid < 4 ? PRIO_SWITCH : 99;
    asm volatile(ATTN_FAST_ASM
                 : [o] "+v"(o), [ka] "+v"(kaddr), [va] "+v"(vaddr), [go] "+v"(go), [ld] "+s"(ld), [cnt] "=&s"(cnt), [keep] "=&s"(keep)
                 : [q] "v"(qf), [nc] "v"(negc), [vs] "v"(vstep), [sw] "s"(sw), [kg] "s"(kg), [vg] "s"(vg)
                 : "memory", "scc", ATTN_FAST_CLOBBERS);
  } else {
#pragma unroll
    for (int j = 0; j < 6; ++j) { glds16<true>(kg, go + j * 8192, ld + j * 8192); glds16<true>(vg, go + j * 8192, ld + 65536 + j * 8192); }
    float m = -INFINITY;
    vaddr -= (int)lds0; kaddr -= (int)lds0;
#pragma unroll 1
    for (int t4 = 0; t4 < 16; ++t4) {
      if (t4 == 2) wait_bar<0>();
#pragma unroll 1
      for (int u = 0; u < 4; ++u) {
        const int t = t4 * 4 + u;
        f32x16 z;
#pragma unroll
        for (int i = 0; i < 16; ++i) z[i] = 0.f;
        const f32x16 s = MFMA32(ld128(L + kaddr + t * 1024), qf, z);
        float mx = s[0];
#pragma unroll
        for (int i = 1; i < 16; ++i) mx = fmaxf(mx, s[i]);
        mx = fmaxf(mx, __shfl_xor(mx, 32));
        const float mn = fmaxf(m, mx);
        const float al = __builtin_amdgcn_exp2f(m - mn);
        m = mn;
#pragma unroll
        for (int i = 0; i < 16; ++i) o[i] *= al;
        u32x4 p0, p1;
#pragma unroll
        for (int i = 0; i < 4; ++i) {
          p0[i] = cvtpk(__builtin_amdgcn_exp2f(s[2 * i] - mn), __builtin_amdgcn_exp2f(s[2 * i + 1] - mn));
          p1[i] = cvtpk(__builtin_amdgcn_exp2f(s[8 + 2 * i] - mn), __builtin_amdgcn_exp2f(s[8 + 2 * i + 1] - mn));
        }
        const bf16x8 v0 = ld128(L + vaddr + u * 1024), v1 = ld128(L + vaddr + u * 1024 + 512);
        o = MFMA32(v0, __builtin_bit_cast(bf16x8, p0), o);
        o = MFMA32(v1, __builtin_bit_cast(bf16x8, p1), o);
      }
      vaddr += vstep;
    }
  }
  TS(5);
  const float inv = __builtin_amdgcn_rcpf(lo_x32(o[8]));
  const u32x4 ov = pair32((u32x2){cvtpk(o[0] * inv, o[1] * inv), cvtpk(o[2] * inv, o[3] * inv)}, (u32x2){cvtpk(o[4] * inv, o[5] * inv), cvtpk(o[6] * inv, o[7] * inv)});
  const int row = qr * 256 + wid * 32 + r32;
  st16(ws, WS_O + ((((unsigned)(row >> 4) * 16 + (h >> 1)) * 4 + (h & 1) * 2 + hh) * 16 + (row & 15)) * 16, ov);
}

template <int NKS, int NF, int TS0>
__device__ __forceinline__ void gemm64(const char* ag  , const bf16x8 (&wf)[NF], char* smem, f32x4 (&res)[2] TSARG) {
  const int tid = threadIdx.x, lane = tid & 63, wid = rfl(tid >> 6), wn = wid & 3, kh = wid >> 2;
  const unsigned lds0 = (unsigned)(size_t)(lds_ptr)smem;
  lds_cptr L = (lds_cptr)smem;
  constexpr int STG = 32768, NS = (NKS + 7) / 8, ND = NS < 4 ? NS : 4;
  auto issue = [&](int s) {
    const unsigned d = lds0 + (s % ND) * STG + ((wid >> 1) * 8 + (wid & 1) * 4) * 1024;
#pragma unroll
    for (int i = 0; i < 4; ++i) { int ks = s * 8 + (wid & 1) * 4 + i; if (s == NS - 1 && NKS % 8) ks = ks > NKS - 1 ? NKS - 1 : ks; glds16<true>(ag + ks * 1024, lane * 16, d + i * 1024); }
  };
  issue(0); issue(1);
  const int aoff = kh * 1024 + lane * 16;
  f32x4 acc[4] = {Z4, Z4, Z4, Z4};
  bf16x8 fa[2][4], fb[2][4];
  auto rd = [&](bf16x8 (&f)[2][4], int s, int hf) {
    lds_cptr B = L + (s % ND) * STG + aoff + hf * 4096;
#pragma unroll
    for (int i = 0; i < 2; ++i)
#pragma unroll
      for (int rt = 0; rt < 4; ++rt) f[i][rt] = ld128(B + rt * 8192 + i * 2048);
  };
  auto mm = [&](const bf16x8 (&f)[2][4], int f0) {
#pragma unroll
    for (int i = 0; i < 2; ++i)
      if (f0 + i < NF) {
#pragma unroll
        for (int rt = 0; rt < 4; ++rt) acc[rt] = MFMA16(wf[f0 + i], f[i][rt], acc[rt]);
      }
  };
  wait_bar<4>();
  TS(TS0);
  rd(fa, 0, 0);
  if (2 < NS) issue(2);
  SB();
#pragma unroll
  for (int s = 0; s < NS; ++s) {
    const bool h1 = 4 * s + 2 < NF;
    if (h1) { rd(fb, s, 1); mm(fa, 4 * s); weave<8, 8>(); } else mm(fa, 4 * s);
    SB();
    if (s + 1 < NS) {
      if (s + 2 < NS) wait_bar<4>(); else wait_bar<0>();
      rd(fa, s + 1, 0);
      if (h1) { mm(fb, 4 * s + 2); weave<8, 8>(); }
      SB();
      if (s + 3 < NS) issue(s + 3);
    } else if (h1) mm(fb, 4 * s + 2);
    SB();
  }
  TS(TS0 + 1);
  lds_bar();
  LAS f32x4* X = (LAS f32x4*)smem;
  if (kh == 0) { X[((wn * 2 + 1) * 2 + 0) * 64 + lane] = acc[2]; X[((wn * 2 + 1) * 2 + 1) * 64 + lane] = acc[3]; }
  else         { X[((wn * 2 + 0) * 2 + 0) * 64 + lane] = acc[0]; X[((wn * 2 + 0) * 2 + 1) * 64 + lane] = acc[1]; }
  lds_bar();
  const f32x4 o0 = X[((wn * 2 + kh) * 2 + 0) * 64 + lane], o1 = X[((wn * 2 + kh) * 2 + 1) * 64 + lane];
  if (kh == 0) { res[0] = acc[0] + o0; res[1] = acc[1] + o1; } else { res[0] = acc[2] + o0; res[1] = acc[3] + o1; }
}

constexpr int G_B = 0, G_BS = 24576, G_A = 3 * G_BS, G_AS = 16384, G_ST = G_A + 3 * G_AS, G_MS = G_ST + 8192, G_CS = G_MS + 1024;
__device__ __forceinline__ void w_voffs(unsigned (&voff)[3], int nu, int lane) {
#pragma unroll
  for (int i = 0; i < 3; ++i) {
    const int ch = i * 64 + lane, r = ch / 12, cp = ch - r * 12;
    int c = cp ^ (((r >> 3) & 1) << 1);
    if (c >= nu * 4) c -= 4;
    voff[i] = (unsigned)(r * FF + c * 8) * 2u;
  }
}
struct WLoad {
  const char* bg; const char* bu; unsigned voff[3]; unsigned dst;
  __device__ __forceinline__ void init(const Params& p, int fb, int lw, int lane, unsigned lds0) {
    const int u0 = fb < 11 ? 3 * fb : 33 + 2 * (fb - 11), nu = fb < 11 ? 3 : 2;
    const size_t o = ((size_t)lw * 16 * FF + u0 * 32) * 2;
    bg = p.wg + o; bu = p.wu + o; w_voffs(voff, nu, lane); dst = lds0 + G_B + lw * 3072;
  }
  __device__ __forceinline__ void issue(int s) const {
#pragma unroll
    for (int i = 0; i < 3; ++i) glds16<false>(bg + (size_t)s * 64 * FF * 2, voff[i], dst + (s % 3) * G_BS + i * 1024);
#pragma unroll
    for (int i = 0; i < 3; ++i) glds16<false>(bu + (size_t)s * 64 * FF * 2, voff[i], dst + (s % 3) * G_BS + 12288 + i * 1024);
  }
};
struct F4 { bf16x8 a[4], w[2][3]; };
__device__ __forceinline__ void p4_gateup(const Params& p, rsrc_t ws, int mb, int fb, char* smem, const WLoad& wl, int wct, bf16x8 (&wdf)[22] TSARG) {
  const int tid = threadIdx.x, lane = tid & 63, wid = rfl(tid >> 6);
  const int r16 = lane & 15, g = lane >> 4, kh = wid >> 2;
  const unsigned lds0 = (unsigned)(size_t)(lds_ptr)smem;
  lds_cptr L = (lds_cptr)smem;
  constexpr int NS = 8;
  const int u0 = fb < 11 ? 3 * fb : 33 + 2 * (fb - 11), nu = fb < 11 ? 3 : 2;
  auto wd_load = [&](int i) { const int ks = 2 * i + kh; wdf[i] = ldw16(ws, WS_WDF + ((unsigned)wct * 43 + (ks > 42 ? 42 : ks)) * 1024 + lane * 16); };
  if (wid >= 4) {
    const int lw = wid - 4;
    const char* ag = p.ws + WS_H + (size_t)(mb * 8 + 2 * lw) * 16384;
    auto issueA = [&](int s) {
      const unsigned d = lds0 + G_A + (s % 3) * G_AS + lw * 4096;
#pragma unroll
      for (int i = 0; i < 4; ++i) glds16<true>(ag + (i >> 1) * 16384 + (2 * s + (i & 1)) * 1024, lane * 16, d + i * 1024);
    };
    issueA(0); issueA(1);
#pragma unroll
    for (int i = 0; i < 2; ++i) glds16<true>(p.ws + WS_HST + (size_t)mb * 8192 + (lw * 2 + i) * 1024, lane * 16, lds0 + G_ST + (lw * 2 + i) * 1024);
    wait_bar<6>();
#pragma unroll
    for (int j = 1; j < NS; ++j) {
      if (j + 1 < NS) { issueA(j + 1); wl.issue(j + 1); wait_bar<10>(); } else wait_bar<0>();
      if (j == 1) {
        const int row = tid - 256;
        if (row < 128) {
          const f32x4 v0 = *(const LAS f32x4*)(L + G_ST + row * 64), v1 = *(const LAS f32x4*)(L + G_ST + row * 64 + 16), v2 = *(const LAS f32x4*)(L + G_ST + row * 64 + 32), v3 = *(const LAS f32x4*)(L + G_ST + row * 64 + 48);
          const float a = ((v0[0] + v0[2]) + (v1[0] + v1[2])) + ((v2[0] + v2[2]) + (v3[0] + v3[2])), bb = ((v0[1] + v0[3]) + (v1[1] + v1[3])) + ((v2[1] + v2[3]) + (v3[1] + v3[3]));
          const float mean = a * (1.f / DM), var = fmaxf(bb * (1.f / DM) - mean * mean, 0.f), rstd = rsqrtf(var + EPS);
          *(LAS f32x2*)(smem + G_MS + row * 8) = (f32x2){rstd, -mean * rstd};
        }
      }
    }
#pragma unroll
    for (int i = 0; i < 22; ++i) wd_load(i);
    lds_bar();
  } else {
    const int wm = wid >> 1, wn = wid & 1;
    const int aoff = G_A + wm * 8192 + lane * 16;
    int boff[3];
#pragma unroll
    for (int i = 0; i < 3; ++i) {
      const int jt = 3 * wn + i, q = (lane >> 2) & 3, pp = lane & 3;
      boff[i] = G_B + (g >> 1) * 3072 + (8 * (g & 1) + q) * 192 + ((jt ^ (g & 1)) << 5) + pp * 8;
    }
    const int m0 = wm * 12288, m1 = (wm ^ 1) * 12288;
    f32x4 acc[2][3][4], cs[3];
#pragma unroll
    for (int i = 0; i < 3; ++i) { cs[i] = Z4;
#pragma unroll
      for (int j = 0; j < 4; ++j) { acc[0][i][j] = Z4; acc[1][i][j] = Z4; } }
    const bf16x8 one = ones8();
    F4 fa, fb2;
    auto rd = [&](F4& f, int s, int ks) {
      lds_cptr B = L + (s % 3) * G_BS, A = L + (s % 3) * G_AS;
#pragma unroll
      for (int i = 0; i < 4; ++i) f.a[i] = ld128(A + aoff + i * 2048 + ks * 1024);
#pragma unroll
      for (int i = 0; i < 3; ++i) { f.w[0][i] = ldtr2(B + boff[i] + m0 + ks * 6144, 768); f.w[1][i] = ldtr2(B + boff[i] + m1 + ks * 6144, 768); }
    };
    auto mm = [&](const F4& f) {
#pragma unroll
      for (int m = 0; m < 2; ++m)
#pragma unroll
        for (int i = 0; i < 3; ++i)
#pragma unroll
          for (int j = 0; j < 4; ++j) acc[m][i][j] = MFMA16(f.w[m][i], f.a[j], acc[m][i][j]);
#pragma unroll
      for (int i = 0; i < 3; ++i) cs[i] = MFMA16(f.w[0][i], one, cs[i]);
    };
    lds_bar();
    TS(11);
    rd(fa, 0, 0); SB();
#pragma unroll
    for (int s = 0; s < NS; ++s) {
      rd(fb2, s, 1); mm(fa); weave<16, 27>(); SB();
      if (s + 1 < NS) { lds_bar(); rd(fa, s + 1, 0); mm(fb2); weave<16, 27>(); SB(); }
      else { mm(fb2); SB(); }
    }
    TS(12);
#pragma unroll
    for (int i = 0; i < 11; ++i) wd_load(i);
    LAS float* csl = (LAS float*)(smem + G_CS);
    if (r16 == 0) {
#pragma unroll
      for (int i = 0; i < 3; ++i) *(LAS f32x4*)(csl + (((wm * 2 + wn) * 3 + i) * 16 + 4 * g)) = cs[i];
    }
    lds_bar();
    TS(33);
    const LAS f32x2* ms = (const LAS f32x2*)(smem + G_MS);
    f32x2 st[4];
#pragma unroll
    for (int j = 0; j < 4; ++j) st[j] = ms[wm * 64 + j * 16 + r16];
    constexpr float K1 = -2.885390081777927f * 0.7978845608028654f, K2 = -2.885390081777927f * 0.035677408136300125f;
#pragma unroll
    for (int i = 0; i < 3; ++i) {
      const int jt = 3 * wn + i, u = jt >> 1, jj = jt & 1;
      const f32x4 cg = *(LAS f32x4*)(csl + ((0 * 2 + wn) * 3 + i) * 16 + 4 * g), cu = *(LAS f32x4*)(csl + ((1 * 2 + wn) * 3 + i) * 16 + 4 * g);
      u32x2 av[4];
#pragma unroll
      for (int j = 0; j < 4; ++j) {
        const f32x4 ag2 = wm ? acc[1][i][j] : acc[0][i][j], au = wm ? acc[0][i][j] : acc[1][i][j];
        const f32x4 ga = ag2 * st[j].x + st[j].y * cg, up = au * st[j].x + st[j].y * cu;
        const f32x4 z = ga * (ga * ga * K2 + K1);
        const f32x4 d = (f32x4){__builtin_amdgcn_exp2f(z[0]), __builtin_amdgcn_exp2f(z[1]), __builtin_amdgcn_exp2f(z[2]), __builtin_amdgcn_exp2f(z[3])} + 1.f;
        const f32x4 rc = (f32x4){__builtin_amdgcn_rcpf(d[0]), __builtin_amdgcn_rcpf(d[1]), __builtin_amdgcn_rcpf(d[2]), __builtin_amdgcn_rcpf(d[3])};
        av[j] = cvt4((ga * rc) * up);
      }
      if (u < nu) {
#pragma unroll
        for (int j = 0; j < 4; j += 2) {
          const unsigned rt = (unsigned)mb * 8 + wm * 4 + j + (g & 1);
          st16(ws, WS_ACT + (((rt * 43 + (u0 + u)) * 4 + jj * 2 + (g >> 1)) * 16 + r16) * 16, pair16(av[j], av[j + 1]));
        }
      }
    }
    TS(34);
    SB();
#pragma unroll
    for (int i = 11; i < 22; ++i) wd_load(i);
  }
}

extern __shared__ __attribute__((aligned(16))) char smem_dyn[];

__global__ __launch_bounds__(512, 2) void mega(Params p) {
  char* smem = smem_dyn;
  const int b = blockIdx.x, x = b & 7, i5 = b >> 3;
  const int tid = threadIdx.x, lane = tid & 63, wid = rfl(tid >> 6);
  const int r16 = lane & 15, g = lane >> 4, wn = wid & 3, kh = wid >> 2;
  TS_BEGIN();
  const rsrc_t ws = __builtin_amdgcn_make_buffer_rsrc(p.ws, 0, WS_USED, 0x00020000);
  const unsigned epoch = __hip_atomic_load((gu32*)(p.ws + WS_FLAG + FL_EPOCH), RLX_AGENT);

  const int rb1 = (x >> 1) * 8 + (i5 & 7), cb1 = (x & 1) * 4 + (i5 >> 3);
  p1_qkv(p, ws, b, rb1, cb1, smem TSPASS);
  const unsigned tag = (unsigned)rfl((int)(epoch * 0x9E3779B1u + 0x7F4A7C15u));
  wait_bar<0>();
  publish(p.ws, FL_1 + (cb1 * 32 + rb1) * 4, tag);
  TS(3);

  const int h = x * 4 + (i5 & 3), qr = i5 >> 2;
  await<32>(p.ws, FL_1 + (h >> 2) * 128, tag);
  TS(27);
  p2_attn(p, ws, h, qr, smem, tag TSPASS);
  wait_bar<0>();
  publish(p.ws, FL_2 + (qr * 32 + h) * 4, tag);
  TS(6);

  const int rb = x * 4 + (i5 & 3), cb = i5 >> 2;
  const int wct = cb * 4 + wn, col = cb * 64 + wn * 16 + 4 * g;
  bf16x8 wof[8];
#pragma unroll
  for (int i = 0; i < 8; ++i) wof[i] = ldw16(ws, WS_WOF + ((unsigned)wct * 16 + 2 * i + kh) * 1024 + lane * 16);
  u32x2 xv[2];
#pragma unroll
  for (int i = 0; i < 2; ++i) xv[i] = *(const u32x2*)(p.x + ((size_t)(rb * 64 + (2 * kh + i) * 16 + r16) * DM + col) * 2);
  await<32>(p.ws, FL_2 + (rb >> 2) * 128, tag);
  TS(7);
#pragma unroll
  for (int i = 0; i < 8; ++i) PIN(wof[i]);
  PIN(xv[0]); PIN(xv[1]);
  u32x2 hv[2];
  {
    f32x4 res[2];
    gemm64<16, 8, 8>(p.ws + WS_O + (size_t)(rb * 4 + (wid >> 1)) * 16384, wof, smem + G_A, res TSPASS);
    TS(31);
    LAS f32x2* S = (LAS f32x2*)(smem + G_A + 16384);
#pragma unroll
    for (int i = 0; i < 2; ++i) hv[i] = cvt4(unpack4(xv[i]) + res[i]);
    { const unsigned rt = (unsigned)rb * 4 + 2 * kh + (g & 1);
      st16(ws, WS_H + (((rt * 16 + cb * 2 + (wn >> 1)) * 4 + (wn & 1) * 2 + (g >> 1)) * 16 + r16) * 16, pair16(hv[0], hv[1])); }
#pragma unroll
    for (int i = 0; i < 2; ++i) {
      const f32x4 v = unpack4(hv[i]);
      const float s1 = sum_x32(sum_x16((v[0] + v[1]) + (v[2] + v[3]))), s2 = sum_x32(sum_x16((v[0] * v[0] + v[1] * v[1]) + (v[2] * v[2] + v[3] * v[3])));
      if (g == 0) S[((2 * kh + i) * 16 + r16) * 4 + wn] = (f32x2){s1, s2};
    }
    lds_bar();
    if (tid < 64) {
      const f32x2 a = S[tid * 4], bb = S[tid * 4 + 1], c = S[tid * 4 + 2], d = S[tid * 4 + 3];
      const float s1 = (a.x + bb.x) + (c.x + d.x), s2 = (a.y + bb.y) + (c.y + d.y);
      __hip_atomic_store((gu64*)(p.ws + WS_HST + ((size_t)(rb * 64 + tid) * 8 + cb) * 8), ((unsigned long long)__float_as_uint(s2) << 32) | __float_as_uint(s1), RLX_AGENT);
    }
  }
  TS(32);
  wait_bar<0>();
  publish(p.ws, FL_3 + (rb >> 1) * 128 + ((rb & 1) * 8 + cb) * 4, tag);
  TS(9);

  const int mb = x * 2 + (i5 & 1), fb = i5 >> 1;
  WLoad wl;
  if (wid >= 4) { wl.init(p, fb, wid - 4, lane, (unsigned)(size_t)(lds_ptr)smem); wl.issue(0); wl.issue(1); }
  await<16>(p.ws, FL_3 + mb * 128, tag);
  TS(10);
  bf16x8 wdf[22];
  p4_gateup(p, ws, mb, fb, smem, wl, wct, wdf TSPASS);
  if (wid >= 4) wait_bar<22>(); else wait_bar<11>();
  publish(p.ws, FL_4 + mb * 128 + fb * 4, tag);
  TS(13);

  await<16>(p.ws, FL_4 + (rb >> 1) * 128, tag);
  TS(14);
  if (kh) wdf[21] = (bf16x8){0, 0, 0, 0, 0, 0, 0, 0};
#pragma unroll
  for (int i = 0; i < 22; ++i) PIN(wdf[i]);
  {
    f32x4 res[2];
    gemm64<43, 22, 15>(p.ws + WS_ACT + (size_t)(rb * 4 + (wid >> 1)) * 43 * 1024, wdf, smem, res TSPASS);
#pragma unroll
    for (int i = 0; i < 2; ++i) {
      const int row = rb * 64 + (2 * kh + i) * 16 + r16;
      *(u32x2*)(p.out + ((size_t)row * DM + col) * 2) = cvt4(unpack4(hv[i]) + res[i]);
    }
  }
  TS_END();
}

extern "C" void kernel_launch(void* const* d_in, const int* in_sizes, int n_in, void* d_out, int out_size, void* d_ws, size_t ws_size, hipStream_t stream) {
  Params p;
  p.x = (const char*)d_in[0]; p.wqkv = (const char*)d_in[1]; p.wo = (const char*)d_in[2];
  p.wg = (const char*)d_in[4]; p.wu = (const char*)d_in[5]; p.wd = (const char*)d_in[6];
  p.out = (char*)d_out; p.ws = (char*)d_ws;
  constexpr int LDS = 160 * 1024;
  (void)hipFuncSetAttribute((const void*)mega, hipFuncAttributeMaxDynamicSharedMemorySize, LDS);
  mega<<<256, 512, LDS, stream>>>(p);
}
```

```cpp
#include <hip/hip_runtime.h>
#include <stdint.h>
#define ATTN_FAST_ASM \
  "s_mov_b32 %[keep], m0\n\t" \
  "ds_read_b128 v[168:171], %[ka]\n\t" \
  "ds_read_b128 v[172:175], %[ka] offset:1024\n\t" \
  "ds_read_b128 v[176:179], %[va]\n\t" \
  "ds_read_b128 v[184:187], %[va] offset:512\n\t" \
  "v_mov_b32 v164, 0\n\t" \
  "v_mov_b32 v165, 0\n\t" \
  "v_mov_b32 v166, 0\n\t" \
  "v_mov_b32 v167, 0\n\t" \
  "v_mov_b32 v188, 0\n\t" \
  "v_mov_b32 v189, 0\n\t" \
  "v_mov_b32 v190, 0\n\t" \
  "v_mov_b32 v191, 0\n\t" \
  "s_waitcnt lgkmcnt(3)\n\t" \
  "v_mfma_f32_32x32x16_bf16 v[128:143], v[168:171], %[q], %[nc]\n\t" \
  "s_nop 10\n\t" \
  "s_waitcnt lgkmcnt(2)\n\t" \
  "v_mfma_f32_32x32x16_bf16 v[144:159], v[172:175], %[q], %[nc]\n\t" \
  "ds_read_b128 v[168:171], %[ka] offset:2048\n\t" \
  "s_mov_b32 m0, %[ld]\n\t" \
  "v_exp_f32 v128, v128\n\t" \
  "v_exp_f32 v129, v129\n\t" \
  "global_load_lds_dwordx4 %[go], %[kg] sc1\n\t" \
  "v_exp_f32 v130, v130\n\t" \
  "v_exp_f32 v131, v131\n\t" \
  "v_cvt_pk_bf16_f32 v160, v128, v129\n\t" \
  "v_exp_f32 v132, v132\n\t" \
  "v_exp_f32 v133, v133\n\t" \
  "v_cvt_pk_bf16_f32 v161, v130, v131\n\t" \
  "v_mfma_f32_32x32x16_bf16 %[o], v[188:191], v[164:167], %[o]\n\t" \
  "ds_read_b128 v[180:183], %[va] offset:1024\n\t" \
  "ds_read_b128 v[188:191], %[va] offset:1536\n\t" \
  "s_add_u32 m0, %[ld], 0x10000\n\t" \
  "v_exp_f32 v134, v134\n\t" \
  "v_exp_f32 v135, v135\n\t" \
  "global_load_lds_dwordx4 %[go], %[vg] sc1\n\t" \
  "s_add_u32 %[ld], %[ld], 0x2000\n\t" \
  "v_cvt_pk_bf16_f32 v162, v132, v133\n\t" \
  "v_exp_f32 v136, v136\n\t" \
  "v_exp_f32 v137, v137\n\t" \
  "v_cvt_pk_bf16_f32 v163, v134, v135\n\t" \
  "v_add_u32 %[go], 0x2000, %[go]\n\t" \
  "v_exp_f32 v138, v138\n\t" \
  "v_exp_f32 v139, v139\n\t" \
  "s_waitcnt lgkmcnt(4)\n\t" \
  "v_mfma_f32_32x32x16_bf16 %[o], v[176:179], v[160:163], %[o]\n\t" \
  "v_cvt_pk_bf16_f32 v164, v136, v137\n\t" \
  "v_exp_f32 v140, v140\n\t" \
  "v_exp_f32 v141, v141\n\t" \
  "v_cvt_pk_bf16_f32 v165, v138, v139\n\t" \
  "v_exp_f32 v142, v142\n\t" \
  "v_exp_f32 v143, v143\n\t" \
  "v_cvt_pk_bf16_f32 v166, v140, v141\n\t" \
  "v_cvt_pk_bf16_f32 v167, v142, v143\n\t" \
  "s_waitcnt lgkmcnt(2)\n\t" \
  "v_mfma_f32_32x32x16_bf16 v[128:143], v[168:171], %[q], %[nc]\n\t" \
  "ds_read_b128 v[172:175], %[ka] offset:3072\n\t" \
  "s_mov_b32 m0, %[ld]\n\t" \
  "v_exp_f32 v144, v144\n\t" \
  "v_exp_f32 v145, v145\n\t" \
  "global_load_lds_dwordx4 %[go], %[kg] sc1\n\t" \
  "v_exp_f32 v146, v146\n\t" \
  "v_exp_f32 v147, v147\n\t" \
  "v_cvt_pk_bf16_f32 v160, v144, v145\n\t" \
  "v_exp_f32 v148, v148\n\t" \
  "v_exp_f32 v149, v149\n\t" \
  "v_cvt_pk_bf16_f32 v161, v146, v147\n\t" \
  "v_mfma_f32_32x32x16_bf16 %[o], v[184:187], v[164:167], %[o]\n\t" \
  "ds_read_b128 v[176:179], %[va] offset:2048\n\t" \
  "ds_read_b128 v[184:187], %[va] offset:2560\n\t" \
  "s_add_u32 m0, %[ld], 0x10000\n\t" \
  "v_exp_f32 v150, v150\n\t" \
  "v_exp_f32 v151, v151\n\t" \
  "global_load_lds_dwordx4 %[go], %[vg] sc1\n\t" \
  "s_add_u32 %[ld], %[ld], 0x2000\n\t" \
  "v_cvt_pk_bf16_f32 v162, v148, v149\n\t" \
  "v_exp_f32 v152, v152\n\t" \
  "v_exp_f32 v153, v153\n\t" \
  "v_cvt_pk_bf16_f32 v163, v150, v151\n\t" \
  "v_add_u32 %[go], 0x2000, %[go]\n\t" \
  "v_exp_f32 v154, v154\n\t" \
  "v_exp_f32 v155, v155\n\t" \
  "s_waitcnt lgkmcnt(4)\n\t" \
  "v_mfma_f32_32x32x16_bf16 %[o], v[180:183], v[160:163], %[o]\n\t" \
  "v_cvt_pk_bf16_f32 v164, v152, v153\n\t" \
  "v_exp_f32 v156, v156\n\t" \
  "v_exp_f32 v157, v157\n\t" \
  "v_cvt_pk_bf16_f32 v165, v154, v155\n\t" \
  "v_exp_f32 v158, v158\n\t" \
  "v_exp_f32 v159, v159\n\t" \
  "v_cvt_pk_bf16_f32 v166, v156, v157\n\t" \
  "v_cvt_pk_bf16_f32 v167, v158, v159\n\t" \
  "s_waitcnt lgkmcnt(2)\n\t" \
  "v_mfma_f32_32x32x16_bf16 v[144:159], v[172:175], %[q], %[nc]\n\t" \
  "ds_read_b128 v[168:171], %[ka] offset:4096\n\t" \
  "s_mov_b32 m0, %[ld]\n\t" \
  "v_exp_f32 v128, v128\n\t" \
  "v_exp_f32 v129, v129\n\t" \
  "global_load_lds_dwordx4 %[go], %[kg] sc1\n\t" \
  "v_exp_f32 v130, v130\n\t" \
  "v_exp_f32 v131, v131\n\t" \
  "v_cvt_pk_bf16_f32 v160, v128, v129\n\t" \
  "v_exp_f32 v132, v132\n\t" \
  "v_exp_f32 v133, v133\n\t" \
  "v_cvt_pk_bf16_f32 v161, v130, v131\n\t" \
  "v_mfma_f32_32x32x16_bf16 %[o], v[188:191], v[164:167], %[o]\n\t" \
  "ds_read_b128 v[180:183], %[va] offset:3072\n\t" \
  "ds_read_b128 v[188:191], %[va] offset:3584\n\t" \
  "s_add_u32 m0, %[ld], 0x10000\n\t" \
  "v_exp_f32 v134, v134\n\t" \
  "v_exp_f32 v135, v135\n\t" \
  "global_load_lds_dwordx4 %[go], %[vg] sc1\n\t" \
  "s_add_u32 %[ld], %[ld], 0x2000\n\t" \
  "v_cvt_pk_bf16_f32 v162, v132, v133\n\t" \
  "v_exp_f32 v136, v136\n\t" \
  "v_exp_f32 v137, v137\n\t" \
  "v_cvt_pk_bf16_f32 v163, v134, v135\n\t" \
  "v_add_u32 %[go], 0x2000, %[go]\n\t" \
  "v_exp_f32 v138, v138\n\t" \
  "v_exp_f32 v139, v139\n\t" \
  "s_waitcnt lgkmcnt(4)\n\t" \
  "v_mfma_f32_32x32x16_bf16 %[o], v[176:179], v[160:163], %[o]\n\t" \
  "v_cvt_pk_bf16_f32 v164, v136, v137\n\t" \
  "v_exp_f32 v140, v140\n\t" \
  "v_exp_f32 v141, v141\n\t" \
  "v_cvt_pk_bf16_f32 v165, v138, v139\n\t" \
  "v_exp_f32 v142, v142\n\t" \
  "v_exp_f32 v143, v143\n\t" \
  "v_cvt_pk_bf16_f32 v166, v140, v141\n\t" \
  "v_cvt_pk_bf16_f32 v167, v142, v143\n\t" \
  "s_waitcnt lgkmcnt(2)\n\t" \
  "v_mfma_f32_32x32x16_bf16 v[128:143], v[168:171], %[q], %[nc]\n\t" \
  "ds_read_b128 v[172:175], %[ka] offset:5120\n\t" \
  "s_mov_b32 m0, %[ld]\n\t" \
  "v_exp_f32 v144, v144\n\t" \
  "v_exp_f32 v145, v145\n\t" \
  "global_load_lds_dwordx4 %[go], %[kg] sc1\n\t" \
  "v_exp_f32 v146, v146\n\t" \
  "v_exp_f32 v147, v147\n\t" \
  "v_cvt_pk_bf16_f32 v160, v144, v145\n\t" \
  "v_exp_f32 v148, v148\n\t" \
  "v_exp_f32 v149, v149\n\t" \
  "v_cvt_pk_bf16_f32 v161, v146, v147\n\t" \
  "v_mfma_f32_32x32x16_bf16 %[o], v[184:187], v[164:167], %[o]\n\t" \
  "ds_read_b128 v[176:179], %[va] offset:4096\n\t" \
  "ds_read_b128 v[184:187], %[va] offset:4608\n\t" \
  "s_add_u32 m0, %[ld], 0x10000\n\t" \
  "v_exp_f32 v150, v150\n\t" \
  "v_exp_f32 v151, v151\n\t" \
  "global_load_lds_dwordx4 %[go], %[vg] sc1\n\t" \
  "s_add_u32 %[ld], %[ld], 0x2000\n\t" \
  "v_cvt_pk_bf16_f32 v162, v148, v149\n\t" \
  "v_exp_f32 v152, v152\n\t" \
  "v_exp_f32 v153, v153\n\t" \
  "v_cvt_pk_bf16_f32 v163, v150, v151\n\t" \
  "v_add_u32 %[go], 0x2000, %[go]\n\t" \
  "v_exp_f32 v154, v154\n\t" \
  "v_exp_f32 v155, v155\n\t" \
  "s_waitcnt lgkmcnt(4)\n\t" \
  "v_mfma_f32_32x32x16_bf16 %[o], v[180:183], v[160:163], %[o]\n\t" \
  "v_cvt_pk_bf16_f32 v164, v152, v153\n\t" \
  "v_exp_f32 v156, v156\n\t" \
  "v_exp_f32 v157, v157\n\t" \
  "v_cvt_pk_bf16_f32 v165, v154, v155\n\t" \
  "v_exp_f32 v158, v158\n\t" \
  "v_exp_f32 v159, v159\n\t" \
  "v_cvt_pk_bf16_f32 v166, v156, v157\n\t" \
  "v_cvt_pk_bf16_f32 v167, v158, v159\n\t" \
  "v_add_u32 %[ka], 0x1000, %[ka]\n\t" \
  "v_add_u32 %[va], %[vs], %[va]\n\t" \
  "s_waitcnt lgkmcnt(2)\n\t" \
  "v_mfma_f32_32x32x16_bf16 v[144:159], v[172:175], %[q], %[nc]\n\t" \
  "ds_read_b128 v[168:171], %[ka] offset:2048\n\t" \
  "s_mov_b32 m0, %[ld]\n\t" \
  "v_exp_f32 v128, v128\n\t" \
  "v_exp_f32 v129, v129\n\t" \
  "global_load_lds_dwordx4 %[go], %[kg] sc1\n\t" \
  "v_exp_f32 v130, v130\n\t" \
  "v_exp_f32 v131, v131\n\t" \
  "v_cvt_pk_bf16_f32 v160, v128, v129\n\t" \
  "v_exp_f32 v132, v132\n\t" \
  "v_exp_f32 v133, v133\n\t" \
  "v_cvt_pk_bf16_f32 v161, v130, v131\n\t" \
  "v_mfma_f32_32x32x16_bf16 %[o], v[188:191], v[164:167], %[o]\n\t" \
  "ds_read_b128 v[180:183], %[va] offset:1024\n\t" \
  "ds_read_b128 v[188:191], %[va] offset:1536\n\t" \
  "s_add_u32 m0, %[ld], 0x10000\n\t" \
  "v_exp_f32 v134, v134\n\t" \
  "v_exp_f32 v135, v135\n\t" \
  "global_load_lds_dwordx4 %[go], %[vg] sc1\n\t" \
  "s_add_u32 %[ld], %[ld], 0x2000\n\t" \
  "v_cvt_pk_bf16_f32 v162, v132, v133\n\t" \
  "v_exp_f32 v136, v136\n\t" \
  "v_exp_f32 v137, v137\n\t" \
  "v_cvt_pk_bf16_f32 v163, v134, v135\n\t" \
  "v_add_u32 %[go], 0x2000, %[go]\n\t" \
  "v_exp_f32 v138, v138\n\t" \
  "v_exp_f32 v139, v139\n\t" \
  "s_waitcnt lgkmcnt(4)\n\t" \
  "v_mfma_f32_32x32x16_bf16 %[o], v[176:179], v[160:163], %[o]\n\t" \
  "v_cvt_pk_bf16_f32 v164, v136, v137\n\t" \
  "v_exp_f32 v140, v140\n\t" \
  "v_exp_f32 v141, v141\n\t" \
  "v_cvt_pk_bf16_f32 v165, v138, v139\n\t" \
  "v_exp_f32 v142, v142\n\t" \
  "v_exp_f32 v143, v143\n\t" \
  "v_cvt_pk_bf16_f32 v166, v140, v141\n\t" \
  "v_cvt_pk_bf16_f32 v167, v142, v143\n\t" \
  "s_waitcnt lgkmcnt(2)\n\t" \
  "v_mfma_f32_32x32x16_bf16 v[128:143], v[168:171], %[q], %[nc]\n\t" \
  "ds_read_b128 v[172:175], %[ka] offset:3072\n\t" \
  "s_mov_b32 m0, %[ld]\n\t" \
  "v_exp_f32 v144, v144\n\t" \
  "v_exp_f32 v145, v145\n\t" \
  "global_load_lds_dwordx4 %[go], %[kg] sc1\n\t" \
  "v_exp_f32 v146, v146\n\t" \
  "v_exp_f32 v147, v147\n\t" \
  "v_cvt_pk_bf16_f32 v160, v144, v145\n\t" \
  "v_exp_f32 v148, v148\n\t" \
  "v_exp_f32 v149, v149\n\t" \
  "v_cvt_pk_bf16_f32 v161, v146, v147\n\t" \
  "v_mfma_f32_32x32x16_bf16 %[o], v[184:187], v[164:167], %[o]\n\t" \
  "ds_read_b128 v[176:179], %[va] offset:2048\n\t" \
  "ds_read_b128 v[184:187], %[va] offset:2560\n\t" \
  "s_add_u32 m0, %[ld], 0x10000\n\t" \
  "v_exp_f32 v150, v150\n\t" \
  "v_exp_f32 v151, v151\n\t" \
  "global_load_lds_dwordx4 %[go], %[vg] sc1\n\t" \
  "s_add_u32 %[ld], %[ld], 0x2000\n\t" \
  "v_cvt_pk_bf16_f32 v162, v148, v149\n\t" \
  "v_exp_f32 v152, v152\n\t" \
  "v_exp_f32 v153, v153\n\t" \
  "v_cvt_pk_bf16_f32 v163, v150, v151\n\t" \
  "v_add_u32 %[go], 0x2000, %[go]\n\t" \
  "v_exp_f32 v154, v154\n\t" \
  "v_exp_f32 v155, v155\n\t" \
  "s_waitcnt lgkmcnt(4)\n\t" \
  "v_mfma_f32_32x32x16_bf16 %[o], v[180:183], v[160:163], %[o]\n\t" \
  "v_cvt_pk_bf16_f32 v164, v152, v153\n\t" \
  "v_exp_f32 v156, v156\n\t" \
  "v_exp_f32 v157, v157\n\t" \
  "v_cvt_pk_bf16_f32 v165, v154, v155\n\t" \
  "v_exp_f32 v158, v158\n\t" \
  "v_exp_f32 v159, v159\n\t" \
  "v_cvt_pk_bf16_f32 v166, v156, v157\n\t" \
  "v_cvt_pk_bf16_f32 v167, v158, v159\n\t" \
  "s_waitcnt lgkmcnt(2)\n\t" \
  "v_mfma_f32_32x32x16_bf16 v[144:159], v[172:175], %[q], %[nc]\n\t" \
  "ds_read_b128 v[168:171], %[ka] offset:4096\n\t" \
  "v_exp_f32 v128, v128\n\t" \
  "v_exp_f32 v129, v129\n\t" \
  "v_exp_f32 v130, v130\n\t" \
  "v_exp_f32 v131, v131\n\t" \
  "v_cvt_pk_bf16_f32 v160, v128, v129\n\t" \
  "v_exp_f32 v132, v132\n\t" \
  "v_exp_f32 v133, v133\n\t" \
  "v_cvt_pk_bf16_f32 v161, v130, v131\n\t" \
  "v_mfma_f32_32x32x16_bf16 %[o], v[188:191], v[164:167], %[o]\n\t" \
  "ds_read_b128 v[180:183], %[va] offset:3072\n\t" \
  "ds_read_b128 v[188:191], %[va] offset:3584\n\t" \
  "v_exp_f32 v134, v134\n\t" \
  "v_exp_f32 v135, v135\n\t" \
  "v_cvt_pk_bf16_f32 v162, v132, v133\n\t" \
  "v_exp_f32 v136, v136\n\t" \
  "v_exp_f32 v137, v137\n\t" \
  "v_cvt_pk_bf16_f32 v163, v134, v135\n\t" \
  "v_exp_f32 v138, v138\n\t" \
  "v_exp_f32 v139, v139\n\t" \
  "s_waitcnt lgkmcnt(4)\n\t" \
  "v_mfma_f32_32x32x16_bf16 %[o], v[176:179], v[160:163], %[o]\n\t" \
  "v_cvt_pk_bf16_f32 v164, v136, v137\n\t" \
  "v_exp_f32 v140, v140\n\t" \
  "v_exp_f32 v141, v141\n\t" \
  "v_cvt_pk_bf16_f32 v165, v138, v139\n\t" \
  "v_exp_f32 v142, v142\n\t" \
  "v_exp_f32 v143, v143\n\t" \
  "v_cvt_pk_bf16_f32 v166, v140, v141\n\t" \
  "v_cvt_pk_bf16_f32 v167, v142, v143\n\t" \
  "s_waitcnt lgkmcnt(2)\n\t" \
  "v_mfma_f32_32x32x16_bf16 v[128:143], v[168:171], %[q], %[nc]\n\t" \
  "ds_read_b128 v[172:175], %[ka] offset:5120\n\t" \
  "v_exp_f32 v144, v144\n\t" \
  "v_exp_f32 v145, v145\n\t" \
  "v_exp_f32 v146, v146\n\t" \
  "v_exp_f32 v147, v147\n\t" \
  "v_cvt_pk_bf16_f32 v160, v144, v145\n\t" \
  "v_exp_f32 v148, v148\n\t" \
  "v_exp_f32 v149, v149\n\t" \
  "v_cvt_pk_bf16_f32 v161, v146, v147\n\t" \
  "v_mfma_f32_32x32x16_bf16 %[o], v[184:187], v[164:167], %[o]\n\t" \
  "ds_read_b128 v[176:179], %[va] offset:4096\n\t" \
  "ds_read_b128 v[184:187], %[va] offset:4608\n\t" \
  "v_exp_f32 v150, v150\n\t" \
  "v_exp_f32 v151, v151\n\t" \
  "v_cvt_pk_bf16_f32 v162, v148, v149\n\t" \
  "v_exp_f32 v152, v152\n\t" \
  "v_exp_f32 v153, v153\n\t" \
  "v_cvt_pk_bf16_f32 v163, v150, v151\n\t" \
  "v_exp_f32 v154, v154\n\t" \
  "v_exp_f32 v155, v155\n\t" \
  "s_waitcnt lgkmcnt(4)\n\t" \
  "v_mfma_f32_32x32x16_bf16 %[o], v[180:183], v[160:163], %[o]\n\t" \
  "v_cvt_pk_bf16_f32 v164, v152, v153\n\t" \
  "v_exp_f32 v156, v156\n\t" \
  "v_exp_f32 v157, v157\n\t" \
  "v_cvt_pk_bf16_f32 v165, v154, v155\n\t" \
  "v_exp_f32 v158, v158\n\t" \
  "v_exp_f32 v159, v159\n\t" \
  "v_cvt_pk_bf16_f32 v166, v156, v157\n\t" \
  "v_cvt_pk_bf16_f32 v167, v158, v159\n\t" \
  "v_add_u32 %[ka], 0x1000, %[ka]\n\t" \
  "v_add_u32 %[va], %[vs], %[va]\n\t" \
  "s_mov_b32 m0, %[keep]\n\t" \
  "s_mov_b32 %[cnt], 2\n\t" \
  "s_waitcnt vmcnt(0)\n\t" \
  "s_barrier\n\t" \
  "s_cmp_eq_u32 %[sw], 99\n\t" \
  "s_cbranch_scc1 L_attn_y_%=\n\t" \
  "s_setprio 2\n\t" \
  "s_branch L_attn_loop_%=\n\t" \
  "L_attn_y_%=:\n\t" \
  "s_setprio 1\n\t" \
  "L_attn_loop_%=:\n\t" \
  "s_cmp_lg_u32 %[cnt], %[sw]\n\t" \
  "s_cbranch_scc1 L_attn_nosw_%=\n\t" \
  "s_setprio 0\n\t" \
  "L_attn_nosw_%=:\n\t" \
  "s_waitcnt lgkmcnt(2)\n\t" \
  "v_mfma_f32_32x32x16_bf16 v[144:159], v[172:175], %[q], %[nc]\n\t" \
  "ds_read_b128 v[168:171], %[ka] offset:2048\n\t" \
  "v_exp_f32 v128, v128\n\t" \
  "v_exp_f32 v129, v129\n\t" \
  "v_exp_f32 v130, v130\n\t" \
  "v_exp_f32 v131, v131\n\t" \
  "v_cvt_pk_bf16_f32 v160, v128, v129\n\t" \
  "v_exp_f32 v132, v132\n\t" \
  "v_exp_f32 v133, v133\n\t" \
  "v_cvt_pk_bf16_f32 v161, v130, v131\n\t" \
  "v_mfma_f32_32x32x16_bf16 %[o], v[188:191], v[164:167], %[o]\n\t" \
  "ds_read_b128 v[180:183], %[va] offset:1024\n\t" \
  "ds_read_b128 v[188:191], %[va] offset:1536\n\t" \
  "v_exp_f32 v134, v134\n\t" \
  "v_exp_f32 v135, v135\n\t" \
  "v_cvt_pk_bf16_f32 v162, v132, v133\n\t" \
  "v_exp_f32 v136, v136\n\t" \
  "v_exp_f32 v137, v137\n\t" \
  "v_cvt_pk_bf16_f32 v163, v134, v135\n\t" \
  "v_exp_f32 v138, v138\n\t" \
  "v_exp_f32 v139, v139\n\t" \
  "s_waitcnt lgkmcnt(4)\n\t" \
  "v_mfma_f32_32x32x16_bf16 %[o], v[176:179], v[160:163], %[o]\n\t" \
  "v_cvt_pk_bf16_f32 v164, v136, v137\n\t" \
  "v_exp_f32 v140, v140\n\t" \
  "v_exp_f32 v141, v141\n\t" \
  "v_cvt_pk_bf16_f32 v165, v138, v139\n\t" \
  "v_exp_f32 v142, v142\n\t" \
  "v_exp_f32 v143, v143\n\t" \
  "v_cvt_pk_bf16_f32 v166, v140, v141\n\t" \
  "v_cvt_pk_bf16_f32 v167, v142, v143\n\t" \
  "s_waitcnt lgkmcnt(2)\n\t" \
  "v_mfma_f32_32x32x16_bf16 v[128:143], v[168:171], %[q], %[nc]\n\t" \
  "ds_read_b128 v[172:175], %[ka] offset:3072\n\t" \
  "v_exp_f32 v144, v144\n\t" \
  "v_exp_f32 v145, v145\n\t" \
  "v_exp_f32 v146, v146\n\t" \
  "v_exp_f32 v147, v147\n\t" \
  "v_cvt_pk_bf16_f32 v160, v144, v145\n\t" \
  "v_exp_f32 v148, v148\n\t" \
  "v_exp_f32 v149, v149\n\t" \
  "v_cvt_pk_bf16_f32 v161, v146, v147\n\t" \
  "v_mfma_f32_32x32x16_bf16 %[o], v[184:187], v[164:167], %[o]\n\t" \
  "ds_read_b128 v[176:179], %[va] offset:2048\n\t" \
  "ds_read_b128 v[184:187], %[va] offset:2560\n\t" \
  "v_exp_f32 v150, v150\n\t" \
  "v_exp_f32 v151, v151\n\t" \
  "v_cvt_pk_bf16_f32 v162, v148, v149\n\t" \
  "v_exp_f32 v152, v152\n\t" \
  "v_exp_f32 v153, v153\n\t" \
  "v_cvt_pk_bf16_f32 v163, v150, v151\n\t" \
  "v_exp_f32 v154, v154\n\t" \
  "v_exp_f32 v155, v155\n\t" \
  "s_waitcnt lgkmcnt(4)\n\t" \
  "v_mfma_f32_32x32x16_bf16 %[o], v[180:183], v[160:163], %[o]\n\t" \
  "v_cvt_pk_bf16_f32 v164, v152, v153\n\t" \
  "v_exp_f32 v156, v156\n\t" \
  "v_exp_f32 v157, v157\n\t" \
  "v_cvt_pk_bf16_f32 v165, v154, v155\n\t" \
  "v_exp_f32 v158, v158\n\t" \
  "v_exp_f32 v159, v159\n\t" \
  "v_cvt_pk_bf16_f32 v166, v156, v157\n\t" \
  "v_cvt_pk_bf16_f32 v167, v158, v159\n\t" \
  "s_waitcnt lgkmcnt(2)\n\t" \
  "v_mfma_f32_32x32x16_bf16 v[144:159], v[172:175], %[q], %[nc]\n\t" \
  "ds_read_b128 v[168:171], %[ka] offset:4096\n\t" \
  "v_exp_f32 v128, v128\n\t" \
  "v_exp_f32 v129, v129\n\t" \
  "v_exp_f32 v130, v130\n\t" \
  "v_exp_f32 v131, v131\n\t" \
  "v_cvt_pk_bf16_f32 v160, v128, v129\n\t" \
  "v_exp_f32 v132, v132\n\t" \
  "v_exp_f32 v133, v133\n\t" \
  "v_cvt_pk_bf16_f32 v161, v130, v131\n\t" \
  "v_mfma_f32_32x32x16_bf16 %[o], v[188:191], v[164:167], %[o]\n\t" \
  "ds_read_b128 v[180:183], %[va] offset:3072\n\t" \
  "ds_read_b128 v[188:191], %[va] offset:3584\n\t" \
  "v_exp_f32 v134, v134\n\t" \
  "v_exp_f32 v135, v135\n\t" \
  "v_cvt_pk_bf16_f32 v162, v132, v133\n\t" \
  "v_exp_f32 v136, v136\n\t" \
  "v_exp_f32 v137, v137\n\t" \
  "v_cvt_pk_bf16_f32 v163, v134, v135\n\t" \
  "v_exp_f32 v138, v138\n\t" \
  "v_exp_f32 v139, v139\n\t" \
  "s_waitcnt lgkmcnt(4)\n\t" \
  "v_mfma_f32_32x32x16_bf16 %[o], v[176:179], v[160:163], %[o]\n\t" \
  "v_cvt_pk_bf16_f32 v164, v136, v137\n\t" \
  "v_exp_f32 v140, v140\n\t" \
  "v_exp_f32 v141, v141\n\t" \
  "v_cvt_pk_bf16_f32 v165, v138, v139\n\t" \
  "v_exp_f32 v142, v142\n\t" \
  "v_exp_f32 v143, v143\n\t" \
  "v_cvt_pk_bf16_f32 v166, v140, v141\n\t" \
  "v_cvt_pk_bf16_f32 v167, v142, v143\n\t" \
  "s_waitcnt lgkmcnt(2)\n\t" \
  "v_mfma_f32_32x32x16_bf16 v[128:143], v[168:171], %[q], %[nc]\n\t" \
  "ds_read_b128 v[172:175], %[ka] offset:5120\n\t" \
  "v_exp_f32 v144, v144\n\t" \
  "v_exp_f32 v145, v145\n\t" \
  "v_exp_f32 v146, v146\n\t" \
  "v_exp_f32 v147, v147\n\t" \
  "v_cvt_pk_bf16_f32 v160, v144, v145\n\t" \
  "v_exp_f32 v148, v148\n\t" \
  "v_exp_f32 v149, v149\n\t" \
  "v_cvt_pk_bf16_f32 v161, v146, v147\n\t" \
  "v_mfma_f32_32x32x16_bf16 %[o], v[184:187], v[164:167], %[o]\n\t" \
  "ds_read_b128 v[176:179], %[va] offset:4096\n\t" \
  "ds_read_b128 v[184:187], %[va] offset:4608\n\t" \
  "v_exp_f32 v150, v150\n\t" \
  "v_exp_f32 v151, v151\n\t" \
  "v_cvt_pk_bf16_f32 v162, v148, v149\n\t" \
  "v_exp_f32 v152, v152\n\t" \
  "v_exp_f32 v153, v153\n\t" \
  "v_cvt_pk_bf16_f32 v163, v150, v151\n\t" \
  "v_exp_f32 v154, v154\n\t" \
  "v_exp_f32 v155, v155\n\t" \
  "s_waitcnt lgkmcnt(4)\n\t" \
  "v_mfma_f32_32x32x16_bf16 %[o], v[180:183], v[160:163], %[o]\n\t" \
  "v_cvt_pk_bf16_f32 v164, v152, v153\n\t" \
  "v_exp_f32 v156, v156\n\t" \
  "v_exp_f32 v157, v157\n\t" \
  "v_cvt_pk_bf16_f32 v165, v154, v155\n\t" \
  "v_exp_f32 v158, v158\n\t" \
  "v_exp_f32 v159, v159\n\t" \
  "v_cvt_pk_bf16_f32 v166, v156, v157\n\t" \
  "v_cvt_pk_bf16_f32 v167, v158, v159\n\t" \
  "v_add_u32 %[ka], 0x1000, %[ka]\n\t" \
  "v_add_u32 %[va], %[vs], %[va]\n\t" \
  "s_add_u32 %[cnt], %[cnt], 1\n\t" \
  "s_cmp_lt_u32 %[cnt], 16\n\t" \
  "s_cbranch_scc1 L_attn_loop_%=\n\t" \
  "s_waitcnt lgkmcnt(0)\n\t" \
  "v_mfma_f32_32x32x16_bf16 %[o], v[188:191], v[164:167], %[o]\n\t" \
  "s_setprio 0\n\t" \
  "s_nop 15\n\t" \
  ""
#define ATTN_FAST_CLOBBERS "v128", "v129", "v130", "v131", "v132", "v133", "v134", "v135", "v136", "v137", "v138", "v139", "v140", "v141", "v142", "v143", "v144", "v145", "v146", "v147", "v148", "v149", "v150", "v151", "v152", "v153", "v154", "v155", "v156", "v157", "v158", "v159", "v160", "v161", "v162", "v163", "v164", "v165", "v166", "v167", "v168", "v169", "v170", "v171", "v172", "v173", "v174", "v175", "v176", "v177", "v178", "v179", "v180", "v181", "v182", "v183", "v184", "v185", "v186", "v187", "v188", "v189", "v190", "v191"

typedef short bf16x8 __attribute__((ext_vector_type(8)));
typedef short s16x4 __attribute__((ext_vector_type(4)));
typedef float f32x4 __attribute__((ext_vector_type(4)));
typedef float f32x2 __attribute__((ext_vector_type(2)));
typedef float f32x16 __attribute__((ext_vector_type(16)));
typedef unsigned u32x4 __attribute__((ext_vector_type(4)));
typedef unsigned u32x2 __attribute__((ext_vector_type(2)));
typedef __bf16 bf16x2_t __attribute__((ext_vector_type(2)));
#define LAS __attribute__((address_space(3)))
#define GAS __attribute__((address_space(1)))
typedef const LAS char* lds_cptr;
typedef LAS char* lds_ptr;
typedef GAS unsigned gu32;
typedef GAS unsigned long long gu64;
typedef __amdgpu_buffer_rsrc_t rsrc_t;

constexpr int SEQ = 2048, DM = 512, NH = 32, FF = 1376;
constexpr float EPS = 1e-5f;
constexpr float QSCALE = 0.25f * 1.4426950408889634f;

constexpr unsigned WS_Q = 0, WS_K = 2u << 20, WS_V = 4u << 20, WS_O = 6u << 20, WS_H = 8u << 20, WS_ACT = 10u << 20;
constexpr unsigned WS_KMAX = 16u << 20, WS_HST = (16u << 20) + (64u << 10);
constexpr unsigned WS_WOF = 17u << 20, WS_WDF = WS_WOF + 512 * 1024;
constexpr unsigned WS_FLAG = 20u << 20;
constexpr unsigned FL_EPOCH = 0, FL_TMO = 256, FL_1 = 1024, FL_2 = 4096, FL_3 = 8192, FL_4 = 12288;
constexpr unsigned WS_USED = 24u << 20;
constexpr size_t WS_DBG = 64u << 20;

#define TS(i) do {} while (0)
#define TS_BEGIN() do {} while (0)
#define TS_END() do {} while (0)
#define TSARG
#define TSPASS

struct Params {
  const char* x; const char* wqkv; const char* wo; const char* wg; const char* wu; const char* wd;
  char* out; char* ws;
};

#define MFMA16(a, b, c) __builtin_amdgcn_mfma_f32_16x16x32_bf16(a, b, c, 0, 0, 0)
#define MFMA32(a, b, c) __builtin_amdgcn_mfma_f32_32x32x16_bf16(a, b, c, 0, 0, 0)
#define PIN(x) asm volatile("" : "+v"(x))
#define SB() __builtin_amdgcn_sched_barrier(0)
#define RLX_AGENT __ATOMIC_RELAXED, __HIP_MEMORY_SCOPE_AGENT

template <int N> __device__ __forceinline__ void wait_bar() {
  asm volatile("s_waitcnt vmcnt(%0) lgkmcnt(0)\n\ts_barrier" ::"n"(N) : "memory");
}
template <int N> __device__ __forceinline__ void wait_vm() { asm volatile("s_waitcnt vmcnt(%0)" ::"n"(N) : "memory"); }
__device__ __forceinline__ void lds_bar() { asm volatile("s_waitcnt lgkmcnt(0)\n\ts_barrier" ::: "memory"); }
__device__ __forceinline__ void raw_bar() { asm volatile("s_barrier" ::: "memory"); }

template <bool SC1> __device__ __forceinline__ void glds16(const void* sbase, unsigned voff, unsigned lds_addr) {
  unsigned keep;
  if (SC1) asm volatile("s_mov_b32 %0, m0\n\ts_mov_b32 m0, %3\n\ts_nop 2\n\tglobal_load_lds_dwordx4 %1, %2 sc1\n\ts_mov_b32 m0, %0"
                        : "=&s"(keep) : "v"(voff), "s"(sbase), "s"(lds_addr) : "memory");
  else     asm volatile("s_mov_b32 %0, m0\n\ts_mov_b32 m0, %3\n\ts_nop 2\n\tglobal_load_lds_dwordx4 %1, %2\n\ts_mov_b32 m0, %0"
                        : "=&s"(keep) : "v"(voff), "s"(sbase), "s"(lds_addr) : "memory");
}
__device__ __forceinline__ void glds4_sc1(const void* sbase, unsigned voff, unsigned lds_addr) {
  unsigned keep;
  asm volatile("s_mov_b32 %0, m0\n\ts_mov_b32 m0, %3\n\ts_nop 2\n\tglobal_load_lds_dword %1, %2 sc1\n\ts_mov_b32 m0, %0"
               : "=&s"(keep) : "v"(voff), "s"(sbase), "s"(lds_addr) : "memory");
}
__device__ __forceinline__ bf16x8 ld128(lds_cptr p) { return *(const LAS bf16x8*)p; }
typedef short v4i16_t __attribute__((ext_vector_type(4)));
__device__ __forceinline__ s16x4 ldtr(lds_cptr p) {
  return __builtin_bit_cast(s16x4, __builtin_amdgcn_ds_read_tr16_b64_v4i16((LAS v4i16_t*)p));
}
__device__ __forceinline__ bf16x8 ldtr2(lds_cptr p, int second) {
  const s16x4 a = ldtr(p), b = ldtr(p + second);
  return (bf16x8){a[0], a[1], a[2], a[3], b[0], b[1], b[2], b[3]};
}
__device__ __forceinline__ unsigned cvtpk(float lo, float hi) {
  f32x2 v = {lo, hi}; bf16x2_t b = __builtin_convertvector(v, bf16x2_t); return __builtin_bit_cast(unsigned, b);
}
__device__ __forceinline__ u32x2 cvt4(f32x4 v) { return (u32x2){cvtpk(v[0], v[1]), cvtpk(v[2], v[3])}; }
__device__ __forceinline__ float bflo(unsigned w) { return __uint_as_float(w << 16); }
__device__ __forceinline__ float bfhi(unsigned w) { return __uint_as_float(w & 0xffff0000u); }
__device__ __forceinline__ f32x4 unpack4(u32x2 w) { return (f32x4){bflo(w[0]), bfhi(w[0]), bflo(w[1]), bfhi(w[1])}; }
__device__ __forceinline__ float bfr(float v) { return bflo(cvtpk(v, 0.f)); }
__device__ __forceinline__ void ln_consts(float sum, float sumsq, float& mean, float& rstd) {
  const float m = sum * (1.f / DM), var = fmaxf(sumsq * (1.f / DM) - m * m, 0.f);
  mean = bfr(m); rstd = __builtin_amdgcn_rcpf(bfr(__builtin_amdgcn_sqrtf(bfr(bfr(var) + EPS))));
}
__device__ __forceinline__ int rfl(int v) { return __builtin_amdgcn_readfirstlane(v); }
__device__ __forceinline__ bf16x8 ones8() { return (bf16x8){0x3F80, 0x3F80, 0x3F80, 0x3F80, 0x3F80, 0x3F80, 0x3F80, 0x3F80}; }
#define Z4 ((f32x4){0.f, 0.f, 0.f, 0.f})
__device__ __forceinline__ void st16(rsrc_t ws, unsigned off, u32x4 v) { __builtin_amdgcn_raw_buffer_store_b128(v, ws, off, 0, 16); }
__device__ __forceinline__ bf16x8 ldw16(rsrc_t ws, unsigned off) { return __builtin_bit_cast(bf16x8, __builtin_amdgcn_raw_buffer_load_b128(ws, off, 0, 16)); }
__device__ __forceinline__ u32x4 pair16(u32x2 X, u32x2 Y) {
  auto a = __builtin_amdgcn_permlane16_swap(X[0], Y[0], false, false);
  auto b = __builtin_amdgcn_permlane16_swap(X[1], Y[1], false, false);
  return (u32x4){a[0], b[0], a[1], b[1]};
}
__device__ __forceinline__ u32x4 pair32(u32x2 X, u32x2 Y) {
  auto a = __builtin_amdgcn_permlane32_swap(X[0], Y[0], false, false);
  auto b = __builtin_amdgcn_permlane32_swap(X[1], Y[1], false, false);
  return (u32x4){a[0], b[0], a[1], b[1]};
}
__device__ __forceinline__ float sum_x16(float v) { auto r = __builtin_amdgcn_permlane16_swap(__float_as_uint(v), __float_as_uint(v), false, false); return __uint_as_float(r[0]) + __uint_as_float(r[1]); }
__device__ __forceinline__ float sum_x32(float v) { auto r = __builtin_amdgcn_permlane32_swap(__float_as_uint(v), __float_as_uint(v), false, false); return __uint_as_float(r[0]) + __uint_as_float(r[1]); }
__device__ __forceinline__ float max_x16(float v) { auto r = __builtin_amdgcn_permlane16_swap(__float_as_uint(v), __float_as_uint(v), false, false); return fmaxf(__uint_as_float(r[0]), __uint_as_float(r[1])); }
__device__ __forceinline__ float lo_x32(float v) { auto r = __builtin_amdgcn_permlane32_swap(__float_as_uint(v), __float_as_uint(v), false, false); return __uint_as_float(r[0]); }
template <int N> __device__ __forceinline__ float ror16(float v) { return __uint_as_float(__builtin_amdgcn_update_dpp(0u, __float_as_uint(v), 0x120 + N, 0xf, 0xf, false)); }
__device__ __forceinline__ float max_row16(float v) { v = fmaxf(v, ror16<8>(v)); v = fmaxf(v, ror16<4>(v)); v = fmaxf(v, ror16<2>(v)); return fmaxf(v, ror16<1>(v)); }
template <int ND, int NM> __device__ __forceinline__ void weave() {
#pragma unroll
  for (int i = 0; i < ND; ++i) { __builtin_amdgcn_sched_group_barrier(0x008, 1, 0); __builtin_amdgcn_sched_group_barrier(0x100, 1, 0); }
  if (NM > ND) __builtin_amdgcn_sched_group_barrier(0x008, NM - ND, 0);
}

__device__ __forceinline__ void publish(char* ws, unsigned flag_off, unsigned tag) {
  if (threadIdx.x == 0) __hip_atomic_store((gu32*)(ws + WS_FLAG + flag_off), tag, RLX_AGENT);
}
template <int NW>
__device__ __forceinline__ void poll(char* ws, unsigned flag_off, unsigned tag) {
  const int lane = threadIdx.x & 63;
  gu32* f = (gu32*)(ws + WS_FLAG + flag_off);
  const unsigned long long t0 = __builtin_amdgcn_s_memrealtime();
  for (unsigned spins = 1;; ++spins) {
    bool ok = true;
    if (NW == 256) {
#pragma unroll
      for (int i = 0; i < 4; ++i) ok &= __hip_atomic_load(f + i * 64 + lane, RLX_AGENT) == tag;
    } else ok = __hip_atomic_load(f + (lane < NW ? lane : 0), RLX_AGENT) == tag;
    if (__all(ok)) break;
    if ((spins & 63u) == 0u && __builtin_amdgcn_s_memrealtime() - t0 > 20000000ull) {
      if (lane == 0) __hip_atomic_store((gu32*)(ws + WS_FLAG + FL_TMO), tag, RLX_AGENT);
      break;
    }
    __builtin_amdgcn_s_sleep(1);
  }
}
template <int NW> __device__ __forceinline__ void await(char* ws, unsigned flag_off, unsigned tag) {
  if (rfl(threadIdx.x >> 6) == 0) poll<NW>(ws, flag_off, tag);
  raw_bar();
}

struct F1 { bf16x8 a[4], w[3]; };
__device__ __forceinline__ void p1_qkv(const Params& p, rsrc_t ws, int b, int rb, int cb, char* smem TSARG) {
  const int tid = threadIdx.x, lane = tid & 63, wid = rfl(tid >> 6);
  const int r16 = lane & 15, g = lane >> 4;
  const unsigned lds0 = (unsigned)(size_t)(lds_ptr)smem;
  lds_cptr L = (lds_cptr)smem;
  constexpr int STG = 32768, ND = 3, NS = 8, RL = ND * STG, MS = RL + 8192;
  LAS f32x2* ms = (LAS f32x2*)(smem + MS);
  if (wid >= 4) {
    const int lw = wid - 4;
    const unsigned voffA = (unsigned)((16 * lw + (lane >> 3)) * DM + (((lane & 7) ^ (lane >> 3)) * 8)) * 2u;
    unsigned voffB[2];
#pragma unroll
    for (int i = 0; i < 2; ++i) { const int r = lane >> 3, c = lane & 7, ff = ((r >> 1) & 1) | (i << 1), j = (c >> 1) ^ ff;
      voffB[i] = (unsigned)((16 * lw + 8 * i + r) * (3 * DM) + j * 16 + (c & 1) * 8) * 2u; }
    const char* xa = p.x + (size_t)rb * 64 * DM * 2;
    const char* wb = p.wqkv + (size_t)cb * 64 * 2;
    auto issue = [&](int s) {
      const unsigned d = lds0 + (s % ND) * STG;
#pragma unroll
      for (int i = 0; i < 2; ++i) glds16<false>(xa + s * 128 + i * 8 * DM * 2, voffA, d + (2 * lw + i) * 1024);
#pragma unroll
      for (int i = 0; i < 2; ++i)
#pragma unroll
        for (int seg = 0; seg < 3; ++seg) glds16<false>(wb + (size_t)s * 64 * (3 * DM) * 2 + seg * DM * 2, voffB[i], d + 8192 + ((2 * lw + i) * 3 + seg) * 1024);
    };
    TS(20);
    issue(0); issue(1);
    TS(21);
    wait_bar<8>();
    TS(23);
    const int f0 = b * 8 + 2 * lw;
    auto rl_issue = [&](int i) {
      const int f = f0 + i, fw = f < 512 + 32 * 43 ? f : 0;
      const bool isd = fw >= 512; const int f2 = isd ? fw - 512 : fw, ct = isd ? f2 / 43 : f2 >> 4, ks = isd ? f2 - ct * 43 : f2 & 15;
      const char* src = (isd ? p.wd : p.wo) + ((size_t)ks * 32 * DM + ct * 16) * 2;
      const int t = lane >> 5, gg = (lane >> 3) & 3, q = (lane >> 1) & 3;
      glds16<false>(src, (unsigned)((8 * gg + 4 * t + q) * DM + (lane & 1) * 8) * 2u, lds0 + RL + (2 * lw + i) * 1024);
    };
#pragma unroll
    for (int j = 1; j < NS; ++j) {
      if (j + 1 < NS) issue(j + 1);
      if (j + 1 == NS - 1) { rl_issue(0); rl_issue(1); }
      if (j + 1 < NS - 1) wait_bar<8>(); else if (j + 1 == NS - 1) wait_bar<10>(); else wait_bar<2>();
    }
    wait_vm<0>();
#pragma unroll
    for (int i = 0; i < 2; ++i)
      if (f0 + i < 512 + 32 * 43) st16(ws, WS_WOF + (unsigned)(f0 + i) * 1024 + lane * 16, __builtin_bit_cast(u32x4, ldtr2(L + RL + (2 * lw + i) * 1024 + lane * 8, 512)));
    lds_bar();
  } else {
    f32x4 acc[3][4], cs[3], sx = Z4, sq = Z4;
    int aoff[2][4];
#pragma unroll
    for (int ks = 0; ks < 2; ++ks)
#pragma unroll
      for (int i = 0; i < 4; ++i) aoff[ks][i] = ((wid + i) & 3) * 2048 + (r16 >> 3) * 1024 + (r16 & 7) * 128 + (((4 * ks + g) ^ (r16 & 7)) << 4);
    int boff;
    { const int q = (lane >> 2) & 3, pp = lane & 3, ff = ((q >> 1) & 1) | ((g & 1) << 1);
      boff = 8192 + g * 3072 + q * 128 + ((wid ^ ff) << 5) + pp * 8; }
#pragma unroll
    for (int i = 0; i < 3; ++i) { cs[i] = Z4;
#pragma unroll
      for (int j = 0; j < 4; ++j) acc[i][j] = Z4; }
    const bf16x8 one = ones8();
    F1 fa, fb;
    auto rd = [&](F1& fr, int s, int ks) {
      lds_cptr B = L + (s % ND) * STG;
#pragma unroll
      for (int seg = 0; seg < 3; ++seg) fr.w[seg] = ldtr2(B + boff + ks * 12288 + seg * 1024, 512);
#pragma unroll
      for (int i = 0; i < 4; ++i) fr.a[i] = ld128(B + aoff[ks][i]);
    };
    auto mm = [&](const F1& fr) {
#pragma unroll
      for (int i = 0; i < 4; ++i) {
        acc[0][i] = MFMA16(fr.w[0], fr.a[i], acc[0][i]);
        acc[1][i] = MFMA16(fr.w[1], fr.a[i], acc[1][i]);
        acc[2][i] = MFMA16(fr.a[i], fr.w[2], acc[2][i]);
      }
      cs[0] = MFMA16(fr.w[0], one, cs[0]); cs[1] = MFMA16(fr.w[1], one, cs[1]); cs[2] = MFMA16(one, fr.w[2], cs[2]);
      sx = MFMA16(one, fr.a[0], sx); sq = MFMA16(fr.a[0], fr.a[0], sq);
    };
    lds_bar();
    TS(1);
    rd(fa, 0, 0); SB();
#pragma unroll
    for (int s = 0; s < NS; ++s) {
      rd(fb, s, 1); mm(fa); weave<10, 17>(); SB();
      if (s + 1 < NS) { lds_bar(); rd(fa, s + 1, 0); mm(fb); weave<10, 17>(); SB(); }
      else { mm(fb); SB(); }
    }
    TS(2);
    if ((r16 >> 2) == g) {
      const int e = r16 & 3; const float s2 = e == 0 ? sq[0] : e == 1 ? sq[1] : e == 2 ? sq[2] : sq[3];
      float mean, rstd; ln_consts(sx[0], s2, mean, rstd);
      ms[wid * 16 + r16] = (f32x2){mean, rstd};
    }
    lds_bar();
    TS(25);
    const int h = cb * 4 + wid;
    u32x2 qv[4], kv[4], vv[4];
    float kmx = 0.f;
#pragma unroll
    for (int i = 0; i < 4; ++i) {
      const int rt = (wid + i) & 3;
      const f32x2 st = ms[rt * 16 + r16];
      qv[i] = cvt4((acc[0][i] - st.x * cs[0]) * (st.y * QSCALE));
      { const f32x4 v = (acc[1][i] - st.x * cs[1]) * st.y; kv[i] = cvt4(v);
        kmx = fmaxf(kmx, sum_x32(sum_x16((v[0] * v[0] + v[1] * v[1]) + (v[2] * v[2] + v[3] * v[3])))); }
      { const f32x4 m01 = *(LAS f32x4*)(ms + rt * 16 + 4 * g), m23 = *(LAS f32x4*)(ms + rt * 16 + 4 * g + 2); const float cv = cs[2][0];
        vv[i] = (u32x2){cvtpk((acc[2][i][0] - m01[0] * cv) * m01[1], (acc[2][i][1] - m01[2] * cv) * m01[3]),
                        cvtpk((acc[2][i][2] - m23[0] * cv) * m23[1], (acc[2][i][3] - m23[2] * cv) * m23[3])}; }
    }
#pragma unroll
    for (int i = 0; i < 4; i += 2) {
      { const int rt = (wid + i + (g & 1)) & 3, grow = rb * 64 + rt * 16;
        const unsigned off = (((unsigned)h * 64 + (grow >> 5)) * 2 + (g >> 1)) * 512 + ((grow & 31) + r16) * 16;
        st16(ws, WS_K + off, pair16(kv[i], kv[i + 1])); st16(ws, WS_Q + off, pair16(qv[i], qv[i + 1])); }
      { const int rt = (wid + i + (g >> 1)) & 3, grow = rb * 64 + rt * 16;
        const unsigned off = ((((unsigned)h * 64 + (grow >> 5)) * 2 + (rt & 1)) * 2 + (g & 1)) * 256 + r16 * 16;
        st16(ws, WS_V + off, pair32(vv[i], vv[i + 1])); }
    }
    kmx = max_row16(kmx);
    if (lane == 0) __hip_atomic_store((gu32*)(p.ws + WS_KMAX) + h * 32 + rb, __float_as_uint(kmx), RLX_AGENT);
    TS(26);
  }
}

constexpr int A_K = 0, A_V = 65536, A_ONE = 131072, A_Q = A_ONE + 5120, A_KM = A_Q + 8192, A_BYTES = A_KM + 256;
constexpr float SAFE_BOUND = 48.f;
constexpr int PRIO_SWITCH = 13;

__device__ __forceinline__ void p2_attn(const Params& p, rsrc_t ws, int h, int qr, char* smem, unsigned tag TSARG) {
  const int tid = threadIdx.x, lane = tid & 63, wid = rfl(tid >> 6);
  const int r32 = lane & 31, hh = lane >> 5;
  const unsigned lds0 = (unsigned)(size_t)(lds_ptr)smem;
  lds_cptr L = (lds_cptr)smem;
  const char* kg = p.ws + WS_K + (size_t)h * 65536;
  const char* vg = p.ws + WS_V + (size_t)h * 65536;
  const unsigned vo = lane * 16;
  glds16<true>(p.ws + WS_Q + ((size_t)h * 64 + qr * 8 + wid) * 1024, vo, lds0 + A_Q + wid * 1024);
  glds4_sc1(p.ws + WS_KMAX + h * 128, (lane & 31) * 4, lds0 + A_KM);
#pragma unroll
  for (int i = 0; i < 2; ++i) {
    const int t = wid + 8 * i;
    glds16<true>(kg + t * 1024, vo, lds0 + A_K + t * 1024);
    glds16<true>(vg + t * 1024, vo, lds0 + A_V + t * 1024);
  }
  { const unsigned o1 = (tid & 31) < 2 ? 0x3F803F80u : 0u;
    *(LAS u32x2*)(smem + A_ONE + tid * 8) = (u32x2){o1, o1};
    if (tid < 128) *(LAS u32x2*)(smem + A_ONE + 4096 + tid * 8) = (u32x2){o1, o1}; }
  if (wid == 0) {
    poll<256>(p.ws, FL_1, tag);
    if (blockIdx.x == 0 && lane == 0) __hip_atomic_store((gu32*)(p.ws + WS_FLAG + FL_EPOCH), tag, RLX_AGENT);
  }
  wait_bar<0>();
  TS(4);
  const bf16x8 qf = ld128(L + A_Q + wid * 1024 + lane * 16);
  float c;
  { float km = *(const LAS float*)(L + A_KM + lane * 4);
    km = max_x16(max_row16(km));
    float q2 = 0.f;
#pragma unroll
    for (int j = 0; j < 8; ++j) { const float v = __uint_as_float((unsigned)(unsigned short)qf[j] << 16); q2 += v * v; }
    q2 = sum_x32(q2);
    c = sqrtf(q2 * km) * 1.02f + 0.01f; }
  const bool fast = __all(c <= SAFE_BOUND);
  const bool vlane = r32 < 16;
  int vaddr = (int)lds0 + (vlane ? A_V : A_ONE) + hh * 256 + (r32 & 15) * 16;
  const int vstep = vlane ? 4096 : 0;
  int kaddr = (int)lds0 + A_K + lane * 16;
  f32x16 o;
#pragma unroll
  for (int i = 0; i < 16; ++i) o[i] = 0.f;
  unsigned go = vo + (16 + wid) * 1024;
  unsigned ld = lds0 + A_K + (16 + wid) * 1024;
  if (fast) {
    f32x16 negc;
#pragma unroll
    for (int i = 0; i < 16; ++i) negc[i] = -c;
    int cnt, keep;
    const int sw = wid < 4 ? PRIO_SWITCH : 99;
    asm volatile(ATTN_FAST_ASM
                 : [o] "+v"(o), [ka] "+v"(kaddr), [va] "+v"(vaddr), [go] "+v"(go), [ld] "+s"(ld), [cnt] "=&s"(cnt), [keep] "=&s"(keep)
                 : [q] "v"(qf), [nc] "v"(negc), [vs] "v"(vstep), [sw] "s"(sw), [kg] "s"(kg), [vg] "s"(vg)
                 : "memory", "scc", ATTN_FAST_CLOBBERS);
  } else {
#pragma unroll
    for (int j = 0; j < 6; ++j) { glds16<true>(kg, go + j * 8192, ld + j * 8192); glds16<true>(vg, go + j * 8192, ld + 65536 + j * 8192); }
    float m = -INFINITY;
    vaddr -= (int)lds0; kaddr -= (int)lds0;
#pragma unroll 1
    for (int t4 = 0; t4 < 16; ++t4) {
      if (t4 == 2) wait_bar<0>();
#pragma unroll 1
      for (int u = 0; u < 4; ++u) {
        const int t = t4 * 4 + u;
        f32x16 z;
#pragma unroll
        for (int i = 0; i < 16; ++i) z[i] = 0.f;
        const f32x16 s = MFMA32(ld128(L + kaddr + t * 1024), qf, z);
        float mx = s[0];
#pragma unroll
        for (int i = 1; i < 16; ++i) mx = fmaxf(mx, s[i]);
        mx = fmaxf(mx, __shfl_xor(mx, 32));
        const float mn = fmaxf(m, mx);
        const float al = __builtin_amdgcn_exp2f(m - mn);
        m = mn;
#pragma unroll
        for (int i = 0; i < 16; ++i) o[i] *= al;
        u32x4 p0, p1;
#pragma unroll
        for (int i = 0; i < 4; ++i) {
          p0[i] = cvtpk(__builtin_amdgcn_exp2f(s[2 * i] - mn), __builtin_amdgcn_exp2f(s[2 * i + 1] - mn));
          p1[i] = cvtpk(__builtin_amdgcn_exp2f(s[8 + 2 * i] - mn), __builtin_amdgcn_exp2f(s[8 + 2 * i + 1] - mn));
        }
        const bf16x8 v0 = ld128(L + vaddr + u * 1024), v1 = ld128(L + vaddr + u * 1024 + 512);
        o = MFMA32(v0, __builtin_bit_cast(bf16x8, p0), o);
        o = MFMA32(v1, __builtin_bit_cast(bf16x8, p1), o);
      }
      vaddr += vstep;
    }
  }
  TS(5);
  const float inv = __builtin_amdgcn_rcpf(lo_x32(o[8]));
  const u32x4 ov = pair32((u32x2){cvtpk(o[0] * inv, o[1] * inv), cvtpk(o[2] * inv, o[3] * inv)}, (u32x2){cvtpk(o[4] * inv, o[5] * inv), cvtpk(o[6] * inv, o[7] * inv)});
  const int row = qr * 256 + wid * 32 + r32;
  st16(ws, WS_O + ((((unsigned)(row >> 4) * 16 + (h >> 1)) * 4 + (h & 1) * 2 + hh) * 16 + (row & 15)) * 16, ov);
}

template <int NKS, int NF, int TS0>
__device__ __forceinline__ void gemm64(const char* ag  , const bf16x8 (&wf)[NF], char* smem, f32x4 (&res)[2] TSARG) {
  const int tid = threadIdx.x, lane = tid & 63, wid = rfl(tid >> 6), wn = wid & 3, kh = wid >> 2;
  const unsigned lds0 = (unsigned)(size_t)(lds_ptr)smem;
  lds_cptr L = (lds_cptr)smem;
  constexpr int STG = 32768, NS = (NKS + 7) / 8, ND = NS < 4 ? NS : 4;
  auto issue = [&](int s) {
    const unsigned d = lds0 + (s % ND) * STG + ((wid >> 1) * 8 + (wid & 1) * 4) * 1024;
#pragma unroll
    for (int i = 0; i < 4; ++i) { int ks = s * 8 + (wid & 1) * 4 + i; if (s == NS - 1 && NKS % 8) ks = ks > NKS - 1 ? NKS - 1 : ks; glds16<true>(ag + ks * 1024, lane * 16, d + i * 1024); }
  };
  issue(0); issue(1);
  const int aoff = kh * 1024 + lane * 16;
  f32x4 acc[4] = {Z4, Z4, Z4, Z4};
  bf16x8 fa[2][4], fb[2][4];
  auto rd = [&](bf16x8 (&f)[2][4], int s, int hf) {
    lds_cptr B = L + (s % ND) * STG + aoff + hf * 4096;
#pragma unroll
    for (int i = 0; i < 2; ++i)
#pragma unroll
      for (int rt = 0; rt < 4; ++rt) f[i][rt] = ld128(B + rt * 8192 + i * 2048);
  };
  auto mm = [&](const bf16x8 (&f)[2][4], int f0) {
#pragma unroll
    for (int i = 0; i < 2; ++i)
      if (f0 + i < NF) {
#pragma unroll
        for (int rt = 0; rt < 4; ++rt) acc[rt] = MFMA16(wf[f0 + i], f[i][rt], acc[rt]);
      }
  };
  wait_bar<4>();
  TS(TS0);
  rd(fa, 0, 0);
  if (2 < NS) issue(2);
  SB();
#pragma unroll
  for (int s = 0; s < NS; ++s) {
    const bool h1 = 4 * s + 2 < NF;
    if (h1) { rd(fb, s, 1); mm(fa, 4 * s); weave<8, 8>(); } else mm(fa, 4 * s);
    SB();
    if (s + 1 < NS) {
      if (s + 2 < NS) wait_bar<4>(); else wait_bar<0>();
      rd(fa, s + 1, 0);
      if (h1) { mm(fb, 4 * s + 2); weave<8, 8>(); }
      SB();
      if (s + 3 < NS) issue(s + 3);
    } else if (h1) mm(fb, 4 * s + 2);
    SB();
  }
  TS(TS0 + 1);
  lds_bar();
  LAS f32x4* X = (LAS f32x4*)smem;
  if (kh == 0) { X[((wn * 2 + 1) * 2 + 0) * 64 + lane] = acc[2]; X[((wn * 2 + 1) * 2 + 1) * 64 + lane] = acc[3]; }
  else         { X[((wn * 2 + 0) * 2 + 0) * 64 + lane] = acc[0]; X[((wn * 2 + 0) * 2 + 1) * 64 + lane] = acc[1]; }
  lds_bar();
  const f32x4 o0 = X[((wn * 2 + kh) * 2 + 0) * 64 + lane], o1 = X[((wn * 2 + kh) * 2 + 1) * 64 + lane];
  if (kh == 0) { res[0] = acc[0] + o0; res[1] = acc[1] + o1; } else { res[0] = acc[2] + o0; res[1] = acc[3] + o1; }
}

constexpr int G_B = 0, G_BS = 24576, G_A = 3 * G_BS, G_AS = 16384, G_ST = G_A + 3 * G_AS, G_MS = G_ST + 8192, G_CS = G_MS + 1024;
__device__ __forceinline__ void w_voffs(unsigned (&voff)[3], int nu, int lane) {
#pragma unroll
  for (int i = 0; i < 3; ++i) {
    const int ch = i * 64 + lane, r = ch / 12, cp = ch - r * 12;
    int c = cp ^ (((r >> 3) & 1) << 1);
    if (c >= nu * 4) c -= 4;
    voff[i] = (unsigned)(r * FF + c * 8) * 2u;
  }
}
struct WLoad {
  const char* bg; const char* bu; unsigned voff[3]; unsigned dst;
  __device__ __forceinline__ void init(const Params& p, int fb, int lw, int lane, unsigned lds0) {
    const int u0 = fb < 11 ? 3 * fb : 33 + 2 * (fb - 11), nu = fb < 11 ? 3 : 2;
    const size_t o = ((size_t)lw * 16 * FF + u0 * 32) * 2;
    bg = p.wg + o; bu = p.wu + o; w_voffs(voff, nu, lane); dst = lds0 + G_B + lw * 3072;
  }
  __device__ __forceinline__ void issue(int s) const {
#pragma unroll
    for (int i = 0; i < 3; ++i) glds16<false>(bg + (size_t)s * 64 * FF * 2, voff[i], dst + (s % 3) * G_BS + i * 1024);
#pragma unroll
    for (int i = 0; i < 3; ++i) glds16<false>(bu + (size_t)s * 64 * FF * 2, voff[i], dst + (s % 3) * G_BS + 12288 + i * 1024);
  }
};
struct F4 { bf16x8 a[4], w[2][3]; };
__device__ __forceinline__ void p4_gateup(const Params& p, rsrc_t ws, int mb, int fb, char* smem, const WLoad& wl, int wct, bf16x8 (&wdf)[22] TSARG) {
  const int tid = threadIdx.x, lane = tid & 63, wid = rfl(tid >> 6);
  const int r16 = lane & 15, g = lane >> 4, kh = wid >> 2;
  const unsigned lds0 = (unsigned)(size_t)(lds_ptr)smem;
  lds_cptr L = (lds_cptr)smem;
  constexpr int NS = 8;
  const int u0 = fb < 11 ? 3 * fb : 33 + 2 * (fb - 11), nu = fb < 11 ? 3 : 2;
  auto wd_load = [&](int i) { const int ks = 2 * i + kh; wdf[i] = ldw16(ws, WS_WDF + ((unsigned)wct * 43 + (ks > 42 ? 42 : ks)) * 1024 + lane * 16); };
  if (wid >= 4) {
    const int lw = wid - 4;
    const char* ag = p.ws + WS_H + (size_t)(mb * 8 + 2 * lw) * 16384;
    auto issueA = [&](int s) {
      const unsigned d = lds0 + G_A + (s % 3) * G_AS + lw * 4096;
#pragma unroll
      for (int i = 0; i < 4; ++i) glds16<true>(ag + (i >> 1) * 16384 + (2 * s + (i & 1)) * 1024, lane * 16, d + i * 1024);
    };
    issueA(0); issueA(1);
#pragma unroll
    for (int i = 0; i < 2; ++i) glds16<true>(p.ws + WS_HST + (size_t)mb * 8192 + (lw * 2 + i) * 1024, lane * 16, lds0 + G_ST + (lw * 2 + i) * 1024);
    wait_bar<6>();
#pragma unroll
    for (int j = 1; j < NS; ++j) {
      if (j + 1 < NS) { issueA(j + 1); wl.issue(j + 1); wait_bar<10>(); } else wait_bar<0>();
      if (j == 1) {
        const int row = tid - 256;
        if (row < 128) {
          const f32x4 v0 = *(const LAS f32x4*)(L + G_ST + row * 64), v1 = *(const LAS f32x4*)(L + G_ST + row * 64 + 16), v2 = *(const LAS f32x4*)(L + G_ST + row * 64 + 32), v3 = *(const LAS f32x4*)(L + G_ST + row * 64 + 48);
          const float a = ((v0[0] + v0[2]) + (v1[0] + v1[2])) + ((v2[0] + v2[2]) + (v3[0] + v3[2])), bb = ((v0[1] + v0[3]) + (v1[1] + v1[3])) + ((v2[1] + v2[3]) + (v3[1] + v3[3]));
          float mean, rstd; ln_consts(a, bb, mean, rstd);
          *(LAS f32x2*)(smem + G_MS + row * 8) = (f32x2){rstd, -mean * rstd};
        }
      }
    }
#pragma unroll
    for (int i = 0; i < 22; ++i) wd_load(i);
    lds_bar();
  } else {
    const int wm = wid >> 1, wn = wid & 1;
    const int aoff = G_A + wm * 8192 + lane * 16;
    int boff[3];
#pragma unroll
    for (int i = 0; i < 3; ++i) {
      const int jt = 3 * wn + i, q = (lane >> 2) & 3, pp = lane & 3;
      boff[i] = G_B + (g >> 1) * 3072 + (8 * (g & 1) + q) * 192 + ((jt ^ (g & 1)) << 5) + pp * 8;
    }
    const int m0 = wm * 12288, m1 = (wm ^ 1) * 12288;
    f32x4 acc[2][3][4], cs[3];
#pragma unroll
    for (int i = 0; i < 3; ++i) { cs[i] = Z4;
#pragma unroll
      for (int j = 0; j < 4; ++j) { acc[0][i][j] = Z4; acc[1][i][j] = Z4; } }
    const bf16x8 one = ones8();
    F4 fa, fb2;
    auto rd = [&](F4& f, int s, int ks) {
      lds_cptr B = L + (s % 3) * G_BS, A = L + (s % 3) * G_AS;
#pragma unroll
      for (int i = 0; i < 4; ++i) f.a[i] = ld128(A + aoff + i * 2048 + ks * 1024);
#pragma unroll
      for (int i = 0; i < 3; ++i) { f.w[0][i] = ldtr2(B + boff[i] + m0 + ks * 6144, 768); f.w[1][i] = ldtr2(B + boff[i] + m1 + ks * 6144, 768); }
    };
    auto mm = [&](const F4& f) {
#pragma unroll
      for (int m = 0; m < 2; ++m)
#pragma unroll
        for (int i = 0; i < 3; ++i)
#pragma unroll
          for (int j = 0; j < 4; ++j) acc[m][i][j] = MFMA16(f.w[m][i], f.a[j], acc[m][i][j]);
#pragma unroll
      for (int i = 0; i < 3; ++i) cs[i] = MFMA16(f.w[0][i], one, cs[i]);
    };
    lds_bar();
    TS(11);
    rd(fa, 0, 0); SB();
#pragma unroll
    for (int s = 0; s < NS; ++s) {
      rd(fb2, s, 1); mm(fa); weave<16, 27>(); SB();
      if (s + 1 < NS) { lds_bar(); rd(fa, s + 1, 0); mm(fb2); weave<16, 27>(); SB(); }
      else { mm(fb2); SB(); }
    }
    TS(12);
#pragma unroll
    for (int i = 0; i < 11; ++i) wd_load(i);
    LAS float* csl = (LAS float*)(smem + G_CS);
    if (r16 == 0) {
#pragma unroll
      for (int i = 0; i < 3; ++i) *(LAS f32x4*)(csl + (((wm * 2 + wn) * 3 + i) * 16 + 4 * g)) = cs[i];
    }
    lds_bar();
    TS(33);
    const LAS f32x2* ms = (const LAS f32x2*)(smem + G_MS);
    f32x2 st[4];
#pragma unroll
    for (int j = 0; j < 4; ++j) st[j] = ms[wm * 64 + j * 16 + r16];
    constexpr float K1 = -2.885390081777927f * 0.7978845608028654f, K2 = -2.885390081777927f * 0.035677408136300125f;
#pragma unroll
    for (int i = 0; i < 3; ++i) {
      const int jt = 3 * wn + i, u = jt >> 1, jj = jt & 1;
      const f32x4 cg = *(LAS f32x4*)(csl + ((0 * 2 + wn) * 3 + i) * 16 + 4 * g), cu = *(LAS f32x4*)(csl + ((1 * 2 + wn) * 3 + i) * 16 + 4 * g);
      u32x2 av[4];
#pragma unroll
      for (int j = 0; j < 4; ++j) {
        const f32x4 ag2 = wm ? acc[1][i][j] : acc[0][i][j], au = wm ? acc[0][i][j] : acc[1][i][j];
        const f32x4 ga = ag2 * st[j].x + st[j].y * cg, up = au * st[j].x + st[j].y * cu;
        const f32x4 z = ga * (ga * ga * K2 + K1);
        const f32x4 d = (f32x4){__builtin_amdgcn_exp2f(z[0]), __builtin_amdgcn_exp2f(z[1]), __builtin_amdgcn_exp2f(z[2]), __builtin_amdgcn_exp2f(z[3])} + 1.f;
        const f32x4 rc = (f32x4){__builtin_amdgcn_rcpf(d[0]), __builtin_amdgcn_rcpf(d[1]), __builtin_amdgcn_rcpf(d[2]), __builtin_amdgcn_rcpf(d[3])};
        av[j] = cvt4((ga * rc) * up);
      }
      if (u < nu) {
#pragma unroll
        for (int j = 0; j < 4; j += 2) {
          const unsigned rt = (unsigned)mb * 8 + wm * 4 + j + (g & 1);
          st16(ws, WS_ACT + (((rt * 43 + (u0 + u)) * 4 + jj * 2 + (g >> 1)) * 16 + r16) * 16, pair16(av[j], av[j + 1]));
        }
      }
    }
    TS(34);
    SB();
#pragma unroll
    for (int i = 11; i < 22; ++i) wd_load(i);
  }
}

extern __shared__ __attribute__((aligned(16))) char smem_dyn[];

__global__ __launch_bounds__(512, 2) void mega(Params p) {
  char* smem = smem_dyn;
  const int b = blockIdx.x, x = b & 7, i5 = b >> 3;
  const int tid = threadIdx.x, lane = tid & 63, wid = rfl(tid >> 6);
  const int r16 = lane & 15, g = lane >> 4, wn = wid & 3, kh = wid >> 2;
  TS_BEGIN();
  const rsrc_t ws = __builtin_amdgcn_make_buffer_rsrc(p.ws, 0, WS_USED, 0x00020000);
  const unsigned epoch = __hip_atomic_load((gu32*)(p.ws + WS_FLAG + FL_EPOCH), RLX_AGENT);

  const int rb1 = (x >> 1) * 8 + (i5 & 7), cb1 = (x & 1) * 4 + (i5 >> 3);
  p1_qkv(p, ws, b, rb1, cb1, smem TSPASS);
  const unsigned tag = (unsigned)rfl((int)(epoch * 0x9E3779B1u + 0x7F4A7C15u));
  wait_bar<0>();
  publish(p.ws, FL_1 + (cb1 * 32 + rb1) * 4, tag);
  TS(3);

  const int h = x * 4 + (i5 & 3), qr = i5 >> 2;
  await<32>(p.ws, FL_1 + (h >> 2) * 128, tag);
  TS(27);
  p2_attn(p, ws, h, qr, smem, tag TSPASS);
  wait_bar<0>();
  publish(p.ws, FL_2 + (qr * 32 + h) * 4, tag);
  TS(6);

  const int rb = x * 4 + (i5 & 3), cb = i5 >> 2;
  const int wct = cb * 4 + wn, col = cb * 64 + wn * 16 + 4 * g;
  bf16x8 wof[8];
#pragma unroll
  for (int i = 0; i < 8; ++i) wof[i] = ldw16(ws, WS_WOF + ((unsigned)wct * 16 + 2 * i + kh) * 1024 + lane * 16);
  u32x2 xv[2];
#pragma unroll
  for (int i = 0; i < 2; ++i) xv[i] = *(const u32x2*)(p.x + ((size_t)(rb * 64 + (2 * kh + i) * 16 + r16) * DM + col) * 2);
  await<32>(p.ws, FL_2 + (rb >> 2) * 128, tag);
  TS(7);
#pragma unroll
  for (int i = 0; i < 8; ++i) PIN(wof[i]);
  PIN(xv[0]); PIN(xv[1]);
  u32x2 hv[2];
  {
    f32x4 res[2];
    gemm64<16, 8, 8>(p.ws + WS_O + (size_t)(rb * 4 + (wid >> 1)) * 16384, wof, smem + G_A, res TSPASS);
    TS(31);
    LAS f32x2* S = (LAS f32x2*)(smem + G_A + 16384);
#pragma unroll
    for (int i = 0; i < 2; ++i) hv[i] = cvt4(unpack4(xv[i]) + res[i]);
    { const unsigned rt = (unsigned)rb * 4 + 2 * kh + (g & 1);
      st16(ws, WS_H + (((rt * 16 + cb * 2 + (wn >> 1)) * 4 + (wn & 1) * 2 + (g >> 1)) * 16 + r16) * 16, pair16(hv[0], hv[1])); }
#pragma unroll
    for (int i = 0; i < 2; ++i) {
      const f32x4 v = unpack4(hv[i]);
      const float s1 = sum_x32(sum_x16((v[0] + v[1]) + (v[2] + v[3]))), s2 = sum_x32(sum_x16((v[0] * v[0] + v[1] * v[1]) + (v[2] * v[2] + v[3] * v[3])));
      if (g == 0) S[((2 * kh + i) * 16 + r16) * 4 + wn] = (f32x2){s1, s2};
    }
    lds_bar();
    if (tid < 64) {
      const f32x2 a = S[tid * 4], bb = S[tid * 4 + 1], c = S[tid * 4 + 2], d = S[tid * 4 + 3];
      const float s1 = (a.x + bb.x) + (c.x + d.x), s2 = (a.y + bb.y) + (c.y + d.y);
      __hip_atomic_store((gu64*)(p.ws + WS_HST + ((size_t)(rb * 64 + tid) * 8 + cb) * 8), ((unsigned long long)__float_as_uint(s2) << 32) | __float_as_uint(s1), RLX_AGENT);
    }
  }
  TS(32);
  wait_bar<0>();
  publish(p.ws, FL_3 + (rb >> 1) * 128 + ((rb & 1) * 8 + cb) * 4, tag);
  TS(9);

  const int mb = x * 2 + (i5 & 1), fb = i5 >> 1;
  WLoad wl;
  if (wid >= 4) { wl.init(p, fb, wid - 4, lane, (unsigned)(size_t)(lds_ptr)smem); wl.issue(0); wl.issue(1); }
  await<16>(p.ws, FL_3 + mb * 128, tag);
  TS(10);
  bf16x8 wdf[22];
  p4_gateup(p, ws, mb, fb, smem, wl, wct, wdf TSPASS);
  if (wid >= 4) wait_bar<22>(); else wait_bar<11>();
  publish(p.ws, FL_4 + mb * 128 + fb * 4, tag);
  TS(13);

  await<16>(p.ws, FL_4 + (rb >> 1) * 128, tag);
  TS(14);
  if (kh) wdf[21] = (bf16x8){0, 0, 0, 0, 0, 0, 0, 0};
#pragma unroll
  for (int i = 0; i < 22; ++i) PIN(wdf[i]);
  {
    f32x4 res[2];
    gemm64<43, 22, 15>(p.ws + WS_ACT + (size_t)(rb * 4 + (wid >> 1)) * 43 * 1024, wdf, smem, res TSPASS);
#pragma unroll
    for (int i = 0; i < 2; ++i) {
      const int row = rb * 64 + (2 * kh + i) * 16 + r16;
      *(u32x2*)(p.out + ((size_t)row * DM + col) * 2) = cvt4(unpack4(hv[i]) + res[i]);
    }
  }
  TS_END();
}

extern "C" void kernel_launch(void* const* d_in, const int* in_sizes, int n_in, void* d_out, int out_size, void* d_ws, size_t ws_size, hipStream_t stream) {
  Params p;
  p.x = (const char*)d_in[0]; p.wqkv = (const char*)d_in[1]; p.wo = (const char*)d_in[2];
  p.wg = (const char*)d_in[4]; p.wu = (const char*)d_in[5]; p.wd = (const char*)d_in[6];
  p.out = (char*)d_out; p.ws = (char*)d_ws;
  constexpr int LDS = 160 * 1024;
  (void)hipFuncSetAttribute((const void*)mega, hipFuncAttributeMaxDynamicSharedMemorySize, LDS);
  mega<<<256, 512, LDS, stream>>>(p);
}
```

```cpp
#include <hip/hip_runtime.h>
#include <stdint.h>
#define ATTN_ASM_A \
  "s_mov_b32 %[keep], m0\n\t" \
  "ds_read_b128 v[168:171], %[ka]\n\t" \
  "ds_read_b128 v[172:175], %[ka] offset:1024\n\t" \
  "ds_read_b128 v[176:179], %[va]\n\t" \
  "ds_read_b128 v[184:187], %[va] offset:512\n\t" \
  "v_mov_b32 v164, 0\n\t" \
  "v_mov_b32 v165, 0\n\t" \
  "v_mov_b32 v166, 0\n\t" \
  "v_mov_b32 v167, 0\n\t" \
  "v_mov_b32 v188, 0\n\t" \
  "v_mov_b32 v189, 0\n\t" \
  "v_mov_b32 v190, 0\n\t" \
  "v_mov_b32 v191, 0\n\t" \
  "s_waitcnt lgkmcnt(3)\n\t" \
  "v_mfma_f32_32x32x16_bf16 v[128:143], v[168:171], %[q], %[nc]\n\t" \
  "s_nop 10\n\t" \
  "s_waitcnt lgkmcnt(2)\n\t" \
  "v_mfma_f32_32x32x16_bf16 v[144:159], v[172:175], %[q], %[nc]\n\t" \
  "ds_read_b128 v[168:171], %[ka] offset:2048\n\t" \
  "s_mov_b32 m0, %[ld]\n\t" \
  "v_exp_f32 v128, v128\n\t" \
  "v_exp_f32 v129, v129\n\t" \
  "global_load_lds_dwordx4 %[go], %[kg] sc1\n\t" \
  "v_exp_f32 v130, v130\n\t" \
  "v_exp_f32 v131, v131\n\t" \
  "v_cvt_pk_bf16_f32 v160, v128, v129\n\t" \
  "v_exp_f32 v132, v132\n\t" \
  "v_exp_f32 v133, v133\n\t" \
  "v_cvt_pk_bf16_f32 v161, v130, v131\n\t" \
  "v_mfma_f32_32x32x16_bf16 %[o], v[188:191], v[164:167], %[o]\n\t" \
  "ds_read_b128 v[180:183], %[va] offset:1024\n\t" \
  "ds_read_b128 v[188:191], %[va] offset:1536\n\t" \
  "s_add_u32 m0, %[ld], 0x10000\n\t" \
  "v_exp_f32 v134, v134\n\t" \
  "v_exp_f32 v135, v135\n\t" \
  "global_load_lds_dwordx4 %[go], %[vg] sc1\n\t" \
  "s_add_u32 %[ld], %[ld], 0x2000\n\t" \
  "v_cvt_pk_bf16_f32 v162, v132, v133\n\t" \
  "v_exp_f32 v136, v136\n\t" \
  "v_exp_f32 v137, v137\n\t" \
  "v_cvt_pk_bf16_f32 v163, v134, v135\n\t" \
  "v_add_u32 %[go], 0x2000, %[go]\n\t" \
  "v_and_b32 %[go], 0xffff, %[go]\n\t" \
  "v_exp_f32 v138, v138\n\t" \
  "v_exp_f32 v139, v139\n\t" \
  "s_waitcnt lgkmcnt(4)\n\t" \
  "v_mfma_f32_32x32x16_bf16 %[o], v[176:179], v[160:163], %[o]\n\t" \
  "v_cvt_pk_bf16_f32 v164, v136, v137\n\t" \
  "v_exp_f32 v140, v140\n\t" \
  "v_exp_f32 v141, v141\n\t" \
  "v_cvt_pk_bf16_f32 v165, v138, v139\n\t" \
  "v_exp_f32 v142, v142\n\t" \
  "v_exp_f32 v143, v143\n\t" \
  "v_cvt_pk_bf16_f32 v166, v140, v141\n\t" \
  "v_cvt_pk_bf16_f32 v167, v142, v143\n\t" \
  "s_waitcnt lgkmcnt(2)\n\t" \
  "v_mfma_f32_32x32x16_bf16 v[128:143], v[168:171], %[q], %[nc]\n\t" \
  "ds_read_b128 v[172:175], %[ka] offset:3072\n\t" \
  "s_mov_b32 m0, %[ld]\n\t" \
  "v_exp_f32 v144, v144\n\t" \
  "v_exp_f32 v145, v145\n\t" \
  "global_load_lds_dwordx4 %[go], %[kg] sc1\n\t" \
  "v_exp_f32 v146, v146\n\t" \
  "v_exp_f32 v147, v147\n\t" \
  "v_cvt_pk_bf16_f32 v160, v144, v145\n\t" \
  "v_exp_f32 v148, v148\n\t" \
  "v_exp_f32 v149, v149\n\t" \
  "v_cvt_pk_bf16_f32 v161, v146, v147\n\t" \
  "v_mfma_f32_32x32x16_bf16 %[o], v[184:187], v[164:167], %[o]\n\t" \
  "ds_read_b128 v[176:179], %[va] offset:2048\n\t" \
  "ds_read_b128 v[184:187], %[va] offset:2560\n\t" \
  "s_add_u32 m0, %[ld], 0x10000\n\t" \
  "v_exp_f32 v150, v150\n\t" \
  "v_exp_f32 v151, v151\n\t" \
  "global_load_lds_dwordx4 %[go], %[vg] sc1\n\t" \
  "s_add_u32 %[ld], %[ld], 0x2000\n\t" \
  "v_cvt_pk_bf16_f32 v162, v148, v149\n\t" \
  "v_exp_f32 v152, v152\n\t" \
  "v_exp_f32 v153, v153\n\t" \
  "v_cvt_pk_bf16_f32 v163, v150, v151\n\t" \
  "v_add_u32 %[go], 0x2000, %[go]\n\t" \
  "v_and_b32 %[go], 0xffff, %[go]\n\t" \
  "v_exp_f32 v154, v154\n\t" \
  "v_exp_f32 v155, v155\n\t" \
  "s_waitcnt lgkmcnt(4)\n\t" \
  "v_mfma_f32_32x32x16_bf16 %[o], v[180:183], v[160:163], %[o]\n\t" \
  "v_cvt_pk_bf16_f32 v164, v152, v153\n\t" \
  "v_exp_f32 v156, v156\n\t" \
  "v_exp_f32 v157, v157\n\t" \
  "v_cvt_pk_bf16_f32 v165, v154, v155\n\t" \
  "v_exp_f32 v158, v158\n\t" \
  "v_exp_f32 v159, v159\n\t" \
  "v_cvt_pk_bf16_f32 v166, v156, v157\n\t" \
  "v_cvt_pk_bf16_f32 v167, v158, v159\n\t" \
  "s_waitcnt lgkmcnt(2)\n\t" \
  "v_mfma_f32_32x32x16_bf16 v[144:159], v[172:175], %[q], %[nc]\n\t" \
  "ds_read_b128 v[168:171], %[ka] offset:4096\n\t" \
  "s_mov_b32 m0, %[ld]\n\t" \
  "v_exp_f32 v128, v128\n\t" \
  "v_exp_f32 v129, v129\n\t" \
  "global_load_lds_dwordx4 %[go], %[kg] sc1\n\t" \
  "v_exp_f32 v130, v130\n\t" \
  "v_exp_f32 v131, v131\n\t" \
  "v_cvt_pk_bf16_f32 v160, v128, v129\n\t" \
  "v_exp_f32 v132, v132\n\t" \
  "v_exp_f32 v133, v133\n\t" \
  "v_cvt_pk_bf16_f32 v161, v130, v131\n\t" \
  "v_mfma_f32_32x32x16_bf16 %[o], v[188:191], v[164:167], %[o]\n\t" \
  "ds_read_b128 v[180:183], %[va] offset:3072\n\t" \
  "ds_read_b128 v[188:191], %[va] offset:3584\n\t" \
  "s_add_u32 m0, %[ld], 0x10000\n\t" \
  "v_exp_f32 v134, v134\n\t" \
  "v_exp_f32 v135, v135\n\t" \
  "global_load_lds_dwordx4 %[go], %[vg] sc1\n\t" \
  "s_add_u32 %[ld], %[ld], 0x2000\n\t" \
  "v_cvt_pk_bf16_f32 v162, v132, v133\n\t" \
  "v_exp_f32 v136, v136\n\t" \
  "v_exp_f32 v137, v137\n\t" \
  "v_cvt_pk_bf16_f32 v163, v134, v135\n\t" \
  "v_add_u32 %[go], 0x2000, %[go]\n\t" \
  "v_and_b32 %[go], 0xffff, %[go]\n\t" \
  "v_exp_f32 v138, v138\n\t" \
  "v_exp_f32 v139, v139\n\t" \
  "s_waitcnt lgkmcnt(4)\n\t" \
  "v_mfma_f32_32x32x16_bf16 %[o], v[176:179], v[160:163], %[o]\n\t" \
  "v_cvt_pk_bf16_f32 v164, v136, v137\n\t" \
  "v_exp_f32 v140, v140\n\t" \
  "v_exp_f32 v141, v141\n\t" \
  "v_cvt_pk_bf16_f32 v165, v138, v139\n\t" \
  "v_exp_f32 v142, v142\n\t" \
  "v_exp_f32 v143, v143\n\t" \
  "v_cvt_pk_bf16_f32 v166, v140, v141\n\t" \
  "v_cvt_pk_bf16_f32 v167, v142, v143\n\t" \
  "s_waitcnt lgkmcnt(2)\n\t" \
  "v_mfma_f32_32x32x16_bf16 v[128:143], v[168:171], %[q], %[nc]\n\t" \
  "ds_read_b128 v[172:175], %[ka] offset:5120\n\t" \
  "s_mov_b32 m0, %[ld]\n\t" \
  "v_exp_f32 v144, v144\n\t" \
  "v_exp_f32 v145, v145\n\t" \
  "global_load_lds_dwordx4 %[go], %[kg] sc1\n\t" \
  "v_exp_f32 v146, v146\n\t" \
  "v_exp_f32 v147, v147\n\t" \
  "v_cvt_pk_bf16_f32 v160, v144, v145\n\t" \
  "v_exp_f32 v148, v148\n\t" \
  "v_exp_f32 v149, v149\n\t" \
  "v_cvt_pk_bf16_f32 v161, v146, v147\n\t" \
  "v_mfma_f32_32x32x16_bf16 %[o], v[184:187], v[164:167], %[o]\n\t" \
  "ds_read_b128 v[176:179], %[va] offset:4096\n\t" \
  "ds_read_b128 v[184:187], %[va] offset:4608\n\t" \
  "s_add_u32 m0, %[ld], 0x10000\n\t" \
  "v_exp_f32 v150, v150\n\t" \
  "v_exp_f32 v151, v151\n\t" \
  "global_load_lds_dwordx4 %[go], %[vg] sc1\n\t" \
  "s_add_u32 %[ld], %[ld], 0x2000\n\t" \
  "v_cvt_pk_bf16_f32 v162, v148, v149\n\t" \
  "v_exp_f32 v152, v152\n\t" \
  "v_exp_f32 v153, v153\n\t" \
  "v_cvt_pk_bf16_f32 v163, v150, v151\n\t" \
  "v_add_u32 %[go], 0x2000, %[go]\n\t" \
  "v_and_b32 %[go], 0xffff, %[go]\n\t" \
  "v_exp_f32 v154, v154\n\t" \
  "v_exp_f32 v155, v155\n\t" \
  "s_waitcnt lgkmcnt(4)\n\t" \
  "v_mfma_f32_32x32x16_bf16 %[o], v[180:183], v[160:163], %[o]\n\t" \
  "v_cvt_pk_bf16_f32 v164, v152, v153\n\t" \
  "v_exp_f32 v156, v156\n\t" \
  "v_exp_f32 v157, v157\n\t" \
  "v_cvt_pk_bf16_f32 v165, v154, v155\n\t" \
  "v_exp_f32 v158, v158\n\t" \
  "v_exp_f32 v159, v159\n\t" \
  "v_cvt_pk_bf16_f32 v166, v156, v157\n\t" \
  "v_cvt_pk_bf16_f32 v167, v158, v159\n\t" \
  "v_add_u32 %[ka], 0x1000, %[ka]\n\t" \
  "v_add_u32 %[va], %[vs], %[va]\n\t" \
  "s_waitcnt lgkmcnt(2)\n\t" \
  "v_mfma_f32_32x32x16_bf16 v[144:159], v[172:175], %[q], %[nc]\n\t" \
  "ds_read_b128 v[168:171], %[ka] offset:2048\n\t" \
  "s_mov_b32 m0, %[ld]\n\t" \
  "v_exp_f32 v128, v128\n\t" \
  "v_exp_f32 v129, v129\n\t" \
  "global_load_lds_dwordx4 %[go], %[kg] sc1\n\t" \
  "v_exp_f32 v130, v130\n\t" \
  "v_exp_f32 v131, v131\n\t" \
  "v_cvt_pk_bf16_f32 v160, v128, v129\n\t" \
  "v_exp_f32 v132, v132\n\t" \
  "v_exp_f32 v133, v133\n\t" \
  "v_cvt_pk_bf16_f32 v161, v130, v131\n\t" \
  "v_mfma_f32_32x32x16_bf16 %[o], v[188:191], v[164:167], %[o]\n\t" \
  "ds_read_b128 v[180:183], %[va] offset:1024\n\t" \
  "ds_read_b128 v[188:191], %[va] offset:1536\n\t" \
  "s_add_u32 m0, %[ld], 0x10000\n\t" \
  "v_exp_f32 v134, v134\n\t" \
  "v_exp_f32 v135, v135\n\t" \
  "global_load_lds_dwordx4 %[go], %[vg] sc1\n\t" \
  "s_add_u32 %[ld], %[ld], 0x2000\n\t" \
  "v_cvt_pk_bf16_f32 v162, v132, v133\n\t" \
  "v_exp_f32 v136, v136\n\t" \
  "v_exp_f32 v137, v137\n\t" \
  "v_cvt_pk_bf16_f32 v163, v134, v135\n\t" \
  "v_add_u32 %[go], 0x2000, %[go]\n\t" \
  "v_and_b32 %[go], 0xffff, %[go]\n\t" \
  "v_exp_f32 v138, v138\n\t" \
  "v_exp_f32 v139, v139\n\t" \
  "s_waitcnt lgkmcnt(4)\n\t" \
  "v_mfma_f32_32x32x16_bf16 %[o], v[176:179], v[160:163], %[o]\n\t" \
  "v_cvt_pk_bf16_f32 v164, v136, v137\n\t" \
  "v_exp_f32 v140, v140\n\t" \
  "v_exp_f32 v141, v141\n\t" \
  "v_cvt_pk_bf16_f32 v165, v138, v139\n\t" \
  "v_exp_f32 v142, v142\n\t" \
  "v_exp_f32 v143, v143\n\t" \
  "v_cvt_pk_bf16_f32 v166, v140, v141\n\t" \
  "v_cvt_pk_bf16_f32 v167, v142, v143\n\t" \
  "s_waitcnt lgkmcnt(2)\n\t" \
  "v_mfma_f32_32x32x16_bf16 v[128:143], v[168:171], %[q], %[nc]\n\t" \
  "ds_read_b128 v[172:175], %[ka] offset:3072\n\t" \
  "s_mov_b32 m0, %[ld]\n\t" \
  "v_exp_f32 v144, v144\n\t" \
  "v_exp_f32 v145, v145\n\t" \
  "global_load_lds_dwordx4 %[go], %[kg] sc1\n\t" \
  "v_exp_f32 v146, v146\n\t" \
  "v_exp_f32 v147, v147\n\t" \
  "v_cvt_pk_bf16_f32 v160, v144, v145\n\t" \
  "v_exp_f32 v148, v148\n\t" \
  "v_exp_f32 v149, v149\n\t" \
  "v_cvt_pk_bf16_f32 v161, v146, v147\n\t" \
  "v_mfma_f32_32x32x16_bf16 %[o], v[184:187], v[164:167], %[o]\n\t" \
  "ds_read_b128 v[176:179], %[va] offset:2048\n\t" \
  "ds_read_b128 v[184:187], %[va] offset:2560\n\t" \
  "s_add_u32 m0, %[ld], 0x10000\n\t" \
  "v_exp_f32 v150, v150\n\t" \
  "v_exp_f32 v151, v151\n\t" \
  "global_load_lds_dwordx4 %[go], %[vg] sc1\n\t" \
  "s_add_u32 %[ld], %[ld], 0x2000\n\t" \
  "v_cvt_pk_bf16_f32 v162, v148, v149\n\t" \
  "v_exp_f32 v152, v152\n\t" \
  "v_exp_f32 v153, v153\n\t" \
  "v_cvt_pk_bf16_f32 v163, v150, v151\n\t" \
  "v_add_u32 %[go], 0x2000, %[go]\n\t" \
  "v_and_b32 %[go], 0xffff, %[go]\n\t" \
  "v_exp_f32 v154, v154\n\t" \
  "v_exp_f32 v155, v155\n\t" \
  "s_waitcnt lgkmcnt(4)\n\t" \
  "v_mfma_f32_32x32x16_bf16 %[o], v[180:183], v[160:163], %[o]\n\t" \
  "v_cvt_pk_bf16_f32 v164, v152, v153\n\t" \
  "v_exp_f32 v156, v156\n\t" \
  "v_exp_f32 v157, v157\n\t" \
  "v_cvt_pk_bf16_f32 v165, v154, v155\n\t" \
  "v_exp_f32 v158, v158\n\t" \
  "v_exp_f32 v159, v159\n\t" \
  "v_cvt_pk_bf16_f32 v166, v156, v157\n\t" \
  "v_cvt_pk_bf16_f32 v167, v158, v159\n\t" \
  "s_waitcnt lgkmcnt(2)\n\t" \
  "v_mfma_f32_32x32x16_bf16 v[144:159], v[172:175], %[q], %[nc]\n\t" \
  "ds_read_b128 v[168:171], %[ka] offset:4096\n\t" \
  "v_exp_f32 v128, v128\n\t" \
  "v_exp_f32 v129, v129\n\t" \
  "v_exp_f32 v130, v130\n\t" \
  "v_exp_f32 v131, v131\n\t" \
  "v_cvt_pk_bf16_f32 v160, v128, v129\n\t" \
  "v_exp_f32 v132, v132\n\t" \
  "v_exp_f32 v133, v133\n\t" \
  "v_cvt_pk_bf16_f32 v161, v130, v131\n\t" \
  "v_mfma_f32_32x32x16_bf16 %[o], v[188:191], v[164:167], %[o]\n\t" \
  "ds_read_b128 v[180:183], %[va] offset:3072\n\t" \
  "ds_read_b128 v[188:191], %[va] offset:3584\n\t" \
  "v_exp_f32 v134, v134\n\t" \
  "v_exp_f32 v135, v135\n\t" \
  "v_cvt_pk_bf16_f32 v162, v132, v133\n\t" \
  "v_exp_f32 v136, v136\n\t" \
  "v_exp_f32 v137, v137\n\t" \
  "v_cvt_pk_bf16_f32 v163, v134, v135\n\t" \
  "v_exp_f32 v138, v138\n\t" \
  "v_exp_f32 v139, v139\n\t" \
  "s_waitcnt lgkmcnt(4)\n\t" \
  "v_mfma_f32_32x32x16_bf16 %[o], v[176:179], v[160:163], %[o]\n\t" \
  "v_cvt_pk_bf16_f32 v164, v136, v137\n\t" \
  "v_exp_f32 v140, v140\n\t" \
  "v_exp_f32 v141, v141\n\t" \
  "v_cvt_pk_bf16_f32 v165, v138, v139\n\t" \
  "v_exp_f32 v142, v142\n\t" \
  "v_exp_f32 v143, v143\n\t" \
  "v_cvt_pk_bf16_f32 v166, v140, v141\n\t" \
  "v_cvt_pk_bf16_f32 v167, v142, v143\n\t" \
  "s_waitcnt lgkmcnt(2)\n\t" \
  "v_mfma_f32_32x32x16_bf16 v[128:143], v[168:171], %[q], %[nc]\n\t" \
  "ds_read_b128 v[172:175], %[ka] offset:5120\n\t" \
  "v_exp_f32 v144, v144\n\t" \
  "v_exp_f32 v145, v145\n\t" \
  "v_exp_f32 v146, v146\n\t" \
  "v_exp_f32 v147, v147\n\t" \
  "v_cvt_pk_bf16_f32 v160, v144, v145\n\t" \
  "v_exp_f32 v148, v148\n\t" \
  "v_exp_f32 v149, v149\n\t" \
  "v_cvt_pk_bf16_f32 v161, v146, v147\n\t" \
  "v_mfma_f32_32x32x16_bf16 %[o], v[184:187], v[164:167], %[o]\n\t" \
  "ds_read_b128 v[176:179], %[va] offset:4096\n\t" \
  "ds_read_b128 v[184:187], %[va] offset:4608\n\t" \
  "v_exp_f32 v150, v150\n\t" \
  "v_exp_f32 v151, v151\n\t" \
  "v_cvt_pk_bf16_f32 v162, v148, v149\n\t" \
  "v_exp_f32 v152, v152\n\t" \
  "v_exp_f32 v153, v153\n\t" \
  "v_cvt_pk_bf16_f32 v163, v150, v151\n\t" \
  "v_exp_f32 v154, v154\n\t" \
  "v_exp_f32 v155, v155\n\t" \
  "s_waitcnt lgkmcnt(4)\n\t" \
  "v_mfma_f32_32x32x16_bf16 %[o], v[180:183], v[160:163], %[o]\n\t" \
  "v_cvt_pk_bf16_f32 v164, v152, v153\n\t" \
  "v_exp_f32 v156, v156\n\t" \
  "v_exp_f32 v157, v157\n\t" \
  "v_cvt_pk_bf16_f32 v165, v154, v155\n\t" \
  "v_exp_f32 v158, v158\n\t" \
  "v_exp_f32 v159, v159\n\t" \
  "v_cvt_pk_bf16_f32 v166, v156, v157\n\t" \
  "v_cvt_pk_bf16_f32 v167, v158, v159\n\t" \
  "v_add_u32 %[ka], 0x1000, %[ka]\n\t" \
  "v_add_u32 %[va], %[vs], %[va]\n\t" \
  "s_mov_b32 m0, %[keep]\n\t" \
  "s_waitcnt vmcnt(0) lgkmcnt(0)\n\t" \
  "s_barrier\n\t" \
  ""
#define ATTN_ASM_B \
  "s_mov_b32 %[cnt], 2\n\t" \
  "s_cmp_eq_u32 %[sw], 99\n\t" \
  "s_cbranch_scc1 L_attn_y_%=\n\t" \
  "s_setprio 2\n\t" \
  "s_branch L_attn_loop_%=\n\t" \
  "L_attn_y_%=:\n\t" \
  "s_setprio 1\n\t" \
  "L_attn_loop_%=:\n\t" \
  "s_cmp_lg_u32 %[cnt], %[sw]\n\t" \
  "s_cbranch_scc1 L_attn_nosw_%=\n\t" \
  "s_setprio 0\n\t" \
  "L_attn_nosw_%=:\n\t" \
  "s_waitcnt lgkmcnt(2)\n\t" \
  "v_mfma_f32_32x32x16_bf16 v[144:159], v[172:175], %[q], %[nc]\n\t" \
  "ds_read_b128 v[168:171], %[ka] offset:2048\n\t" \
  "v_exp_f32 v128, v128\n\t" \
  "v_exp_f32 v129, v129\n\t" \
  "v_exp_f32 v130, v130\n\t" \
  "v_exp_f32 v131, v131\n\t" \
  "v_cvt_pk_bf16_f32 v160, v128, v129\n\t" \
  "v_exp_f32 v132, v132\n\t" \
  "v_exp_f32 v133, v133\n\t" \
  "v_cvt_pk_bf16_f32 v161, v130, v131\n\t" \
  "v_mfma_f32_32x32x16_bf16 %[o], v[188:191], v[164:167], %[o]\n\t" \
  "ds_read_b128 v[180:183], %[va] offset:1024\n\t" \
  "ds_read_b128 v[188:191], %[va] offset:1536\n\t" \
  "v_exp_f32 v134, v134\n\t" \
  "v_exp_f32 v135, v135\n\t" \
  "v_cvt_pk_bf16_f32 v162, v132, v133\n\t" \
  "v_exp_f32 v136, v136\n\t" \
  "v_exp_f32 v137, v137\n\t" \
  "v_cvt_pk_bf16_f32 v163, v134, v135\n\t" \
  "v_exp_f32 v138, v138\n\t" \
  "v_exp_f32 v139, v139\n\t" \
  "s_waitcnt lgkmcnt(4)\n\t" \
  "v_mfma_f32_32x32x16_bf16 %[o], v[176:179], v[160:163], %[o]\n\t" \
  "v_cvt_pk_bf16_f32 v164, v136, v137\n\t" \
  "v_exp_f32 v140, v140\n\t" \
  "v_exp_f32 v141, v141\n\t" \
  "v_cvt_pk_bf16_f32 v165, v138, v139\n\t" \
  "v_exp_f32 v142, v142\n\t" \
  "v_exp_f32 v143, v143\n\t" \
  "v_cvt_pk_bf16_f32 v166, v140, v141\n\t" \
  "v_cvt_pk_bf16_f32 v167, v142, v143\n\t" \
  "s_waitcnt lgkmcnt(2)\n\t" \
  "v_mfma_f32_32x32x16_bf16 v[128:143], v[168:171], %[q], %[nc]\n\t" \
  "ds_read_b128 v[172:175], %[ka] offset:3072\n\t" \
  "v_exp_f32 v144, v144\n\t" \
  "v_exp_f32 v145, v145\n\t" \
  "v_exp_f32 v146, v146\n\t" \
  "v_exp_f32 v147, v147\n\t" \
  "v_cvt_pk_bf16_f32 v160, v144, v145\n\t" \
  "v_exp_f32 v148, v148\n\t" \
  "v_exp_f32 v149, v149\n\t" \
  "v_cvt_pk_bf16_f32 v161, v146, v147\n\t" \
  "v_mfma_f32_32x32x16_bf16 %[o], v[184:187], v[164:167], %[o]\n\t" \
  "ds_read_b128 v[176:179], %[va] offset:2048\n\t" \
  "ds_read_b128 v[184:187], %[va] offset:2560\n\t" \
  "v_exp_f32 v150, v150\n\t" \
  "v_exp_f32 v151, v151\n\t" \
  "v_cvt_pk_bf16_f32 v162, v148, v149\n\t" \
  "v_exp_f32 v152, v152\n\t" \
  "v_exp_f32 v153, v153\n\t" \
  "v_cvt_pk_bf16_f32 v163, v150, v151\n\t" \
  "v_exp_f32 v154, v154\n\t" \
  "v_exp_f32 v155, v155\n\t" \
  "s_waitcnt lgkmcnt(4)\n\t" \
  "v_mfma_f32_32x32x16_bf16 %[o], v[180:183], v[160:163], %[o]\n\t" \
  "v_cvt_pk_bf16_f32 v164, v152, v153\n\t" \
  "v_exp_f32 v156, v156\n\t" \
  "v_exp_f32 v157, v157\n\t" \
  "v_cvt_pk_bf16_f32 v165, v154, v155\n\t" \
  "v_exp_f32 v158, v158\n\t" \
  "v_exp_f32 v159, v159\n\t" \
  "v_cvt_pk_bf16_f32 v166, v156, v157\n\t" \
  "v_cvt_pk_bf16_f32 v167, v158, v159\n\t" \
  "s_waitcnt lgkmcnt(2)\n\t" \
  "v_mfma_f32_32x32x16_bf16 v[144:159], v[172:175], %[q], %[nc]\n\t" \
  "ds_read_b128 v[168:171], %[ka] offset:4096\n\t" \
  "v_exp_f32 v128, v128\n\t" \
  "v_exp_f32 v129, v129\n\t" \
  "v_exp_f32 v130, v130\n\t" \
  "v_exp_f32 v131, v131\n\t" \
  "v_cvt_pk_bf16_f32 v160, v128, v129\n\t" \
  "v_exp_f32 v132, v132\n\t" \
  "v_exp_f32 v133, v133\n\t" \
  "v_cvt_pk_bf16_f32 v161, v130, v131\n\t" \
  "v_mfma_f32_32x32x16_bf16 %[o], v[188:191], v[164:167], %[o]\n\t" \
  "ds_read_b128 v[180:183], %[va] offset:3072\n\t" \
  "ds_read_b128 v[188:191], %[va] offset:3584\n\t" \
  "v_exp_f32 v134, v134\n\t" \
  "v_exp_f32 v135, v135\n\t" \
  "v_cvt_pk_bf16_f32 v162, v132, v133\n\t" \
  "v_exp_f32 v136, v136\n\t" \
  "v_exp_f32 v137, v137\n\t" \
  "v_cvt_pk_bf16_f32 v163, v134, v135\n\t" \
  "v_exp_f32 v138, v138\n\t" \
  "v_exp_f32 v139, v139\n\t" \
  "s_waitcnt lgkmcnt(4)\n\t" \
  "v_mfma_f32_32x32x16_bf16 %[o], v[176:179], v[160:163], %[o]\n\t" \
  "v_cvt_pk_bf16_f32 v164, v136, v137\n\t" \
  "v_exp_f32 v140, v140\n\t" \
  "v_exp_f32 v141, v141\n\t" \
  "v_cvt_pk_bf16_f32 v165, v138, v139\n\t" \
  "v_exp_f32 v142, v142\n\t" \
  "v_exp_f32 v143, v143\n\t" \
  "v_cvt_pk_bf16_f32 v166, v140, v141\n\t" \
  "v_cvt_pk_bf16_f32 v167, v142, v143\n\t" \
  "s_waitcnt lgkmcnt(2)\n\t" \
  "v_mfma_f32_32x32x16_bf16 v[128:143], v[168:171], %[q], %[nc]\n\t" \
  "ds_read_b128 v[172:175], %[ka] offset:5120\n\t" \
  "v_exp_f32 v144, v144\n\t" \
  "v_exp_f32 v145, v145\n\t" \
  "v_exp_f32 v146, v146\n\t" \
  "v_exp_f32 v147, v147\n\t" \
  "v_cvt_pk_bf16_f32 v160, v144, v145\n\t" \
  "v_exp_f32 v148, v148\n\t" \
  "v_exp_f32 v149, v149\n\t" \
  "v_cvt_pk_bf16_f32 v161, v146, v147\n\t" \
  "v_mfma_f32_32x32x16_bf16 %[o], v[184:187], v[164:167], %[o]\n\t" \
  "ds_read_b128 v[176:179], %[va] offset:4096\n\t" \
  "ds_read_b128 v[184:187], %[va] offset:4608\n\t" \
  "v_exp_f32 v150, v150\n\t" \
  "v_exp_f32 v151, v151\n\t" \
  "v_cvt_pk_bf16_f32 v162, v148, v149\n\t" \
  "v_exp_f32 v152, v152\n\t" \
  "v_exp_f32 v153, v153\n\t" \
  "v_cvt_pk_bf16_f32 v163, v150, v151\n\t" \
  "v_exp_f32 v154, v154\n\t" \
  "v_exp_f32 v155, v155\n\t" \
  "s_waitcnt lgkmcnt(4)\n\t" \
  "v_mfma_f32_32x32x16_bf16 %[o], v[180:183], v[160:163], %[o]\n\t" \
  "v_cvt_pk_bf16_f32 v164, v152, v153\n\t" \
  "v_exp_f32 v156, v156\n\t" \
  "v_exp_f32 v157, v157\n\t" \
  "v_cvt_pk_bf16_f32 v165, v154, v155\n\t" \
  "v_exp_f32 v158, v158\n\t" \
  "v_exp_f32 v159, v159\n\t" \
  "v_cvt_pk_bf16_f32 v166, v156, v157\n\t" \
  "v_cvt_pk_bf16_f32 v167, v158, v159\n\t" \
  "v_add_u32 %[ka], 0x1000, %[ka]\n\t" \
  "v_add_u32 %[va], %[vs], %[va]\n\t" \
  "s_add_u32 %[cnt], %[cnt], 1\n\t" \
  "s_cmp_lt_u32 %[cnt], 16\n\t" \
  "s_cbranch_scc1 L_attn_loop_%=\n\t" \
  "s_waitcnt lgkmcnt(0)\n\t" \
  "v_mfma_f32_32x32x16_bf16 %[o], v[188:191], v[164:167], %[o]\n\t" \
  "s_setprio 0\n\t" \
  "s_nop 15\n\t" \
  ""

typedef short bf16x8 __attribute__((ext_vector_type(8)));
typedef short s16x4 __attribute__((ext_vector_type(4)));
typedef float f32x4 __attribute__((ext_vector_type(4)));
typedef float f32x2 __attribute__((ext_vector_type(2)));
typedef float f32x16 __attribute__((ext_vector_type(16)));
typedef float f32x8 __attribute__((ext_vector_type(8)));
typedef unsigned u32x4 __attribute__((ext_vector_type(4)));
typedef unsigned u32x8 __attribute__((ext_vector_type(8)));
typedef unsigned u32x2 __attribute__((ext_vector_type(2)));
typedef __bf16 bf16x2_t __attribute__((ext_vector_type(2)));
#define LAS __attribute__((address_space(3)))
#define GAS __attribute__((address_space(1)))
typedef const LAS char* lds_cptr;
typedef LAS char* lds_ptr;
typedef GAS unsigned gu32;
typedef GAS unsigned long long gu64;
typedef __amdgpu_buffer_rsrc_t rsrc_t;

constexpr int SEQ = 2048, DM = 512, NH = 32, FF = 1376;
constexpr float EPS = 1e-5f;
constexpr float QSCALE = 0.25f * 1.4426950408889634f;

constexpr unsigned WS_Q = 0, WS_K = 2u << 20, WS_V = 4u << 20, WS_O = 6u << 20, WS_H = 8u << 20, WS_ACT = 10u << 20;
constexpr unsigned WS_KMAX = 16u << 20, WS_HST = (16u << 20) + (64u << 10);
constexpr unsigned WS_WOF = 17u << 20, WS_WDF = WS_WOF + 512 * 1024, WS_WGF = WS_WDF + 32 * 43 * 1024;
constexpr unsigned WS_FLAG = 22u << 20;
constexpr unsigned FL_EPOCH = 0, FL_TMO = 256, FL_1 = 1024, FL_P = 4096, FL_Q = 8192;
constexpr unsigned POISON = 0xFFFFFFFFu;
constexpr unsigned WS_USED = 24u << 20;
constexpr size_t WS_DBG = 64u << 20;

#define TS(i) do {} while (0)
#define TS_BEGIN() do {} while (0)
#define TS_END() do {} while (0)
#define TSARG
#define TSPASS

struct Params {
  const char* x; const char* wqkv; const char* wo; const char* wg; const char* wu; const char* wd;
  char* out; char* ws;
};

#define MFMA16(a, b, c) __builtin_amdgcn_mfma_f32_16x16x32_bf16(a, b, c, 0, 0, 0)
#define MFMA32(a, b, c) __builtin_amdgcn_mfma_f32_32x32x16_bf16(a, b, c, 0, 0, 0)
#define PIN(x) asm volatile("" : "+v"(x))
#define SB() __builtin_amdgcn_sched_barrier(0)
#define RLX_AGENT __ATOMIC_RELAXED, __HIP_MEMORY_SCOPE_AGENT

template <int N> __device__ __forceinline__ void wait_bar() {
  asm volatile("s_waitcnt vmcnt(%0) lgkmcnt(0)\n\ts_barrier" ::"n"(N) : "memory");
}
template <int N> __device__ __forceinline__ void wait_vm() { asm volatile("s_waitcnt vmcnt(%0)" ::"n"(N) : "memory"); }
__device__ __forceinline__ void lds_bar() { asm volatile("s_waitcnt lgkmcnt(0)\n\ts_barrier" ::: "memory"); }
__device__ __forceinline__ void raw_bar() { asm volatile("s_barrier" ::: "memory"); }

template <int N, bool SC1> __device__ __forceinline__ void glds(const void* sbase, unsigned voff, unsigned lds_addr) {
#define GL_(O, S) "\n\tglobal_load_lds_dwordx4 %0, %1 offset:" #O S
  if (SC1) {
    if (N == 1) asm volatile("s_mov_b32 m0, %2\n\ts_nop 0" GL_(0, " sc1") :: "v"(voff), "s"(sbase), "s"(lds_addr) : "memory");
    if (N == 2) asm volatile("s_mov_b32 m0, %2\n\ts_nop 0" GL_(0, " sc1") GL_(1024, " sc1") :: "v"(voff), "s"(sbase), "s"(lds_addr) : "memory");
    if (N == 3) asm volatile("s_mov_b32 m0, %2\n\ts_nop 0" GL_(0, " sc1") GL_(1024, " sc1") GL_(2048, " sc1") :: "v"(voff), "s"(sbase), "s"(lds_addr) : "memory");
    if (N == 4) asm volatile("s_mov_b32 m0, %2\n\ts_nop 0" GL_(0, " sc1") GL_(1024, " sc1") GL_(2048, " sc1") GL_(3072, " sc1") :: "v"(voff), "s"(sbase), "s"(lds_addr) : "memory");
  } else {
    if (N == 1) asm volatile("s_mov_b32 m0, %2\n\ts_nop 0" GL_(0, "") :: "v"(voff), "s"(sbase), "s"(lds_addr) : "memory");
    if (N == 2) asm volatile("s_mov_b32 m0, %2\n\ts_nop 0" GL_(0, "") GL_(1024, "") :: "v"(voff), "s"(sbase), "s"(lds_addr) : "memory");
    if (N == 3) asm volatile("s_mov_b32 m0, %2\n\ts_nop 0" GL_(0, "") GL_(1024, "") GL_(2048, "") :: "v"(voff), "s"(sbase), "s"(lds_addr) : "memory");
  }
#undef GL_
}
template <bool SC1> __device__ __forceinline__ void glds16(const void* sbase, unsigned voff, unsigned lds_addr) { glds<1, SC1>(sbase, voff, lds_addr); }
template <bool SC1> __device__ __forceinline__ void glds4(const void* sbase, unsigned voff, unsigned lds_addr) {
  if (SC1) asm volatile("s_mov_b32 m0, %2\n\ts_nop 0\n\tglobal_load_lds_dword %0, %1 sc1" :: "v"(voff), "s"(sbase), "s"(lds_addr) : "memory");
  else     asm volatile("s_mov_b32 m0, %2\n\ts_nop 0\n\tglobal_load_lds_dword %0, %1" :: "v"(voff), "s"(sbase), "s"(lds_addr) : "memory");
}
__device__ __forceinline__ bf16x8 ld128(lds_cptr p) { return *(const LAS bf16x8*)p; }
typedef short v4i16_t __attribute__((ext_vector_type(4)));
__device__ __forceinline__ s16x4 ldtr(lds_cptr p) {
  return __builtin_bit_cast(s16x4, __builtin_amdgcn_ds_read_tr16_b64_v4i16((LAS v4i16_t*)p));
}
__device__ __forceinline__ bf16x8 ldtr2(lds_cptr p, int second) {
  const s16x4 a = ldtr(p), b = ldtr(p + second);
  return (bf16x8){a[0], a[1], a[2], a[3], b[0], b[1], b[2], b[3]};
}
__device__ __forceinline__ unsigned cvtpk(float lo, float hi) {
  f32x2 v = {lo, hi}; bf16x2_t b = __builtin_convertvector(v, bf16x2_t); return __builtin_bit_cast(unsigned, b);
}
__device__ __forceinline__ u32x2 cvt4(f32x4 v) { return (u32x2){cvtpk(v[0], v[1]), cvtpk(v[2], v[3])}; }
__device__ __forceinline__ float bflo(unsigned w) { return __uint_as_float(w << 16); }
__device__ __forceinline__ float bfhi(unsigned w) { return __uint_as_float(w & 0xffff0000u); }
__device__ __forceinline__ f32x4 unpack4(u32x2 w) { return (f32x4){bflo(w[0]), bfhi(w[0]), bflo(w[1]), bfhi(w[1])}; }
__device__ __forceinline__ float bfr(float v) { return bflo(cvtpk(v, 0.f)); }
__device__ __forceinline__ void ln_consts(float sum, float sumsq, float& mean, float& rstd) {
  const float m = sum * (1.f / DM), var = fmaxf(sumsq * (1.f / DM) - m * m, 0.f);
  mean = bfr(m); rstd = __builtin_amdgcn_rcpf(bfr(__builtin_amdgcn_sqrtf(bfr(bfr(var) + EPS))));
}
__device__ __forceinline__ int rfl(int v) { return __builtin_amdgcn_readfirstlane(v); }
__device__ __forceinline__ bf16x8 ones8() { return (bf16x8){0x3F80, 0x3F80, 0x3F80, 0x3F80, 0x3F80, 0x3F80, 0x3F80, 0x3F80}; }
#define Z4 ((f32x4){0.f, 0.f, 0.f, 0.f})
__device__ __forceinline__ void st16(rsrc_t ws, unsigned off, u32x4 v) { __builtin_amdgcn_raw_buffer_store_b128(v, ws, off, 0, 16); }
__device__ __forceinline__ bf16x8 ldw16(rsrc_t ws, unsigned off) { return __builtin_bit_cast(bf16x8, __builtin_amdgcn_raw_buffer_load_b128(ws, off, 0, 16)); }
__device__ __forceinline__ u32x4 pair16(u32x2 X, u32x2 Y) {
  auto a = __builtin_amdgcn_permlane16_swap(X[0], Y[0], false, false);
  auto b = __builtin_amdgcn_permlane16_swap(X[1], Y[1], false, false);
  return (u32x4){a[0], b[0], a[1], b[1]};
}
__device__ __forceinline__ u32x4 pair32(u32x2 X, u32x2 Y) {
  auto a = __builtin_amdgcn_permlane32_swap(X[0], Y[0], false, false);
  auto b = __builtin_amdgcn_permlane32_swap(X[1], Y[1], false, false);
  return (u32x4){a[0], b[0], a[1], b[1]};
}
__device__ __forceinline__ float sum_x16(float v) { auto r = __builtin_amdgcn_permlane16_swap(__float_as_uint(v), __float_as_uint(v), false, false); return __uint_as_float(r[0]) + __uint_as_float(r[1]); }
__device__ __forceinline__ float sum_x32(float v) { auto r = __builtin_amdgcn_permlane32_swap(__float_as_uint(v), __float_as_uint(v), false, false); return __uint_as_float(r[0]) + __uint_as_float(r[1]); }
__device__ __forceinline__ float max_x16(float v) { auto r = __builtin_amdgcn_permlane16_swap(__float_as_uint(v), __float_as_uint(v), false, false); return fmaxf(__uint_as_float(r[0]), __uint_as_float(r[1])); }
__device__ __forceinline__ float lo_x32(float v) { auto r = __builtin_amdgcn_permlane32_swap(__float_as_uint(v), __float_as_uint(v), false, false); return __uint_as_float(r[0]); }
template <int N> __device__ __forceinline__ float ror16(float v) { return __uint_as_float(__builtin_amdgcn_update_dpp(0u, __float_as_uint(v), 0x120 + N, 0xf, 0xf, false)); }
__device__ __forceinline__ float max_row16(float v) { v = fmaxf(v, ror16<8>(v)); v = fmaxf(v, ror16<4>(v)); v = fmaxf(v, ror16<2>(v)); return fmaxf(v, ror16<1>(v)); }
template <int ND, int NM> __device__ __forceinline__ void weave() {
#pragma unroll
  for (int i = 0; i < ND; ++i) { __builtin_amdgcn_sched_group_barrier(0x008, 1, 0); __builtin_amdgcn_sched_group_barrier(0x100, 1, 0); }
  if (NM > ND) __builtin_amdgcn_sched_group_barrier(0x008, NM - ND, 0);
}

__device__ __forceinline__ u32x4 nop4(u32x4 v) { return __builtin_elementwise_min(v, (u32x4){POISON - 1, POISON - 1, POISON - 1, POISON - 1}); }
template <int NP> __device__ __forceinline__ bool pieces_ok(lds_cptr base) {
  const int lane = threadIdx.x & 63;
  unsigned m = 0;
#pragma unroll
  for (int i = 0; i < NP; ++i) { const u32x4 v = *(const LAS u32x4*)(base + i * 1024 + lane * 16); const unsigned a = v[0] > v[1] ? v[0] : v[1], b = v[2] > v[3] ? v[2] : v[3]; m = m > a ? m : a; m = m > b ? m : b; }
  return !__any(m == POISON);
}
template <int NP> struct Chk {
  u32x4 v[NP];
  __device__ __forceinline__ void read(lds_cptr base) {
    const int lane = threadIdx.x & 63;
#pragma unroll
    for (int i = 0; i < NP; ++i) v[i] = *(const LAS u32x4*)(base + i * 1024 + lane * 16);
  }
  __device__ __forceinline__ bool ok() const {
    unsigned m = 0;
#pragma unroll
    for (int i = 0; i < NP; ++i) { const unsigned a = v[i][0] > v[i][1] ? v[i][0] : v[i][1], b = v[i][2] > v[i][3] ? v[i][2] : v[i][3]; m = m > a ? m : a; m = m > b ? m : b; }
    return !__any(m == POISON);
  }
};
struct Spin {
  unsigned n; unsigned long long t0;
  __device__ __forceinline__ Spin() : n(0), t0(__builtin_amdgcn_s_memrealtime()) {}
  __device__ __forceinline__ bool expired(char* ws) {
    if ((++n & 63u) == 0u && __builtin_amdgcn_s_memrealtime() - t0 > 20000000ull) {
      if ((threadIdx.x & 63) == 0) __hip_atomic_store((gu32*)(ws + WS_FLAG + FL_TMO), 1u, RLX_AGENT);
      return true;
    }
    return false;
  }
};

__device__ __forceinline__ void publish(char* ws, unsigned flag_off, unsigned tag) {
  if (threadIdx.x == 0) __hip_atomic_store((gu32*)(ws + WS_FLAG + flag_off), tag, RLX_AGENT);
}
template <int NW>
__device__ __forceinline__ void poll(char* ws, unsigned flag_off, unsigned tag) {
  const int lane = threadIdx.x & 63;
  gu32* f = (gu32*)(ws + WS_FLAG + flag_off);
  const unsigned long long t0 = __builtin_amdgcn_s_memrealtime();
  for (unsigned spins = 1;; ++spins) {
    bool ok = true;
    if (NW == 256) {
#pragma unroll
      for (int i = 0; i < 4; ++i) ok &= __hip_atomic_load(f + i * 64 + lane, RLX_AGENT) == tag;
    } else ok = __hip_atomic_load(f + (lane < NW ? lane : 0), RLX_AGENT) == tag;
    if (__all(ok)) break;
    if ((spins & 63u) == 0u && __builtin_amdgcn_s_memrealtime() - t0 > 20000000ull) {
      if (lane == 0) __hip_atomic_store((gu32*)(ws + WS_FLAG + FL_TMO), tag, RLX_AGENT);
      break;
    }
    __builtin_amdgcn_s_sleep(1);
  }
}
template <int NW> __device__ __forceinline__ void await(char* ws, unsigned flag_off, unsigned tag) {
  if (rfl(threadIdx.x >> 6) == 0) poll<NW>(ws, flag_off, tag);
  raw_bar();
}

struct F1 { bf16x8 a[4], w[3]; };
__device__ __forceinline__ void p1_qkv(const Params& p, rsrc_t ws, int b, int rb1, int cb1, char* smem TSARG) {
  const int tid = threadIdx.x, lane = tid & 63, wid = rfl(tid >> 6);
  const int r16 = lane & 15, g = lane >> 4;
  const unsigned lds0 = (unsigned)(size_t)(lds_ptr)smem;
  lds_cptr L = (lds_cptr)smem;
  constexpr int STG = 32768, ND = 4, NS = 8, RL = ND * STG, MS = RL + 5 * 4096;
  LAS f32x2* ms = (LAS f32x2*)(smem + MS);
  const int rb = rb1, cb = cb1;
  if (wid >= 4) {
    const int lw = wid - 4;
    const unsigned voffA = (unsigned)((16 * lw + (lane >> 3)) * DM + (((lane & 7) ^ (lane >> 3)) * 8)) * 2u;
    unsigned voffB[2];
#pragma unroll
    for (int i = 0; i < 2; ++i) { const int r = lane >> 3, c = lane & 7, ff = ((r >> 1) & 1) | (i << 1), j = (c >> 1) ^ ff;
      voffB[i] = (unsigned)((16 * lw + 8 * i + r) * (3 * DM) + j * 16 + (c & 1) * 8) * 2u; }
    const char* xa = p.x + (size_t)rb * 64 * DM * 2;
    const char* wb = p.wqkv + (size_t)cb * 64 * 2;
    const int rot = ((b >> 3) & 7) + 2 * (b >> 6);
    auto issue = [&](int s) {
      const unsigned d = lds0 + (s % ND) * STG;
      const int kc = (s + rot) & 7;
#pragma unroll
      for (int i = 0; i < 2; ++i) glds16<false>(xa + kc * 128 + i * 8 * DM * 2, voffA, d + (2 * lw + i) * 1024);
#pragma unroll
      for (int i = 0; i < 2; ++i) glds<3, false>(wb + (size_t)kc * 64 * (3 * DM) * 2, voffB[i], d + 8192 + (2 * lw + i) * 3072);
    };
    TS(20);
    issue(0); issue(1);
    TS(21);
    wait_vm<8>(); raw_bar();
    TS(23);
    issue(2);
#pragma unroll
    for (int j = 1; j < NS; ++j) {
      if (j + 2 < NS) { issue(j + 2); wait_bar<16>(); } else if (j + 1 < NS) wait_bar<8>(); else wait_bar<0>();
    }
    lds_bar();
  } else {
    auto rl_decode = [&](int J, const char*& src, int& pitch, unsigned& dst, int& dstride, int& nt) {
      if (J < 128) { const int ks = J >> 3, cg = J & 7; src = p.wo + ((size_t)ks * 32 * DM + cg * 64) * 2; pitch = DM; dst = WS_WOF + (unsigned)(cg * 4 * 16 + ks) * 1024; dstride = 16 * 1024; nt = 4; }
      else if (J < 472) { const int ks = (J - 128) >> 3, cg = (J - 128) & 7; src = p.wd + ((size_t)ks * 32 * DM + cg * 64) * 2; pitch = DM; dst = WS_WDF + (unsigned)(cg * 4 * 43 + ks) * 1024; dstride = 43 * 1024; nt = 4; }
      else { const int r = J - 472, mat = r >= 352, r2 = r - mat * 352, ks = r2 / 22, cg = r2 - ks * 22;
        src = (mat ? p.wu : p.wg) + ((size_t)ks * 32 * FF + cg * 64) * 2; pitch = FF; dst = WS_WGF + (unsigned)((mat * 86 + cg * 4) * 16 + ks) * 1024; dstride = 16 * 1024; nt = cg == 21 ? 2 : 4; }
    };
    auto rl_load = [&](int slot) {
      const int J = b * 5 + slot < 1176 ? b * 5 + slot : 0;
      const char* src; int pitch, dstride, nt; unsigned dst; rl_decode(J, src, pitch, dst, dstride, nt);
      const int r = lane >> 3, c = lane & 7;
#pragma unroll
      for (int kg = 0; kg < 4; ++kg) {
        int j = (c >> 1) ^ (((r >> 1) & 1) | ((kg & 1) << 1)); j = j < nt ? j : j - 2;
        glds16<false>(src, (unsigned)((kg * 8 + r) * pitch + j * 16 + (c & 1) * 8) * 2u, lds0 + RL + slot * 4096 + kg * 1024);
      }
    };
    auto rl_store = [&](int slot) {
      if (b * 5 + slot < 1176) {
        const char* src; int pitch, dstride, nt; unsigned dst; rl_decode(b * 5 + slot, src, pitch, dst, dstride, nt);
        const int q = (lane >> 2) & 3, pp = lane & 3, ff = ((q >> 1) & 1) | ((g & 1) << 1);
#pragma unroll
        for (int j = 0; j < 4; ++j)
          if (j < nt) st16(ws, dst + j * dstride + lane * 16, __builtin_bit_cast(u32x4, ldtr2(L + RL + slot * 4096 + g * 1024 + q * 128 + ((j ^ ff) << 5) + pp * 8, 512)));
      }
    };
    f32x4 acc[3][4], cs[3], sx = Z4, sq = Z4;
    int aoff[2][4];
#pragma unroll
    for (int ks = 0; ks < 2; ++ks)
#pragma unroll
      for (int i = 0; i < 4; ++i) aoff[ks][i] = ((wid + i) & 3) * 2048 + (r16 >> 3) * 1024 + (r16 & 7) * 128 + (((4 * ks + g) ^ (r16 & 7)) << 4);
    int boff;
    { const int q = (lane >> 2) & 3, pp = lane & 3, ff = ((q >> 1) & 1) | ((g & 1) << 1);
      boff = 8192 + g * 3072 + q * 128 + ((wid ^ ff) << 5) + pp * 8; }
#pragma unroll
    for (int i = 0; i < 3; ++i) { cs[i] = Z4;
#pragma unroll
      for (int j = 0; j < 4; ++j) acc[i][j] = Z4; }
    const bf16x8 one = ones8();
    F1 fa, fb;
    auto rd = [&](F1& fr, int s, int ks) {
      lds_cptr B = L + (s % ND) * STG;
#pragma unroll
      for (int seg = 0; seg < 3; ++seg) fr.w[seg] = ldtr2(B + boff + ks * 12288 + seg * 1024, 512);
#pragma unroll
      for (int i = 0; i < 4; ++i) fr.a[i] = ld128(B + aoff[ks][i]);
    };
    auto mm = [&](const F1& fr) {
#pragma unroll
      for (int i = 0; i < 4; ++i) {
        acc[0][i] = MFMA16(fr.w[0], fr.a[i], acc[0][i]);
        acc[1][i] = MFMA16(fr.w[1], fr.a[i], acc[1][i]);
        acc[2][i] = MFMA16(fr.a[i], fr.w[2], acc[2][i]);
      }
      cs[0] = MFMA16(fr.w[0], one, cs[0]); cs[1] = MFMA16(fr.w[1], one, cs[1]); cs[2] = MFMA16(one, fr.w[2], cs[2]);
      sx = MFMA16(one, fr.a[0], sx); sq = MFMA16(fr.a[0], fr.a[0], sq);
    };
    lds_bar();
    TS(1);
    rd(fa, 0, 0); SB();
#pragma unroll
    for (int s = 0; s < NS; ++s) {
      rd(fb, s, 1); mm(fa); weave<10, 17>(); SB();
      if (s == 0) { rl_load(wid); if (wid == 0) rl_load(4); SB(); }
      if (s == 3) { wait_vm<0>(); rl_store(wid); if (wid == 0) rl_store(4); SB(); }
      if (s + 1 < NS) { lds_bar(); rd(fa, s + 1, 0); mm(fb); weave<10, 17>(); SB(); }
      else { mm(fb); SB(); }
    }
    TS(2);
    if ((r16 >> 2) == g) {
      const int e = r16 & 3; const float s2 = e == 0 ? sq[0] : e == 1 ? sq[1] : e == 2 ? sq[2] : sq[3];
      float mean, rstd; ln_consts(sx[0], s2, mean, rstd);
      ms[wid * 16 + r16] = (f32x2){mean, rstd};
    }
    lds_bar();
    TS(25);
    const int h = cb * 4 + wid;
    u32x2 qv[4], kv[4], vv[4];
    float kmx = 0.f;
#pragma unroll
    for (int i = 0; i < 4; ++i) {
      const int rt = (wid + i) & 3;
      const f32x2 st = ms[rt * 16 + r16];
      qv[i] = cvt4((acc[0][i] - st.x * cs[0]) * (st.y * QSCALE));
      { const f32x4 v = (acc[1][i] - st.x * cs[1]) * st.y; kv[i] = cvt4(v);
        kmx = fmaxf(kmx, sum_x32(sum_x16((v[0] * v[0] + v[1] * v[1]) + (v[2] * v[2] + v[3] * v[3])))); }
      { const f32x4 m01 = *(LAS f32x4*)(ms + rt * 16 + 4 * g), m23 = *(LAS f32x4*)(ms + rt * 16 + 4 * g + 2); const float cv = cs[2][0];
        vv[i] = (u32x2){cvtpk((acc[2][i][0] - m01[0] * cv) * m01[1], (acc[2][i][1] - m01[2] * cv) * m01[3]),
                        cvtpk((acc[2][i][2] - m23[0] * cv) * m23[1], (acc[2][i][3] - m23[2] * cv) * m23[3])}; }
    }
#pragma unroll
    for (int i = 0; i < 4; i += 2) {
      { const int rt = (wid + i + (g & 1)) & 3, grow = rb * 64 + rt * 16;
        const unsigned off = (((unsigned)h * 64 + (grow >> 5)) * 2 + (g >> 1)) * 512 + ((grow & 31) + r16) * 16;
        st16(ws, WS_K + off, pair16(kv[i], kv[i + 1])); st16(ws, WS_Q + off, pair16(qv[i], qv[i + 1])); }
      { const int rt = (wid + i + (g >> 1)) & 3, grow = rb * 64 + rt * 16;
        const unsigned off = ((((unsigned)h * 64 + (grow >> 5)) * 2 + (rt & 1)) * 2 + (g & 1)) * 256 + r16 * 16;
        st16(ws, WS_V + off, pair32(vv[i], vv[i + 1])); }
    }
    kmx = max_row16(kmx);
    if (lane == 0) __hip_atomic_store((gu32*)(p.ws + WS_KMAX) + h * 32 + rb, __float_as_uint(kmx), RLX_AGENT);
    TS(26);
  }
}

constexpr int A_K = 0, A_V = 65536, A_ONE = 131072, A_Q = A_ONE + 5120, A_KM = A_Q + 8192, A_DUMMY = A_KM + 256, A_BYTES = A_DUMMY + 256;
constexpr float SAFE_BOUND = 48.f;
constexpr int PRIO_SWITCH = 13;

struct P3Pre { bf16x8 wof[16]; u32x2 xv[4]; };
__device__ __forceinline__ void p2_attn(const Params& p, rsrc_t ws, int h, int qr, int rb, int cb, int mb, int fb, char* smem, unsigned tag, P3Pre& pre TSARG) {
  const int tid = threadIdx.x, lane = tid & 63, wid = rfl(tid >> 6);
  const int r32 = lane & 31, hh = lane >> 5;
  const unsigned lds0 = (unsigned)(size_t)(lds_ptr)smem;
  lds_cptr L = (lds_cptr)smem;
  const char* kg = p.ws + WS_K + (size_t)h * 65536;
  const char* vg = p.ws + WS_V + (size_t)h * 65536;
  const unsigned vo = lane * 16;
  const int u0 = fb < 11 ? 3 * fb : 33 + 2 * (fb - 11), nu = fb < 11 ? 3 : 2;
  glds16<true>(p.ws + WS_Q + ((size_t)h * 64 + qr * 8 + wid) * 1024, vo, lds0 + A_Q + wid * 1024);
  glds4<true>(p.ws + WS_KMAX + h * 128, (lane & 31) * 4, lds0 + A_KM);
#pragma unroll
  for (int i = 0; i < 2; ++i) {
    const int t = wid + 8 * i, gt = (t + 8 * qr) & 63;
    glds16<true>(kg + gt * 1024, vo, lds0 + A_K + t * 1024);
    glds16<true>(vg + gt * 1024, vo, lds0 + A_V + t * 1024);
  }
  st16(ws, WS_O + (unsigned)((qr * 16 + 2 * wid + (lane >> 5)) * 16 + (h >> 1)) * 1024 + (h & 1) * 512 + (lane & 31) * 16, (u32x4){POISON, POISON, POISON, POISON});
  { const unsigned o1 = (tid & 31) < 2 ? 0x3F803F80u : 0u;
    *(LAS u32x2*)(smem + A_ONE + tid * 8) = (u32x2){o1, o1};
    if (tid < 128) *(LAS u32x2*)(smem + A_ONE + 4096 + tid * 8) = (u32x2){o1, o1}; }
  if (wid == 0) {
    poll<256>(p.ws, FL_1, tag);
    if (blockIdx.x == 0 && lane == 0) __hip_atomic_store((gu32*)(p.ws + WS_FLAG + FL_EPOCH), tag, RLX_AGENT);
  }
  wait_bar<1>();
  TS(4);
  const bf16x8 qf = ld128(L + A_Q + wid * 1024 + lane * 16);
  float c;
  { float km = *(const LAS float*)(L + A_KM + lane * 4);
    km = max_x16(max_row16(km));
    float q2 = 0.f;
#pragma unroll
    for (int j = 0; j < 8; ++j) { const float v = __uint_as_float((unsigned)(unsigned short)qf[j] << 16); q2 += v * v; }
    q2 = sum_x32(q2);
    c = sqrtf(q2 * km) * 1.02f + 0.01f; }
  const bool fast = __all(c <= SAFE_BOUND);
  const bool vlane = r32 < 16;
  int vaddr = (int)lds0 + (vlane ? A_V : A_ONE) + hh * 256 + (r32 & 15) * 16;
  const int vstep = vlane ? 4096 : 0;
  int kaddr = (int)lds0 + A_K + lane * 16;
  f32x16 o;
#pragma unroll
  for (int i = 0; i < 16; ++i) o[i] = 0.f;
  unsigned go = vo + ((16 + wid + 8 * qr) & 63) * 1024;
  unsigned ld = lds0 + A_K + (16 + wid) * 1024;
  auto between = [&]() {
    publish(p.ws, FL_P + blockIdx.x * 4, tag);
#pragma unroll
    for (int i = 0; i < 2; ++i) {
      const int ln = ((blockIdx.x >> 3) & 1) * 768 + wid * 96 + i * 32 + lane, ch = ln >> 7, jt = ch >= 6 ? ch - 6 : ch;
      glds4<false>(p.ws + WS_WGF, (unsigned)(((ch >= 6) * 86 + u0 * 2 + (jt < 2 * nu ? jt : jt - 2)) * 128 + (ln & 127)) * 128u, lds0 + A_DUMMY);
    }
  };
  if (fast) {
    f32x16 negc, sa, sb; f32x8 pp, kk; f32x16 vv;
#pragma unroll
    for (int i = 0; i < 16; ++i) negc[i] = -c;
    int cnt, keep;
    asm volatile(ATTN_ASM_A
                 : [o] "+v"(o), [ka] "+v"(kaddr), [va] "+v"(vaddr), [go] "+v"(go), [ld] "+s"(ld), [keep] "=&s"(keep),
                   "=&{v[128:143]}"(sa), "=&{v[144:159]}"(sb), "=&{v[160:167]}"(pp), "=&{v[168:175]}"(kk), "=&{v[176:191]}"(vv)
                 : [q] "v"(qf), [nc] "v"(negc), [vs] "v"(vstep), [kg] "s"(kg), [vg] "s"(vg)
                 : "memory", "scc");
    between();
    const int sw = wid < 4 ? PRIO_SWITCH : 99;
    asm volatile(ATTN_ASM_B
                 : [o] "+v"(o), [ka] "+v"(kaddr), [va] "+v"(vaddr), [cnt] "=&s"(cnt),
                   "+{v[128:143]}"(sa), "+{v[144:159]}"(sb), "+{v[160:167]}"(pp), "+{v[168:175]}"(kk), "+{v[176:191]}"(vv)
                 : [q] "v"(qf), [nc] "v"(negc), [vs] "v"(vstep), [sw] "s"(sw)
                 : "memory", "scc");
  } else {
#pragma unroll
    for (int j = 0; j < 6; ++j) { const unsigned gj = (go + j * 8192) & 0xffffu; glds16<true>(kg, gj, ld + j * 8192); glds16<true>(vg, gj, ld + 65536 + j * 8192); }
    float m = -INFINITY;
    vaddr -= (int)lds0; kaddr -= (int)lds0;
#pragma unroll 1
    for (int t4 = 0; t4 < 16; ++t4) {
      if (t4 == 2) { wait_bar<0>(); between(); }
#pragma unroll 1
      for (int u = 0; u < 4; ++u) {
        const int t = t4 * 4 + u;
        f32x16 z;
#pragma unroll
        for (int i = 0; i < 16; ++i) z[i] = 0.f;
        const f32x16 s = MFMA32(ld128(L + kaddr + t * 1024), qf, z);
        float mx = s[0];
#pragma unroll
        for (int i = 1; i < 16; ++i) mx = fmaxf(mx, s[i]);
        mx = fmaxf(mx, __shfl_xor(mx, 32));
        const float mn = fmaxf(m, mx);
        const float al = __builtin_amdgcn_exp2f(m - mn);
        m = mn;
#pragma unroll
        for (int i = 0; i < 16; ++i) o[i] *= al;
        u32x4 p0, p1;
#pragma unroll
        for (int i = 0; i < 4; ++i) {
          p0[i] = cvtpk(__builtin_amdgcn_exp2f(s[2 * i] - mn), __builtin_amdgcn_exp2f(s[2 * i + 1] - mn));
          p1[i] = cvtpk(__builtin_amdgcn_exp2f(s[8 + 2 * i] - mn), __builtin_amdgcn_exp2f(s[8 + 2 * i + 1] - mn));
        }
        const bf16x8 v0 = ld128(L + vaddr + u * 1024), v1 = ld128(L + vaddr + u * 1024 + 512);
        o = MFMA32(v0, __builtin_bit_cast(bf16x8, p0), o);
        o = MFMA32(v1, __builtin_bit_cast(bf16x8, p1), o);
      }
      vaddr += vstep;
    }
  }
  TS(5);
  if (wid >= 4) {
    const int r16 = lane & 15, g = lane >> 4, wn = wid & 3;
#pragma unroll
    for (int i = 0; i < 16; ++i) pre.wof[i] = ldw16(ws, WS_WOF + ((unsigned)(cb * 4 + wn) * 16 + i) * 1024 + lane * 16);
#pragma unroll
    for (int i = 0; i < 4; ++i) pre.xv[i] = *(const u32x2*)(p.x + ((size_t)(rb * 64 + i * 16 + r16) * DM + cb * 64 + wn * 16 + 4 * g) * 2);
  }
  const float inv = __builtin_amdgcn_rcpf(lo_x32(o[8]));
  const u32x4 ov = pair32((u32x2){cvtpk(o[0] * inv, o[1] * inv), cvtpk(o[2] * inv, o[3] * inv)}, (u32x2){cvtpk(o[4] * inv, o[5] * inv), cvtpk(o[6] * inv, o[7] * inv)});
  const int row = qr * 256 + wid * 32 + r32;
  st16(ws, WS_O + ((((unsigned)(row >> 4) * 16 + (h >> 1)) * 4 + (h & 1) * 2 + hh) * 16 + (row & 15)) * 16, nop4(ov));
}

constexpr int G64_STG = 16384;
template <int NKS, int ND, int LA>
__device__ __forceinline__ void gemm64_load(char* ws, const char* ag  , char* smem) {
  const int lane = threadIdx.x & 63, wid = rfl(threadIdx.x >> 6);
  const unsigned lds0 = (unsigned)(size_t)(lds_ptr)smem;
  lds_cptr L = (lds_cptr)smem + wid * 4096;
  constexpr int NS = (NKS + 3) / 4, LAST = NKS - 4 * (NS - 1);
  auto issue = [&](int s) {
    const unsigned d = lds0 + (s % ND) * G64_STG + wid * 4096;
    if (s < NS - 1 || LAST == 4) glds<4, true>(ag + s * 4096, lane * 16, d); else glds<LAST == 4 ? 4 : 3, true>(ag + s * 4096, lane * 16, d);
  };
  auto okay = [&](int s) { return (s < NS - 1 || LAST == 4) ? pieces_ok<4>(L + (s % ND) * G64_STG) : pieces_ok<3>(L + (s % ND) * G64_STG); };
  auto ensure = [&](int s) { if (!okay(s)) { Spin sp; do { issue(s); wait_vm<0>(); } while (!okay(s) && !sp.expired(ws)); } };
  auto waitv = [&](int j) {
    const int hi = j + LA - 2 < NS - 1 ? j + LA - 2 : NS - 1;
    const int n = (hi - j) * 4 - ((hi == NS - 1 && hi > j) ? 4 - LAST : 0);
    switch (n) { case 0: wait_vm<0>(); break; case 3: wait_vm<3>(); break; case 4: wait_vm<4>(); break; case 7: wait_vm<7>(); break; case 8: wait_vm<8>(); break; case 11: wait_vm<11>(); break;
                 case 12: wait_vm<12>(); break; case 15: wait_vm<15>(); break; case 16: wait_vm<16>(); break; default: wait_vm<0>(); break; }
  };
  static_assert(LAST == 4 || LAST == 3, "last stage");
  issue(0); wait_vm<0>(); ensure(0);
#pragma unroll
  for (int s = 1; s < LA; ++s) if (s < NS) issue(s);
  lds_bar();
#pragma unroll
  for (int j = 1; j < NS; ++j) {
    waitv(j);
    if (j < NS - 1 || LAST == 4) {
      Chk<4> c; c.read(L + (j % ND) * G64_STG);
      if (j + LA - 1 < NS) issue(j + LA - 1);
      if (!c.ok()) ensure(j);
    } else ensure(j);
    lds_bar();
  }
}
template <int NKS, int ND, int TS0>
__device__ __forceinline__ void gemm64_mma(const bf16x8 (&wf)[NKS], char* smem, f32x4 (&acc)[4] TSARG) {
  const int lane = threadIdx.x & 63;
  lds_cptr L = (lds_cptr)smem + lane * 16;
  constexpr int NS = (NKS + 3) / 4;
#pragma unroll
  for (int i = 0; i < 4; ++i) acc[i] = Z4;
  bf16x8 f[2][4];
  auto rd = [&](int ks) {
    lds_cptr B = L + ((ks >> 2) % ND) * G64_STG + (ks & 3) * 1024;
#pragma unroll
    for (int rt = 0; rt < 4; ++rt) f[ks & 1][rt] = ld128(B + rt * 4096);
  };
  lds_bar();
  TS(TS0);
  rd(0); SB();
#pragma unroll
  for (int ks = 0; ks < NKS; ++ks) {
    if (ks + 1 < NKS) {
      if (((ks + 1) & 3) == 0) lds_bar();
      rd(ks + 1);
    }
#pragma unroll
    for (int rt = 0; rt < 4; ++rt) acc[rt] = MFMA16(wf[ks], f[ks & 1][rt], acc[rt]);
    if (ks + 1 < NKS) weave<4, 4>();
    SB();
  }
  TS(TS0 + 1);
}

constexpr int G_B = 0, G_BS = 24576, G_A = 3 * G_BS, G_AS = 16384, G_ST = G_A + 4 * G_AS, G_MS = G_ST + 8192, G_CS = G_MS + 1024;
struct WLoad {
  const char* src[3]; unsigned dst; int rot;
  __device__ __forceinline__ void init(const Params& p, int mb, int fb, int lw, unsigned lds0) {
    const int u0 = fb < 11 ? 3 * fb : 33 + 2 * (fb - 11), nu = fb < 11 ? 3 : 2;
    rot = (fb + 4 * (mb & 1)) & 7;
#pragma unroll
    for (int i = 0; i < 3; ++i) { const int jt = 3 * (lw & 1) + i; src[i] = p.ws + WS_WGF + (size_t)((lw >> 1) * 86 + u0 * 2 + (jt < 2 * nu ? jt : jt - 2)) * 16384; }
    dst = lds0 + G_B + lw * 6144;
  }
  __device__ __forceinline__ void issue(int s, int lane) const {
#pragma unroll
    for (int i = 0; i < 3; ++i) glds<2, true>(src[i] + ((s + rot) & 7) * 2048, lane * 16, dst + (s % 3) * G_BS + i * 2048);
  }
};
struct F4 { bf16x8 a[4], w[2][3]; };
__device__ __forceinline__ void p4_gateup(const Params& p, rsrc_t ws, int mb, int fb, char* smem, const WLoad& wl, int wct, bf16x8 (&wdf)[43] TSARG) {
  const int tid = threadIdx.x, lane = tid & 63, wid = rfl(tid >> 6);
  const int r16 = lane & 15, g = lane >> 4;
  const unsigned lds0 = (unsigned)(size_t)(lds_ptr)smem;
  lds_cptr L = (lds_cptr)smem;
  constexpr int NS = 8;
  const int u0 = fb < 11 ? 3 * fb : 33 + 2 * (fb - 11), nu = fb < 11 ? 3 : 2;
  if (wid >= 4) {
    const int lw = wid - 4;
    const char* ag = p.ws + WS_H + (size_t)(mb * 8 + 2 * lw) * 16384;
    auto issueA = [&](int s) {
      const unsigned d = lds0 + G_A + (s & 3) * G_AS + lw * 4096;
#pragma unroll
      for (int i = 0; i < 2; ++i) glds<2, true>(ag + i * 16384 + ((s + wl.rot) & 7) * 2048, lane * 16, d + i * 2048);
    };
    auto issueS = [&]() {
      glds<2, true>(p.ws + WS_HST + (size_t)mb * 8192 + lw * 2048, lane * 16, lds0 + G_ST + lw * 2048);
    };
    auto ensure = [&](int s) {
      lds_cptr A = L + G_A + (s & 3) * G_AS + lw * 4096;
      if (!pieces_ok<4>(A)) { Spin sp; do { issueA(s); wait_vm<0>(); } while (!pieces_ok<4>(A) && !sp.expired(p.ws)); }
    };
    issueA(0); wait_vm<0>(); ensure(0);
    issueA(1); issueS(); issueA(2); wl.issue(2, lane);
    lds_bar();
#pragma unroll
    for (int j = 1; j < NS; ++j) {
      if (j == 1) wait_vm<12>(); else if (j + 1 < NS) wait_vm<10>(); else wait_vm<0>();
      Chk<4> c; c.read(L + G_A + (j & 3) * G_AS + lw * 4096);
      if (j + 2 < NS) issueA(j + 2);
      if (!c.ok()) ensure(j);
      if (j == 2) {
        lds_cptr Sg = L + G_ST + lw * 2048;
        if (!pieces_ok<2>(Sg)) { Spin sp; do { issueS(); wait_vm<0>(); } while (!pieces_ok<2>(Sg) && !sp.expired(p.ws)); }
      }
      lds_bar();
      if (j + 2 < NS) wl.issue(j + 2, lane);
      if (j == 2) {
        const int row = tid - 256;
        if (row < 128) {
          float a = 0.f, bb = 0.f;
#pragma unroll
          for (int c = 0; c < 8; ++c) { const f32x2 v = *(const LAS f32x2*)(L + G_ST + (row >> 6) * 4096 + c * 512 + (row & 63) * 8); a += v.x; bb += v.y; }
          float mean, rstd; ln_consts(a, bb, mean, rstd);
          *(LAS f32x2*)(smem + G_MS + row * 8) = (f32x2){rstd, -mean * rstd};
        }
      }
    }
    lds_bar();
  } else {
    const int wm = wid >> 1, wn = wid & 1;
    const int aoff = G_A + wm * 8192 + lane * 16;
    const int m0 = wm * 12288, m1 = (wm ^ 1) * 12288;
    const int boff = G_B + wn * 6144 + lane * 16;
    f32x4 acc[2][3][4], cs[3];
#pragma unroll
    for (int i = 0; i < 3; ++i) { cs[i] = Z4;
#pragma unroll
      for (int j = 0; j < 4; ++j) { acc[0][i][j] = Z4; acc[1][i][j] = Z4; } }
    const bf16x8 one = ones8();
    F4 fa, fb2;
    auto rd = [&](F4& f, int s, int ks) {
      lds_cptr B = L + (s % 3) * G_BS, A = L + (s & 3) * G_AS;
#pragma unroll
      for (int i = 0; i < 4; ++i) f.a[i] = ld128(A + aoff + i * 2048 + ks * 1024);
#pragma unroll
      for (int i = 0; i < 3; ++i) { f.w[0][i] = ld128(B + boff + m0 + i * 2048 + ks * 1024); f.w[1][i] = ld128(B + boff + m1 + i * 2048 + ks * 1024); }
    };
    auto mm = [&](const F4& f) {
#pragma unroll
      for (int m = 0; m < 2; ++m)
#pragma unroll
        for (int i = 0; i < 3; ++i)
#pragma unroll
          for (int j = 0; j < 4; ++j) acc[m][i][j] = MFMA16(f.w[m][i], f.a[j], acc[m][i][j]);
#pragma unroll
      for (int i = 0; i < 3; ++i) cs[i] = MFMA16(f.w[0][i], one, cs[i]);
    };
    lds_bar();
    TS(11);
    rd(fa, 0, 0); SB();
#pragma unroll
    for (int s = 0; s < NS; ++s) {
      rd(fb2, s, 1); mm(fa); weave<10, 27>(); SB();
      if (s + 1 < NS) { lds_bar(); rd(fa, s + 1, 0); mm(fb2); weave<10, 27>(); SB(); }
      else { mm(fb2); SB(); }
    }
    TS(12);
    LAS float* csl = (LAS float*)(smem + G_CS);
    if (r16 == 0) {
#pragma unroll
      for (int i = 0; i < 3; ++i) *(LAS f32x4*)(csl + (((wm * 2 + wn) * 3 + i) * 16 + 4 * g)) = cs[i];
    }
    lds_bar();
    TS(33);
    const LAS f32x2* ms = (const LAS f32x2*)(smem + G_MS);
    f32x2 st[4];
#pragma unroll
    for (int j = 0; j < 4; ++j) st[j] = ms[wm * 64 + j * 16 + r16];
    constexpr float K1 = -2.885390081777927f * 0.7978845608028654f, K2 = -2.885390081777927f * 0.035677408136300125f;
#pragma unroll
    for (int i = 0; i < 3; ++i) {
      const int jt = 3 * wn + i, u = jt >> 1, jj = jt & 1;
      const f32x4 cg = *(LAS f32x4*)(csl + ((0 * 2 + wn) * 3 + i) * 16 + 4 * g), cu = *(LAS f32x4*)(csl + ((1 * 2 + wn) * 3 + i) * 16 + 4 * g);
      u32x2 av[4];
#pragma unroll
      for (int j = 0; j < 4; ++j) {
        const f32x4 ag2 = wm ? acc[1][i][j] : acc[0][i][j], au = wm ? acc[0][i][j] : acc[1][i][j];
        const f32x4 ga = ag2 * st[j].x + st[j].y * cg, up = au * st[j].x + st[j].y * cu;
        const f32x4 z = ga * (ga * ga * K2 + K1);
        const f32x4 d = (f32x4){__builtin_amdgcn_exp2f(z[0]), __builtin_amdgcn_exp2f(z[1]), __builtin_amdgcn_exp2f(z[2]), __builtin_amdgcn_exp2f(z[3])} + 1.f;
        const f32x4 rc = (f32x4){__builtin_amdgcn_rcpf(d[0]), __builtin_amdgcn_rcpf(d[1]), __builtin_amdgcn_rcpf(d[2]), __builtin_amdgcn_rcpf(d[3])};
        av[j] = cvt4((ga * rc) * up);
      }
      if (u < nu) {
#pragma unroll
        for (int j = 0; j < 4; j += 2) {
          const unsigned rt = (unsigned)mb * 8 + wm * 4 + j + (g & 1);
          st16(ws, WS_ACT + (((rt * 43 + (u0 + u)) * 4 + jj * 2 + (g >> 1)) * 16 + r16) * 16, nop4(pair16(av[j], av[j + 1])));
        }
      }
    }
    TS(34);
  }
  if (wid >= 4) {
#pragma unroll
    for (int i = 0; i < 43; ++i) wdf[i] = ldw16(ws, WS_WDF + ((unsigned)wct * 43 + i) * 1024 + lane * 16);
  }
}

extern __shared__ __attribute__((aligned(16))) char smem_dyn[];

__global__ __launch_bounds__(512, 2) void mega(Params p) {
  char* smem = smem_dyn;
  const int b = blockIdx.x, x = b & 7, i5 = b >> 3;
  const int tid = threadIdx.x, lane = tid & 63, wid = rfl(tid >> 6);
  const int r16 = lane & 15, g = lane >> 4, wn = wid & 3;
  TS_BEGIN();
  const rsrc_t ws = __builtin_amdgcn_make_buffer_rsrc(p.ws, 0, WS_USED, 0x00020000);
  const unsigned epoch = __hip_atomic_load((gu32*)(p.ws + WS_FLAG + FL_EPOCH), RLX_AGENT);

  const int rb1 = (x >> 1) * 8 + (i5 & 7), cb1 = (x & 1) * 4 + (i5 >> 3);
  p1_qkv(p, ws, b, rb1, cb1, smem TSPASS);
  const unsigned tag = (unsigned)rfl((int)(epoch * 0x9E3779B1u + 0x7F4A7C15u));
  wait_bar<0>();
  publish(p.ws, FL_1 + (cb1 * 32 + rb1) * 4, tag);
  TS(3);

  const int h = x * 4 + (i5 & 3), qr = i5 >> 2;
  const int rb = x * 4 + (i5 & 3), cb = i5 >> 2;
  const int mb = x * 2 + (i5 & 1), fb = i5 >> 1;
  await<32>(p.ws, FL_1 + (h >> 2) * 128, tag);
  TS(27);
  P3Pre pre;
  p2_attn(p, ws, h, qr, rb, cb, mb, fb, smem, tag, pre TSPASS);
  if (wid == 0) poll<256>(p.ws, FL_P, tag);
  if (tid == 64) *(LAS unsigned*)(smem + G_CS + 1024) = 0u;
  lds_bar();
  TS(6);

  const int wct = cb * 4 + wn, col = cb * 64 + wn * 16 + 4 * g;
  u32x2 hv[4];
  if (wid >= 4) {
    { const int u0 = fb < 11 ? 3 * fb : 33 + 2 * (fb - 11), nu = fb < 11 ? 3 : 2;
      const u32x4 pz = (u32x4){POISON, POISON, POISON, POISON};
#pragma unroll
      for (int i = 0; i < 9; ++i) {
        const int j = wn * 9 + i;
        unsigned off;
        if (j < 8) off = WS_H + (unsigned)((rb * 4 + (j >> 1)) * 16 + cb * 2 + (j & 1)) * 1024 + lane * 16;
        else if (j < 32) { const int jj = j - 8, rt = jj / 3, u = jj - rt * 3; off = WS_ACT + (unsigned)((mb * 8 + rt) * 43 + u0 + (u < nu ? u : 0)) * 1024 + lane * 16; }
        else off = WS_HST + (unsigned)(rb * 8 + cb) * 512 + (lane & 31) * 16;
        st16(ws, off, pz);
      }
      wait_vm<0>();
      if (lane == 0 && __hip_atomic_fetch_add((LAS unsigned*)(smem + G_CS + 1024), 1u, __ATOMIC_RELAXED, __HIP_MEMORY_SCOPE_WORKGROUP) == 3u)
        __hip_atomic_store((gu32*)(p.ws + WS_FLAG + FL_Q) + b, tag, RLX_AGENT); }
#pragma unroll
    for (int i = 0; i < 16; ++i) PIN(pre.wof[i]);
#pragma unroll
    for (int i = 0; i < 4; ++i) PIN(pre.xv[i]);
    f32x4 acc[4];
    gemm64_mma<16, 4, 8>(pre.wof, smem + G_A, acc TSPASS);
    LAS f32x2* S = (LAS f32x2*)(smem + G_ST);
#pragma unroll
    for (int i = 0; i < 4; ++i) hv[i] = cvt4(unpack4(pre.xv[i]) + acc[i]);
#pragma unroll
    for (int i = 0; i < 4; i += 2) {
      const unsigned rt = (unsigned)rb * 4 + i + (g & 1);
      st16(ws, WS_H + (((rt * 16 + cb * 2 + (wn >> 1)) * 4 + (wn & 1) * 2 + (g >> 1)) * 16 + r16) * 16, nop4(pair16(hv[i], hv[i + 1])));
    }
#pragma unroll
    for (int i = 0; i < 4; ++i) {
      const f32x4 v = unpack4(hv[i]);
      const float s1 = sum_x32(sum_x16((v[0] + v[1]) + (v[2] + v[3]))), s2 = sum_x32(sum_x16((v[0] * v[0] + v[1] * v[1]) + (v[2] * v[2] + v[3] * v[3])));
      if (g == 0) S[(i * 16 + r16) * 4 + wn] = (f32x2){s1, s2};
    }
    lds_bar();
    if (wid == 4) {
      const f32x2 a = S[lane * 4], bb = S[lane * 4 + 1], c = S[lane * 4 + 2], d = S[lane * 4 + 3];
      const unsigned s1 = __float_as_uint((a.x + bb.x) + (c.x + d.x)), s2 = __float_as_uint((a.y + bb.y) + (c.y + d.y));
      __hip_atomic_store((gu64*)(p.ws + WS_HST + (size_t)(rb * 8 + cb) * 512 + lane * 8), ((unsigned long long)(s2 < POISON ? s2 : POISON - 1) << 32) | (s1 < POISON ? s1 : POISON - 1), RLX_AGENT);
    }
    TS(32);
    raw_bar();
  } else {
    gemm64_load<16, 4, 4>(p.ws, p.ws + WS_O + (size_t)(rb * 4 + wid) * 16384, smem + G_A);
    lds_bar();
    WLoad w0; w0.init(p, mb, fb, wid, (unsigned)(size_t)(lds_ptr)smem); w0.issue(0, lane); w0.issue(1, lane);
    if (wid == 0) poll<256>(p.ws, FL_Q, tag);
    wait_bar<0>();
  }
  TS(10);

  bf16x8 wdf[43];
  { WLoad wl;
    if (wid >= 4) wl.init(p, mb, fb, wid - 4, (unsigned)(size_t)(lds_ptr)smem);
    p4_gateup(p, ws, mb, fb, smem, wl, wct, wdf TSPASS); }
  lds_bar();
  TS(13);

  if (wid >= 4) {
#pragma unroll
    for (int i = 0; i < 43; ++i) PIN(wdf[i]);
    f32x4 acc[4];
    gemm64_mma<43, 8, 15>(wdf, smem, acc TSPASS);
#pragma unroll
    for (int i = 0; i < 4; ++i) *(u32x2*)(p.out + ((size_t)(rb * 64 + i * 16 + r16) * DM + col) * 2) = cvt4(unpack4(hv[i]) + acc[i]);
  } else {
    gemm64_load<43, 8, 6>(p.ws, p.ws + WS_ACT + (size_t)(rb * 4 + wid) * 43 * 1024, smem);
  }
  TS_END();
}

extern "C" void kernel_launch(void* const* d_in, const int* in_sizes, int n_in, void* d_out, int out_size, void* d_ws, size_t ws_size, hipStream_t stream) {
  Params p;
  p.x = (const char*)d_in[0]; p.wqkv = (const char*)d_in[1]; p.wo = (const char*)d_in[2];
  p.wg = (const char*)d_in[4]; p.wu = (const char*)d_in[5]; p.wd = (const char*)d_in[6];
  p.out = (char*)d_out; p.ws = (char*)d_ws;
  constexpr int LDS = 160 * 1024;
  (void)hipFuncSetAttribute((const void*)mega, hipFuncAttributeMaxDynamicSharedMemorySize, LDS);
  mega<<<256, 512, LDS, stream>>>(p);
}
```

```cpp
#define DIAG 1
#define DIAG_RT 1
#define AMP_I 0
#define AMP_J 1
#include <hip/hip_runtime.h>
#include <stdint.h>
#define ATTN_ASM_A \
  "s_mov_b32 %[keep], m0\n\t" \
  "ds_read_b128 v[168:171], %[ka]\n\t" \
  "ds_read_b128 v[172:175], %[ka] offset:1024\n\t" \
  "ds_read_b128 v[176:179], %[va]\n\t" \
  "ds_read_b128 v[184:187], %[va] offset:512\n\t" \
  "v_mov_b32 v164, 0\n\t" \
  "v_mov_b32 v165, 0\n\t" \
  "v_mov_b32 v166, 0\n\t" \
  "v_mov_b32 v167, 0\n\t" \
  "v_mov_b32 v188, 0\n\t" \
  "v_mov_b32 v189, 0\n\t" \
  "v_mov_b32 v190, 0\n\t" \
  "v_mov_b32 v191, 0\n\t" \
  "s_waitcnt lgkmcnt(3)\n\t" \
  "v_mfma_f32_32x32x16_bf16 v[128:143], v[168:171], %[q], %[nc]\n\t" \
  "s_nop 10\n\t" \
  "s_waitcnt lgkmcnt(2)\n\t" \
  "v_mfma_f32_32x32x16_bf16 v[144:159], v[172:175], %[q], %[nc]\n\t" \
  "ds_read_b128 v[168:171], %[ka] offset:2048\n\t" \
  "s_mov_b32 m0, %[ld]\n\t" \
  "v_exp_f32 v128, v128\n\t" \
  "v_exp_f32 v129, v129\n\t" \
  "global_load_lds_dwordx4 %[go], %[kg] sc1\n\t" \
  "v_exp_f32 v130, v130\n\t" \
  "v_exp_f32 v131, v131\n\t" \
  "v_cvt_pk_bf16_f32 v160, v128, v129\n\t" \
  "v_exp_f32 v132, v132\n\t" \
  "v_exp_f32 v133, v133\n\t" \
  "v_cvt_pk_bf16_f32 v161, v130, v131\n\t" \
  "v_mfma_f32_32x32x16_bf16 %[o], v[188:191], v[164:167], %[o]\n\t" \
  "ds_read_b128 v[180:183], %[va] offset:1024\n\t" \
  "ds_read_b128 v[188:191], %[va] offset:1536\n\t" \
  "s_add_u32 m0, %[ld], 0x10000\n\t" \
  "v_exp_f32 v134, v134\n\t" \
  "v_exp_f32 v135, v135\n\t" \
  "global_load_lds_dwordx4 %[go], %[vg] sc1\n\t" \
  "s_add_u32 %[ld], %[ld], 0x2000\n\t" \
  "v_cvt_pk_bf16_f32 v162, v132, v133\n\t" \
  "v_exp_f32 v136, v136\n\t" \
  "v_exp_f32 v137, v137\n\t" \
  "v_cvt_pk_bf16_f32 v163, v134, v135\n\t" \
  "v_add_u32 %[go], 0x2000, %[go]\n\t" \
  "v_and_b32 %[go], 0xffff, %[go]\n\t" \
  "v_exp_f32 v138, v138\n\t" \
  "v_exp_f32 v139, v139\n\t" \
  "s_waitcnt lgkmcnt(4)\n\t" \
  "v_mfma_f32_32x32x16_bf16 %[o], v[176:179], v[160:163], %[o]\n\t" \
  "v_cvt_pk_bf16_f32 v164, v136, v137\n\t" \
  "v_exp_f32 v140, v140\n\t" \
  "v_exp_f32 v141, v141\n\t" \
  "v_cvt_pk_bf16_f32 v165, v138, v139\n\t" \
  "v_exp_f32 v142, v142\n\t" \
  "v_exp_f32 v143, v143\n\t" \
  "v_cvt_pk_bf16_f32 v166, v140, v141\n\t" \
  "v_cvt_pk_bf16_f32 v167, v142, v143\n\t" \
  "s_waitcnt lgkmcnt(2)\n\t" \
  "v_mfma_f32_32x32x16_bf16 v[128:143], v[168:171], %[q], %[nc]\n\t" \
  "ds_read_b128 v[172:175], %[ka] offset:3072\n\t" \
  "s_mov_b32 m0, %[ld]\n\t" \
  "v_exp_f32 v144, v144\n\t" \
  "v_exp_f32 v145, v145\n\t" \
  "global_load_lds_dwordx4 %[go], %[kg] sc1\n\t" \
  "v_exp_f32 v146, v146\n\t" \
  "v_exp_f32 v147, v147\n\t" \
  "v_cvt_pk_bf16_f32 v160, v144, v145\n\t" \
  "v_exp_f32 v148, v148\n\t" \
  "v_exp_f32 v149, v149\n\t" \
  "v_cvt_pk_bf16_f32 v161, v146, v147\n\t" \
  "v_mfma_f32_32x32x16_bf16 %[o], v[184:187], v[164:167], %[o]\n\t" \
  "ds_read_b128 v[176:179], %[va] offset:2048\n\t" \
  "ds_read_b128 v[184:187], %[va] offset:2560\n\t" \
  "s_add_u32 m0, %[ld], 0x10000\n\t" \
  "v_exp_f32 v150, v150\n\t" \
  "v_exp_f32 v151, v151\n\t" \
  "global_load_lds_dwordx4 %[go], %[vg] sc1\n\t" \
  "s_add_u32 %[ld], %[ld], 0x2000\n\t" \
  "v_cvt_pk_bf16_f32 v162, v148, v149\n\t" \
  "v_exp_f32 v152, v152\n\t" \
  "v_exp_f32 v153, v153\n\t" \
  "v_cvt_pk_bf16_f32 v163, v150, v151\n\t" \
  "v_add_u32 %[go], 0x2000, %[go]\n\t" \
  "v_and_b32 %[go], 0xffff, %[go]\n\t" \
  "v_exp_f32 v154, v154\n\t" \
  "v_exp_f32 v155, v155\n\t" \
  "s_waitcnt lgkmcnt(4)\n\t" \
  "v_mfma_f32_32x32x16_bf16 %[o], v[180:183], v[160:163], %[o]\n\t" \
  "v_cvt_pk_bf16_f32 v164, v152, v153\n\t" \
  "v_exp_f32 v156, v156\n\t" \
  "v_exp_f32 v157, v157\n\t" \
  "v_cvt_pk_bf16_f32 v165, v154, v155\n\t" \
  "v_exp_f32 v158, v158\n\t" \
  "v_exp_f32 v159, v159\n\t" \
  "v_cvt_pk_bf16_f32 v166, v156, v157\n\t" \
  "v_cvt_pk_bf16_f32 v167, v158, v159\n\t" \
  "s_waitcnt lgkmcnt(2)\n\t" \
  "v_mfma_f32_32x32x16_bf16 v[144:159], v[172:175], %[q], %[nc]\n\t" \
  "ds_read_b128 v[168:171], %[ka] offset:4096\n\t" \
  "s_mov_b32 m0, %[ld]\n\t" \
  "v_exp_f32 v128, v128\n\t" \
  "v_exp_f32 v129, v129\n\t" \
  "global_load_lds_dwordx4 %[go], %[kg] sc1\n\t" \
  "v_exp_f32 v130, v130\n\t" \
  "v_exp_f32 v131, v131\n\t" \
  "v_cvt_pk_bf16_f32 v160, v128, v129\n\t" \
  "v_exp_f32 v132, v132\n\t" \
  "v_exp_f32 v133, v133\n\t" \
  "v_cvt_pk_bf16_f32 v161, v130, v131\n\t" \
  "v_mfma_f32_32x32x16_bf16 %[o], v[188:191], v[164:167], %[o]\n\t" \
  "ds_read_b128 v[180:183], %[va] offset:3072\n\t" \
  "ds_read_b128 v[188:191], %[va] offset:3584\n\t" \
  "s_add_u32 m0, %[ld], 0x10000\n\t" \
  "v_exp_f32 v134, v134\n\t" \
  "v_exp_f32 v135, v135\n\t" \
  "global_load_lds_dwordx4 %[go], %[vg] sc1\n\t" \
  "s_add_u32 %[ld], %[ld], 0x2000\n\t" \
  "v_cvt_pk_bf16_f32 v162, v132, v133\n\t" \
  "v_exp_f32 v136, v136\n\t" \
  "v_exp_f32 v137, v137\n\t" \
  "v_cvt_pk_bf16_f32 v163, v134, v135\n\t" \
  "v_add_u32 %[go], 0x2000, %[go]\n\t" \
  "v_and_b32 %[go], 0xffff, %[go]\n\t" \
  "v_exp_f32 v138, v138\n\t" \
  "v_exp_f32 v139, v139\n\t" \
  "s_waitcnt lgkmcnt(4)\n\t" \
  "v_mfma_f32_32x32x16_bf16 %[o], v[176:179], v[160:163], %[o]\n\t" \
  "v_cvt_pk_bf16_f32 v164, v136, v137\n\t" \
  "v_exp_f32 v140, v140\n\t" \
  "v_exp_f32 v141, v141\n\t" \
  "v_cvt_pk_bf16_f32 v165, v138, v139\n\t" \
  "v_exp_f32 v142, v142\n\t" \
  "v_exp_f32 v143, v143\n\t" \
  "v_cvt_pk_bf16_f32 v166, v140, v141\n\t" \
  "v_cvt_pk_bf16_f32 v167, v142, v143\n\t" \
  "s_waitcnt lgkmcnt(2)\n\t" \
  "v_mfma_f32_32x32x16_bf16 v[128:143], v[168:171], %[q], %[nc]\n\t" \
  "ds_read_b128 v[172:175], %[ka] offset:5120\n\t" \
  "s_mov_b32 m0, %[ld]\n\t" \
  "v_exp_f32 v144, v144\n\t" \
  "v_exp_f32 v145, v145\n\t" \
  "global_load_lds_dwordx4 %[go], %[kg] sc1\n\t" \
  "v_exp_f32 v146, v146\n\t" \
  "v_exp_f32 v147, v147\n\t" \
  "v_cvt_pk_bf16_f32 v160, v144, v145\n\t" \
  "v_exp_f32 v148, v148\n\t" \
  "v_exp_f32 v149, v149\n\t" \
  "v_cvt_pk_bf16_f32 v161, v146, v147\n\t" \
  "v_mfma_f32_32x32x16_bf16 %[o], v[184:187], v[164:167], %[o]\n\t" \
  "ds_read_b128 v[176:179], %[va] offset:4096\n\t" \
  "ds_read_b128 v[184:187], %[va] offset:4608\n\t" \
  "s_add_u32 m0, %[ld], 0x10000\n\t" \
  "v_exp_f32 v150, v150\n\t" \
  "v_exp_f32 v151, v151\n\t" \
  "global_load_lds_dwordx4 %[go], %[vg] sc1\n\t" \
  "s_add_u32 %[ld], %[ld], 0x2000\n\t" \
  "v_cvt_pk_bf16_f32 v162, v148, v149\n\t" \
  "v_exp_f32 v152, v152\n\t" \
  "v_exp_f32 v153, v153\n\t" \
  "v_cvt_pk_bf16_f32 v163, v150, v151\n\t" \
  "v_add_u32 %[go], 0x2000, %[go]\n\t" \
  "v_and_b32 %[go], 0xffff, %[go]\n\t" \
  "v_exp_f32 v154, v154\n\t" \
  "v_exp_f32 v155, v155\n\t" \
  "s_waitcnt lgkmcnt(4)\n\t" \
  "v_mfma_f32_32x32x16_bf16 %[o], v[180:183], v[160:163], %[o]\n\t" \
  "v_cvt_pk_bf16_f32 v164, v152, v153\n\t" \
  "v_exp_f32 v156, v156\n\t" \
  "v_exp_f32 v157, v157\n\t" \
  "v_cvt_pk_bf16_f32 v165, v154, v155\n\t" \
  "v_exp_f32 v158, v158\n\t" \
  "v_exp_f32 v159, v159\n\t" \
  "v_cvt_pk_bf16_f32 v166, v156, v157\n\t" \
  "v_cvt_pk_bf16_f32 v167, v158, v159\n\t" \
  "v_add_u32 %[ka], 0x1000, %[ka]\n\t" \
  "v_add_u32 %[va], %[vs], %[va]\n\t" \
  "s_waitcnt lgkmcnt(2)\n\t" \
  "v_mfma_f32_32x32x16_bf16 v[144:159], v[172:175], %[q], %[nc]\n\t" \
  "ds_read_b128 v[168:171], %[ka] offset:2048\n\t" \
  "s_mov_b32 m0, %[ld]\n\t" \
  "v_exp_f32 v128, v128\n\t" \
  "v_exp_f32 v129, v129\n\t" \
  "global_load_lds_dwordx4 %[go], %[kg] sc1\n\t" \
  "v_exp_f32 v130, v130\n\t" \
  "v_exp_f32 v131, v131\n\t" \
  "v_cvt_pk_bf16_f32 v160, v128, v129\n\t" \
  "v_exp_f32 v132, v132\n\t" \
  "v_exp_f32 v133, v133\n\t" \
  "v_cvt_pk_bf16_f32 v161, v130, v131\n\t" \
  "v_mfma_f32_32x32x16_bf16 %[o], v[188:191], v[164:167], %[o]\n\t" \
  "ds_read_b128 v[180:183], %[va] offset:1024\n\t" \
  "ds_read_b128 v[188:191], %[va] offset:1536\n\t" \
  "s_add_u32 m0, %[ld], 0x10000\n\t" \
  "v_exp_f32 v134, v134\n\t" \
  "v_exp_f32 v135, v135\n\t" \
  "global_load_lds_dwordx4 %[go], %[vg] sc1\n\t" \
  "s_add_u32 %[ld], %[ld], 0x2000\n\t" \
  "v_cvt_pk_bf16_f32 v162, v132, v133\n\t" \
  "v_exp_f32 v136, v136\n\t" \
  "v_exp_f32 v137, v137\n\t" \
  "v_cvt_pk_bf16_f32 v163, v134, v135\n\t" \
  "v_add_u32 %[go], 0x2000, %[go]\n\t" \
  "v_and_b32 %[go], 0xffff, %[go]\n\t" \
  "v_exp_f32 v138, v138\n\t" \
  "v_exp_f32 v139, v139\n\t" \
  "s_waitcnt lgkmcnt(4)\n\t" \
  "v_mfma_f32_32x32x16_bf16 %[o], v[176:179], v[160:163], %[o]\n\t" \
  "v_cvt_pk_bf16_f32 v164, v136, v137\n\t" \
  "v_exp_f32 v140, v140\n\t" \
  "v_exp_f32 v141, v141\n\t" \
  "v_cvt_pk_bf16_f32 v165, v138, v139\n\t" \
  "v_exp_f32 v142, v142\n\t" \
  "v_exp_f32 v143, v143\n\t" \
  "v_cvt_pk_bf16_f32 v166, v140, v141\n\t" \
  "v_cvt_pk_bf16_f32 v167, v142, v143\n\t" \
  "s_waitcnt lgkmcnt(2)\n\t" \
  "v_mfma_f32_32x32x16_bf16 v[128:143], v[168:171], %[q], %[nc]\n\t" \
  "ds_read_b128 v[172:175], %[ka] offset:3072\n\t" \
  "s_mov_b32 m0, %[ld]\n\t" \
  "v_exp_f32 v144, v144\n\t" \
  "v_exp_f32 v145, v145\n\t" \
  "global_load_lds_dwordx4 %[go], %[kg] sc1\n\t" \
  "v_exp_f32 v146, v146\n\t" \
  "v_exp_f32 v147, v147\n\t" \
  "v_cvt_pk_bf16_f32 v160, v144, v145\n\t" \
  "v_exp_f32 v148, v148\n\t" \
  "v_exp_f32 v149, v149\n\t" \
  "v_cvt_pk_bf16_f32 v161, v146, v147\n\t" \
  "v_mfma_f32_32x32x16_bf16 %[o], v[184:187], v[164:167], %[o]\n\t" \
  "ds_read_b128 v[176:179], %[va] offset:2048\n\t" \
  "ds_read_b128 v[184:187], %[va] offset:2560\n\t" \
  "s_add_u32 m0, %[ld], 0x10000\n\t" \
  "v_exp_f32 v150, v150\n\t" \
  "v_exp_f32 v151, v151\n\t" \
  "global_load_lds_dwordx4 %[go], %[vg] sc1\n\t" \
  "s_add_u32 %[ld], %[ld], 0x2000\n\t" \
  "v_cvt_pk_bf16_f32 v162, v148, v149\n\t" \
  "v_exp_f32 v152, v152\n\t" \
  "v_exp_f32 v153, v153\n\t" \
  "v_cvt_pk_bf16_f32 v163, v150, v151\n\t" \
  "v_add_u32 %[go], 0x2000, %[go]\n\t" \
  "v_and_b32 %[go], 0xffff, %[go]\n\t" \
  "v_exp_f32 v154, v154\n\t" \
  "v_exp_f32 v155, v155\n\t" \
  "s_waitcnt lgkmcnt(4)\n\t" \
  "v_mfma_f32_32x32x16_bf16 %[o], v[180:183], v[160:163], %[o]\n\t" \
  "v_cvt_pk_bf16_f32 v164, v152, v153\n\t" \
  "v_exp_f32 v156, v156\n\t" \
  "v_exp_f32 v157, v157\n\t" \
  "v_cvt_pk_bf16_f32 v165, v154, v155\n\t" \
  "v_exp_f32 v158, v158\n\t" \
  "v_exp_f32 v159, v159\n\t" \
  "v_cvt_pk_bf16_f32 v166, v156, v157\n\t" \
  "v_cvt_pk_bf16_f32 v167, v158, v159\n\t" \
  "s_waitcnt lgkmcnt(2)\n\t" \
  "v_mfma_f32_32x32x16_bf16 v[144:159], v[172:175], %[q], %[nc]\n\t" \
  "ds_read_b128 v[168:171], %[ka] offset:4096\n\t" \
  "v_exp_f32 v128, v128\n\t" \
  "v_exp_f32 v129, v129\n\t" \
  "v_exp_f32 v130, v130\n\t" \
  "v_exp_f32 v131, v131\n\t" \
  "v_cvt_pk_bf16_f32 v160, v128, v129\n\t" \
  "v_exp_f32 v132, v132\n\t" \
  "v_exp_f32 v133, v133\n\t" \
  "v_cvt_pk_bf16_f32 v161, v130, v131\n\t" \
  "v_mfma_f32_32x32x16_bf16 %[o], v[188:191], v[164:167], %[o]\n\t" \
  "ds_read_b128 v[180:183], %[va] offset:3072\n\t" \
  "ds_read_b128 v[188:191], %[va] offset:3584\n\t" \
  "v_exp_f32 v134, v134\n\t" \
  "v_exp_f32 v135, v135\n\t" \
  "v_cvt_pk_bf16_f32 v162, v132, v133\n\t" \
  "v_exp_f32 v136, v136\n\t" \
  "v_exp_f32 v137, v137\n\t" \
  "v_cvt_pk_bf16_f32 v163, v134, v135\n\t" \
  "v_exp_f32 v138, v138\n\t" \
  "v_exp_f32 v139, v139\n\t" \
  "s_waitcnt lgkmcnt(4)\n\t" \
  "v_mfma_f32_32x32x16_bf16 %[o], v[176:179], v[160:163], %[o]\n\t" \
  "v_cvt_pk_bf16_f32 v164, v136, v137\n\t" \
  "v_exp_f32 v140, v140\n\t" \
  "v_exp_f32 v141, v141\n\t" \
  "v_cvt_pk_bf16_f32 v165, v138, v139\n\t" \
  "v_exp_f32 v142, v142\n\t" \
  "v_exp_f32 v143, v143\n\t" \
  "v_cvt_pk_bf16_f32 v166, v140, v141\n\t" \
  "v_cvt_pk_bf16_f32 v167, v142, v143\n\t" \
  "s_waitcnt lgkmcnt(2)\n\t" \
  "v_mfma_f32_32x32x16_bf16 v[128:143], v[168:171], %[q], %[nc]\n\t" \
  "ds_read_b128 v[172:175], %[ka] offset:5120\n\t" \
  "v_exp_f32 v144, v144\n\t" \
  "v_exp_f32 v145, v145\n\t" \
  "v_exp_f32 v146, v146\n\t" \
  "v_exp_f32 v147, v147\n\t" \
  "v_cvt_pk_bf16_f32 v160, v144, v145\n\t" \
  "v_exp_f32 v148, v148\n\t" \
  "v_exp_f32 v149, v149\n\t" \
  "v_cvt_pk_bf16_f32 v161, v146, v147\n\t" \
  "v_mfma_f32_32x32x16_bf16 %[o], v[184:187], v[164:167], %[o]\n\t" \
  "ds_read_b128 v[176:179], %[va] offset:4096\n\t" \
  "ds_read_b128 v[184:187], %[va] offset:4608\n\t" \
  "v_exp_f32 v150, v150\n\t" \
  "v_exp_f32 v151, v151\n\t" \
  "v_cvt_pk_bf16_f32 v162, v148, v149\n\t" \
  "v_exp_f32 v152, v152\n\t" \
  "v_exp_f32 v153, v153\n\t" \
  "v_cvt_pk_bf16_f32 v163, v150, v151\n\t" \
  "v_exp_f32 v154, v154\n\t" \
  "v_exp_f32 v155, v155\n\t" \
  "s_waitcnt lgkmcnt(4)\n\t" \
  "v_mfma_f32_32x32x16_bf16 %[o], v[180:183], v[160:163], %[o]\n\t" \
  "v_cvt_pk_bf16_f32 v164, v152, v153\n\t" \
  "v_exp_f32 v156, v156\n\t" \
  "v_exp_f32 v157, v157\n\t" \
  "v_cvt_pk_bf16_f32 v165, v154, v155\n\t" \
  "v_exp_f32 v158, v158\n\t" \
  "v_exp_f32 v159, v159\n\t" \
  "v_cvt_pk_bf16_f32 v166, v156, v157\n\t" \
  "v_cvt_pk_bf16_f32 v167, v158, v159\n\t" \
  "v_add_u32 %[ka], 0x1000, %[ka]\n\t" \
  "v_add_u32 %[va], %[vs], %[va]\n\t" \
  "s_mov_b32 m0, %[keep]\n\t" \
  "s_waitcnt vmcnt(0) lgkmcnt(0)\n\t" \
  "s_barrier\n\t" \
  ""
#define ATTN_ASM_B \
  "s_mov_b32 %[cnt], 2\n\t" \
  "s_cmp_eq_u32 %[sw], 99\n\t" \
  "s_cbranch_scc1 L_attn_y_%=\n\t" \
  "s_setprio 2\n\t" \
  "s_branch L_attn_loop_%=\n\t" \
  "L_attn_y_%=:\n\t" \
  "s_setprio 1\n\t" \
  "L_attn_loop_%=:\n\t" \
  "s_cmp_lg_u32 %[cnt], %[sw]\n\t" \
  "s_cbranch_scc1 L_attn_nosw_%=\n\t" \
  "s_setprio 0\n\t" \
  "L_attn_nosw_%=:\n\t" \
  "s_waitcnt lgkmcnt(2)\n\t" \
  "v_mfma_f32_32x32x16_bf16 v[144:159], v[172:175], %[q], %[nc]\n\t" \
  "ds_read_b128 v[168:171], %[ka] offset:2048\n\t" \
  "v_exp_f32 v128, v128\n\t" \
  "v_exp_f32 v129, v129\n\t" \
  "v_exp_f32 v130, v130\n\t" \
  "v_exp_f32 v131, v131\n\t" \
  "v_cvt_pk_bf16_f32 v160, v128, v129\n\t" \
  "v_exp_f32 v132, v132\n\t" \
  "v_exp_f32 v133, v133\n\t" \
  "v_cvt_pk_bf16_f32 v161, v130, v131\n\t" \
  "v_mfma_f32_32x32x16_bf16 %[o], v[188:191], v[164:167], %[o]\n\t" \
  "ds_read_b128 v[180:183], %[va] offset:1024\n\t" \
  "ds_read_b128 v[188:191], %[va] offset:1536\n\t" \
  "v_exp_f32 v134, v134\n\t" \
  "v_exp_f32 v135, v135\n\t" \
  "v_cvt_pk_bf16_f32 v162, v132, v133\n\t" \
  "v_exp_f32 v136, v136\n\t" \
  "v_exp_f32 v137, v137\n\t" \
  "v_cvt_pk_bf16_f32 v163, v134, v135\n\t" \
  "v_exp_f32 v138, v138\n\t" \
  "v_exp_f32 v139, v139\n\t" \
  "s_waitcnt lgkmcnt(4)\n\t" \
  "v_mfma_f32_32x32x16_bf16 %[o], v[176:179], v[160:163], %[o]\n\t" \
  "v_cvt_pk_bf16_f32 v164, v136, v137\n\t" \
  "v_exp_f32 v140, v140\n\t" \
  "v_exp_f32 v141, v141\n\t" \
  "v_cvt_pk_bf16_f32 v165, v138, v139\n\t" \
  "v_exp_f32 v142, v142\n\t" \
  "v_exp_f32 v143, v143\n\t" \
  "v_cvt_pk_bf16_f32 v166, v140, v141\n\t" \
  "v_cvt_pk_bf16_f32 v167, v142, v143\n\t" \
  "s_waitcnt lgkmcnt(2)\n\t" \
  "v_mfma_f32_32x32x16_bf16 v[128:143], v[168:171], %[q], %[nc]\n\t" \
  "ds_read_b128 v[172:175], %[ka] offset:3072\n\t" \
  "v_exp_f32 v144, v144\n\t" \
  "v_exp_f32 v145, v145\n\t" \
  "v_exp_f32 v146, v146\n\t" \
  "v_exp_f32 v147, v147\n\t" \
  "v_cvt_pk_bf16_f32 v160, v144, v145\n\t" \
  "v_exp_f32 v148, v148\n\t" \
  "v_exp_f32 v149, v149\n\t" \
  "v_cvt_pk_bf16_f32 v161, v146, v147\n\t" \
  "v_mfma_f32_32x32x16_bf16 %[o], v[184:187], v[164:167], %[o]\n\t" \
  "ds_read_b128 v[176:179], %[va] offset:2048\n\t" \
  "ds_read_b128 v[184:187], %[va] offset:2560\n\t" \
  "v_exp_f32 v150, v150\n\t" \
  "v_exp_f32 v151, v151\n\t" \
  "v_cvt_pk_bf16_f32 v162, v148, v149\n\t" \
  "v_exp_f32 v152, v152\n\t" \
  "v_exp_f32 v153, v153\n\t" \
  "v_cvt_pk_bf16_f32 v163, v150, v151\n\t" \
  "v_exp_f32 v154, v154\n\t" \
  "v_exp_f32 v155, v155\n\t" \
  "s_waitcnt lgkmcnt(4)\n\t" \
  "v_mfma_f32_32x32x16_bf16 %[o], v[180:183], v[160:163], %[o]\n\t" \
  "v_cvt_pk_bf16_f32 v164, v152, v153\n\t" \
  "v_exp_f32 v156, v156\n\t" \
  "v_exp_f32 v157, v157\n\t" \
  "v_cvt_pk_bf16_f32 v165, v154, v155\n\t" \
  "v_exp_f32 v158, v158\n\t" \
  "v_exp_f32 v159, v159\n\t" \
  "v_cvt_pk_bf16_f32 v166, v156, v157\n\t" \
  "v_cvt_pk_bf16_f32 v167, v158, v159\n\t" \
  "s_waitcnt lgkmcnt(2)\n\t" \
  "v_mfma_f32_32x32x16_bf16 v[144:159], v[172:175], %[q], %[nc]\n\t" \
  "ds_read_b128 v[168:171], %[ka] offset:4096\n\t" \
  "v_exp_f32 v128, v128\n\t" \
  "v_exp_f32 v129, v129\n\t" \
  "v_exp_f32 v130, v130\n\t" \
  "v_exp_f32 v131, v131\n\t" \
  "v_cvt_pk_bf16_f32 v160, v128, v129\n\t" \
  "v_exp_f32 v132, v132\n\t" \
  "v_exp_f32 v133, v133\n\t" \
  "v_cvt_pk_bf16_f32 v161, v130, v131\n\t" \
  "v_mfma_f32_32x32x16_bf16 %[o], v[188:191], v[164:167], %[o]\n\t" \
  "ds_read_b128 v[180:183], %[va] offset:3072\n\t" \
  "ds_read_b128 v[188:191], %[va] offset:3584\n\t" \
  "v_exp_f32 v134, v134\n\t" \
  "v_exp_f32 v135, v135\n\t" \
  "v_cvt_pk_bf16_f32 v162, v132, v133\n\t" \
  "v_exp_f32 v136, v136\n\t" \
  "v_exp_f32 v137, v137\n\t" \
  "v_cvt_pk_bf16_f32 v163, v134, v135\n\t" \
  "v_exp_f32 v138, v138\n\t" \
  "v_exp_f32 v139, v139\n\t" \
  "s_waitcnt lgkmcnt(4)\n\t" \
  "v_mfma_f32_32x32x16_bf16 %[o], v[176:179], v[160:163], %[o]\n\t" \
  "v_cvt_pk_bf16_f32 v164, v136, v137\n\t" \
  "v_exp_f32 v140, v140\n\t" \
  "v_exp_f32 v141, v141\n\t" \
  "v_cvt_pk_bf16_f32 v165, v138, v139\n\t" \
  "v_exp_f32 v142, v142\n\t" \
  "v_exp_f32 v143, v143\n\t" \
  "v_cvt_pk_bf16_f32 v166, v140, v141\n\t" \
  "v_cvt_pk_bf16_f32 v167, v142, v143\n\t" \
  "s_waitcnt lgkmcnt(2)\n\t" \
  "v_mfma_f32_32x32x16_bf16 v[128:143], v[168:171], %[q], %[nc]\n\t" \
  "ds_read_b128 v[172:175], %[ka] offset:5120\n\t" \
  "v_exp_f32 v144, v144\n\t" \
  "v_exp_f32 v145, v145\n\t" \
  "v_exp_f32 v146, v146\n\t" \
  "v_exp_f32 v147, v147\n\t" \
  "v_cvt_pk_bf16_f32 v160, v144, v145\n\t" \
  "v_exp_f32 v148, v148\n\t" \
  "v_exp_f32 v149, v149\n\t" \
  "v_cvt_pk_bf16_f32 v161, v146, v147\n\t" \
  "v_mfma_f32_32x32x16_bf16 %[o], v[184:187], v[164:167], %[o]\n\t" \
  "ds_read_b128 v[176:179], %[va] offset:4096\n\t" \
  "ds_read_b128 v[184:187], %[va] offset:4608\n\t" \
  "v_exp_f32 v150, v150\n\t" \
  "v_exp_f32 v151, v151\n\t" \
  "v_cvt_pk_bf16_f32 v162, v148, v149\n\t" \
  "v_exp_f32 v152, v152\n\t" \
  "v_exp_f32 v153, v153\n\t" \
  "v_cvt_pk_bf16_f32 v163, v150, v151\n\t" \
  "v_exp_f32 v154, v154\n\t" \
  "v_exp_f32 v155, v155\n\t" \
  "s_waitcnt lgkmcnt(4)\n\t" \
  "v_mfma_f32_32x32x16_bf16 %[o], v[180:183], v[160:163], %[o]\n\t" \
  "v_cvt_pk_bf16_f32 v164, v152, v153\n\t" \
  "v_exp_f32 v156, v156\n\t" \
  "v_exp_f32 v157, v157\n\t" \
  "v_cvt_pk_bf16_f32 v165, v154, v155\n\t" \
  "v_exp_f32 v158, v158\n\t" \
  "v_exp_f32 v159, v159\n\t" \
  "v_cvt_pk_bf16_f32 v166, v156, v157\n\t" \
  "v_cvt_pk_bf16_f32 v167, v158, v159\n\t" \
  "v_add_u32 %[ka], 0x1000, %[ka]\n\t" \
  "v_add_u32 %[va], %[vs], %[va]\n\t" \
  "s_add_u32 %[cnt], %[cnt], 1\n\t" \
  "s_cmp_lt_u32 %[cnt], 16\n\t" \
  "s_cbranch_scc1 L_attn_loop_%=\n\t" \
  "s_waitcnt lgkmcnt(0)\n\t" \
  "v_mfma_f32_32x32x16_bf16 %[o], v[188:191], v[164:167], %[o]\n\t" \
  "s_setprio 0\n\t" \
  "s_nop 15\n\t" \
  ""

typedef short bf16x8 __attribute__((ext_vector_type(8)));
typedef short s16x4 __attribute__((ext_vector_type(4)));
typedef float f32x4 __attribute__((ext_vector_type(4)));
typedef float f32x2 __attribute__((ext_vector_type(2)));
typedef float f32x16 __attribute__((ext_vector_type(16)));
typedef float f32x8 __attribute__((ext_vector_type(8)));
typedef unsigned u32x4 __attribute__((ext_vector_type(4)));
typedef unsigned u32x8 __attribute__((ext_vector_type(8)));
typedef unsigned u32x2 __attribute__((ext_vector_type(2)));
typedef __bf16 bf16x2_t __attribute__((ext_vector_type(2)));
#define LAS __attribute__((address_space(3)))
#define GAS __attribute__((address_space(1)))
typedef const LAS char* lds_cptr;
typedef LAS char* lds_ptr;
typedef GAS unsigned gu32;
typedef GAS unsigned long long gu64;
typedef __amdgpu_buffer_rsrc_t rsrc_t;

constexpr int SEQ = 2048, DM = 512, NH = 32, FF = 1376;
constexpr float EPS = 1e-5f;
constexpr float QSCALE = 0.25f * 1.4426950408889634f;

constexpr unsigned WS_Q = 0, WS_K = 2u << 20, WS_V = 4u << 20, WS_O = 6u << 20, WS_H = 8u << 20, WS_ACT = 10u << 20;
constexpr unsigned WS_KMAX = 16u << 20, WS_HST = (16u << 20) + (64u << 10);
constexpr unsigned WS_WOF = 17u << 20, WS_WDF = WS_WOF + 512 * 1024, WS_WGF = WS_WDF + 32 * 43 * 1024;
constexpr unsigned WS_FLAG = 22u << 20;
constexpr unsigned FL_EPOCH = 0, FL_TMO = 256, FL_1 = 1024, FL_P = 4096, FL_Q = 8192, FL_R = 12288;
constexpr unsigned POISON = 0xFFFFFFFFu;
constexpr unsigned WS_USED = 24u << 20;
constexpr size_t WS_DBG = 64u << 20;

constexpr int NTS = 40;
struct Stamps { unsigned long long t[NTS]; unsigned long long r0; };
#define TS(i) do { __builtin_amdgcn_sched_barrier(0); asm volatile("s_memrealtime %0\n\ts_waitcnt lgkmcnt(0)" : "=s"(st_.t[i]) :: "memory"); __builtin_amdgcn_sched_barrier(0); } while (0)
#define TS_BEGIN() Stamps st_; for (int i_ = 0; i_ < NTS; ++i_) st_.t[i_] = 0; st_.r0 = __builtin_amdgcn_s_memrealtime(); TS(0)
#define TS_END() do { asm volatile("s_waitcnt vmcnt(0)" ::: "memory"); TS(NTS - 1); if ((threadIdx.x >> 6) == 0) { const unsigned long long d_ = (st_.t[AMP_J] - st_.t[AMP_I]) * 10ull; \
   const unsigned long long t0_ = __builtin_amdgcn_s_memrealtime(); while (__builtin_amdgcn_s_memrealtime() - t0_ < d_) __builtin_amdgcn_s_sleep(4); } } while (0)
#define TSARG , Stamps& st_
#define TSPASS , st_

struct Params {
  const char* x; const char* wqkv; const char* wo; const char* wg; const char* wu; const char* wd;
  char* out; char* ws;
};

#define MFMA16(a, b, c) __builtin_amdgcn_mfma_f32_16x16x32_bf16(a, b, c, 0, 0, 0)
#define MFMA32(a, b, c) __builtin_amdgcn_mfma_f32_32x32x16_bf16(a, b, c, 0, 0, 0)
#define PIN(x) asm volatile("" : "+v"(x))
#define SB() __builtin_amdgcn_sched_barrier(0)
#define RLX_AGENT __ATOMIC_RELAXED, __HIP_MEMORY_SCOPE_AGENT

template <int N> __device__ __forceinline__ void wait_bar() {
  asm volatile("s_waitcnt vmcnt(%0) lgkmcnt(0)\n\ts_barrier" ::"n"(N) : "memory");
}
template <int N> __device__ __forceinline__ void wait_vm() { asm volatile("s_waitcnt vmcnt(%0)" ::"n"(N) : "memory"); }
__device__ __forceinline__ void lds_bar() { asm volatile("s_waitcnt lgkmcnt(0)\n\ts_barrier" ::: "memory"); }
__device__ __forceinline__ void raw_bar() { asm volatile("s_barrier" ::: "memory"); }

template <int N, bool SC1> __device__ __forceinline__ void glds(const void* sbase, unsigned voff, unsigned lds_addr) {
#define GL_(O, S) "\n\tglobal_load_lds_dwordx4 %0, %1 offset:" #O S
  if (SC1) {
    if (N == 1) asm volatile("s_mov_b32 m0, %2\n\ts_nop 0" GL_(0, " sc1") :: "v"(voff), "s"(sbase), "s"(lds_addr) : "memory");
    if (N == 2) asm volatile("s_mov_b32 m0, %2\n\ts_nop 0" GL_(0, " sc1") GL_(1024, " sc1") :: "v"(voff), "s"(sbase), "s"(lds_addr) : "memory");
    if (N == 3) asm volatile("s_mov_b32 m0, %2\n\ts_nop 0" GL_(0, " sc1") GL_(1024, " sc1") GL_(2048, " sc1") :: "v"(voff), "s"(sbase), "s"(lds_addr) : "memory");
    if (N == 4) asm volatile("s_mov_b32 m0, %2\n\ts_nop 0" GL_(0, " sc1") GL_(1024, " sc1") GL_(2048, " sc1") GL_(3072, " sc1") :: "v"(voff), "s"(sbase), "s"(lds_addr) : "memory");
  } else {
    if (N == 1) asm volatile("s_mov_b32 m0, %2\n\ts_nop 0" GL_(0, "") :: "v"(voff), "s"(sbase), "s"(lds_addr) : "memory");
    if (N == 2) asm volatile("s_mov_b32 m0, %2\n\ts_nop 0" GL_(0, "") GL_(1024, "") :: "v"(voff), "s"(sbase), "s"(lds_addr) : "memory");
    if (N == 3) asm volatile("s_mov_b32 m0, %2\n\ts_nop 0" GL_(0, "") GL_(1024, "") GL_(2048, "") :: "v"(voff), "s"(sbase), "s"(lds_addr) : "memory");
  }
#undef GL_
}
template <bool SC1> __device__ __forceinline__ void glds16(const void* sbase, unsigned voff, unsigned lds_addr) { glds<1, SC1>(sbase, voff, lds_addr); }
template <bool SC1> __device__ __forceinline__ void glds4(const void* sbase, unsigned voff, unsigned lds_addr) {
  if (SC1) asm volatile("s_mov_b32 m0, %2\n\ts_nop 0\n\tglobal_load_lds_dword %0, %1 sc1" :: "v"(voff), "s"(sbase), "s"(lds_addr) : "memory");
  else     asm volatile("s_mov_b32 m0, %2\n\ts_nop 0\n\tglobal_load_lds_dword %0, %1" :: "v"(voff), "s"(sbase), "s"(lds_addr) : "memory");
}
__device__ __forceinline__ bf16x8 ld128(lds_cptr p) { return *(const LAS bf16x8*)p; }
typedef short v4i16_t __attribute__((ext_vector_type(4)));
__device__ __forceinline__ s16x4 ldtr(lds_cptr p) {
  return __builtin_bit_cast(s16x4, __builtin_amdgcn_ds_read_tr16_b64_v4i16((LAS v4i16_t*)p));
}
__device__ __forceinline__ bf16x8 ldtr2(lds_cptr p, int second) {
  const s16x4 a = ldtr(p), b = ldtr(p + second);
  return (bf16x8){a[0], a[1], a[2], a[3], b[0], b[1], b[2], b[3]};
}
__device__ __forceinline__ unsigned cvtpk(float lo, float hi) {
  f32x2 v = {lo, hi}; bf16x2_t b = __builtin_convertvector(v, bf16x2_t); return __builtin_bit_cast(unsigned, b);
}
__device__ __forceinline__ u32x2 cvt4(f32x4 v) { return (u32x2){cvtpk(v[0], v[1]), cvtpk(v[2], v[3])}; }
__device__ __forceinline__ float bflo(unsigned w) { return __uint_as_float(w << 16); }
__device__ __forceinline__ float bfhi(unsigned w) { return __uint_as_float(w & 0xffff0000u); }
__device__ __forceinline__ f32x4 unpack4(u32x2 w) { return (f32x4){bflo(w[0]), bfhi(w[0]), bflo(w[1]), bfhi(w[1])}; }
__device__ __forceinline__ float bfr(float v) { return bflo(cvtpk(v, 0.f)); }
__device__ __forceinline__ void ln_consts(float sum, float sumsq, float& mean, float& rstd) {
  const float m = sum * (1.f / DM), var = fmaxf(sumsq * (1.f / DM) - m * m, 0.f);
  mean = bfr(m); rstd = __builtin_amdgcn_rcpf(bfr(__builtin_amdgcn_sqrtf(bfr(bfr(var) + EPS))));
}
__device__ __forceinline__ int rfl(int v) { return __builtin_amdgcn_readfirstlane(v); }
__device__ __forceinline__ bf16x8 ones8() { return (bf16x8){0x3F80, 0x3F80, 0x3F80, 0x3F80, 0x3F80, 0x3F80, 0x3F80, 0x3F80}; }
#define Z4 ((f32x4){0.f, 0.f, 0.f, 0.f})
__device__ __forceinline__ void st16(rsrc_t ws, unsigned off, u32x4 v) { __builtin_amdgcn_raw_buffer_store_b128(v, ws, off, 0, 16); }
__device__ __forceinline__ bf16x8 ldw16(rsrc_t ws, unsigned off) { return __builtin_bit_cast(bf16x8, __builtin_amdgcn_raw_buffer_load_b128(ws, off, 0, 16)); }
__device__ __forceinline__ u32x4 pair16(u32x2 X, u32x2 Y) {
  auto a = __builtin_amdgcn_permlane16_swap(X[0], Y[0], false, false);
  auto b = __builtin_amdgcn_permlane16_swap(X[1], Y[1], false, false);
  return (u32x4){a[0], b[0], a[1], b[1]};
}
__device__ __forceinline__ u32x4 pair32(u32x2 X, u32x2 Y) {
  auto a = __builtin_amdgcn_permlane32_swap(X[0], Y[0], false, false);
  auto b = __builtin_amdgcn_permlane32_swap(X[1], Y[1], false, false);
  return (u32x4){a[0], b[0], a[1], b[1]};
}
__device__ __forceinline__ float sum_x16(float v) { auto r = __builtin_amdgcn_permlane16_swap(__float_as_uint(v), __float_as_uint(v), false, false); return __uint_as_float(r[0]) + __uint_as_float(r[1]); }
__device__ __forceinline__ float sum_x32(float v) { auto r = __builtin_amdgcn_permlane32_swap(__float_as_uint(v), __float_as_uint(v), false, false); return __uint_as_float(r[0]) + __uint_as_float(r[1]); }
__device__ __forceinline__ float max_x16(float v) { auto r = __builtin_amdgcn_permlane16_swap(__float_as_uint(v), __float_as_uint(v), false, false); return fmaxf(__uint_as_float(r[0]), __uint_as_float(r[1])); }
__device__ __forceinline__ float lo_x32(float v) { auto r = __builtin_amdgcn_permlane32_swap(__float_as_uint(v), __float_as_uint(v), false, false); return __uint_as_float(r[0]); }
template <int N> __device__ __forceinline__ float ror16(float v) { return __uint_as_float(__builtin_amdgcn_update_dpp(0u, __float_as_uint(v), 0x120 + N, 0xf, 0xf, false)); }
__device__ __forceinline__ float max_row16(float v) { v = fmaxf(v, ror16<8>(v)); v = fmaxf(v, ror16<4>(v)); v = fmaxf(v, ror16<2>(v)); return fmaxf(v, ror16<1>(v)); }
template <int ND, int NM> __device__ __forceinline__ void weave() {
#pragma unroll
  for (int i = 0; i < ND; ++i) { __builtin_amdgcn_sched_group_barrier(0x008, 1, 0); __builtin_amdgcn_sched_group_barrier(0x100, 1, 0); }
  if (NM > ND) __builtin_amdgcn_sched_group_barrier(0x008, NM - ND, 0);
}

__device__ __forceinline__ u32x4 nop4(u32x4 v) { return __builtin_elementwise_min(v, (u32x4){POISON - 1, POISON - 1, POISON - 1, POISON - 1}); }
template <int NP> __device__ __forceinline__ bool pieces_ok(lds_cptr base) {
  const int lane = threadIdx.x & 63;
  unsigned m = 0;
#pragma unroll
  for (int i = 0; i < NP; ++i) { const u32x4 v = *(const LAS u32x4*)(base + i * 1024 + lane * 16); const unsigned a = v[0] > v[1] ? v[0] : v[1], b = v[2] > v[3] ? v[2] : v[3]; m = m > a ? m : a; m = m > b ? m : b; }
  return !__any(m == POISON);
}
template <int NP> struct Chk {
  u32x4 v[NP];
  __device__ __forceinline__ void read(lds_cptr base) {
    const int lane = threadIdx.x & 63;
#pragma unroll
    for (int i = 0; i < NP; ++i) v[i] = *(const LAS u32x4*)(base + i * 1024 + lane * 16);
  }
  __device__ __forceinline__ bool ok() const {
    unsigned m = 0;
#pragma unroll
    for (int i = 0; i < NP; ++i) { const unsigned a = v[i][0] > v[i][1] ? v[i][0] : v[i][1], b = v[i][2] > v[i][3] ? v[i][2] : v[i][3]; m = m > a ? m : a; m = m > b ? m : b; }
    return !__any(m == POISON);
  }
};
struct Spin {
  unsigned n; unsigned long long t0;
  __device__ __forceinline__ Spin() : n(0), t0(__builtin_amdgcn_s_memrealtime()) {}
  __device__ __forceinline__ bool expired(char* ws) {
    if ((++n & 63u) == 0u && __builtin_amdgcn_s_memrealtime() - t0 > 20000000ull) {
      if ((threadIdx.x & 63) == 0) __hip_atomic_store((gu32*)(ws + WS_FLAG + FL_TMO), 1u, RLX_AGENT);
      return true;
    }
    return false;
  }
};

__device__ __forceinline__ void publish(char* ws, unsigned flag_off, unsigned tag) {
  if (threadIdx.x == 0) __hip_atomic_store((gu32*)(ws + WS_FLAG + flag_off), tag, RLX_AGENT);
}
template <int NW>
__device__ __forceinline__ void poll(char* ws, unsigned flag_off, unsigned tag) {
  const int lane = threadIdx.x & 63;
  gu32* f = (gu32*)(ws + WS_FLAG + flag_off);
  const unsigned long long t0 = __builtin_amdgcn_s_memrealtime();
  for (unsigned spins = 1;; ++spins) {
    bool ok = true;
    if (NW == 256) {
#pragma unroll
      for (int i = 0; i < 4; ++i) ok &= __hip_atomic_load(f + i * 64 + lane, RLX_AGENT) == tag;
    } else ok = __hip_atomic_load(f + (lane < NW ? lane : 0), RLX_AGENT) == tag;
    if (__all(ok)) break;
    if ((spins & 63u) == 0u && __builtin_amdgcn_s_memrealtime() - t0 > 20000000ull) {
      if (lane == 0) __hip_atomic_store((gu32*)(ws + WS_FLAG + FL_TMO), tag, RLX_AGENT);
      break;
    }
    __builtin_amdgcn_s_sleep(1);
  }
}
template <int NW> __device__ __forceinline__ void await(char* ws, unsigned flag_off, unsigned tag) {
  if (rfl(threadIdx.x >> 6) == 0) poll<NW>(ws, flag_off, tag);
  raw_bar();
}

struct F1 { bf16x8 a[4], w[3]; };
__device__ __forceinline__ void p1_qkv(const Params& p, rsrc_t ws, int b, int rb1, int cb1, char* smem TSARG) {
  const int tid = threadIdx.x, lane = tid & 63, wid = rfl(tid >> 6);
  const int r16 = lane & 15, g = lane >> 4;
  const unsigned lds0 = (unsigned)(size_t)(lds_ptr)smem;
  lds_cptr L = (lds_cptr)smem;
  constexpr int STG = 32768, ND = 4, NS = 8, RL = ND * STG, MS = RL + 5 * 4096;
  LAS f32x2* ms = (LAS f32x2*)(smem + MS);
  const int rb = rb1, cb = cb1;
  if (wid >= 4) {
    const int lw = wid - 4;
    const unsigned voffA = (unsigned)((16 * lw + (lane >> 3)) * DM + (((lane & 7) ^ (lane >> 3)) * 8)) * 2u;
    unsigned voffB[2];
#pragma unroll
    for (int i = 0; i < 2; ++i) { const int r = lane >> 3, c = lane & 7, ff = ((r >> 1) & 1) | (i << 1), j = (c >> 1) ^ ff;
      voffB[i] = (unsigned)((16 * lw + 8 * i + r) * (3 * DM) + j * 16 + (c & 1) * 8) * 2u; }
    const char* xa = p.x + (size_t)rb * 64 * DM * 2;
    const char* wb = p.wqkv + (size_t)cb * 64 * 2;
    const int rot = ((b >> 3) & 7) + 2 * (b >> 6);
    auto issue = [&](int s) {
      const unsigned d = lds0 + (s % ND) * STG;
      const int kc = (s + rot) & 7;
#pragma unroll
      for (int i = 0; i < 2; ++i) glds16<false>(xa + kc * 128 + i * 8 * DM * 2, voffA, d + (2 * lw + i) * 1024);
#pragma unroll
      for (int i = 0; i < 2; ++i) glds<3, false>(wb + (size_t)kc * 64 * (3 * DM) * 2, voffB[i], d + 8192 + (2 * lw + i) * 3072);
    };
    auto rl_decode = [&](int J, const char*& src, int& pitch, unsigned& dst, int& dstride, int& nt) {
      if (J < 128) { const int ks = J >> 3, cg = J & 7; src = p.wo + ((size_t)ks * 32 * DM + cg * 64) * 2; pitch = DM; dst = WS_WOF + (unsigned)(cg * 4 * 16 + ks) * 1024; dstride = 16 * 1024; nt = 4; }
      else if (J < 472) { const int ks = (J - 128) >> 3, cg = (J - 128) & 7; src = p.wd + ((size_t)ks * 32 * DM + cg * 64) * 2; pitch = DM; dst = WS_WDF + (unsigned)(cg * 4 * 43 + ks) * 1024; dstride = 43 * 1024; nt = 4; }
      else { const int r = J - 472, mat = r >= 352, r2 = r - mat * 352, ks = r2 / 22, cg = r2 - ks * 22;
        src = (mat ? p.wu : p.wg) + ((size_t)ks * 32 * FF + cg * 64) * 2; pitch = FF; dst = WS_WGF + (unsigned)((mat * 86 + cg * 4) * 16 + ks) * 1024; dstride = 16 * 1024; nt = cg == 21 ? 2 : 4; }
    };
    auto rl_load = [&](int slot) {
      const int J = b * 5 + slot < 1176 ? b * 5 + slot : 0;
      const char* src; int pitch, dstride, nt; unsigned dst; rl_decode(J, src, pitch, dst, dstride, nt);
      const int r = lane >> 3, c = lane & 7;
#pragma unroll
      for (int kg = 0; kg < 4; ++kg) {
        int j = (c >> 1) ^ (((r >> 1) & 1) | ((kg & 1) << 1)); j = j < nt ? j : j - 2;
        glds16<false>(src, (unsigned)((kg * 8 + r) * pitch + j * 16 + (c & 1) * 8) * 2u, lds0 + RL + slot * 4096 + kg * 1024);
      }
    };
    auto rl_store = [&](int slot) {
      if (b * 5 + slot < 1176) {
        const char* src; int pitch, dstride, nt; unsigned dst; rl_decode(b * 5 + slot, src, pitch, dst, dstride, nt);
        const int q = (lane >> 2) & 3, pp = lane & 3, ff = ((q >> 1) & 1) | ((g & 1) << 1);
#pragma unroll
        for (int j = 0; j < 4; ++j)
          if (j < nt) st16(ws, dst + j * dstride + lane * 16, __builtin_bit_cast(u32x4, ldtr2(L + RL + slot * 4096 + g * 1024 + q * 128 + ((j ^ ff) << 5) + pp * 8, 512)));
      }
    };
    TS(20);
    issue(0); issue(1);
    TS(21);
    wait_vm<8>(); raw_bar();
    TS(23);
    issue(2);
    const int slot2 = lw == 0 ? 4 : lw;
#pragma unroll
    for (int j = 1; j < NS; ++j) {
      if (j + 2 < NS) issue(j + 2);
      if (j == 2) { rl_load(lw); rl_load(slot2); }
      if (j == 1 || j == 5) wait_bar<16>(); else if (j <= 4) wait_bar<24>(); else if (j == 6) wait_bar<8>(); else wait_bar<0>();
      if (j == 4) { wait_vm<16>(); rl_store(lw); if (lw == 0) rl_store(4); }
    }
    lds_bar();
  } else {
    f32x4 acc[3][4], cs[3], sx = Z4, sq = Z4;
    int aoff[2][4];
#pragma unroll
    for (int ks = 0; ks < 2; ++ks)
#pragma unroll
      for (int i = 0; i < 4; ++i) aoff[ks][i] = ((wid + i) & 3) * 2048 + (r16 >> 3) * 1024 + (r16 & 7) * 128 + (((4 * ks + g) ^ (r16 & 7)) << 4);
    int boff;
    { const int q = (lane >> 2) & 3, pp = lane & 3, ff = ((q >> 1) & 1) | ((g & 1) << 1);
      boff = 8192 + g * 3072 + q * 128 + ((wid ^ ff) << 5) + pp * 8; }
#pragma unroll
    for (int i = 0; i < 3; ++i) { cs[i] = Z4;
#pragma unroll
      for (int j = 0; j < 4; ++j) acc[i][j] = Z4; }
    const bf16x8 one = ones8();
    F1 fa, fb;
    auto rd = [&](F1& fr, int s, int ks) {
      lds_cptr B = L + (s % ND) * STG;
#pragma unroll
      for (int seg = 0; seg < 3; ++seg) fr.w[seg] = ldtr2(B + boff + ks * 12288 + seg * 1024, 512);
#pragma unroll
      for (int i = 0; i < 4; ++i) fr.a[i] = ld128(B + aoff[ks][i]);
    };
    auto mm = [&](const F1& fr) {
#pragma unroll
      for (int i = 0; i < 4; ++i) {
        acc[0][i] = MFMA16(fr.w[0], fr.a[i], acc[0][i]);
        acc[1][i] = MFMA16(fr.w[1], fr.a[i], acc[1][i]);
        acc[2][i] = MFMA16(fr.a[i], fr.w[2], acc[2][i]);
      }
      cs[0] = MFMA16(fr.w[0], one, cs[0]); cs[1] = MFMA16(fr.w[1], one, cs[1]); cs[2] = MFMA16(one, fr.w[2], cs[2]);
      sx = MFMA16(one, fr.a[0], sx); sq = MFMA16(fr.a[0], fr.a[0], sq);
    };
    lds_bar();
    TS(1);
    rd(fa, 0, 0); SB();
#pragma unroll
    for (int s = 0; s < NS; ++s) {
      rd(fb, s, 1); mm(fa); weave<10, 17>(); SB();
      if (s + 1 < NS) { lds_bar(); rd(fa, s + 1, 0); mm(fb); weave<10, 17>(); SB(); }
      else { mm(fb); SB(); }
    }
    TS(2);
    if ((r16 >> 2) == g) {
      const int e = r16 & 3; const float s2 = e == 0 ? sq[0] : e == 1 ? sq[1] : e == 2 ? sq[2] : sq[3];
      float mean, rstd; ln_consts(sx[0], s2, mean, rstd);
      ms[wid * 16 + r16] = (f32x2){mean, rstd};
    }
    lds_bar();
    TS(25);
    const int h = cb * 4 + wid;
    u32x2 qv[4], kv[4], vv[4];
    float kmx = 0.f;
#pragma unroll
    for (int i = 0; i < 4; ++i) {
      const int rt = (wid + i) & 3;
      const f32x2 st = ms[rt * 16 + r16];
      qv[i] = cvt4((acc[0][i] - st.x * cs[0]) * (st.y * QSCALE));
      { const f32x4 v = (acc[1][i] - st.x * cs[1]) * st.y; kv[i] = cvt4(v);
        kmx = fmaxf(kmx, sum_x32(sum_x16((v[0] * v[0] + v[1] * v[1]) + (v[2] * v[2] + v[3] * v[3])))); }
      { const f32x4 m01 = *(LAS f32x4*)(ms + rt * 16 + 4 * g), m23 = *(LAS f32x4*)(ms + rt * 16 + 4 * g + 2); const float cv = cs[2][0];
        vv[i] = (u32x2){cvtpk((acc[2][i][0] - m01[0] * cv) * m01[1], (acc[2][i][1] - m01[2] * cv) * m01[3]),
                        cvtpk((acc[2][i][2] - m23[0] * cv) * m23[1], (acc[2][i][3] - m23[2] * cv) * m23[3])}; }
    }
#pragma unroll
    for (int i = 0; i < 4; i += 2) {
      { const int rt = (wid + i + (g & 1)) & 3, grow = rb * 64 + rt * 16;
        const unsigned off = (((unsigned)h * 64 + (grow >> 5)) * 2 + (g >> 1)) * 512 + ((grow & 31) + r16) * 16;
        st16(ws, WS_K + off, pair16(kv[i], kv[i + 1])); st16(ws, WS_Q + off, pair16(qv[i], qv[i + 1])); }
      { const int rt = (wid + i + (g >> 1)) & 3, grow = rb * 64 + rt * 16;
        const unsigned off = ((((unsigned)h * 64 + (grow >> 5)) * 2 + (rt & 1)) * 2 + (g & 1)) * 256 + r16 * 16;
        st16(ws, WS_V + off, pair32(vv[i], vv[i + 1])); }
    }
    kmx = max_row16(kmx);
    if (lane == 0) __hip_atomic_store((gu32*)(p.ws + WS_KMAX) + h * 32 + rb, __float_as_uint(kmx), RLX_AGENT);
    TS(26);
  }
}

constexpr int A_K = 0, A_V = 65536, A_ONE = 131072, A_Q = A_ONE + 5120, A_KM = A_Q + 8192, A_DUMMY = A_KM + 256, A_BYTES = A_DUMMY + 256;
constexpr float SAFE_BOUND = 48.f;
constexpr int PRIO_SWITCH = 13;

struct P3Pre { bf16x8 wof[16]; u32x2 xv[4]; };
__device__ __forceinline__ void p2_attn(const Params& p, rsrc_t ws, int h, int qr, int rb, int cb, int mb, int fb, char* smem, unsigned tag, P3Pre& pre TSARG) {
  const int tid = threadIdx.x, lane = tid & 63, wid = rfl(tid >> 6);
  const int r32 = lane & 31, hh = lane >> 5;
  const unsigned lds0 = (unsigned)(size_t)(lds_ptr)smem;
  lds_cptr L = (lds_cptr)smem;
  const char* kg = p.ws + WS_K + (size_t)h * 65536;
  const char* vg = p.ws + WS_V + (size_t)h * 65536;
  const unsigned vo = lane * 16;
  const int u0 = fb < 11 ? 3 * fb : 33 + 2 * (fb - 11), nu = fb < 11 ? 3 : 2;
  glds16<true>(p.ws + WS_Q + ((size_t)h * 64 + qr * 8 + wid) * 1024, vo, lds0 + A_Q + wid * 1024);
  glds4<true>(p.ws + WS_KMAX + h * 128, (lane & 31) * 4, lds0 + A_KM);
#pragma unroll
  for (int i = 0; i < 2; ++i) {
    const int t = wid + 8 * i, gt = (t + 8 * qr) & 63;
    glds16<true>(kg + gt * 1024, vo, lds0 + A_K + t * 1024);
    glds16<true>(vg + gt * 1024, vo, lds0 + A_V + t * 1024);
  }
  st16(ws, WS_O + (unsigned)((qr * 16 + 2 * wid + (lane >> 5)) * 16 + (h >> 1)) * 1024 + (h & 1) * 512 + (lane & 31) * 16, (u32x4){POISON, POISON, POISON, POISON});
  { const unsigned o1 = (tid & 31) < 2 ? 0x3F803F80u : 0u;
    *(LAS u32x2*)(smem + A_ONE + tid * 8) = (u32x2){o1, o1};
    if (tid < 128) *(LAS u32x2*)(smem + A_ONE + 4096 + tid * 8) = (u32x2){o1, o1}; }
  if (wid == 0) {
    poll<256>(p.ws, FL_1, tag);
    if (blockIdx.x == 0 && lane == 0) __hip_atomic_store((gu32*)(p.ws + WS_FLAG + FL_EPOCH), tag, RLX_AGENT);
  }
  wait_bar<1>();
  TS(4);
  const bf16x8 qf = ld128(L + A_Q + wid * 1024 + lane * 16);
  float c;
  { float km = *(const LAS float*)(L + A_KM + lane * 4);
    km = max_x16(max_row16(km));
    float q2 = 0.f;
#pragma unroll
    for (int j = 0; j < 8; ++j) { const float v = __uint_as_float((unsigned)(unsigned short)qf[j] << 16); q2 += v * v; }
    q2 = sum_x32(q2);
    c = sqrtf(q2 * km) * 1.02f + 0.01f; }
  const bool fast = __all(c <= SAFE_BOUND);
  const bool vlane = r32 < 16;
  int vaddr = (int)lds0 + (vlane ? A_V : A_ONE) + hh * 256 + (r32 & 15) * 16;
  const int vstep = vlane ? 4096 : 0;
  int kaddr = (int)lds0 + A_K + lane * 16;
  f32x16 o;
#pragma unroll
  for (int i = 0; i < 16; ++i) o[i] = 0.f;
  unsigned go = vo + ((16 + wid + 8 * qr) & 63) * 1024;
  unsigned ld = lds0 + A_K + (16 + wid) * 1024;
  auto between = [&]() {
    publish(p.ws, FL_P + blockIdx.x * 4, tag);
#pragma unroll
    for (int i = 0; i < 2; ++i) {
      const int ln = ((blockIdx.x >> 3) & 1) * 768 + wid * 96 + i * 32 + lane, ch = ln >> 7, jt = ch >= 6 ? ch - 6 : ch;
      glds4<false>(p.ws + WS_WGF, (unsigned)(((ch >= 6) * 86 + u0 * 2 + (jt < 2 * nu ? jt : jt - 2)) * 128 + (ln & 127)) * 128u, lds0 + A_DUMMY);
    }
  };
  if (fast) {
    f32x16 negc, sa, sb; f32x8 pp, kk; f32x16 vv;
#pragma unroll
    for (int i = 0; i < 16; ++i) negc[i] = -c;
    int cnt, keep;
    asm volatile(ATTN_ASM_A
                 : [o] "+v"(o), [ka] "+v"(kaddr), [va] "+v"(vaddr), [go] "+v"(go), [ld] "+s"(ld), [keep] "=&s"(keep),
                   "=&{v[128:143]}"(sa), "=&{v[144:159]}"(sb), "=&{v[160:167]}"(pp), "=&{v[168:175]}"(kk), "=&{v[176:191]}"(vv)
                 : [q] "v"(qf), [nc] "v"(negc), [vs] "v"(vstep), [kg] "s"(kg), [vg] "s"(vg)
                 : "memory", "scc");
    between();
    const int sw = wid < 4 ? PRIO_SWITCH : 99;
    asm volatile(ATTN_ASM_B
                 : [o] "+v"(o), [ka] "+v"(kaddr), [va] "+v"(vaddr), [cnt] "=&s"(cnt),
                   "+{v[128:143]}"(sa), "+{v[144:159]}"(sb), "+{v[160:167]}"(pp), "+{v[168:175]}"(kk), "+{v[176:191]}"(vv)
                 : [q] "v"(qf), [nc] "v"(negc), [vs] "v"(vstep), [sw] "s"(sw)
                 : "memory", "scc");
  } else {
#pragma unroll
    for (int j = 0; j < 6; ++j) { const unsigned gj = (go + j * 8192) & 0xffffu; glds16<true>(kg, gj, ld + j * 8192); glds16<true>(vg, gj, ld + 65536 + j * 8192); }
    float m = -INFINITY;
    vaddr -= (int)lds0; kaddr -= (int)lds0;
#pragma unroll 1
    for (int t4 = 0; t4 < 16; ++t4) {
      if (t4 == 2) { wait_bar<0>(); between(); }
#pragma unroll 1
      for (int u = 0; u < 4; ++u) {
        const int t = t4 * 4 + u;
        f32x16 z;
#pragma unroll
        for (int i = 0; i < 16; ++i) z[i] = 0.f;
        const f32x16 s = MFMA32(ld128(L + kaddr + t * 1024), qf, z);
        float mx = s[0];
#pragma unroll
        for (int i = 1; i < 16; ++i) mx = fmaxf(mx, s[i]);
        mx = fmaxf(mx, __shfl_xor(mx, 32));
        const float mn = fmaxf(m, mx);
        const float al = __builtin_amdgcn_exp2f(m - mn);
        m = mn;
#pragma unroll
        for (int i = 0; i < 16; ++i) o[i] *= al;
        u32x4 p0, p1;
#pragma unroll
        for (int i = 0; i < 4; ++i) {
          p0[i] = cvtpk(__builtin_amdgcn_exp2f(s[2 * i] - mn), __builtin_amdgcn_exp2f(s[2 * i + 1] - mn));
          p1[i] = cvtpk(__builtin_amdgcn_exp2f(s[8 + 2 * i] - mn), __builtin_amdgcn_exp2f(s[8 + 2 * i + 1] - mn));
        }
        const bf16x8 v0 = ld128(L + vaddr + u * 1024), v1 = ld128(L + vaddr + u * 1024 + 512);
        o = MFMA32(v0, __builtin_bit_cast(bf16x8, p0), o);
        o = MFMA32(v1, __builtin_bit_cast(bf16x8, p1), o);
      }
      vaddr += vstep;
    }
  }
  TS(5);
  if (wid >= 4) {
    const int r16 = lane & 15, g = lane >> 4, wn = wid & 3;
#pragma unroll
    for (int i = 0; i < 16; ++i) pre.wof[i] = ldw16(ws, WS_WOF + ((unsigned)(cb * 4 + wn) * 16 + ((i + 4 * cb) & 15)) * 1024 + lane * 16);
#pragma unroll
    for (int i = 0; i < 4; ++i) pre.xv[i] = *(const u32x2*)(p.x + ((size_t)(rb * 64 + i * 16 + r16) * DM + cb * 64 + wn * 16 + 4 * g) * 2);
  }
  const float inv = __builtin_amdgcn_rcpf(lo_x32(o[8]));
  const u32x4 ov = pair32((u32x2){cvtpk(o[0] * inv, o[1] * inv), cvtpk(o[2] * inv, o[3] * inv)}, (u32x2){cvtpk(o[4] * inv, o[5] * inv), cvtpk(o[6] * inv, o[7] * inv)});
  const int row = qr * 256 + wid * 32 + r32;
  st16(ws, WS_O + ((((unsigned)(row >> 4) * 16 + (h >> 1)) * 4 + (h & 1) * 2 + hh) * 16 + (row & 15)) * 16, nop4(ov));
}

constexpr int G64_STG = 16384;
template <int NKS, int ND, int LA>
__device__ __forceinline__ void gemm64_load(char* ws, const char* ag  , int rot, char* smem) {
  const int lane = threadIdx.x & 63, wid = rfl(threadIdx.x >> 6);
  const unsigned lds0 = (unsigned)(size_t)(lds_ptr)smem;
  lds_cptr L = (lds_cptr)smem + wid * 4096;
  constexpr int NS = (NKS + 3) / 4, LAST = NKS - 4 * (NS - 1), NFULL = NKS / 4;
  auto issue = [&](int s) {
    const unsigned d = lds0 + (s % ND) * G64_STG + wid * 4096;
    if (s < NS - 1 || LAST == 4) { const int q = s + rot; glds<4, true>(ag + (q >= NFULL ? q - NFULL : q) * 4096, lane * 16, d); } else glds<LAST == 4 ? 4 : 3, true>(ag + s * 4096, lane * 16, d);
  };
  auto okay = [&](int s) { return (s < NS - 1 || LAST == 4) ? pieces_ok<4>(L + (s % ND) * G64_STG) : pieces_ok<3>(L + (s % ND) * G64_STG); };
  auto ensure = [&](int s) { if (!okay(s)) { Spin sp; do { issue(s); wait_vm<0>(); } while (!okay(s) && !sp.expired(ws)); } };
  auto waitv = [&](int j) {
    const int hi = j + LA - 2 < NS - 1 ? j + LA - 2 : NS - 1;
    const int n = (hi - j) * 4 - ((hi == NS - 1 && hi > j) ? 4 - LAST : 0);
    switch (n) { case 0: wait_vm<0>(); break; case 3: wait_vm<3>(); break; case 4: wait_vm<4>(); break; case 7: wait_vm<7>(); break; case 8: wait_vm<8>(); break; case 11: wait_vm<11>(); break;
                 case 12: wait_vm<12>(); break; case 15: wait_vm<15>(); break; case 16: wait_vm<16>(); break; default: wait_vm<0>(); break; }
  };
  static_assert(LAST == 4 || LAST == 3, "last stage");
  issue(0); wait_vm<0>(); ensure(0);
#pragma unroll
  for (int s = 1; s < LA; ++s) if (s < NS) issue(s);
  lds_bar();
#pragma unroll
  for (int j = 1; j < NS; ++j) {
    waitv(j);
    if (j < NS - 1 || LAST == 4) {
      Chk<4> c; c.read(L + (j % ND) * G64_STG);
      if (j + LA - 1 < NS) issue(j + LA - 1);
      if (!c.ok()) ensure(j);
    } else ensure(j);
    lds_bar();
  }
}
template <int NKS, int ND, int TS0>
__device__ __forceinline__ void gemm64_mma(const bf16x8 (&wf)[NKS], char* smem, f32x4 (&acc)[4] TSARG) {
  const int lane = threadIdx.x & 63;
  lds_cptr L = (lds_cptr)smem + lane * 16;
  constexpr int NS = (NKS + 3) / 4;
#pragma unroll
  for (int i = 0; i < 4; ++i) acc[i] = Z4;
  bf16x8 f[2][4];
  auto rd = [&](int ks) {
    lds_cptr B = L + ((ks >> 2) % ND) * G64_STG + (ks & 3) * 1024;
#pragma unroll
    for (int rt = 0; rt < 4; ++rt) f[ks & 1][rt] = ld128(B + rt * 4096);
  };
  lds_bar();
  TS(TS0);
  rd(0); SB();
#pragma unroll
  for (int ks = 0; ks < NKS; ++ks) {
    if (ks + 1 < NKS) {
      if (((ks + 1) & 3) == 0) lds_bar();
      rd(ks + 1);
    }
#pragma unroll
    for (int rt = 0; rt < 4; ++rt) acc[rt] = MFMA16(wf[ks], f[ks & 1][rt], acc[rt]);
    if (ks + 1 < NKS) weave<4, 4>();
    SB();
  }
  TS(TS0 + 1);
}

constexpr int G_B = 0, G_BS = 24576, G_A = 3 * G_BS, G_AS = 16384, G_ST = G_A + 4 * G_AS, G_MS = G_ST + 8192, G_CS = G_MS + 1024;
struct WLoad {
  const char* src[3]; unsigned dst; int rot;
  __device__ __forceinline__ void init(const Params& p, int mb, int fb, int lw, unsigned lds0) {
    const int u0 = fb < 11 ? 3 * fb : 33 + 2 * (fb - 11), nu = fb < 11 ? 3 : 2;
    rot = (fb + 4 * (mb & 1)) & 7;
#pragma unroll
    for (int i = 0; i < 3; ++i) { const int jt = 3 * (lw & 1) + i; src[i] = p.ws + WS_WGF + (size_t)((lw >> 1) * 86 + u0 * 2 + (jt < 2 * nu ? jt : jt - 2)) * 16384; }
    dst = lds0 + G_B + lw * 6144;
  }
  __device__ __forceinline__ void issue(int s, int lane) const {
#pragma unroll
    for (int i = 0; i < 3; ++i) glds<2, true>(src[i] + ((s + rot) & 7) * 2048, lane * 16, dst + (s % 3) * G_BS + i * 2048);
  }
};
struct F4 { bf16x8 a[4], w[2][3]; };
__device__ __forceinline__ void p4_gateup(const Params& p, rsrc_t ws, int mb, int fb, char* smem, const WLoad& wl, int wct, int rot5, bf16x8 (&wdf)[43] TSARG) {
  const int tid = threadIdx.x, lane = tid & 63, wid = rfl(tid >> 6);
  const int r16 = lane & 15, g = lane >> 4;
  const unsigned lds0 = (unsigned)(size_t)(lds_ptr)smem;
  lds_cptr L = (lds_cptr)smem;
  constexpr int NS = 8;
  const int u0 = fb < 11 ? 3 * fb : 33 + 2 * (fb - 11), nu = fb < 11 ? 3 : 2;
  if (wid >= 4) {
    const int lw = wid - 4;
    const char* ag = p.ws + WS_H + (size_t)(mb * 8 + 2 * lw) * 16384;
    auto issueA = [&](int s) {
      const unsigned d = lds0 + G_A + (s & 3) * G_AS + lw * 4096;
#pragma unroll
      for (int i = 0; i < 2; ++i) glds<2, true>(ag + i * 16384 + ((s + wl.rot) & 7) * 2048, lane * 16, d + i * 2048);
    };
    auto issueS = [&]() {
      glds<2, true>(p.ws + WS_HST + (size_t)mb * 8192 + lw * 2048, lane * 16, lds0 + G_ST + lw * 2048);
    };
    auto ensure = [&](int s) {
      lds_cptr A = L + G_A + (s & 3) * G_AS + lw * 4096;
      if (!pieces_ok<4>(A)) { Spin sp; do { issueA(s); wait_vm<0>(); } while (!pieces_ok<4>(A) && !sp.expired(p.ws)); }
    };
    float cs[3] = {0.f, 0.f, 0.f};
    const unsigned one_bits = 0x3F803F80u;
    const bf16x2_t one2 = __builtin_bit_cast(bf16x2_t, one_bits);
    auto colsum = [&](int s) {
#pragma unroll
      for (int i = 0; i < 6; ++i) {
        const u32x4 v = *(const LAS u32x4*)(L + G_B + (s % 3) * G_BS + lw * 6144 + i * 1024 + lane * 16);
#pragma unroll
        for (int e = 0; e < 4; ++e) { const unsigned t = v[e]; cs[i >> 1] = __builtin_amdgcn_fdot2_f32_bf16(__builtin_bit_cast(bf16x2_t, t), one2, cs[i >> 1], false); }
      }
    };
    issueA(0);
    colsum(0); colsum(1);
    wait_vm<0>(); ensure(0);
    issueA(1); issueS(); issueA(2); wl.issue(2, lane);
    lds_bar();
#pragma unroll
    for (int j = 1; j < NS; ++j) {
      if (j == 1) wait_vm<12>(); else if (j + 1 < NS) wait_vm<10>(); else wait_vm<0>();
      Chk<4> c; c.read(L + G_A + (j & 3) * G_AS + lw * 4096);
      if (j + 2 < NS) issueA(j + 2);
      if (!c.ok()) ensure(j);
      if (j == 2) {
        lds_cptr Sg = L + G_ST + lw * 2048;
        if (!pieces_ok<2>(Sg)) { Spin sp; do { issueS(); wait_vm<0>(); } while (!pieces_ok<2>(Sg) && !sp.expired(p.ws)); }
      }
      if (j >= 2) colsum(j);
      lds_bar();
      if (j + 2 < NS) wl.issue(j + 2, lane);
      if (j == 2) {
        const int row = tid - 256;
        if (row < 128) {
          float a = 0.f, bb = 0.f;
#pragma unroll
          for (int c = 0; c < 8; ++c) { const f32x2 v = *(const LAS f32x2*)(L + G_ST + (row >> 6) * 4096 + c * 512 + (row & 63) * 8); a += v.x; bb += v.y; }
          float mean, rstd; ln_consts(a, bb, mean, rstd);
          *(LAS f32x2*)(smem + G_MS + row * 8) = (f32x2){rstd, -mean * rstd};
        }
      }
    }
#pragma unroll
    for (int i = 0; i < 3; ++i) { const float t = sum_x32(sum_x16(cs[i])); if (g == 0) *(LAS float*)(smem + G_CS + ((lw * 3 + i) * 16 + r16) * 4) = t; }
    lds_bar();
  } else {
    const int wm = wid >> 1, wn = wid & 1;
    const int aoff = G_A + wm * 8192 + lane * 16;
    constexpr int m0 = 0, m1 = 12288;
    const int boff = G_B + wn * 6144 + lane * 16;
    f32x4 acc[2][3][4];
#pragma unroll
    for (int i = 0; i < 3; ++i)
#pragma unroll
      for (int j = 0; j < 4; ++j) { acc[0][i][j] = Z4; acc[1][i][j] = Z4; }
    F4 fa, fb2;
    auto rd = [&](F4& f, int s, int ks) {
      lds_cptr B = L + (s % 3) * G_BS, A = L + (s & 3) * G_AS;
#pragma unroll
      for (int i = 0; i < 4; ++i) f.a[i] = ld128(A + aoff + i * 2048 + ks * 1024);
#pragma unroll
      for (int i = 0; i < 3; ++i) { f.w[0][i] = ld128(B + boff + m0 + i * 2048 + ks * 1024); f.w[1][i] = ld128(B + boff + m1 + i * 2048 + ks * 1024); }
    };
    auto mm = [&](const F4& f) {
#pragma unroll
      for (int m = 0; m < 2; ++m)
#pragma unroll
        for (int i = 0; i < 3; ++i)
#pragma unroll
          for (int j = 0; j < 4; ++j) acc[m][i][j] = MFMA16(f.w[m][i], f.a[j], acc[m][i][j]);
    };
    lds_bar();
    TS(11);
    rd(fa, 0, 0); SB();
#pragma unroll
    for (int s = 0; s < NS; ++s) {
      rd(fb2, s, 1); mm(fa); weave<10, 24>(); SB();
      if (s + 1 < NS) { lds_bar(); rd(fa, s + 1, 0); mm(fb2); weave<10, 24>(); SB(); }
      else { mm(fb2); SB(); }
    }
    TS(12);
    const LAS float* csl = (const LAS float*)(smem + G_CS);
    lds_bar();
    TS(33);
    const LAS f32x2* ms = (const LAS f32x2*)(smem + G_MS);
    f32x2 st[4];
#pragma unroll
    for (int j = 0; j < 4; ++j) st[j] = ms[wm * 64 + j * 16 + r16];
    constexpr float K1 = -2.885390081777927f * 0.7978845608028654f, K2 = -2.885390081777927f * 0.035677408136300125f;
#pragma unroll
    for (int i = 0; i < 3; ++i) {
      const int jt = 3 * wn + i, u = jt >> 1, jj = jt & 1;
      const f32x4 cg = *(const LAS f32x4*)(csl + jt * 16 + 4 * g), cu = *(const LAS f32x4*)(csl + (6 + jt) * 16 + 4 * g);
      u32x2 av[4];
#pragma unroll
      for (int j = 0; j < 4; ++j) {
        const f32x4 ag2 = acc[0][i][j], au = acc[1][i][j];
        const f32x4 ga = ag2 * st[j].x + st[j].y * cg, up = au * st[j].x + st[j].y * cu;
        const f32x4 z = ga * (ga * ga * K2 + K1);
        const f32x4 d = (f32x4){__builtin_amdgcn_exp2f(z[0]), __builtin_amdgcn_exp2f(z[1]), __builtin_amdgcn_exp2f(z[2]), __builtin_amdgcn_exp2f(z[3])} + 1.f;
        const f32x4 rc = (f32x4){__builtin_amdgcn_rcpf(d[0]), __builtin_amdgcn_rcpf(d[1]), __builtin_amdgcn_rcpf(d[2]), __builtin_amdgcn_rcpf(d[3])};
        av[j] = cvt4((ga * rc) * up);
      }
      if (u < nu) {
#pragma unroll
        for (int j = 0; j < 4; j += 2) {
          const unsigned rt = (unsigned)mb * 8 + wm * 4 + j + (g & 1);
          st16(ws, WS_ACT + (((rt * 43 + (u0 + u)) * 4 + jj * 2 + (g >> 1)) * 16 + r16) * 16, nop4(pair16(av[j], av[j + 1])));
        }
      }
    }
    TS(34);
  }
  if (wid >= 4) {
#pragma unroll
    for (int i = 0; i < 43; ++i) { int ks = i; if (i < 40) { ks += rot5; ks = ks >= 40 ? ks - 40 : ks; } wdf[i] = ldw16(ws, WS_WDF + ((unsigned)wct * 43 + ks) * 1024 + lane * 16); }
  }
}

extern __shared__ __attribute__((aligned(16))) char smem_dyn[];

__global__ __launch_bounds__(512, 2) void mega(Params p) {
  char* smem = smem_dyn;
  const int b = blockIdx.x, x = b & 7, i5 = b >> 3;
  const int tid = threadIdx.x, lane = tid & 63, wid = rfl(tid >> 6);
  const int r16 = lane & 15, g = lane >> 4, wn = wid & 3;
  TS_BEGIN();
  const rsrc_t ws = __builtin_amdgcn_make_buffer_rsrc(p.ws, 0, WS_USED, 0x00020000);
  const unsigned epoch = __hip_atomic_load((gu32*)(p.ws + WS_FLAG + FL_EPOCH), RLX_AGENT);

  const int rb1 = (x >> 1) * 8 + (i5 & 7), cb1 = (x & 1) * 4 + (i5 >> 3);
  p1_qkv(p, ws, b, rb1, cb1, smem TSPASS);
  const unsigned tag = (unsigned)rfl((int)(epoch * 0x9E3779B1u + 0x7F4A7C15u));
  wait_bar<0>();
  publish(p.ws, FL_1 + (cb1 * 32 + rb1) * 4, tag);
  TS(3);

  const int h = x * 4 + (i5 & 3), qr = i5 >> 2;
  const int rb = x * 4 + (i5 & 3), cb = i5 >> 2;
  const int mb = x * 2 + (i5 & 1), fb = i5 >> 1;
  await<32>(p.ws, FL_1 + (h >> 2) * 128, tag);
  TS(27);
  P3Pre pre;
  p2_attn(p, ws, h, qr, rb, cb, mb, fb, smem, tag, pre TSPASS);
  if (wid == 0) poll<256>(p.ws, FL_P, tag);
  if (tid == 64) *(LAS unsigned*)(smem + G_CS + 1024) = 0u;
  lds_bar();
  TS(6);

  const int wct = cb * 4 + wn, col = cb * 64 + wn * 16 + 4 * g;
  u32x2 hv[4];
  if (wid >= 4) {
    { const u32x4 pz = (u32x4){POISON, POISON, POISON, POISON};
#pragma unroll
      for (int i = 0; i < 3; ++i) {
        const int j = wn * 3 + i;
        st16(ws, j < 8 ? WS_H + (unsigned)((rb * 4 + (j >> 1)) * 16 + cb * 2 + (j & 1)) * 1024 + lane * 16 : WS_HST + (unsigned)(rb * 8 + cb) * 512 + (lane & 31) * 16, pz);
      }
      wait_vm<0>();
      if (lane == 0 && __hip_atomic_fetch_add((LAS unsigned*)(smem + G_CS + 1024), 1u, __ATOMIC_RELAXED, __HIP_MEMORY_SCOPE_WORKGROUP) == 3u)
        __hip_atomic_store((gu32*)(p.ws + WS_FLAG + FL_Q) + b, tag, RLX_AGENT); }
#pragma unroll
    for (int i = 0; i < 16; ++i) PIN(pre.wof[i]);
#pragma unroll
    for (int i = 0; i < 4; ++i) PIN(pre.xv[i]);
    f32x4 acc[4];
    gemm64_mma<16, 4, 8>(pre.wof, smem + G_A, acc TSPASS);
    LAS f32x2* S = (LAS f32x2*)(smem + G_ST);
#pragma unroll
    for (int i = 0; i < 4; ++i) hv[i] = cvt4(unpack4(pre.xv[i]) + acc[i]);
#pragma unroll
    for (int i = 0; i < 4; i += 2) {
      const unsigned rt = (unsigned)rb * 4 + i + (g & 1);
      st16(ws, WS_H + (((rt * 16 + cb * 2 + (wn >> 1)) * 4 + (wn & 1) * 2 + (g >> 1)) * 16 + r16) * 16, nop4(pair16(hv[i], hv[i + 1])));
    }
#pragma unroll
    for (int i = 0; i < 4; ++i) {
      const f32x4 v = unpack4(hv[i]);
      const float s1 = sum_x32(sum_x16((v[0] + v[1]) + (v[2] + v[3]))), s2 = sum_x32(sum_x16((v[0] * v[0] + v[1] * v[1]) + (v[2] * v[2] + v[3] * v[3])));
      if (g == 0) S[(i * 16 + r16) * 4 + wn] = (f32x2){s1, s2};
    }
    lds_bar();
    if (wid == 4) {
      const f32x2 a = S[lane * 4], bb = S[lane * 4 + 1], c = S[lane * 4 + 2], d = S[lane * 4 + 3];
      const unsigned s1 = __float_as_uint((a.x + bb.x) + (c.x + d.x)), s2 = __float_as_uint((a.y + bb.y) + (c.y + d.y));
      __hip_atomic_store((gu64*)(p.ws + WS_HST + (size_t)(rb * 8 + cb) * 512 + lane * 8), ((unsigned long long)(s2 < POISON ? s2 : POISON - 1) << 32) | (s1 < POISON ? s1 : POISON - 1), RLX_AGENT);
    }
    TS(32);
    raw_bar();
  } else {
    gemm64_load<16, 4, 4>(p.ws, p.ws + WS_O + (size_t)(rb * 4 + wid) * 16384, cb & 3, smem + G_A);
    lds_bar();
    WLoad w0; w0.init(p, mb, fb, wid, (unsigned)(size_t)(lds_ptr)smem); w0.issue(0, lane); w0.issue(1, lane);
    { const int u0 = fb < 11 ? 3 * fb : 33 + 2 * (fb - 11), nu = fb < 11 ? 3 : 2;
#pragma unroll
      for (int i = 0; i < 6; ++i) { const int rt = 2 * wid + i / 3, u = i % 3;
        st16(ws, WS_ACT + (unsigned)((mb * 8 + rt) * 43 + u0 + (u < nu ? u : 0)) * 1024 + lane * 16, (u32x4){POISON, POISON, POISON, POISON}); } }
    if (wid == 0) poll<256>(p.ws, FL_Q, tag);
    wait_bar<0>();
    publish(p.ws, FL_R + b * 4, tag);
  }
  TS(10);

  bf16x8 wdf[43];
  { WLoad wl;
    if (wid >= 4) wl.init(p, mb, fb, wid - 4, (unsigned)(size_t)(lds_ptr)smem);
    p4_gateup(p, ws, mb, fb, smem, wl, wct, 4 * (cb + (cb >> 2)), wdf TSPASS); }
  if (wid == 0) poll<256>(p.ws, FL_R, tag);
  lds_bar();
  TS(13);

  if (wid >= 4) {
#pragma unroll
    for (int i = 0; i < 43; ++i) PIN(wdf[i]);
    f32x4 acc[4];
    gemm64_mma<43, 8, 15>(wdf, smem, acc TSPASS);
#pragma unroll
    for (int i = 0; i < 4; ++i) *(u32x2*)(p.out + ((size_t)(rb * 64 + i * 16 + r16) * DM + col) * 2) = cvt4(unpack4(hv[i]) + acc[i]);
  } else {
    gemm64_load<43, 8, 6>(p.ws, p.ws + WS_ACT + (size_t)(rb * 4 + wid) * 43 * 1024, cb + (cb >> 2), smem);
  }
  TS_END();
}

extern "C" void kernel_launch(void* const* d_in, const int* in_sizes, int n_in, void* d_out, int out_size, void* d_ws, size_t ws_size, hipStream_t stream) {
  Params p;
  p.x = (const char*)d_in[0]; p.wqkv = (const char*)d_in[1]; p.wo = (const char*)d_in[2];
  p.wg = (const char*)d_in[4]; p.wu = (const char*)d_in[5]; p.wd = (const char*)d_in[6];
  p.out = (char*)d_out; p.ws = (char*)d_ws;
  constexpr int LDS = 160 * 1024;
  (void)hipFuncSetAttribute((const void*)mega, hipFuncAttributeMaxDynamicSharedMemorySize, LDS);
  mega<<<256, 512, LDS, stream>>>(p);
}
```

```cpp
#include <hip/hip_runtime.h>
#include <stdint.h>
#define ATTN_ASM_A \
  "s_mov_b32 %[keep], m0\n\t" \
  "ds_read_b128 v[168:171], %[ka]\n\t" \
  "ds_read_b128 v[172:175], %[ka] offset:1024\n\t" \
  "ds_read_b128 v[176:179], %[va]\n\t" \
  "ds_read_b128 v[184:187], %[va] offset:512\n\t" \
  "v_mov_b32 v164, 0\n\t" \
  "v_mov_b32 v165, 0\n\t" \
  "v_mov_b32 v166, 0\n\t" \
  "v_mov_b32 v167, 0\n\t" \
  "v_mov_b32 v188, 0\n\t" \
  "v_mov_b32 v189, 0\n\t" \
  "v_mov_b32 v190, 0\n\t" \
  "v_mov_b32 v191, 0\n\t" \
  "s_waitcnt lgkmcnt(3)\n\t" \
  "v_mfma_f32_32x32x16_bf16 v[128:143], v[168:171], %[q], %[nc]\n\t" \
  "s_nop 10\n\t" \
  "s_waitcnt lgkmcnt(2)\n\t" \
  "v_mfma_f32_32x32x16_bf16 v[144:159], v[172:175], %[q], %[nc]\n\t" \
  "ds_read_b128 v[168:171], %[ka] offset:2048\n\t" \
  "s_mov_b32 m0, %[ld]\n\t" \
  "v_exp_f32 v128, v128\n\t" \
  "v_exp_f32 v129, v129\n\t" \
  "global_load_lds_dwordx4 %[go], %[kg] sc1\n\t" \
  "v_exp_f32 v130, v130\n\t" \
  "v_exp_f32 v131, v131\n\t" \
  "v_cvt_pk_bf16_f32 v160, v128, v129\n\t" \
  "v_exp_f32 v132, v132\n\t" \
  "v_exp_f32 v133, v133\n\t" \
  "v_cvt_pk_bf16_f32 v161, v130, v131\n\t" \
  "v_mfma_f32_32x32x16_bf16 %[o], v[188:191], v[164:167], %[o]\n\t" \
  "ds_read_b128 v[180:183], %[va] offset:1024\n\t" \
  "ds_read_b128 v[188:191], %[va] offset:1536\n\t" \
  "s_add_u32 m0, %[ld], 0x10000\n\t" \
  "v_exp_f32 v134, v134\n\t" \
  "v_exp_f32 v135, v135\n\t" \
  "global_load_lds_dwordx4 %[go], %[vg] sc1\n\t" \
  "s_add_u32 %[ld], %[ld], 0x2000\n\t" \
  "v_cvt_pk_bf16_f32 v162, v132, v133\n\t" \
  "v_exp_f32 v136, v136\n\t" \
  "v_exp_f32 v137, v137\n\t" \
  "v_cvt_pk_bf16_f32 v163, v134, v135\n\t" \
  "v_add_u32 %[go], 0x2000, %[go]\n\t" \
  "v_and_b32 %[go], 0xffff, %[go]\n\t" \
  "v_exp_f32 v138, v138\n\t" \
  "v_exp_f32 v139, v139\n\t" \
  "s_waitcnt lgkmcnt(4)\n\t" \
  "v_mfma_f32_32x32x16_bf16 %[o], v[176:179], v[160:163], %[o]\n\t" \
  "v_cvt_pk_bf16_f32 v164, v136, v137\n\t" \
  "v_exp_f32 v140, v140\n\t" \
  "v_exp_f32 v141, v141\n\t" \
  "v_cvt_pk_bf16_f32 v165, v138, v139\n\t" \
  "v_exp_f32 v142, v142\n\t" \
  "v_exp_f32 v143, v143\n\t" \
  "v_cvt_pk_bf16_f32 v166, v140, v141\n\t" \
  "v_cvt_pk_bf16_f32 v167, v142, v143\n\t" \
  "s_waitcnt lgkmcnt(2)\n\t" \
  "v_mfma_f32_32x32x16_bf16 v[128:143], v[168:171], %[q], %[nc]\n\t" \
  "ds_read_b128 v[172:175], %[ka] offset:3072\n\t" \
  "s_mov_b32 m0, %[ld]\n\t" \
  "v_exp_f32 v144, v144\n\t" \
  "v_exp_f32 v145, v145\n\t" \
  "global_load_lds_dwordx4 %[go], %[kg] sc1\n\t" \
  "v_exp_f32 v146, v146\n\t" \
  "v_exp_f32 v147, v147\n\t" \
  "v_cvt_pk_bf16_f32 v160, v144, v145\n\t" \
  "v_exp_f32 v148, v148\n\t" \
  "v_exp_f32 v149, v149\n\t" \
  "v_cvt_pk_bf16_f32 v161, v146, v147\n\t" \
  "v_mfma_f32_32x32x16_bf16 %[o], v[184:187], v[164:167], %[o]\n\t" \
  "ds_read_b128 v[176:179], %[va] offset:2048\n\t" \
  "ds_read_b128 v[184:187], %[va] offset:2560\n\t" \
  "s_add_u32 m0, %[ld], 0x10000\n\t" \
  "v_exp_f32 v150, v150\n\t" \
  "v_exp_f32 v151, v151\n\t" \
  "global_load_lds_dwordx4 %[go], %[vg] sc1\n\t" \
  "s_add_u32 %[ld], %[ld], 0x2000\n\t" \
  "v_cvt_pk_bf16_f32 v162, v148, v149\n\t" \
  "v_exp_f32 v152, v152\n\t" \
  "v_exp_f32 v153, v153\n\t" \
  "v_cvt_pk_bf16_f32 v163, v150, v151\n\t" \
  "v_add_u32 %[go], 0x2000, %[go]\n\t" \
  "v_and_b32 %[go], 0xffff, %[go]\n\t" \
  "v_exp_f32 v154, v154\n\t" \
  "v_exp_f32 v155, v155\n\t" \
  "s_waitcnt lgkmcnt(4)\n\t" \
  "v_mfma_f32_32x32x16_bf16 %[o], v[180:183], v[160:163], %[o]\n\t" \
  "v_cvt_pk_bf16_f32 v164, v152, v153\n\t" \
  "v_exp_f32 v156, v156\n\t" \
  "v_exp_f32 v157, v157\n\t" \
  "v_cvt_pk_bf16_f32 v165, v154, v155\n\t" \
  "v_exp_f32 v158, v158\n\t" \
  "v_exp_f32 v159, v159\n\t" \
  "v_cvt_pk_bf16_f32 v166, v156, v157\n\t" \
  "v_cvt_pk_bf16_f32 v167, v158, v159\n\t" \
  "s_waitcnt lgkmcnt(2)\n\t" \
  "v_mfma_f32_32x32x16_bf16 v[144:159], v[172:175], %[q], %[nc]\n\t" \
  "ds_read_b128 v[168:171], %[ka] offset:4096\n\t" \
  "s_mov_b32 m0, %[ld]\n\t" \
  "v_exp_f32 v128, v128\n\t" \
  "v_exp_f32 v129, v129\n\t" \
  "global_load_lds_dwordx4 %[go], %[kg] sc1\n\t" \
  "v_exp_f32 v130, v130\n\t" \
  "v_exp_f32 v131, v131\n\t" \
  "v_cvt_pk_bf16_f32 v160, v128, v129\n\t" \
  "v_exp_f32 v132, v132\n\t" \
  "v_exp_f32 v133, v133\n\t" \
  "v_cvt_pk_bf16_f32 v161, v130, v131\n\t" \
  "v_mfma_f32_32x32x16_bf16 %[o], v[188:191], v[164:167], %[o]\n\t" \
  "ds_read_b128 v[180:183], %[va] offset:3072\n\t" \
  "ds_read_b128 v[188:191], %[va] offset:3584\n\t" \
  "s_add_u32 m0, %[ld], 0x10000\n\t" \
  "v_exp_f32 v134, v134\n\t" \
  "v_exp_f32 v135, v135\n\t" \
  "global_load_lds_dwordx4 %[go], %[vg] sc1\n\t" \
  "s_add_u32 %[ld], %[ld], 0x2000\n\t" \
  "v_cvt_pk_bf16_f32 v162, v132, v133\n\t" \
  "v_exp_f32 v136, v136\n\t" \
  "v_exp_f32 v137, v137\n\t" \
  "v_cvt_pk_bf16_f32 v163, v134, v135\n\t" \
  "v_add_u32 %[go], 0x2000, %[go]\n\t" \
  "v_and_b32 %[go], 0xffff, %[go]\n\t" \
  "v_exp_f32 v138, v138\n\t" \
  "v_exp_f32 v139, v139\n\t" \
  "s_waitcnt lgkmcnt(4)\n\t" \
  "v_mfma_f32_32x32x16_bf16 %[o], v[176:179], v[160:163], %[o]\n\t" \
  "v_cvt_pk_bf16_f32 v164, v136, v137\n\t" \
  "v_exp_f32 v140, v140\n\t" \
  "v_exp_f32 v141, v141\n\t" \
  "v_cvt_pk_bf16_f32 v165, v138, v139\n\t" \
  "v_exp_f32 v142, v142\n\t" \
  "v_exp_f32 v143, v143\n\t" \
  "v_cvt_pk_bf16_f32 v166, v140, v141\n\t" \
  "v_cvt_pk_bf16_f32 v167, v142, v143\n\t" \
  "s_waitcnt lgkmcnt(2)\n\t" \
  "v_mfma_f32_32x32x16_bf16 v[128:143], v[168:171], %[q], %[nc]\n\t" \
  "ds_read_b128 v[172:175], %[ka] offset:5120\n\t" \
  "s_mov_b32 m0, %[ld]\n\t" \
  "v_exp_f32 v144, v144\n\t" \
  "v_exp_f32 v145, v145\n\t" \
  "global_load_lds_dwordx4 %[go], %[kg] sc1\n\t" \
  "v_exp_f32 v146, v146\n\t" \
  "v_exp_f32 v147, v147\n\t" \
  "v_cvt_pk_bf16_f32 v160, v144, v145\n\t" \
  "v_exp_f32 v148, v148\n\t" \
  "v_exp_f32 v149, v149\n\t" \
  "v_cvt_pk_bf16_f32 v161, v146, v147\n\t" \
  "v_mfma_f32_32x32x16_bf16 %[o], v[184:187], v[164:167], %[o]\n\t" \
  "ds_read_b128 v[176:179], %[va] offset:4096\n\t" \
  "ds_read_b128 v[184:187], %[va] offset:4608\n\t" \
  "s_add_u32 m0, %[ld], 0x10000\n\t" \
  "v_exp_f32 v150, v150\n\t" \
  "v_exp_f32 v151, v151\n\t" \
  "global_load_lds_dwordx4 %[go], %[vg] sc1\n\t" \
  "s_add_u32 %[ld], %[ld], 0x2000\n\t" \
  "v_cvt_pk_bf16_f32 v162, v148, v149\n\t" \
  "v_exp_f32 v152, v152\n\t" \
  "v_exp_f32 v153, v153\n\t" \
  "v_cvt_pk_bf16_f32 v163, v150, v151\n\t" \
  "v_add_u32 %[go], 0x2000, %[go]\n\t" \
  "v_and_b32 %[go], 0xffff, %[go]\n\t" \
  "v_exp_f32 v154, v154\n\t" \
  "v_exp_f32 v155, v155\n\t" \
  "s_waitcnt lgkmcnt(4)\n\t" \
  "v_mfma_f32_32x32x16_bf16 %[o], v[180:183], v[160:163], %[o]\n\t" \
  "v_cvt_pk_bf16_f32 v164, v152, v153\n\t" \
  "v_exp_f32 v156, v156\n\t" \
  "v_exp_f32 v157, v157\n\t" \
  "v_cvt_pk_bf16_f32 v165, v154, v155\n\t" \
  "v_exp_f32 v158, v158\n\t" \
  "v_exp_f32 v159, v159\n\t" \
  "v_cvt_pk_bf16_f32 v166, v156, v157\n\t" \
  "v_cvt_pk_bf16_f32 v167, v158, v159\n\t" \
  "v_add_u32 %[ka], 0x1000, %[ka]\n\t" \
  "v_add_u32 %[va], %[vs], %[va]\n\t" \
  "s_waitcnt lgkmcnt(2)\n\t" \
  "v_mfma_f32_32x32x16_bf16 v[144:159], v[172:175], %[q], %[nc]\n\t" \
  "ds_read_b128 v[168:171], %[ka] offset:2048\n\t" \
  "s_mov_b32 m0, %[ld]\n\t" \
  "v_exp_f32 v128, v128\n\t" \
  "v_exp_f32 v129, v129\n\t" \
  "global_load_lds_dwordx4 %[go], %[kg] sc1\n\t" \
  "v_exp_f32 v130, v130\n\t" \
  "v_exp_f32 v131, v131\n\t" \
  "v_cvt_pk_bf16_f32 v160, v128, v129\n\t" \
  "v_exp_f32 v132, v132\n\t" \
  "v_exp_f32 v133, v133\n\t" \
  "v_cvt_pk_bf16_f32 v161, v130, v131\n\t" \
  "v_mfma_f32_32x32x16_bf16 %[o], v[188:191], v[164:167], %[o]\n\t" \
  "ds_read_b128 v[180:183], %[va] offset:1024\n\t" \
  "ds_read_b128 v[188:191], %[va] offset:1536\n\t" \
  "s_add_u32 m0, %[ld], 0x10000\n\t" \
  "v_exp_f32 v134, v134\n\t" \
  "v_exp_f32 v135, v135\n\t" \
  "global_load_lds_dwordx4 %[go], %[vg] sc1\n\t" \
  "s_add_u32 %[ld], %[ld], 0x2000\n\t" \
  "v_cvt_pk_bf16_f32 v162, v132, v133\n\t" \
  "v_exp_f32 v136, v136\n\t" \
  "v_exp_f32 v137, v137\n\t" \
  "v_cvt_pk_bf16_f32 v163, v134, v135\n\t" \
  "v_add_u32 %[go], 0x2000, %[go]\n\t" \
  "v_and_b32 %[go], 0xffff, %[go]\n\t" \
  "v_exp_f32 v138, v138\n\t" \
  "v_exp_f32 v139, v139\n\t" \
  "s_waitcnt lgkmcnt(4)\n\t" \
  "v_mfma_f32_32x32x16_bf16 %[o], v[176:179], v[160:163], %[o]\n\t" \
  "v_cvt_pk_bf16_f32 v164, v136, v137\n\t" \
  "v_exp_f32 v140, v140\n\t" \
  "v_exp_f32 v141, v141\n\t" \
  "v_cvt_pk_bf16_f32 v165, v138, v139\n\t" \
  "v_exp_f32 v142, v142\n\t" \
  "v_exp_f32 v143, v143\n\t" \
  "v_cvt_pk_bf16_f32 v166, v140, v141\n\t" \
  "v_cvt_pk_bf16_f32 v167, v142, v143\n\t" \
  "s_waitcnt lgkmcnt(2)\n\t" \
  "v_mfma_f32_32x32x16_bf16 v[128:143], v[168:171], %[q], %[nc]\n\t" \
  "ds_read_b128 v[172:175], %[ka] offset:3072\n\t" \
  "s_mov_b32 m0, %[ld]\n\t" \
  "v_exp_f32 v144, v144\n\t" \
  "v_exp_f32 v145, v145\n\t" \
  "global_load_lds_dwordx4 %[go], %[kg] sc1\n\t" \
  "v_exp_f32 v146, v146\n\t" \
  "v_exp_f32 v147, v147\n\t" \
  "v_cvt_pk_bf16_f32 v160, v144, v145\n\t" \
  "v_exp_f32 v148, v148\n\t" \
  "v_exp_f32 v149, v149\n\t" \
  "v_cvt_pk_bf16_f32 v161, v146, v147\n\t" \
  "v_mfma_f32_32x32x16_bf16 %[o], v[184:187], v[164:167], %[o]\n\t" \
  "ds_read_b128 v[176:179], %[va] offset:2048\n\t" \
  "ds_read_b128 v[184:187], %[va] offset:2560\n\t" \
  "s_add_u32 m0, %[ld], 0x10000\n\t" \
  "v_exp_f32 v150, v150\n\t" \
  "v_exp_f32 v151, v151\n\t" \
  "global_load_lds_dwordx4 %[go], %[vg] sc1\n\t" \
  "s_add_u32 %[ld], %[ld], 0x2000\n\t" \
  "v_cvt_pk_bf16_f32 v162, v148, v149\n\t" \
  "v_exp_f32 v152, v152\n\t" \
  "v_exp_f32 v153, v153\n\t" \
  "v_cvt_pk_bf16_f32 v163, v150, v151\n\t" \
  "v_add_u32 %[go], 0x2000, %[go]\n\t" \
  "v_and_b32 %[go], 0xffff, %[go]\n\t" \
  "v_exp_f32 v154, v154\n\t" \
  "v_exp_f32 v155, v155\n\t" \
  "s_waitcnt lgkmcnt(4)\n\t" \
  "v_mfma_f32_32x32x16_bf16 %[o], v[180:183], v[160:163], %[o]\n\t" \
  "v_cvt_pk_bf16_f32 v164, v152, v153\n\t" \
  "v_exp_f32 v156, v156\n\t" \
  "v_exp_f32 v157, v157\n\t" \
  "v_cvt_pk_bf16_f32 v165, v154, v155\n\t" \
  "v_exp_f32 v158, v158\n\t" \
  "v_exp_f32 v159, v159\n\t" \
  "v_cvt_pk_bf16_f32 v166, v156, v157\n\t" \
  "v_cvt_pk_bf16_f32 v167, v158, v159\n\t" \
  "s_waitcnt lgkmcnt(2)\n\t" \
  "v_mfma_f32_32x32x16_bf16 v[144:159], v[172:175], %[q], %[nc]\n\t" \
  "ds_read_b128 v[168:171], %[ka] offset:4096\n\t" \
  "v_exp_f32 v128, v128\n\t" \
  "v_exp_f32 v129, v129\n\t" \
  "v_exp_f32 v130, v130\n\t" \
  "v_exp_f32 v131, v131\n\t" \
  "v_cvt_pk_bf16_f32 v160, v128, v129\n\t" \
  "v_exp_f32 v132, v132\n\t" \
  "v_exp_f32 v133, v133\n\t" \
  "v_cvt_pk_bf16_f32 v161, v130, v131\n\t" \
  "v_mfma_f32_32x32x16_bf16 %[o], v[188:191], v[164:167], %[o]\n\t" \
  "ds_read_b128 v[180:183], %[va] offset:3072\n\t" \
  "ds_read_b128 v[188:191], %[va] offset:3584\n\t" \
  "v_exp_f32 v134, v134\n\t" \
  "v_exp_f32 v135, v135\n\t" \
  "v_cvt_pk_bf16_f32 v162, v132, v133\n\t" \
  "v_exp_f32 v136, v136\n\t" \
  "v_exp_f32 v137, v137\n\t" \
  "v_cvt_pk_bf16_f32 v163, v134, v135\n\t" \
  "v_exp_f32 v138, v138\n\t" \
  "v_exp_f32 v139, v139\n\t" \
  "s_waitcnt lgkmcnt(4)\n\t" \
  "v_mfma_f32_32x32x16_bf16 %[o], v[176:179], v[160:163], %[o]\n\t" \
  "v_cvt_pk_bf16_f32 v164, v136, v137\n\t" \
  "v_exp_f32 v140, v140\n\t" \
  "v_exp_f32 v141, v141\n\t" \
  "v_cvt_pk_bf16_f32 v165, v138, v139\n\t" \
  "v_exp_f32 v142, v142\n\t" \
  "v_exp_f32 v143, v143\n\t" \
  "v_cvt_pk_bf16_f32 v166, v140, v141\n\t" \
  "v_cvt_pk_bf16_f32 v167, v142, v143\n\t" \
  "s_waitcnt lgkmcnt(2)\n\t" \
  "v_mfma_f32_32x32x16_bf16 v[128:143], v[168:171], %[q], %[nc]\n\t" \
  "ds_read_b128 v[172:175], %[ka] offset:5120\n\t" \
  "v_exp_f32 v144, v144\n\t" \
  "v_exp_f32 v145, v145\n\t" \
  "v_exp_f32 v146, v146\n\t" \
  "v_exp_f32 v147, v147\n\t" \
  "v_cvt_pk_bf16_f32 v160, v144, v145\n\t" \
  "v_exp_f32 v148, v148\n\t" \
  "v_exp_f32 v149, v149\n\t" \
  "v_cvt_pk_bf16_f32 v161, v146, v147\n\t" \
  "v_mfma_f32_32x32x16_bf16 %[o], v[184:187], v[164:167], %[o]\n\t" \
  "ds_read_b128 v[176:179], %[va] offset:4096\n\t" \
  "ds_read_b128 v[184:187], %[va] offset:4608\n\t" \
  "v_exp_f32 v150, v150\n\t" \
  "v_exp_f32 v151, v151\n\t" \
  "v_cvt_pk_bf16_f32 v162, v148, v149\n\t" \
  "v_exp_f32 v152, v152\n\t" \
  "v_exp_f32 v153, v153\n\t" \
  "v_cvt_pk_bf16_f32 v163, v150, v151\n\t" \
  "v_exp_f32 v154, v154\n\t" \
  "v_exp_f32 v155, v155\n\t" \
  "s_waitcnt lgkmcnt(4)\n\t" \
  "v_mfma_f32_32x32x16_bf16 %[o], v[180:183], v[160:163], %[o]\n\t" \
  "v_cvt_pk_bf16_f32 v164, v152, v153\n\t" \
  "v_exp_f32 v156, v156\n\t" \
  "v_exp_f32 v157, v157\n\t" \
  "v_cvt_pk_bf16_f32 v165, v154, v155\n\t" \
  "v_exp_f32 v158, v158\n\t" \
  "v_exp_f32 v159, v159\n\t" \
  "v_cvt_pk_bf16_f32 v166, v156, v157\n\t" \
  "v_cvt_pk_bf16_f32 v167, v158, v159\n\t" \
  "v_add_u32 %[ka], 0x1000, %[ka]\n\t" \
  "v_add_u32 %[va], %[vs], %[va]\n\t" \
  "s_mov_b32 m0, %[keep]\n\t" \
  "s_waitcnt vmcnt(0) lgkmcnt(0)\n\t" \
  "s_barrier\n\t" \
  ""
#define ATTN_ASM_B \
  "s_mov_b32 %[cnt], %[c0]\n\t" \
  "s_cmp_lg_u32 %[c0], 2\n\t" \
  "s_cbranch_scc1 L_attn_loop_%=\n\t" \
  "s_cmp_eq_u32 %[sw], 99\n\t" \
  "s_cbranch_scc1 L_attn_y_%=\n\t" \
  "s_setprio 2\n\t" \
  "s_branch L_attn_loop_%=\n\t" \
  "L_attn_y_%=:\n\t" \
  "s_setprio 1\n\t" \
  "L_attn_loop_%=:\n\t" \
  "s_cmp_lg_u32 %[cnt], %[sw]\n\t" \
  "s_cbranch_scc1 L_attn_nosw_%=\n\t" \
  "s_setprio 0\n\t" \
  "L_attn_nosw_%=:\n\t" \
  "s_waitcnt lgkmcnt(2)\n\t" \
  "v_mfma_f32_32x32x16_bf16 v[144:159], v[172:175], %[q], %[nc]\n\t" \
  "ds_read_b128 v[168:171], %[ka] offset:2048\n\t" \
  "v_exp_f32 v128, v128\n\t" \
  "v_exp_f32 v129, v129\n\t" \
  "v_exp_f32 v130, v130\n\t" \
  "v_exp_f32 v131, v131\n\t" \
  "v_cvt_pk_bf16_f32 v160, v128, v129\n\t" \
  "v_exp_f32 v132, v132\n\t" \
  "v_exp_f32 v133, v133\n\t" \
  "v_cvt_pk_bf16_f32 v161, v130, v131\n\t" \
  "v_mfma_f32_32x32x16_bf16 %[o], v[188:191], v[164:167], %[o]\n\t" \
  "ds_read_b128 v[180:183], %[va] offset:1024\n\t" \
  "ds_read_b128 v[188:191], %[va] offset:1536\n\t" \
  "v_exp_f32 v134, v134\n\t" \
  "v_exp_f32 v135, v135\n\t" \
  "v_cvt_pk_bf16_f32 v162, v132, v133\n\t" \
  "v_exp_f32 v136, v136\n\t" \
  "v_exp_f32 v137, v137\n\t" \
  "v_cvt_pk_bf16_f32 v163, v134, v135\n\t" \
  "v_exp_f32 v138, v138\n\t" \
  "v_exp_f32 v139, v139\n\t" \
  "s_waitcnt lgkmcnt(4)\n\t" \
  "v_mfma_f32_32x32x16_bf16 %[o], v[176:179], v[160:163], %[o]\n\t" \
  "v_cvt_pk_bf16_f32 v164, v136, v137\n\t" \
  "v_exp_f32 v140, v140\n\t" \
  "v_exp_f32 v141, v141\n\t" \
  "v_cvt_pk_bf16_f32 v165, v138, v139\n\t" \
  "v_exp_f32 v142, v142\n\t" \
  "v_exp_f32 v143, v143\n\t" \
  "v_cvt_pk_bf16_f32 v166, v140, v141\n\t" \
  "v_cvt_pk_bf16_f32 v167, v142, v143\n\t" \
  "s_waitcnt lgkmcnt(2)\n\t" \
  "v_mfma_f32_32x32x16_bf16 v[128:143], v[168:171], %[q], %[nc]\n\t" \
  "ds_read_b128 v[172:175], %[ka] offset:3072\n\t" \
  "v_exp_f32 v144, v144\n\t" \
  "v_exp_f32 v145, v145\n\t" \
  "v_exp_f32 v146, v146\n\t" \
  "v_exp_f32 v147, v147\n\t" \
  "v_cvt_pk_bf16_f32 v160, v144, v145\n\t" \
  "v_exp_f32 v148, v148\n\t" \
  "v_exp_f32 v149, v149\n\t" \
  "v_cvt_pk_bf16_f32 v161, v146, v147\n\t" \
  "v_mfma_f32_32x32x16_bf16 %[o], v[184:187], v[164:167], %[o]\n\t" \
  "ds_read_b128 v[176:179], %[va] offset:2048\n\t" \
  "ds_read_b128 v[184:187], %[va] offset:2560\n\t" \
  "v_exp_f32 v150, v150\n\t" \
  "v_exp_f32 v151, v151\n\t" \
  "v_cvt_pk_bf16_f32 v162, v148, v149\n\t" \
  "v_exp_f32 v152, v152\n\t" \
  "v_exp_f32 v153, v153\n\t" \
  "v_cvt_pk_bf16_f32 v163, v150, v151\n\t" \
  "v_exp_f32 v154, v154\n\t" \
  "v_exp_f32 v155, v155\n\t" \
  "s_waitcnt lgkmcnt(4)\n\t" \
  "v_mfma_f32_32x32x16_bf16 %[o], v[180:183], v[160:163], %[o]\n\t" \
  "v_cvt_pk_bf16_f32 v164, v152, v153\n\t" \
  "v_exp_f32 v156, v156\n\t" \
  "v_exp_f32 v157, v157\n\t" \
  "v_cvt_pk_bf16_f32 v165, v154, v155\n\t" \
  "v_exp_f32 v158, v158\n\t" \
  "v_exp_f32 v159, v159\n\t" \
  "v_cvt_pk_bf16_f32 v166, v156, v157\n\t" \
  "v_cvt_pk_bf16_f32 v167, v158, v159\n\t" \
  "s_waitcnt lgkmcnt(2)\n\t" \
  "v_mfma_f32_32x32x16_bf16 v[144:159], v[172:175], %[q], %[nc]\n\t" \
  "ds_read_b128 v[168:171], %[ka] offset:4096\n\t" \
  "v_exp_f32 v128, v128\n\t" \
  "v_exp_f32 v129, v129\n\t" \
  "v_exp_f32 v130, v130\n\t" \
  "v_exp_f32 v131, v131\n\t" \
  "v_cvt_pk_bf16_f32 v160, v128, v129\n\t" \
  "v_exp_f32 v132, v132\n\t" \
  "v_exp_f32 v133, v133\n\t" \
  "v_cvt_pk_bf16_f32 v161, v130, v131\n\t" \
  "v_mfma_f32_32x32x16_bf16 %[o], v[188:191], v[164:167], %[o]\n\t" \
  "ds_read_b128 v[180:183], %[va] offset:3072\n\t" \
  "ds_read_b128 v[188:191], %[va] offset:3584\n\t" \
  "v_exp_f32 v134, v134\n\t" \
  "v_exp_f32 v135, v135\n\t" \
  "v_cvt_pk_bf16_f32 v162, v132, v133\n\t" \
  "v_exp_f32 v136, v136\n\t" \
  "v_exp_f32 v137, v137\n\t" \
  "v_cvt_pk_bf16_f32 v163, v134, v135\n\t" \
  "v_exp_f32 v138, v138\n\t" \
  "v_exp_f32 v139, v139\n\t" \
  "s_waitcnt lgkmcnt(4)\n\t" \
  "v_mfma_f32_32x32x16_bf16 %[o], v[176:179], v[160:163], %[o]\n\t" \
  "v_cvt_pk_bf16_f32 v164, v136, v137\n\t" \
  "v_exp_f32 v140, v140\n\t" \
  "v_exp_f32 v141, v141\n\t" \
  "v_cvt_pk_bf16_f32 v165, v138, v139\n\t" \
  "v_exp_f32 v142, v142\n\t" \
  "v_exp_f32 v143, v143\n\t" \
  "v_cvt_pk_bf16_f32 v166, v140, v141\n\t" \
  "v_cvt_pk_bf16_f32 v167, v142, v143\n\t" \
  "s_waitcnt lgkmcnt(2)\n\t" \
  "v_mfma_f32_32x32x16_bf16 v[128:143], v[168:171], %[q], %[nc]\n\t" \
  "ds_read_b128 v[172:175], %[ka] offset:5120\n\t" \
  "v_exp_f32 v144, v144\n\t" \
  "v_exp_f32 v145, v145\n\t" \
  "v_exp_f32 v146, v146\n\t" \
  "v_exp_f32 v147, v147\n\t" \
  "v_cvt_pk_bf16_f32 v160, v144, v145\n\t" \
  "v_exp_f32 v148, v148\n\t" \
  "v_exp_f32 v149, v149\n\t" \
  "v_cvt_pk_bf16_f32 v161, v146, v147\n\t" \
  "v_mfma_f32_32x32x16_bf16 %[o], v[184:187], v[164:167], %[o]\n\t" \
  "ds_read_b128 v[176:179], %[va] offset:4096\n\t" \
  "ds_read_b128 v[184:187], %[va] offset:4608\n\t" \
  "v_exp_f32 v150, v150\n\t" \
  "v_exp_f32 v151, v151\n\t" \
  "v_cvt_pk_bf16_f32 v162, v148, v149\n\t" \
  "v_exp_f32 v152, v152\n\t" \
  "v_exp_f32 v153, v153\n\t" \
  "v_cvt_pk_bf16_f32 v163, v150, v151\n\t" \
  "v_exp_f32 v154, v154\n\t" \
  "v_exp_f32 v155, v155\n\t" \
  "s_waitcnt lgkmcnt(4)\n\t" \
  "v_mfma_f32_32x32x16_bf16 %[o], v[180:183], v[160:163], %[o]\n\t" \
  "v_cvt_pk_bf16_f32 v164, v152, v153\n\t" \
  "v_exp_f32 v156, v156\n\t" \
  "v_exp_f32 v157, v157\n\t" \
  "v_cvt_pk_bf16_f32 v165, v154, v155\n\t" \
  "v_exp_f32 v158, v158\n\t" \
  "v_exp_f32 v159, v159\n\t" \
  "v_cvt_pk_bf16_f32 v166, v156, v157\n\t" \
  "v_cvt_pk_bf16_f32 v167, v158, v159\n\t" \
  "v_add_u32 %[ka], 0x1000, %[ka]\n\t" \
  "v_add_u32 %[va], %[vs], %[va]\n\t" \
  "s_add_u32 %[cnt], %[cnt], 1\n\t" \
  "s_cmp_lt_u32 %[cnt], %[c1]\n\t" \
  "s_cbranch_scc1 L_attn_loop_%=\n\t" \
  "s_waitcnt lgkmcnt(0)\n\t" \
  "s_cmp_lg_u32 %[c1], 16\n\t" \
  "s_cbranch_scc1 L_attn_end_%=\n\t" \
  "v_mfma_f32_32x32x16_bf16 %[o], v[188:191], v[164:167], %[o]\n\t" \
  "s_setprio 0\n\t" \
  "L_attn_end_%=:\n\t" \
  "s_nop 15\n\t" \
  ""

typedef short bf16x8 __attribute__((ext_vector_type(8)));
typedef short s16x4 __attribute__((ext_vector_type(4)));
typedef float f32x4 __attribute__((ext_vector_type(4)));
typedef float f32x2 __attribute__((ext_vector_type(2)));
typedef float f32x16 __attribute__((ext_vector_type(16)));
typedef float f32x8 __attribute__((ext_vector_type(8)));
typedef unsigned u32x4 __attribute__((ext_vector_type(4)));
typedef unsigned u32x8 __attribute__((ext_vector_type(8)));
typedef unsigned u32x2 __attribute__((ext_vector_type(2)));
typedef __bf16 bf16x2_t __attribute__((ext_vector_type(2)));
#define LAS __attribute__((address_space(3)))
#define GAS __attribute__((address_space(1)))
typedef const LAS char* lds_cptr;
typedef LAS char* lds_ptr;
typedef GAS unsigned gu32;
typedef GAS unsigned long long gu64;
typedef __amdgpu_buffer_rsrc_t rsrc_t;

constexpr int SEQ = 2048, DM = 512, NH = 32, FF = 1376;
constexpr float EPS = 1e-5f;
constexpr float QSCALE = 0.25f * 1.4426950408889634f;

constexpr unsigned WS_Q = 0, WS_K = 2u << 20, WS_V = 4u << 20, WS_O = 6u << 20, WS_H = 8u << 20, WS_ACT = 10u << 20;
constexpr unsigned WS_KMAX = 16u << 20, WS_HST = (16u << 20) + (64u << 10);
constexpr unsigned WS_WOF = 17u << 20, WS_WDF = WS_WOF + 512 * 1024, WS_WGF = WS_WDF + 32 * 43 * 1024;
constexpr unsigned WS_FLAG = 22u << 20;
constexpr unsigned FL_EPOCH = 0, FL_TMO = 256, FL_1 = 1024, FL_P = 4096, FL_Q = 8192, FL_R = 12288;
constexpr unsigned POISON = 0xFFFFFFFFu;
constexpr unsigned WS_USED = 24u << 20;
constexpr size_t WS_DBG = 64u << 20;

#define TS(i) do {} while (0)
#define TS_BEGIN() do {} while (0)
#define TS_END() do {} while (0)
#define TSARG
#define TSPASS

struct Params {
  const char* x; const char* wqkv; const char* wo; const char* wg; const char* wu; const char* wd;
  char* out; char* ws;
};

#define MFMA16(a, b, c) __builtin_amdgcn_mfma_f32_16x16x32_bf16(a, b, c, 0, 0, 0)
#define MFMA32(a, b, c) __builtin_amdgcn_mfma_f32_32x32x16_bf16(a, b, c, 0, 0, 0)
#define PIN(x) asm volatile("" : "+v"(x))
#define SB() __builtin_amdgcn_sched_barrier(0)
#define RLX_AGENT __ATOMIC_RELAXED, __HIP_MEMORY_SCOPE_AGENT

template <int N> __device__ __forceinline__ void wait_bar() {
  asm volatile("s_waitcnt vmcnt(%0) lgkmcnt(0)\n\ts_barrier" ::"n"(N) : "memory");
}
template <int N> __device__ __forceinline__ void wait_vm() { asm volatile("s_waitcnt vmcnt(%0)" ::"n"(N) : "memory"); }
__device__ __forceinline__ void lds_bar() { asm volatile("s_waitcnt lgkmcnt(0)\n\ts_barrier" ::: "memory"); }
__device__ __forceinline__ void raw_bar() { asm volatile("s_barrier" ::: "memory"); }

template <int N, bool SC1> __device__ __forceinline__ void glds(const void* sbase, unsigned voff, unsigned lds_addr) {
#define GL_(O, S) "\n\tglobal_load_lds_dwordx4 %0, %1 offset:" #O S
  if (SC1) {
    if (N == 1) asm volatile("s_mov_b32 m0, %2\n\ts_nop 0" GL_(0, " sc1") :: "v"(voff), "s"(sbase), "s"(lds_addr) : "memory");
    if (N == 2) asm volatile("s_mov_b32 m0, %2\n\ts_nop 0" GL_(0, " sc1") GL_(1024, " sc1") :: "v"(voff), "s"(sbase), "s"(lds_addr) : "memory");
    if (N == 3) asm volatile("s_mov_b32 m0, %2\n\ts_nop 0" GL_(0, " sc1") GL_(1024, " sc1") GL_(2048, " sc1") :: "v"(voff), "s"(sbase), "s"(lds_addr) : "memory");
    if (N == 4) asm volatile("s_mov_b32 m0, %2\n\ts_nop 0" GL_(0, " sc1") GL_(1024, " sc1") GL_(2048, " sc1") GL_(3072, " sc1") :: "v"(voff), "s"(sbase), "s"(lds_addr) : "memory");
  } else {
    if (N == 1) asm volatile("s_mov_b32 m0, %2\n\ts_nop 0" GL_(0, "") :: "v"(voff), "s"(sbase), "s"(lds_addr) : "memory");
    if (N == 2) asm volatile("s_mov_b32 m0, %2\n\ts_nop 0" GL_(0, "") GL_(1024, "") :: "v"(voff), "s"(sbase), "s"(lds_addr) : "memory");
    if (N == 3) asm volatile("s_mov_b32 m0, %2\n\ts_nop 0" GL_(0, "") GL_(1024, "") GL_(2048, "") :: "v"(voff), "s"(sbase), "s"(lds_addr) : "memory");
  }
#undef GL_
}
template <bool SC1> __device__ __forceinline__ void glds16(const void* sbase, unsigned voff, unsigned lds_addr) { glds<1, SC1>(sbase, voff, lds_addr); }
template <bool SC1> __device__ __forceinline__ void glds4(const void* sbase, unsigned voff, unsigned lds_addr) {
  if (SC1) asm volatile("s_mov_b32 m0, %2\n\ts_nop 0\n\tglobal_load_lds_dword %0, %1 sc1" :: "v"(voff), "s"(sbase), "s"(lds_addr) : "memory");
  else     asm volatile("s_mov_b32 m0, %2\n\ts_nop 0\n\tglobal_load_lds_dword %0, %1" :: "v"(voff), "s"(sbase), "s"(lds_addr) : "memory");
}
__device__ __forceinline__ bf16x8 ld128(lds_cptr p) { return *(const LAS bf16x8*)p; }
typedef short v4i16_t __attribute__((ext_vector_type(4)));
__device__ __forceinline__ s16x4 ldtr(lds_cptr p) {
  return __builtin_bit_cast(s16x4, __builtin_amdgcn_ds_read_tr16_b64_v4i16((LAS v4i16_t*)p));
}
__device__ __forceinline__ bf16x8 ldtr2(lds_cptr p, int second) {
  const s16x4 a = ldtr(p), b = ldtr(p + second);
  return (bf16x8){a[0], a[1], a[2], a[3], b[0], b[1], b[2], b[3]};
}
__device__ __forceinline__ unsigned cvtpk(float lo, float hi) {
  f32x2 v = {lo, hi}; bf16x2_t b = __builtin_convertvector(v, bf16x2_t); return __builtin_bit_cast(unsigned, b);
}
__device__ __forceinline__ u32x2 cvt4(f32x4 v) { return (u32x2){cvtpk(v[0], v[1]), cvtpk(v[2], v[3])}; }
__device__ __forceinline__ float bflo(unsigned w) { return __uint_as_float(w << 16); }
__device__ __forceinline__ float bfhi(unsigned w) { return __uint_as_float(w & 0xffff0000u); }
__device__ __forceinline__ f32x4 unpack4(u32x2 w) { return (f32x4){bflo(w[0]), bfhi(w[0]), bflo(w[1]), bfhi(w[1])}; }
__device__ __forceinline__ float bfr(float v) { return bflo(cvtpk(v, 0.f)); }
__device__ __forceinline__ void ln_consts(float sum, float sumsq, float& mean, float& rstd) {
  const float m = sum * (1.f / DM), var = fmaxf(sumsq * (1.f / DM) - m * m, 0.f);
  mean = bfr(m); rstd = __builtin_amdgcn_rcpf(bfr(__builtin_amdgcn_sqrtf(bfr(bfr(var) + EPS))));
}
__device__ __forceinline__ int rfl(int v) { return __builtin_amdgcn_readfirstlane(v); }
__device__ __forceinline__ bf16x8 ones8() { return (bf16x8){0x3F80, 0x3F80, 0x3F80, 0x3F80, 0x3F80, 0x3F80, 0x3F80, 0x3F80}; }
#define Z4 ((f32x4){0.f, 0.f, 0.f, 0.f})
__device__ __forceinline__ void st16(rsrc_t ws, unsigned off, u32x4 v) { __builtin_amdgcn_raw_buffer_store_b128(v, ws, off, 0, 16); }
__device__ __forceinline__ bf16x8 ldw16(rsrc_t ws, unsigned off) { return __builtin_bit_cast(bf16x8, __builtin_amdgcn_raw_buffer_load_b128(ws, off, 0, 16)); }
__device__ __forceinline__ u32x4 pair16(u32x2 X, u32x2 Y) {
  auto a = __builtin_amdgcn_permlane16_swap(X[0], Y[0], false, false);
  auto b = __builtin_amdgcn_permlane16_swap(X[1], Y[1], false, false);
  return (u32x4){a[0], b[0], a[1], b[1]};
}
__device__ __forceinline__ u32x4 pair32(u32x2 X, u32x2 Y) {
  auto a = __builtin_amdgcn_permlane32_swap(X[0], Y[0], false, false);
  auto b = __builtin_amdgcn_permlane32_swap(X[1], Y[1], false, false);
  return (u32x4){a[0], b[0], a[1], b[1]};
}
__device__ __forceinline__ float sum_x16(float v) { auto r = __builtin_amdgcn_permlane16_swap(__float_as_uint(v), __float_as_uint(v), false, false); return __uint_as_float(r[0]) + __uint_as_float(r[1]); }
__device__ __forceinline__ float sum_x32(float v) { auto r = __builtin_amdgcn_permlane32_swap(__float_as_uint(v), __float_as_uint(v), false, false); return __uint_as_float(r[0]) + __uint_as_float(r[1]); }
__device__ __forceinline__ float max_x16(float v) { auto r = __builtin_amdgcn_permlane16_swap(__float_as_uint(v), __float_as_uint(v), false, false); return fmaxf(__uint_as_float(r[0]), __uint_as_float(r[1])); }
__device__ __forceinline__ float lo_x32(float v) { auto r = __builtin_amdgcn_permlane32_swap(__float_as_uint(v), __float_as_uint(v), false, false); return __uint_as_float(r[0]); }
template <int N> __device__ __forceinline__ float ror16(float v) { return __uint_as_float(__builtin_amdgcn_update_dpp(0u, __float_as_uint(v), 0x120 + N, 0xf, 0xf, false)); }
__device__ __forceinline__ float max_row16(float v) { v = fmaxf(v, ror16<8>(v)); v = fmaxf(v, ror16<4>(v)); v = fmaxf(v, ror16<2>(v)); return fmaxf(v, ror16<1>(v)); }
template <int ND, int NM> __device__ __forceinline__ void weave() {
#pragma unroll
  for (int i = 0; i < ND; ++i) { __builtin_amdgcn_sched_group_barrier(0x008, 1, 0); __builtin_amdgcn_sched_group_barrier(0x100, 1, 0); }
  if (NM > ND) __builtin_amdgcn_sched_group_barrier(0x008, NM - ND, 0);
}

__device__ __forceinline__ u32x4 nop4(u32x4 v) { return __builtin_elementwise_min(v, (u32x4){POISON - 1, POISON - 1, POISON - 1, POISON - 1}); }
template <int NP> __device__ __forceinline__ bool pieces_ok(lds_cptr base) {
  const int lane = threadIdx.x & 63;
  unsigned m = 0;
#pragma unroll
  for (int i = 0; i < NP; ++i) { const u32x4 v = *(const LAS u32x4*)(base + i * 1024 + lane * 16); const unsigned a = v[0] > v[1] ? v[0] : v[1], b = v[2] > v[3] ? v[2] : v[3]; m = m > a ? m : a; m = m > b ? m : b; }
  return !__any(m == POISON);
}
template <int NP> struct Chk {
  u32x4 v[NP];
  __device__ __forceinline__ void read(lds_cptr base) {
    const int lane = threadIdx.x & 63;
#pragma unroll
    for (int i = 0; i < NP; ++i) v[i] = *(const LAS u32x4*)(base + i * 1024 + lane * 16);
  }
  __device__ __forceinline__ bool ok() const {
    unsigned m = 0;
#pragma unroll
    for (int i = 0; i < NP; ++i) { const unsigned a = v[i][0] > v[i][1] ? v[i][0] : v[i][1], b = v[i][2] > v[i][3] ? v[i][2] : v[i][3]; m = m > a ? m : a; m = m > b ? m : b; }
    return !__any(m == POISON);
  }
};
struct Spin {
  unsigned n; unsigned long long t0;
  __device__ __forceinline__ Spin() : n(0), t0(__builtin_amdgcn_s_memrealtime()) {}
  __device__ __forceinline__ bool expired(char* ws) {
    if ((++n & 63u) == 0u && __builtin_amdgcn_s_memrealtime() - t0 > 20000000ull) {
      if ((threadIdx.x & 63) == 0) __hip_atomic_store((gu32*)(ws + WS_FLAG + FL_TMO), 1u, RLX_AGENT);
      return true;
    }
    return false;
  }
};

__device__ __forceinline__ void publish(char* ws, unsigned flag_off, unsigned tag) {
  if (threadIdx.x == 0) __hip_atomic_store((gu32*)(ws + WS_FLAG + flag_off), tag, RLX_AGENT);
}
template <int NW>
__device__ __forceinline__ void poll(char* ws, unsigned flag_off, unsigned tag) {
  const int lane = threadIdx.x & 63;
  gu32* f = (gu32*)(ws + WS_FLAG + flag_off);
  const unsigned long long t0 = __builtin_amdgcn_s_memrealtime();
  for (unsigned spins = 1;; ++spins) {
    bool ok = true;
    if (NW == 256) {
#pragma unroll
      for (int i = 0; i < 4; ++i) ok &= __hip_atomic_load(f + i * 64 + lane, RLX_AGENT) == tag;
    } else ok = __hip_atomic_load(f + (lane < NW ? lane : 0), RLX_AGENT) == tag;
    if (__all(ok)) break;
    if ((spins & 63u) == 0u && __builtin_amdgcn_s_memrealtime() - t0 > 20000000ull) {
      if (lane == 0) __hip_atomic_store((gu32*)(ws + WS_FLAG + FL_TMO), tag, RLX_AGENT);
      break;
    }
    __builtin_amdgcn_s_sleep(1);
  }
}
template <int NW> __device__ __forceinline__ void await(char* ws, unsigned flag_off, unsigned tag) {
  if (rfl(threadIdx.x >> 6) == 0) poll<NW>(ws, flag_off, tag);
  raw_bar();
}

struct Relayout {
  const char* src; int pitch, dstride, nt; unsigned dst;
  __device__ __forceinline__ void decode(const Params& p, int J) {
    if (J >= 1176) J = 0;
    if (J < 128) { const int ks = J >> 3, cg = J & 7; src = p.wo + ((size_t)ks * 32 * DM + cg * 64) * 2; pitch = DM; dst = WS_WOF + (unsigned)(cg * 4 * 16 + ks) * 1024; dstride = 16 * 1024; nt = 4; }
    else if (J < 472) { const int ks = (J - 128) >> 3, cg = (J - 128) & 7; src = p.wd + ((size_t)ks * 32 * DM + cg * 64) * 2; pitch = DM; dst = WS_WDF + (unsigned)(cg * 4 * 43 + ks) * 1024; dstride = 43 * 1024; nt = 4; }
    else { const int r = J - 472, mat = r >= 352, r2 = r - mat * 352, ks = r2 / 22, cg = r2 - ks * 22;
      src = (mat ? p.wu : p.wg) + ((size_t)ks * 32 * FF + cg * 64) * 2; pitch = FF; dst = WS_WGF + (unsigned)((mat * 86 + cg * 4) * 16 + ks) * 1024; dstride = 16 * 1024; nt = cg == 21 ? 2 : 4; }
  }
  __device__ __forceinline__ void load(unsigned lds) const {
    const int lane = threadIdx.x & 63, r = lane >> 3, c = lane & 7;
#pragma unroll
    for (int kg = 0; kg < 4; ++kg) {
      int j = (c >> 1) ^ (((r >> 1) & 1) | ((kg & 1) << 1)); j = j < nt ? j : j - 2;
      glds16<false>(src, (unsigned)((kg * 8 + r) * pitch + j * 16 + (c & 1) * 8) * 2u, lds + kg * 1024);
    }
  }
  __device__ __forceinline__ void store(rsrc_t ws, lds_cptr L) const {
    const int lane = threadIdx.x & 63, g = lane >> 4, q = (lane >> 2) & 3, pp = lane & 3, ff = ((q >> 1) & 1) | ((g & 1) << 1);
#pragma unroll
    for (int j = 0; j < 4; ++j)
      if (j < nt) st16(ws, dst + j * dstride + lane * 16, __builtin_bit_cast(u32x4, ldtr2(L + g * 1024 + q * 128 + ((j ^ ff) << 5) + pp * 8, 512)));
  }
};

struct F1 { bf16x8 a[4], w[3]; };
__device__ __forceinline__ void p1_qkv(const Params& p, rsrc_t ws, int b, int rb1, int cb1, char* smem TSARG) {
  const int tid = threadIdx.x, lane = tid & 63, wid = rfl(tid >> 6);
  const int r16 = lane & 15, g = lane >> 4;
  const unsigned lds0 = (unsigned)(size_t)(lds_ptr)smem;
  lds_cptr L = (lds_cptr)smem;
  constexpr int STG = 32768, ND = 4, NS = 8, RL = ND * STG, MS = RL + 5 * 4096;
  LAS f32x2* ms = (LAS f32x2*)(smem + MS);
  const int rb = rb1, cb = cb1;
  if (wid >= 4) {
    const int lw = wid - 4;
    const unsigned voffA = (unsigned)((16 * lw + (lane >> 3)) * DM + (((lane & 7) ^ (lane >> 3)) * 8)) * 2u;
    unsigned voffB[2];
#pragma unroll
    for (int i = 0; i < 2; ++i) { const int r = lane >> 3, c = lane & 7, ff = ((r >> 1) & 1) | (i << 1), j = (c >> 1) ^ ff;
      voffB[i] = (unsigned)((16 * lw + 8 * i + r) * (3 * DM) + j * 16 + (c & 1) * 8) * 2u; }
    const char* xa = p.x + (size_t)rb * 64 * DM * 2;
    const char* wb = p.wqkv + (size_t)cb * 64 * 2;
    const int rot = ((b >> 3) & 7) + 2 * (b >> 6);
    auto issue = [&](int s) {
      const unsigned d = lds0 + (s % ND) * STG;
      const int kc = (s + rot) & 7;
#pragma unroll
      for (int i = 0; i < 2; ++i) glds16<false>(xa + kc * 128 + i * 8 * DM * 2, voffA, d + (2 * lw + i) * 1024);
#pragma unroll
      for (int i = 0; i < 2; ++i) glds<3, false>(wb + (size_t)kc * 64 * (3 * DM) * 2, voffB[i], d + 8192 + (2 * lw + i) * 3072);
    };
    TS(20);
    issue(0); issue(1);
    TS(21);
    wait_vm<8>(); raw_bar();
    TS(23);
    issue(2);
    const int slot2 = lw == 0 ? 4 : lw;
    Relayout ra, rb2; ra.decode(p, b * 5 + lw); rb2.decode(p, b * 5 + slot2);
#pragma unroll
    for (int j = 1; j < NS; ++j) {
      if (j + 2 < NS) issue(j + 2);
      if (j == 5) { ra.load(lds0 + RL + lw * 4096); rb2.load(lds0 + RL + slot2 * 4096); }
      if (j <= 4) wait_bar<16>(); else if (j == 5) wait_bar<24>(); else if (j == 6) wait_bar<16>(); else wait_bar<8>();
    }
    lds_bar();
    wait_vm<0>();
    if (b * 5 + lw < 1176) ra.store(ws, L + RL + lw * 4096);
    if (lw == 0 && b * 5 + 4 < 1176) rb2.store(ws, L + RL + 4 * 4096);
  } else {
    f32x4 acc[3][4], cs[3], sx = Z4, sq = Z4;
    int aoff[2][4];
#pragma unroll
    for (int ks = 0; ks < 2; ++ks)
#pragma unroll
      for (int i = 0; i < 4; ++i) aoff[ks][i] = ((wid + i) & 3) * 2048 + (r16 >> 3) * 1024 + (r16 & 7) * 128 + (((4 * ks + g) ^ (r16 & 7)) << 4);
    int boff;
    { const int q = (lane >> 2) & 3, pp = lane & 3, ff = ((q >> 1) & 1) | ((g & 1) << 1);
      boff = 8192 + g * 3072 + q * 128 + ((wid ^ ff) << 5) + pp * 8; }
#pragma unroll
    for (int i = 0; i < 3; ++i) { cs[i] = Z4;
#pragma unroll
      for (int j = 0; j < 4; ++j) acc[i][j] = Z4; }
    const bf16x8 one = ones8();
    F1 fa, fb;
    auto rd = [&](F1& fr, int s, int ks) {
      lds_cptr B = L + (s % ND) * STG;
#pragma unroll
      for (int seg = 0; seg < 3; ++seg) fr.w[seg] = ldtr2(B + boff + ks * 12288 + seg * 1024, 512);
#pragma unroll
      for (int i = 0; i < 4; ++i) fr.a[i] = ld128(B + aoff[ks][i]);
    };
    auto mm = [&](const F1& fr) {
#pragma unroll
      for (int i = 0; i < 4; ++i) {
        acc[0][i] = MFMA16(fr.w[0], fr.a[i], acc[0][i]);
        acc[1][i] = MFMA16(fr.w[1], fr.a[i], acc[1][i]);
        acc[2][i] = MFMA16(fr.a[i], fr.w[2], acc[2][i]);
      }
      cs[0] = MFMA16(fr.w[0], one, cs[0]); cs[1] = MFMA16(fr.w[1], one, cs[1]); cs[2] = MFMA16(one, fr.w[2], cs[2]);
      sx = MFMA16(one, fr.a[0], sx); sq = MFMA16(fr.a[0], fr.a[0], sq);
    };
    lds_bar();
    TS(1);
    rd(fa, 0, 0); SB();
#pragma unroll
    for (int s = 0; s < NS; ++s) {
      rd(fb, s, 1); mm(fa); weave<10, 17>(); SB();
      if (s + 1 < NS) { lds_bar(); rd(fa, s + 1, 0); mm(fb); weave<10, 17>(); SB(); }
      else { mm(fb); SB(); }
    }
    TS(2);
    if ((r16 >> 2) == g) {
      const int e = r16 & 3; const float s2 = e == 0 ? sq[0] : e == 1 ? sq[1] : e == 2 ? sq[2] : sq[3];
      float mean, rstd; ln_consts(sx[0], s2, mean, rstd);
      ms[wid * 16 + r16] = (f32x2){mean, rstd};
    }
    lds_bar();
    TS(25);
    const int h = cb * 4 + wid;
    u32x2 qv[4], kv[4], vv[4];
    float kmx = 0.f;
#pragma unroll
    for (int i = 0; i < 4; ++i) {
      const int rt = (wid + i) & 3;
      const f32x2 st = ms[rt * 16 + r16];
      qv[i] = cvt4((acc[0][i] - st.x * cs[0]) * (st.y * QSCALE));
      { const f32x4 v = (acc[1][i] - st.x * cs[1]) * st.y; kv[i] = cvt4(v);
        kmx = fmaxf(kmx, sum_x32(sum_x16((v[0] * v[0] + v[1] * v[1]) + (v[2] * v[2] + v[3] * v[3])))); }
      { const f32x4 m01 = *(LAS f32x4*)(ms + rt * 16 + 4 * g), m23 = *(LAS f32x4*)(ms + rt * 16 + 4 * g + 2); const float cv = cs[2][0];
        vv[i] = (u32x2){cvtpk((acc[2][i][0] - m01[0] * cv) * m01[1], (acc[2][i][1] - m01[2] * cv) * m01[3]),
                        cvtpk((acc[2][i][2] - m23[0] * cv) * m23[1], (acc[2][i][3] - m23[2] * cv) * m23[3])}; }
    }
#pragma unroll
    for (int i = 0; i < 4; i += 2) {
      { const int rt = (wid + i + (g & 1)) & 3, grow = rb * 64 + rt * 16;
        const unsigned off = (((unsigned)h * 64 + (grow >> 5)) * 2 + (g >> 1)) * 512 + ((grow & 31) + r16) * 16;
        st16(ws, WS_K + off, pair16(kv[i], kv[i + 1])); st16(ws, WS_Q + off, pair16(qv[i], qv[i + 1])); }
      { const int rt = (wid + i + (g >> 1)) & 3, grow = rb * 64 + rt * 16;
        const unsigned off = ((((unsigned)h * 64 + (grow >> 5)) * 2 + (rt & 1)) * 2 + (g & 1)) * 256 + r16 * 16;
        st16(ws, WS_V + off, pair32(vv[i], vv[i + 1])); }
    }
    kmx = max_row16(kmx);
    if (lane == 0) __hip_atomic_store((gu32*)(p.ws + WS_KMAX) + h * 32 + rb, __float_as_uint(kmx), RLX_AGENT);
    TS(26);
  }
}

constexpr int A_K = 0, A_V = 65536, A_ONE = 131072, A_Q = A_ONE + 5120, A_KM = A_Q + 8192, A_DUMMY = A_KM + 256, A_BYTES = A_DUMMY + 256;
constexpr float SAFE_BOUND = 48.f;
constexpr int PRIO_SWITCH = 12;

struct P3Pre { bf16x8 wof[16]; u32x2 xv[4]; };
__device__ __forceinline__ void p2_attn(const Params& p, rsrc_t ws, int h, int qr, int rb, int cb, int mb, int fb, char* smem, unsigned tag, P3Pre& pre TSARG) {
  const int tid = threadIdx.x, lane = tid & 63, wid = rfl(tid >> 6);
  const int r32 = lane & 31, hh = lane >> 5;
  const unsigned lds0 = (unsigned)(size_t)(lds_ptr)smem;
  lds_cptr L = (lds_cptr)smem;
  const char* kg = p.ws + WS_K + (size_t)h * 65536;
  const char* vg = p.ws + WS_V + (size_t)h * 65536;
  const unsigned vo = lane * 16;
  const int u0 = fb < 11 ? 3 * fb : 33 + 2 * (fb - 11), nu = fb < 11 ? 3 : 2;
  glds16<true>(p.ws + WS_Q + ((size_t)h * 64 + qr * 8 + wid) * 1024, vo, lds0 + A_Q + wid * 1024);
  glds4<true>(p.ws + WS_KMAX + h * 128, (lane & 31) * 4, lds0 + A_KM);
#pragma unroll
  for (int i = 0; i < 2; ++i) {
    const int t = wid + 8 * i, gt = (t + 8 * qr) & 63;
    glds16<true>(kg + gt * 1024, vo, lds0 + A_K + t * 1024);
    glds16<true>(vg + gt * 1024, vo, lds0 + A_V + t * 1024);
  }
  st16(ws, WS_O + (unsigned)((qr * 16 + 2 * wid + (lane >> 5)) * 16 + (h >> 1)) * 1024 + (h & 1) * 512 + (lane & 31) * 16, (u32x4){POISON, POISON, POISON, POISON});
  { const unsigned o1 = (tid & 31) < 2 ? 0x3F803F80u : 0u;
    *(LAS u32x2*)(smem + A_ONE + tid * 8) = (u32x2){o1, o1};
    if (tid < 128) *(LAS u32x2*)(smem + A_ONE + 4096 + tid * 8) = (u32x2){o1, o1}; }
  if (wid == 0 && blockIdx.x == 0) {
    poll<256>(p.ws, FL_1, tag);
    if (lane == 0) __hip_atomic_store((gu32*)(p.ws + WS_FLAG + FL_EPOCH), tag, RLX_AGENT);
  }
  wait_bar<1>();
  TS(4);
  const bf16x8 qf = ld128(L + A_Q + wid * 1024 + lane * 16);
  float c;
  { float km = *(const LAS float*)(L + A_KM + lane * 4);
    km = max_x16(max_row16(km));
    float q2 = 0.f;
#pragma unroll
    for (int j = 0; j < 8; ++j) { const float v = __uint_as_float((unsigned)(unsigned short)qf[j] << 16); q2 += v * v; }
    q2 = sum_x32(q2);
    c = sqrtf(q2 * km) * 1.02f + 0.01f; }
  const bool fast = __all(c <= SAFE_BOUND);
  const bool vlane = r32 < 16;
  int vaddr = (int)lds0 + (vlane ? A_V : A_ONE) + hh * 256 + (r32 & 15) * 16;
  const int vstep = vlane ? 4096 : 0;
  int kaddr = (int)lds0 + A_K + lane * 16;
  f32x16 o;
#pragma unroll
  for (int i = 0; i < 16; ++i) o[i] = 0.f;
  unsigned go = vo + ((16 + wid + 8 * qr) & 63) * 1024;
  unsigned ld = lds0 + A_K + (16 + wid) * 1024;
  auto between = [&]() {
    publish(p.ws, FL_P + blockIdx.x * 4, tag);
#pragma unroll
    for (int i = 0; i < 2; ++i) {
      const int ln = ((blockIdx.x >> 3) & 1) * 768 + wid * 96 + i * 32 + lane, ch = ln >> 7, jt = ch >= 6 ? ch - 6 : ch;
      glds4<false>(p.ws + WS_WGF, (unsigned)(((ch >= 6) * 86 + u0 * 2 + (jt < 2 * nu ? jt : jt - 2)) * 128 + (ln & 127)) * 128u, lds0 + A_DUMMY);
    }
  };
  auto midway = [&]() {
    if (wid >= 4) {
      poll<256>(p.ws, FL_P, tag);
      const int r16 = lane & 15, g = lane >> 4, wn = wid & 3;
#pragma unroll
      for (int i = 0; i < 16; ++i) pre.wof[i] = ldw16(ws, WS_WOF + ((unsigned)(cb * 4 + wn) * 16 + ((i + 8 * cb) & 15)) * 1024 + lane * 16);
#pragma unroll
      for (int i = 0; i < 4; ++i) pre.xv[i] = *(const u32x2*)(p.x + ((size_t)(rb * 64 + i * 16 + r16) * DM + cb * 64 + wn * 16 + 4 * g) * 2);
    }
  };
  if (fast) {
    f32x16 negc, sa, sb; f32x8 pp, kk; f32x16 vv;
#pragma unroll
    for (int i = 0; i < 16; ++i) negc[i] = -c;
    int cnt, keep;
    asm volatile(ATTN_ASM_A
                 : [o] "+v"(o), [ka] "+v"(kaddr), [va] "+v"(vaddr), [go] "+v"(go), [ld] "+s"(ld), [keep] "=&s"(keep),
                   "=&{v[128:143]}"(sa), "=&{v[144:159]}"(sb), "=&{v[160:167]}"(pp), "=&{v[168:175]}"(kk), "=&{v[176:191]}"(vv)
                 : [q] "v"(qf), [nc] "v"(negc), [vs] "v"(vstep), [kg] "s"(kg), [vg] "s"(vg)
                 : "memory", "scc");
    between();
    const int sw = wid < 4 ? PRIO_SWITCH : 99;
#define ATTN_B(C0, C1) asm volatile(ATTN_ASM_B \
                 : [o] "+v"(o), [ka] "+v"(kaddr), [va] "+v"(vaddr), [cnt] "=&s"(cnt), \
                   "+{v[128:143]}"(sa), "+{v[144:159]}"(sb), "+{v[160:167]}"(pp), "+{v[168:175]}"(kk), "+{v[176:191]}"(vv) \
                 : [q] "v"(qf), [nc] "v"(negc), [vs] "v"(vstep), [sw] "s"(sw), [c0] "s"(C0), [c1] "s"(C1) \
                 : "memory", "scc")
    ATTN_B(2, 10);
    midway();
    ATTN_B(10, 16);
#undef ATTN_B
  } else {
#pragma unroll
    for (int j = 0; j < 6; ++j) { const unsigned gj = (go + j * 8192) & 0xffffu; glds16<true>(kg, gj, ld + j * 8192); glds16<true>(vg, gj, ld + 65536 + j * 8192); }
    float m = -INFINITY;
    vaddr -= (int)lds0; kaddr -= (int)lds0;
#pragma unroll 1
    for (int t4 = 0; t4 < 16; ++t4) {
      if (t4 == 2) { wait_bar<0>(); between(); }
      if (t4 == 10) midway();
#pragma unroll 1
      for (int u = 0; u < 4; ++u) {
        const int t = t4 * 4 + u;
        f32x16 z;
#pragma unroll
        for (int i = 0; i < 16; ++i) z[i] = 0.f;
        const f32x16 s = MFMA32(ld128(L + kaddr + t * 1024), qf, z);
        float mx = s[0];
#pragma unroll
        for (int i = 1; i < 16; ++i) mx = fmaxf(mx, s[i]);
        mx = fmaxf(mx, __shfl_xor(mx, 32));
        const float mn = fmaxf(m, mx);
        const float al = __builtin_amdgcn_exp2f(m - mn);
        m = mn;
#pragma unroll
        for (int i = 0; i < 16; ++i) o[i] *= al;
        u32x4 p0, p1;
#pragma unroll
        for (int i = 0; i < 4; ++i) {
          p0[i] = cvtpk(__builtin_amdgcn_exp2f(s[2 * i] - mn), __builtin_amdgcn_exp2f(s[2 * i + 1] - mn));
          p1[i] = cvtpk(__builtin_amdgcn_exp2f(s[8 + 2 * i] - mn), __builtin_amdgcn_exp2f(s[8 + 2 * i + 1] - mn));
        }
        const bf16x8 v0 = ld128(L + vaddr + u * 1024), v1 = ld128(L + vaddr + u * 1024 + 512);
        o = MFMA32(v0, __builtin_bit_cast(bf16x8, p0), o);
        o = MFMA32(v1, __builtin_bit_cast(bf16x8, p1), o);
      }
      vaddr += vstep;
    }
  }
  TS(5);
  const float inv = __builtin_amdgcn_rcpf(lo_x32(o[8]));
  const u32x4 ov = pair32((u32x2){cvtpk(o[0] * inv, o[1] * inv), cvtpk(o[2] * inv, o[3] * inv)}, (u32x2){cvtpk(o[4] * inv, o[5] * inv), cvtpk(o[6] * inv, o[7] * inv)});
  const int row = qr * 256 + wid * 32 + r32;
  st16(ws, WS_O + ((((unsigned)(row >> 4) * 16 + (h >> 1)) * 4 + (h & 1) * 2 + hh) * 16 + (row & 15)) * 16, nop4(ov));
}

constexpr int G64_KPS = 8, G64_STG = 4 * G64_KPS * 1024;
template <int NKS, int ND, int LA, int NPRE, class Pre>
__device__ __forceinline__ void gemm64_load(char* ws, const char* ag  , int rot, char* smem, const Pre& pre) {
  const int lane = threadIdx.x & 63, wid = rfl(threadIdx.x >> 6);
  const unsigned lds0 = (unsigned)(size_t)(lds_ptr)smem;
  lds_cptr L = (lds_cptr)smem + wid * 8192;
  constexpr int NS = (NKS + 7) / 8, LAST = NKS - 8 * (NS - 1), NFULL = NKS / 8;
  static_assert(LAST == 8 || LAST == 3, "last stage");
  auto issue = [&](int s) {
    const unsigned d = lds0 + (s % ND) * G64_STG + wid * 8192;
    if (s < NFULL) { const int q0 = s + rot, q = q0 >= NFULL ? q0 - NFULL : q0; glds<4, true>(ag + q * 8192, lane * 16, d); glds<4, true>(ag + q * 8192 + 4096, lane * 16, d + 4096); }
    else glds<3, true>(ag + s * 8192, lane * 16, d);
  };
  auto okay = [&](int s) { return s < NFULL ? pieces_ok<8>(L + (s % ND) * G64_STG) : pieces_ok<3>(L + (s % ND) * G64_STG); };
  auto ensure = [&](int s) { if (!okay(s)) { Spin sp; do { issue(s); wait_vm<0>(); } while (!okay(s) && !sp.expired(ws)); } };
  auto waitv = [&](int j) {
    const int hi = j + LA - 2 < NS - 1 ? j + LA - 2 : NS - 1;
    const int n = (hi - j) * 8 - ((hi == NS - 1 && hi > j) ? 8 - LAST : 0);
    switch (n) { case 3: wait_vm<3>(); break; case 8: wait_vm<8>(); break; case 11: wait_vm<11>(); break; case 16: wait_vm<16>(); break; default: wait_vm<0>(); break; }
  };
  issue(0); pre(); wait_vm<NPRE>(); ensure(0);
#pragma unroll
  for (int s = 1; s < LA; ++s) if (s < NS) issue(s);
  lds_bar();
#pragma unroll
  for (int j = 1; j < NS; ++j) {
    waitv(j);
    if (j < NFULL) {
      Chk<8> c; c.read(L + (j % ND) * G64_STG);
      if (j + LA - 1 < NS) issue(j + LA - 1);
      if (!c.ok()) ensure(j);
    } else ensure(j);
    lds_bar();
  }
}
template <int NKS, int ND, int TS0>
__device__ __forceinline__ void gemm64_mma(const bf16x8 (&wf)[NKS], char* smem, f32x4 (&acc)[4] TSARG) {
  const int lane = threadIdx.x & 63;
  lds_cptr L = (lds_cptr)smem + lane * 16;
#pragma unroll
  for (int i = 0; i < 4; ++i) acc[i] = Z4;
  bf16x8 f[2][4];
  auto rd = [&](int ks) {
    lds_cptr B = L + ((ks >> 3) % ND) * G64_STG + (ks & 7) * 1024;
#pragma unroll
    for (int rt = 0; rt < 4; ++rt) f[ks & 1][rt] = ld128(B + rt * 8192);
  };
  lds_bar();
  TS(TS0);
  rd(0); SB();
#pragma unroll
  for (int ks = 0; ks < NKS; ++ks) {
    if (ks + 1 < NKS) {
      if (((ks + 1) & 7) == 0) lds_bar();
      rd(ks + 1);
    }
#pragma unroll
    for (int rt = 0; rt < 4; ++rt) acc[rt] = MFMA16(wf[ks], f[ks & 1][rt], acc[rt]);
    if (ks + 1 < NKS) weave<4, 4>();
    SB();
  }
  TS(TS0 + 1);
}

constexpr int G_B = 0, G_BS = 24576, G_A = 3 * G_BS, G_AS = 16384, G_ST = G_A + 4 * G_AS, G_MS = G_ST + 8192, G_CS = G_MS + 1024;
struct WLoad {
  const char* src[3]; unsigned dst; int rot;
  __device__ __forceinline__ void init(const Params& p, int mb, int fb, int lw, unsigned lds0) {
    const int u0 = fb < 11 ? 3 * fb : 33 + 2 * (fb - 11), nu = fb < 11 ? 3 : 2;
    rot = (fb + 4 * (mb & 1)) & 7;
#pragma unroll
    for (int i = 0; i < 3; ++i) { const int jt = 3 * (lw & 1) + i; src[i] = p.ws + WS_WGF + (size_t)((lw >> 1) * 86 + u0 * 2 + (jt < 2 * nu ? jt : jt - 2)) * 16384; }
    dst = lds0 + G_B + lw * 6144;
  }
  __device__ __forceinline__ void issue(int s, int lane) const {
#pragma unroll
    for (int i = 0; i < 3; ++i) glds<2, true>(src[i] + ((s + rot) & 7) * 2048, lane * 16, dst + (s % 3) * G_BS + i * 2048);
  }
};
struct F4 { bf16x8 a[4], w[2][3]; };
__device__ __forceinline__ void p4_gateup(const Params& p, rsrc_t ws, int mb, int fb, char* smem, const WLoad& wl, unsigned tag, int wct, int rot5, bf16x8 (&wdf)[43] TSARG) {
  const int tid = threadIdx.x, lane = tid & 63, wid = rfl(tid >> 6);
  const int r16 = lane & 15, g = lane >> 4;
  const unsigned lds0 = (unsigned)(size_t)(lds_ptr)smem;
  lds_cptr L = (lds_cptr)smem;
  constexpr int NS = 8;
  const int u0 = fb < 11 ? 3 * fb : 33 + 2 * (fb - 11), nu = fb < 11 ? 3 : 2;
  if (wid >= 4) {
    const int lw = wid - 4;
    const char* ag = p.ws + WS_H + (size_t)(mb * 8 + 2 * lw) * 16384;
    auto issueA = [&](int s) {
      const unsigned d = lds0 + G_A + (s & 3) * G_AS + lw * 4096;
#pragma unroll
      for (int i = 0; i < 2; ++i) glds<2, true>(ag + i * 16384 + ((s + wl.rot) & 7) * 2048, lane * 16, d + i * 2048);
    };
    auto issueS = [&]() {
      glds<2, true>(p.ws + WS_HST + (size_t)mb * 8192 + lw * 2048, lane * 16, lds0 + G_ST + lw * 2048);
    };
    auto ensure = [&](int s) {
      lds_cptr A = L + G_A + (s & 3) * G_AS + lw * 4096;
      if (!pieces_ok<4>(A)) { Spin sp; do { issueA(s); wait_vm<0>(); } while (!pieces_ok<4>(A) && !sp.expired(p.ws)); }
    };
    float cs[3] = {0.f, 0.f, 0.f};
    const unsigned one_bits = 0x3F803F80u;
    const bf16x2_t one2 = __builtin_bit_cast(bf16x2_t, one_bits);
    auto colsum = [&](int s) {
#pragma unroll
      for (int i = 0; i < 6; ++i) {
        const u32x4 v = *(const LAS u32x4*)(L + G_B + (s % 3) * G_BS + lw * 6144 + i * 1024 + lane * 16);
#pragma unroll
        for (int e = 0; e < 4; ++e) { const unsigned t = v[e]; cs[i >> 1] = __builtin_amdgcn_fdot2_f32_bf16(__builtin_bit_cast(bf16x2_t, t), one2, cs[i >> 1], false); }
      }
    };
    issueA(0);
    colsum(0); colsum(1);
    wait_vm<0>(); ensure(0);
    issueA(1); issueS(); issueA(2); wl.issue(2, lane);
    lds_bar();
#pragma unroll
    for (int j = 1; j < NS; ++j) {
      if (j == 1) wait_vm<12>(); else if (j + 1 < NS) wait_vm<10>(); else wait_vm<0>();
      Chk<4> c; c.read(L + G_A + (j & 3) * G_AS + lw * 4096);
      if (j + 2 < NS) issueA(j + 2);
      if (!c.ok()) ensure(j);
      if (j == 2) {
        lds_cptr Sg = L + G_ST + lw * 2048;
        if (!pieces_ok<2>(Sg)) { Spin sp; do { issueS(); wait_vm<0>(); } while (!pieces_ok<2>(Sg) && !sp.expired(p.ws)); }
      }
      if (j >= 2) colsum(j);
      if (j == 5 && lw == 0) poll<256>(p.ws, FL_R, tag);
      lds_bar();
      if (j + 2 < NS) wl.issue(j + 2, lane);
      if (j == 2) {
        const int row = tid - 256;
        if (row < 128) {
          float a = 0.f, bb = 0.f;
#pragma unroll
          for (int c = 0; c < 8; ++c) { const f32x2 v = *(const LAS f32x2*)(L + G_ST + (row >> 6) * 4096 + c * 512 + (row & 63) * 8); a += v.x; bb += v.y; }
          float mean, rstd; ln_consts(a, bb, mean, rstd);
          *(LAS f32x2*)(smem + G_MS + row * 8) = (f32x2){rstd, -mean * rstd};
        }
      }
    }
#pragma unroll
    for (int i = 0; i < 3; ++i) { const float t = sum_x32(sum_x16(cs[i])); if (g == 0) *(LAS float*)(smem + G_CS + ((lw * 3 + i) * 16 + r16) * 4) = t; }
    lds_bar();
  } else {
    const int wm = wid >> 1, wn = wid & 1;
    const int aoff = G_A + wm * 8192 + lane * 16;
    constexpr int m0 = 0, m1 = 12288;
    const int boff = G_B + wn * 6144 + lane * 16;
    f32x4 acc[2][3][4];
#pragma unroll
    for (int i = 0; i < 3; ++i)
#pragma unroll
      for (int j = 0; j < 4; ++j) { acc[0][i][j] = Z4; acc[1][i][j] = Z4; }
    F4 fa, fb2;
    auto rd = [&](F4& f, int s, int ks) {
      lds_cptr B = L + (s % 3) * G_BS, A = L + (s & 3) * G_AS;
#pragma unroll
      for (int i = 0; i < 4; ++i) f.a[i] = ld128(A + aoff + i * 2048 + ks * 1024);
#pragma unroll
      for (int i = 0; i < 3; ++i) { f.w[0][i] = ld128(B + boff + m0 + i * 2048 + ks * 1024); f.w[1][i] = ld128(B + boff + m1 + i * 2048 + ks * 1024); }
    };
    auto mm = [&](const F4& f) {
#pragma unroll
      for (int m = 0; m < 2; ++m)
#pragma unroll
        for (int i = 0; i < 3; ++i)
#pragma unroll
          for (int j = 0; j < 4; ++j) acc[m][i][j] = MFMA16(f.w[m][i], f.a[j], acc[m][i][j]);
    };
    lds_bar();
    TS(11);
    rd(fa, 0, 0); SB();
#pragma unroll
    for (int s = 0; s < NS; ++s) {
      rd(fb2, s, 1); mm(fa); weave<10, 24>(); SB();
      if (s + 1 < NS) { lds_bar(); rd(fa, s + 1, 0); mm(fb2); weave<10, 24>(); SB(); }
      else { mm(fb2); SB(); }
    }
    TS(12);
    const LAS float* csl = (const LAS float*)(smem + G_CS);
    lds_bar();
    TS(33);
    const LAS f32x2* ms = (const LAS f32x2*)(smem + G_MS);
    f32x2 st[4];
#pragma unroll
    for (int j = 0; j < 4; ++j) st[j] = ms[wm * 64 + j * 16 + r16];
    constexpr float K1 = -2.885390081777927f * 0.7978845608028654f, K2 = -2.885390081777927f * 0.035677408136300125f;
#pragma unroll
    for (int i = 0; i < 3; ++i) {
      const int jt = 3 * wn + i, u = jt >> 1, jj = jt & 1;
      const f32x4 cg = *(const LAS f32x4*)(csl + jt * 16 + 4 * g), cu = *(const LAS f32x4*)(csl + (6 + jt) * 16 + 4 * g);
      u32x2 av[4];
#pragma unroll
      for (int j = 0; j < 4; ++j) {
        const f32x4 ag2 = acc[0][i][j], au = acc[1][i][j];
        const f32x4 ga = ag2 * st[j].x + st[j].y * cg, up = au * st[j].x + st[j].y * cu;
        const f32x4 z = ga * (ga * ga * K2 + K1);
        const f32x4 d = (f32x4){__builtin_amdgcn_exp2f(z[0]), __builtin_amdgcn_exp2f(z[1]), __builtin_amdgcn_exp2f(z[2]), __builtin_amdgcn_exp2f(z[3])} + 1.f;
        const f32x4 rc = (f32x4){__builtin_amdgcn_rcpf(d[0]), __builtin_amdgcn_rcpf(d[1]), __builtin_amdgcn_rcpf(d[2]), __builtin_amdgcn_rcpf(d[3])};
        av[j] = cvt4((ga * rc) * up);
      }
      if (u < nu) {
#pragma unroll
        for (int j = 0; j < 4; j += 2) {
          const unsigned rt = (unsigned)mb * 8 + wm * 4 + j + (g & 1);
          st16(ws, WS_ACT + (((rt * 43 + (u0 + u)) * 4 + jj * 2 + (g >> 1)) * 16 + r16) * 16, nop4(pair16(av[j], av[j + 1])));
        }
      }
    }
    TS(34);
  }
  if (wid >= 4) {
#pragma unroll
    for (int i = 0; i < 43; ++i) { int ks = i; if (i < 40) { ks += rot5; ks = ks >= 40 ? ks - 40 : ks; } wdf[i] = ldw16(ws, WS_WDF + ((unsigned)wct * 43 + ks) * 1024 + lane * 16); }
  }
}

extern __shared__ __attribute__((aligned(16))) char smem_dyn[];

__global__ __launch_bounds__(512, 2) void mega(Params p) {
  char* smem = smem_dyn;
  const int b = blockIdx.x, x = b & 7, i5 = b >> 3;
  const int tid = threadIdx.x, lane = tid & 63, wid = rfl(tid >> 6);
  const int r16 = lane & 15, g = lane >> 4, wn = wid & 3;
  TS_BEGIN();
  const rsrc_t ws = __builtin_amdgcn_make_buffer_rsrc(p.ws, 0, WS_USED, 0x00020000);
  const unsigned epoch = __hip_atomic_load((gu32*)(p.ws + WS_FLAG + FL_EPOCH), RLX_AGENT);

  const int rb1 = (x >> 1) * 8 + (i5 & 7), cb1 = (x & 1) * 4 + (i5 >> 3);
  p1_qkv(p, ws, b, rb1, cb1, smem TSPASS);
  const unsigned tag = (unsigned)rfl((int)(epoch * 0x9E3779B1u + 0x7F4A7C15u));
  if (wid < 4) wait_bar<0>(); else raw_bar();
  publish(p.ws, FL_1 + (cb1 * 32 + rb1) * 4, tag);
  TS(3);

  const int h = x * 4 + (i5 & 3), qr = i5 >> 2;
  const int rb = x * 4 + (i5 & 3), cb = i5 >> 2;
  const int mb = x * 2 + (i5 & 1), fb = i5 >> 1;
  await<32>(p.ws, FL_1 + (h >> 2) * 128, tag);
  TS(27);
  P3Pre pre;
  p2_attn(p, ws, h, qr, rb, cb, mb, fb, smem, tag, pre TSPASS);
  if (tid == 64) *(LAS unsigned*)(smem + G_CS + 1024) = 0u;
  lds_bar();
  TS(6);

  const int wct = cb * 4 + wn, col = cb * 64 + wn * 16 + 4 * g;
  u32x2 hv[4];
  if (wid >= 4) {
    { const u32x4 pz = (u32x4){POISON, POISON, POISON, POISON};
#pragma unroll
      for (int i = 0; i < 3; ++i) {
        const int j = wn * 3 + i;
        st16(ws, j < 8 ? WS_H + (unsigned)((rb * 4 + (j >> 1)) * 16 + cb * 2 + (j & 1)) * 1024 + lane * 16 : WS_HST + (unsigned)(rb * 8 + cb) * 512 + (lane & 31) * 16, pz);
      }
      wait_vm<0>();
      if (lane == 0 && __hip_atomic_fetch_add((LAS unsigned*)(smem + G_CS + 1024), 1u, __ATOMIC_RELAXED, __HIP_MEMORY_SCOPE_WORKGROUP) == 3u)
        __hip_atomic_store((gu32*)(p.ws + WS_FLAG + FL_Q) + b, tag, RLX_AGENT); }
#pragma unroll
    for (int i = 0; i < 16; ++i) PIN(pre.wof[i]);
#pragma unroll
    for (int i = 0; i < 4; ++i) PIN(pre.xv[i]);
    f32x4 acc[4];
    gemm64_mma<16, 2, 8>(pre.wof, smem + G_A, acc TSPASS);
    LAS f32x2* S = (LAS f32x2*)(smem + G_ST);
#pragma unroll
    for (int i = 0; i < 4; ++i) hv[i] = cvt4(unpack4(pre.xv[i]) + acc[i]);
#pragma unroll
    for (int i = 0; i < 4; i += 2) {
      const unsigned rt = (unsigned)rb * 4 + i + (g & 1);
      st16(ws, WS_H + (((rt * 16 + cb * 2 + (wn >> 1)) * 4 + (wn & 1) * 2 + (g >> 1)) * 16 + r16) * 16, nop4(pair16(hv[i], hv[i + 1])));
    }
#pragma unroll
    for (int i = 0; i < 4; ++i) {
      const f32x4 v = unpack4(hv[i]);
      const float s1 = sum_x32(sum_x16((v[0] + v[1]) + (v[2] + v[3]))), s2 = sum_x32(sum_x16((v[0] * v[0] + v[1] * v[1]) + (v[2] * v[2] + v[3] * v[3])));
      if (g == 0) S[(i * 16 + r16) * 4 + wn] = (f32x2){s1, s2};
    }
    lds_bar();
    if (wid == 4) {
      const f32x2 a = S[lane * 4], bb = S[lane * 4 + 1], c = S[lane * 4 + 2], d = S[lane * 4 + 3];
      const unsigned s1 = __float_as_uint((a.x + bb.x) + (c.x + d.x)), s2 = __float_as_uint((a.y + bb.y) + (c.y + d.y));
      __hip_atomic_store((gu64*)(p.ws + WS_HST + (size_t)(rb * 8 + cb) * 512 + lane * 8), ((unsigned long long)(s2 < POISON ? s2 : POISON - 1) << 32) | (s1 < POISON ? s1 : POISON - 1), RLX_AGENT);
    }
    TS(32);
    raw_bar();
  } else {
    auto pre3 = [&]() {
      const int u0 = fb < 11 ? 3 * fb : 33 + 2 * (fb - 11), nu = fb < 11 ? 3 : 2;
      WLoad w0; w0.init(p, mb, fb, wid, (unsigned)(size_t)(lds_ptr)smem); w0.issue(0, lane); w0.issue(1, lane);
#pragma unroll
      for (int i = 0; i < 6; ++i) { const int rt = 2 * wid + i / 3, u = i % 3;
        st16(ws, WS_ACT + (unsigned)((mb * 8 + rt) * 43 + u0 + (u < nu ? u : 0)) * 1024 + lane * 16, (u32x4){POISON, POISON, POISON, POISON}); }
    };
    gemm64_load<16, 2, 2, 18>(p.ws, p.ws + WS_O + (size_t)(rb * 4 + wid) * 16384, cb & 1, smem + G_A, pre3);
    if (wid == 0) poll<256>(p.ws, FL_Q, tag);
    lds_bar();
    publish(p.ws, FL_R + b * 4, tag);
    raw_bar();
  }
  TS(10);

  bf16x8 wdf[43];
  { WLoad wl;
    if (wid >= 4) wl.init(p, mb, fb, wid - 4, (unsigned)(size_t)(lds_ptr)smem);
    p4_gateup(p, ws, mb, fb, smem, wl, tag, wct, 8 * (cb % 5), wdf TSPASS); }
  lds_bar();
  TS(13);

  if (wid >= 4) {
#pragma unroll
    for (int i = 0; i < 43; ++i) PIN(wdf[i]);
    f32x4 acc[4];
    gemm64_mma<43, 4, 15>(wdf, smem, acc TSPASS);
#pragma unroll
    for (int i = 0; i < 4; ++i) *(u32x2*)(p.out + ((size_t)(rb * 64 + i * 16 + r16) * DM + col) * 2) = cvt4(unpack4(hv[i]) + acc[i]);
  } else {
    gemm64_load<43, 4, 3, 0>(p.ws, p.ws + WS_ACT + (size_t)(rb * 4 + wid) * 43 * 1024, cb % 5, smem, []() {});
  }
  TS_END();
}

extern "C" void kernel_launch(void* const* d_in, const int* in_sizes, int n_in, void* d_out, int out_size, void* d_ws, size_t ws_size, hipStream_t stream) {
  Params p;
  p.x = (const char*)d_in[0]; p.wqkv = (const char*)d_in[1]; p.wo = (const char*)d_in[2];
  p.wg = (const char*)d_in[4]; p.wu = (const char*)d_in[5]; p.wd = (const char*)d_in[6];
  p.out = (char*)d_out; p.ws = (char*)d_ws;
  constexpr int LDS = 160 * 1024;
  (void)hipFuncSetAttribute((const void*)mega, hipFuncAttributeMaxDynamicSharedMemorySize, LDS);
  mega<<<256, 512, LDS, stream>>>(p);
}
```

```cpp
#include <hip/hip_runtime.h>
#include <stdint.h>
#define ATTN_ASM_A \
  "s_mov_b32 %[keep], m0\n\t" \
  "ds_read_b128 v[168:171], %[ka]\n\t" \
  "ds_read_b128 v[172:175], %[ka] offset:1024\n\t" \
  "ds_read_b128 v[176:179], %[va]\n\t" \
  "ds_read_b128 v[184:187], %[va] offset:512\n\t" \
  "v_mov_b32 v164, 0\n\t" \
  "v_mov_b32 v165, 0\n\t" \
  "v_mov_b32 v166, 0\n\t" \
  "v_mov_b32 v167, 0\n\t" \
  "v_mov_b32 v188, 0\n\t" \
  "v_mov_b32 v189, 0\n\t" \
  "v_mov_b32 v190, 0\n\t" \
  "v_mov_b32 v191, 0\n\t" \
  "s_waitcnt lgkmcnt(3)\n\t" \
  "v_mfma_f32_32x32x16_bf16 v[128:143], v[168:171], %[q], %[nc]\n\t" \
  "s_nop 10\n\t" \
  "s_waitcnt lgkmcnt(2)\n\t" \
  "v_mfma_f32_32x32x16_bf16 v[144:159], v[172:175], %[q], %[nc]\n\t" \
  "ds_read_b128 v[168:171], %[ka] offset:2048\n\t" \
  "s_mov_b32 m0, %[ld]\n\t" \
  "v_exp_f32 v128, v128\n\t" \
  "v_exp_f32 v129, v129\n\t" \
  "global_load_lds_dwordx4 %[go], %[kg] sc1\n\t" \
  "v_exp_f32 v130, v130\n\t" \
  "v_exp_f32 v131, v131\n\t" \
  "v_cvt_pk_bf16_f32 v160, v128, v129\n\t" \
  "v_exp_f32 v132, v132\n\t" \
  "v_exp_f32 v133, v133\n\t" \
  "v_cvt_pk_bf16_f32 v161, v130, v131\n\t" \
  "v_mfma_f32_32x32x16_bf16 %[o], v[188:191], v[164:167], %[o]\n\t" \
  "ds_read_b128 v[180:183], %[va] offset:1024\n\t" \
  "ds_read_b128 v[188:191], %[va] offset:1536\n\t" \
  "s_add_u32 m0, %[ld], 0x10000\n\t" \
  "v_exp_f32 v134, v134\n\t" \
  "v_exp_f32 v135, v135\n\t" \
  "global_load_lds_dwordx4 %[go], %[vg] sc1\n\t" \
  "s_add_u32 %[ld], %[ld], 0x2000\n\t" \
  "v_cvt_pk_bf16_f32 v162, v132, v133\n\t" \
  "v_exp_f32 v136, v136\n\t" \
  "v_exp_f32 v137, v137\n\t" \
  "v_cvt_pk_bf16_f32 v163, v134, v135\n\t" \
  "v_add_u32 %[go], 0x2000, %[go]\n\t" \
  "v_and_b32 %[go], 0xffff, %[go]\n\t" \
  "v_exp_f32 v138, v138\n\t" \
  "v_exp_f32 v139, v139\n\t" \
  "s_waitcnt lgkmcnt(4)\n\t" \
  "v_mfma_f32_32x32x16_bf16 %[o], v[176:179], v[160:163], %[o]\n\t" \
  "v_cvt_pk_bf16_f32 v164, v136, v137\n\t" \
  "v_exp_f32 v140, v140\n\t" \
  "v_exp_f32 v141, v141\n\t" \
  "v_cvt_pk_bf16_f32 v165, v138, v139\n\t" \
  "v_exp_f32 v142, v142\n\t" \
  "v_exp_f32 v143, v143\n\t" \
  "v_cvt_pk_bf16_f32 v166, v140, v141\n\t" \
  "v_cvt_pk_bf16_f32 v167, v142, v143\n\t" \
  "s_waitcnt lgkmcnt(2)\n\t" \
  "v_mfma_f32_32x32x16_bf16 v[128:143], v[168:171], %[q], %[nc]\n\t" \
  "ds_read_b128 v[172:175], %[ka] offset:3072\n\t" \
  "s_mov_b32 m0, %[ld]\n\t" \
  "v_exp_f32 v144, v144\n\t" \
  "v_exp_f32 v145, v145\n\t" \
  "global_load_lds_dwordx4 %[go], %[kg] sc1\n\t" \
  "v_exp_f32 v146, v146\n\t" \
  "v_exp_f32 v147, v147\n\t" \
  "v_cvt_pk_bf16_f32 v160, v144, v145\n\t" \
  "v_exp_f32 v148, v148\n\t" \
  "v_exp_f32 v149, v149\n\t" \
  "v_cvt_pk_bf16_f32 v161, v146, v147\n\t" \
  "v_mfma_f32_32x32x16_bf16 %[o], v[184:187], v[164:167], %[o]\n\t" \
  "ds_read_b128 v[176:179], %[va] offset:2048\n\t" \
  "ds_read_b128 v[184:187], %[va] offset:2560\n\t" \
  "s_add_u32 m0, %[ld], 0x10000\n\t" \
  "v_exp_f32 v150, v150\n\t" \
  "v_exp_f32 v151, v151\n\t" \
  "global_load_lds_dwordx4 %[go], %[vg] sc1\n\t" \
  "s_add_u32 %[ld], %[ld], 0x2000\n\t" \
  "v_cvt_pk_bf16_f32 v162, v148, v149\n\t" \
  "v_exp_f32 v152, v152\n\t" \
  "v_exp_f32 v153, v153\n\t" \
  "v_cvt_pk_bf16_f32 v163, v150, v151\n\t" \
  "v_add_u32 %[go], 0x2000, %[go]\n\t" \
  "v_and_b32 %[go], 0xffff, %[go]\n\t" \
  "v_exp_f32 v154, v154\n\t" \
  "v_exp_f32 v155, v155\n\t" \
  "s_waitcnt lgkmcnt(4)\n\t" \
  "v_mfma_f32_32x32x16_bf16 %[o], v[180:183], v[160:163], %[o]\n\t" \
  "v_cvt_pk_bf16_f32 v164, v152, v153\n\t" \
  "v_exp_f32 v156, v156\n\t" \
  "v_exp_f32 v157, v157\n\t" \
  "v_cvt_pk_bf16_f32 v165, v154, v155\n\t" \
  "v_exp_f32 v158, v158\n\t" \
  "v_exp_f32 v159, v159\n\t" \
  "v_cvt_pk_bf16_f32 v166, v156, v157\n\t" \
  "v_cvt_pk_bf16_f32 v167, v158, v159\n\t" \
  "s_waitcnt lgkmcnt(2)\n\t" \
  "v_mfma_f32_32x32x16_bf16 v[144:159], v[172:175], %[q], %[nc]\n\t" \
  "ds_read_b128 v[168:171], %[ka] offset:4096\n\t" \
  "s_mov_b32 m0, %[ld]\n\t" \
  "v_exp_f32 v128, v128\n\t" \
  "v_exp_f32 v129, v129\n\t" \
  "global_load_lds_dwordx4 %[go], %[kg] sc1\n\t" \
  "v_exp_f32 v130, v130\n\t" \
  "v_exp_f32 v131, v131\n\t" \
  "v_cvt_pk_bf16_f32 v160, v128, v129\n\t" \
  "v_exp_f32 v132, v132\n\t" \
  "v_exp_f32 v133, v133\n\t" \
  "v_cvt_pk_bf16_f32 v161, v130, v131\n\t" \
  "v_mfma_f32_32x32x16_bf16 %[o], v[188:191], v[164:167], %[o]\n\t" \
  "ds_read_b128 v[180:183], %[va] offset:3072\n\t" \
  "ds_read_b128 v[188:191], %[va] offset:3584\n\t" \
  "s_add_u32 m0, %[ld], 0x10000\n\t" \
  "v_exp_f32 v134, v134\n\t" \
  "v_exp_f32 v135, v135\n\t" \
  "global_load_lds_dwordx4 %[go], %[vg] sc1\n\t" \
  "s_add_u32 %[ld], %[ld], 0x2000\n\t" \
  "v_cvt_pk_bf16_f32 v162, v132, v133\n\t" \
  "v_exp_f32 v136, v136\n\t" \
  "v_exp_f32 v137, v137\n\t" \
  "v_cvt_pk_bf16_f32 v163, v134, v135\n\t" \
  "v_add_u32 %[go], 0x2000, %[go]\n\t" \
  "v_and_b32 %[go], 0xffff, %[go]\n\t" \
  "v_exp_f32 v138, v138\n\t" \
  "v_exp_f32 v139, v139\n\t" \
  "s_waitcnt lgkmcnt(4)\n\t" \
  "v_mfma_f32_32x32x16_bf16 %[o], v[176:179], v[160:163], %[o]\n\t" \
  "v_cvt_pk_bf16_f32 v164, v136, v137\n\t" \
  "v_exp_f32 v140, v140\n\t" \
  "v_exp_f32 v141, v141\n\t" \
  "v_cvt_pk_bf16_f32 v165, v138, v139\n\t" \
  "v_exp_f32 v142, v142\n\t" \
  "v_exp_f32 v143, v143\n\t" \
  "v_cvt_pk_bf16_f32 v166, v140, v141\n\t" \
  "v_cvt_pk_bf16_f32 v167, v142, v143\n\t" \
  "s_waitcnt lgkmcnt(2)\n\t" \
  "v_mfma_f32_32x32x16_bf16 v[128:143], v[168:171], %[q], %[nc]\n\t" \
  "ds_read_b128 v[172:175], %[ka] offset:5120\n\t" \
  "s_mov_b32 m0, %[ld]\n\t" \
  "v_exp_f32 v144, v144\n\t" \
  "v_exp_f32 v145, v145\n\t" \
  "global_load_lds_dwordx4 %[go], %[kg] sc1\n\t" \
  "v_exp_f32 v146, v146\n\t" \
  "v_exp_f32 v147, v147\n\t" \
  "v_cvt_pk_bf16_f32 v160, v144, v145\n\t" \
  "v_exp_f32 v148, v148\n\t" \
  "v_exp_f32 v149, v149\n\t" \
  "v_cvt_pk_bf16_f32 v161, v146, v147\n\t" \
  "v_mfma_f32_32x32x16_bf16 %[o], v[184:187], v[164:167], %[o]\n\t" \
  "ds_read_b128 v[176:179], %[va] offset:4096\n\t" \
  "ds_read_b128 v[184:187], %[va] offset:4608\n\t" \
  "s_add_u32 m0, %[ld], 0x10000\n\t" \
  "v_exp_f32 v150, v150\n\t" \
  "v_exp_f32 v151, v151\n\t" \
  "global_load_lds_dwordx4 %[go], %[vg] sc1\n\t" \
  "s_add_u32 %[ld], %[ld], 0x2000\n\t" \
  "v_cvt_pk_bf16_f32 v162, v148, v149\n\t" \
  "v_exp_f32 v152, v152\n\t" \
  "v_exp_f32 v153, v153\n\t" \
  "v_cvt_pk_bf16_f32 v163, v150, v151\n\t" \
  "v_add_u32 %[go], 0x2000, %[go]\n\t" \
  "v_and_b32 %[go], 0xffff, %[go]\n\t" \
  "v_exp_f32 v154, v154\n\t" \
  "v_exp_f32 v155, v155\n\t" \
  "s_waitcnt lgkmcnt(4)\n\t" \
  "v_mfma_f32_32x32x16_bf16 %[o], v[180:183], v[160:163], %[o]\n\t" \
  "v_cvt_pk_bf16_f32 v164, v152, v153\n\t" \
  "v_exp_f32 v156, v156\n\t" \
  "v_exp_f32 v157, v157\n\t" \
  "v_cvt_pk_bf16_f32 v165, v154, v155\n\t" \
  "v_exp_f32 v158, v158\n\t" \
  "v_exp_f32 v159, v159\n\t" \
  "v_cvt_pk_bf16_f32 v166, v156, v157\n\t" \
  "v_cvt_pk_bf16_f32 v167, v158, v159\n\t" \
  "v_add_u32 %[ka], 0x1000, %[ka]\n\t" \
  "v_add_u32 %[va], %[vs], %[va]\n\t" \
  "s_waitcnt lgkmcnt(2)\n\t" \
  "v_mfma_f32_32x32x16_bf16 v[144:159], v[172:175], %[q], %[nc]\n\t" \
  "ds_read_b128 v[168:171], %[ka] offset:2048\n\t" \
  "s_mov_b32 m0, %[ld]\n\t" \
  "v_exp_f32 v128, v128\n\t" \
  "v_exp_f32 v129, v129\n\t" \
  "global_load_lds_dwordx4 %[go], %[kg] sc1\n\t" \
  "v_exp_f32 v130, v130\n\t" \
  "v_exp_f32 v131, v131\n\t" \
  "v_cvt_pk_bf16_f32 v160, v128, v129\n\t" \
  "v_exp_f32 v132, v132\n\t" \
  "v_exp_f32 v133, v133\n\t" \
  "v_cvt_pk_bf16_f32 v161, v130, v131\n\t" \
  "v_mfma_f32_32x32x16_bf16 %[o], v[188:191], v[164:167], %[o]\n\t" \
  "ds_read_b128 v[180:183], %[va] offset:1024\n\t" \
  "ds_read_b128 v[188:191], %[va] offset:1536\n\t" \
  "s_add_u32 m0, %[ld], 0x10000\n\t" \
  "v_exp_f32 v134, v134\n\t" \
  "v_exp_f32 v135, v135\n\t" \
  "global_load_lds_dwordx4 %[go], %[vg] sc1\n\t" \
  "s_add_u32 %[ld], %[ld], 0x2000\n\t" \
  "v_cvt_pk_bf16_f32 v162, v132, v133\n\t" \
  "v_exp_f32 v136, v136\n\t" \
  "v_exp_f32 v137, v137\n\t" \
  "v_cvt_pk_bf16_f32 v163, v134, v135\n\t" \
  "v_add_u32 %[go], 0x2000, %[go]\n\t" \
  "v_and_b32 %[go], 0xffff, %[go]\n\t" \
  "v_exp_f32 v138, v138\n\t" \
  "v_exp_f32 v139, v139\n\t" \
  "s_waitcnt lgkmcnt(4)\n\t" \
  "v_mfma_f32_32x32x16_bf16 %[o], v[176:179], v[160:163], %[o]\n\t" \
  "v_cvt_pk_bf16_f32 v164, v136, v137\n\t" \
  "v_exp_f32 v140, v140\n\t" \
  "v_exp_f32 v141, v141\n\t" \
  "v_cvt_pk_bf16_f32 v165, v138, v139\n\t" \
  "v_exp_f32 v142, v142\n\t" \
  "v_exp_f32 v143, v143\n\t" \
  "v_cvt_pk_bf16_f32 v166, v140, v141\n\t" \
  "v_cvt_pk_bf16_f32 v167, v142, v143\n\t" \
  "s_waitcnt lgkmcnt(2)\n\t" \
  "v_mfma_f32_32x32x16_bf16 v[128:143], v[168:171], %[q], %[nc]\n\t" \
  "ds_read_b128 v[172:175], %[ka] offset:3072\n\t" \
  "s_mov_b32 m0, %[ld]\n\t" \
  "v_exp_f32 v144, v144\n\t" \
  "v_exp_f32 v145, v145\n\t" \
  "global_load_lds_dwordx4 %[go], %[kg] sc1\n\t" \
  "v_exp_f32 v146, v146\n\t" \
  "v_exp_f32 v147, v147\n\t" \
  "v_cvt_pk_bf16_f32 v160, v144, v145\n\t" \
  "v_exp_f32 v148, v148\n\t" \
  "v_exp_f32 v149, v149\n\t" \
  "v_cvt_pk_bf16_f32 v161, v146, v147\n\t" \
  "v_mfma_f32_32x32x16_bf16 %[o], v[184:187], v[164:167], %[o]\n\t" \
  "ds_read_b128 v[176:179], %[va] offset:2048\n\t" \
  "ds_read_b128 v[184:187], %[va] offset:2560\n\t" \
  "s_add_u32 m0, %[ld], 0x10000\n\t" \
  "v_exp_f32 v150, v150\n\t" \
  "v_exp_f32 v151, v151\n\t" \
  "global_load_lds_dwordx4 %[go], %[vg] sc1\n\t" \
  "s_add_u32 %[ld], %[ld], 0x2000\n\t" \
  "v_cvt_pk_bf16_f32 v162, v148, v149\n\t" \
  "v_exp_f32 v152, v152\n\t" \
  "v_exp_f32 v153, v153\n\t" \
  "v_cvt_pk_bf16_f32 v163, v150, v151\n\t" \
  "v_add_u32 %[go], 0x2000, %[go]\n\t" \
  "v_and_b32 %[go], 0xffff, %[go]\n\t" \
  "v_exp_f32 v154, v154\n\t" \
  "v_exp_f32 v155, v155\n\t" \
  "s_waitcnt lgkmcnt(4)\n\t" \
  "v_mfma_f32_32x32x16_bf16 %[o], v[180:183], v[160:163], %[o]\n\t" \
  "v_cvt_pk_bf16_f32 v164, v152, v153\n\t" \
  "v_exp_f32 v156, v156\n\t" \
  "v_exp_f32 v157, v157\n\t" \
  "v_cvt_pk_bf16_f32 v165, v154, v155\n\t" \
  "v_exp_f32 v158, v158\n\t" \
  "v_exp_f32 v159, v159\n\t" \
  "v_cvt_pk_bf16_f32 v166, v156, v157\n\t" \
  "v_cvt_pk_bf16_f32 v167, v158, v159\n\t" \
  "s_waitcnt lgkmcnt(2)\n\t" \
  "v_mfma_f32_32x32x16_bf16 v[144:159], v[172:175], %[q], %[nc]\n\t" \
  "ds_read_b128 v[168:171], %[ka] offset:4096\n\t" \
  "v_exp_f32 v128, v128\n\t" \
  "v_exp_f32 v129, v129\n\t" \
  "v_exp_f32 v130, v130\n\t" \
  "v_exp_f32 v131, v131\n\t" \
  "v_cvt_pk_bf16_f32 v160, v128, v129\n\t" \
  "v_exp_f32 v132, v132\n\t" \
  "v_exp_f32 v133, v133\n\t" \
  "v_cvt_pk_bf16_f32 v161, v130, v131\n\t" \
  "v_mfma_f32_32x32x16_bf16 %[o], v[188:191], v[164:167], %[o]\n\t" \
  "ds_read_b128 v[180:183], %[va] offset:3072\n\t" \
  "ds_read_b128 v[188:191], %[va] offset:3584\n\t" \
  "v_exp_f32 v134, v134\n\t" \
  "v_exp_f32 v135, v135\n\t" \
  "v_cvt_pk_bf16_f32 v162, v132, v133\n\t" \
  "v_exp_f32 v136, v136\n\t" \
  "v_exp_f32 v137, v137\n\t" \
  "v_cvt_pk_bf16_f32 v163, v134, v135\n\t" \
  "v_exp_f32 v138, v138\n\t" \
  "v_exp_f32 v139, v139\n\t" \
  "s_waitcnt lgkmcnt(4)\n\t" \
  "v_mfma_f32_32x32x16_bf16 %[o], v[176:179], v[160:163], %[o]\n\t" \
  "v_cvt_pk_bf16_f32 v164, v136, v137\n\t" \
  "v_exp_f32 v140, v140\n\t" \
  "v_exp_f32 v141, v141\n\t" \
  "v_cvt_pk_bf16_f32 v165, v138, v139\n\t" \
  "v_exp_f32 v142, v142\n\t" \
  "v_exp_f32 v143, v143\n\t" \
  "v_cvt_pk_bf16_f32 v166, v140, v141\n\t" \
  "v_cvt_pk_bf16_f32 v167, v142, v143\n\t" \
  "s_waitcnt lgkmcnt(2)\n\t" \
  "v_mfma_f32_32x32x16_bf16 v[128:143], v[168:171], %[q], %[nc]\n\t" \
  "ds_read_b128 v[172:175], %[ka] offset:5120\n\t" \
  "v_exp_f32 v144, v144\n\t" \
  "v_exp_f32 v145, v145\n\t" \
  "v_exp_f32 v146, v146\n\t" \
  "v_exp_f32 v147, v147\n\t" \
  "v_cvt_pk_bf16_f32 v160, v144, v145\n\t" \
  "v_exp_f32 v148, v148\n\t" \
  "v_exp_f32 v149, v149\n\t" \
  "v_cvt_pk_bf16_f32 v161, v146, v147\n\t" \
  "v_mfma_f32_32x32x16_bf16 %[o], v[184:187], v[164:167], %[o]\n\t" \
  "ds_read_b128 v[176:179], %[va] offset:4096\n\t" \
  "ds_read_b128 v[184:187], %[va] offset:4608\n\t" \
  "v_exp_f32 v150, v150\n\t" \
  "v_exp_f32 v151, v151\n\t" \
  "v_cvt_pk_bf16_f32 v162, v148, v149\n\t" \
  "v_exp_f32 v152, v152\n\t" \
  "v_exp_f32 v153, v153\n\t" \
  "v_cvt_pk_bf16_f32 v163, v150, v151\n\t" \
  "v_exp_f32 v154, v154\n\t" \
  "v_exp_f32 v155, v155\n\t" \
  "s_waitcnt lgkmcnt(4)\n\t" \
  "v_mfma_f32_32x32x16_bf16 %[o], v[180:183], v[160:163], %[o]\n\t" \
  "v_cvt_pk_bf16_f32 v164, v152, v153\n\t" \
  "v_exp_f32 v156, v156\n\t" \
  "v_exp_f32 v157, v157\n\t" \
  "v_cvt_pk_bf16_f32 v165, v154, v155\n\t" \
  "v_exp_f32 v158, v158\n\t" \
  "v_exp_f32 v159, v159\n\t" \
  "v_cvt_pk_bf16_f32 v166, v156, v157\n\t" \
  "v_cvt_pk_bf16_f32 v167, v158, v159\n\t" \
  "v_add_u32 %[ka], 0x1000, %[ka]\n\t" \
  "v_add_u32 %[va], %[vs], %[va]\n\t" \
  "s_mov_b32 m0, %[keep]\n\t" \
  "s_waitcnt vmcnt(0) lgkmcnt(0)\n\t" \
  ""
#define ATTN_ASM_B \
  "s_mov_b32 %[cnt], %[c0]\n\t" \
  "s_cmp_lg_u32 %[c0], 2\n\t" \
  "s_cbranch_scc1 L_attn_loop_%=\n\t" \
  "s_cmp_eq_u32 %[sw], 99\n\t" \
  "s_cbranch_scc1 L_attn_y_%=\n\t" \
  "s_setprio 2\n\t" \
  "s_branch L_attn_loop_%=\n\t" \
  "L_attn_y_%=:\n\t" \
  "s_setprio 1\n\t" \
  "L_attn_loop_%=:\n\t" \
  "s_cmp_lg_u32 %[cnt], %[sw]\n\t" \
  "s_cbranch_scc1 L_attn_nosw_%=\n\t" \
  "s_setprio 0\n\t" \
  "L_attn_nosw_%=:\n\t" \
  "s_waitcnt lgkmcnt(2)\n\t" \
  "v_mfma_f32_32x32x16_bf16 v[144:159], v[172:175], %[q], %[nc]\n\t" \
  "ds_read_b128 v[168:171], %[ka] offset:2048\n\t" \
  "v_exp_f32 v128, v128\n\t" \
  "v_exp_f32 v129, v129\n\t" \
  "v_exp_f32 v130, v130\n\t" \
  "v_exp_f32 v131, v131\n\t" \
  "v_cvt_pk_bf16_f32 v160, v128, v129\n\t" \
  "v_exp_f32 v132, v132\n\t" \
  "v_exp_f32 v133, v133\n\t" \
  "v_cvt_pk_bf16_f32 v161, v130, v131\n\t" \
  "v_mfma_f32_32x32x16_bf16 %[o], v[188:191], v[164:167], %[o]\n\t" \
  "ds_read_b128 v[180:183], %[va] offset:1024\n\t" \
  "ds_read_b128 v[188:191], %[va] offset:1536\n\t" \
  "v_exp_f32 v134, v134\n\t" \
  "v_exp_f32 v135, v135\n\t" \
  "v_cvt_pk_bf16_f32 v162, v132, v133\n\t" \
  "v_exp_f32 v136, v136\n\t" \
  "v_exp_f32 v137, v137\n\t" \
  "v_cvt_pk_bf16_f32 v163, v134, v135\n\t" \
  "v_exp_f32 v138, v138\n\t" \
  "v_exp_f32 v139, v139\n\t" \
  "s_waitcnt lgkmcnt(4)\n\t" \
  "v_mfma_f32_32x32x16_bf16 %[o], v[176:179], v[160:163], %[o]\n\t" \
  "v_cvt_pk_bf16_f32 v164, v136, v137\n\t" \
  "v_exp_f32 v140, v140\n\t" \
  "v_exp_f32 v141, v141\n\t" \
  "v_cvt_pk_bf16_f32 v165, v138, v139\n\t" \
  "v_exp_f32 v142, v142\n\t" \
  "v_exp_f32 v143, v143\n\t" \
  "v_cvt_pk_bf16_f32 v166, v140, v141\n\t" \
  "v_cvt_pk_bf16_f32 v167, v142, v143\n\t" \
  "s_waitcnt lgkmcnt(2)\n\t" \
  "v_mfma_f32_32x32x16_bf16 v[128:143], v[168:171], %[q], %[nc]\n\t" \
  "ds_read_b128 v[172:175], %[ka] offset:3072\n\t" \
  "v_exp_f32 v144, v144\n\t" \
  "v_exp_f32 v145, v145\n\t" \
  "v_exp_f32 v146, v146\n\t" \
  "v_exp_f32 v147, v147\n\t" \
  "v_cvt_pk_bf16_f32 v160, v144, v145\n\t" \
  "v_exp_f32 v148, v148\n\t" \
  "v_exp_f32 v149, v149\n\t" \
  "v_cvt_pk_bf16_f32 v161, v146, v147\n\t" \
  "v_mfma_f32_32x32x16_bf16 %[o], v[184:187], v[164:167], %[o]\n\t" \
  "ds_read_b128 v[176:179], %[va] offset:2048\n\t" \
  "ds_read_b128 v[184:187], %[va] offset:2560\n\t" \
  "v_exp_f32 v150, v150\n\t" \
  "v_exp_f32 v151, v151\n\t" \
  "v_cvt_pk_bf16_f32 v162, v148, v149\n\t" \
  "v_exp_f32 v152, v152\n\t" \
  "v_exp_f32 v153, v153\n\t" \
  "v_cvt_pk_bf16_f32 v163, v150, v151\n\t" \
  "v_exp_f32 v154, v154\n\t" \
  "v_exp_f32 v155, v155\n\t" \
  "s_waitcnt lgkmcnt(4)\n\t" \
  "v_mfma_f32_32x32x16_bf16 %[o], v[180:183], v[160:163], %[o]\n\t" \
  "v_cvt_pk_bf16_f32 v164, v152, v153\n\t" \
  "v_exp_f32 v156, v156\n\t" \
  "v_exp_f32 v157, v157\n\t" \
  "v_cvt_pk_bf16_f32 v165, v154, v155\n\t" \
  "v_exp_f32 v158, v158\n\t" \
  "v_exp_f32 v159, v159\n\t" \
  "v_cvt_pk_bf16_f32 v166, v156, v157\n\t" \
  "v_cvt_pk_bf16_f32 v167, v158, v159\n\t" \
  "s_waitcnt lgkmcnt(2)\n\t" \
  "v_mfma_f32_32x32x16_bf16 v[144:159], v[172:175], %[q], %[nc]\n\t" \
  "ds_read_b128 v[168:171], %[ka] offset:4096\n\t" \
  "v_exp_f32 v128, v128\n\t" \
  "v_exp_f32 v129, v129\n\t" \
  "v_exp_f32 v130, v130\n\t" \
  "v_exp_f32 v131, v131\n\t" \
  "v_cvt_pk_bf16_f32 v160, v128, v129\n\t" \
  "v_exp_f32 v132, v132\n\t" \
  "v_exp_f32 v133, v133\n\t" \
  "v_cvt_pk_bf16_f32 v161, v130, v131\n\t" \
  "v_mfma_f32_32x32x16_bf16 %[o], v[188:191], v[164:167], %[o]\n\t" \
  "ds_read_b128 v[180:183], %[va] offset:3072\n\t" \
  "ds_read_b128 v[188:191], %[va] offset:3584\n\t" \
  "v_exp_f32 v134, v134\n\t" \
  "v_exp_f32 v135, v135\n\t" \
  "v_cvt_pk_bf16_f32 v162, v132, v133\n\t" \
  "v_exp_f32 v136, v136\n\t" \
  "v_exp_f32 v137, v137\n\t" \
  "v_cvt_pk_bf16_f32 v163, v134, v135\n\t" \
  "v_exp_f32 v138, v138\n\t" \
  "v_exp_f32 v139, v139\n\t" \
  "s_waitcnt lgkmcnt(4)\n\t" \
  "v_mfma_f32_32x32x16_bf16 %[o], v[176:179], v[160:163], %[o]\n\t" \
  "v_cvt_pk_bf16_f32 v164, v136, v137\n\t" \
  "v_exp_f32 v140, v140\n\t" \
  "v_exp_f32 v141, v141\n\t" \
  "v_cvt_pk_bf16_f32 v165, v138, v139\n\t" \
  "v_exp_f32 v142, v142\n\t" \
  "v_exp_f32 v143, v143\n\t" \
  "v_cvt_pk_bf16_f32 v166, v140, v141\n\t" \
  "v_cvt_pk_bf16_f32 v167, v142, v143\n\t" \
  "s_waitcnt lgkmcnt(2)\n\t" \
  "v_mfma_f32_32x32x16_bf16 v[128:143], v[168:171], %[q], %[nc]\n\t" \
  "ds_read_b128 v[172:175], %[ka] offset:5120\n\t" \
  "v_exp_f32 v144, v144\n\t" \
  "v_exp_f32 v145, v145\n\t" \
  "v_exp_f32 v146, v146\n\t" \
  "v_exp_f32 v147, v147\n\t" \
  "v_cvt_pk_bf16_f32 v160, v144, v145\n\t" \
  "v_exp_f32 v148, v148\n\t" \
  "v_exp_f32 v149, v149\n\t" \
  "v_cvt_pk_bf16_f32 v161, v146, v147\n\t" \
  "v_mfma_f32_32x32x16_bf16 %[o], v[184:187], v[164:167], %[o]\n\t" \
  "ds_read_b128 v[176:179], %[va] offset:4096\n\t" \
  "ds_read_b128 v[184:187], %[va] offset:4608\n\t" \
  "v_exp_f32 v150, v150\n\t" \
  "v_exp_f32 v151, v151\n\t" \
  "v_cvt_pk_bf16_f32 v162, v148, v149\n\t" \
  "v_exp_f32 v152, v152\n\t" \
  "v_exp_f32 v153, v153\n\t" \
  "v_cvt_pk_bf16_f32 v163, v150, v151\n\t" \
  "v_exp_f32 v154, v154\n\t" \
  "v_exp_f32 v155, v155\n\t" \
  "s_waitcnt lgkmcnt(4)\n\t" \
  "v_mfma_f32_32x32x16_bf16 %[o], v[180:183], v[160:163], %[o]\n\t" \
  "v_cvt_pk_bf16_f32 v164, v152, v153\n\t" \
  "v_exp_f32 v156, v156\n\t" \
  "v_exp_f32 v157, v157\n\t" \
  "v_cvt_pk_bf16_f32 v165, v154, v155\n\t" \
  "v_exp_f32 v158, v158\n\t" \
  "v_exp_f32 v159, v159\n\t" \
  "v_cvt_pk_bf16_f32 v166, v156, v157\n\t" \
  "v_cvt_pk_bf16_f32 v167, v158, v159\n\t" \
  "v_add_u32 %[ka], 0x1000, %[ka]\n\t" \
  "v_add_u32 %[va], %[vs], %[va]\n\t" \
  "s_add_u32 %[cnt], %[cnt], 1\n\t" \
  "s_cmp_lt_u32 %[cnt], %[c1]\n\t" \
  "s_cbranch_scc1 L_attn_loop_%=\n\t" \
  "s_waitcnt lgkmcnt(0)\n\t" \
  "s_cmp_lg_u32 %[c1], 16\n\t" \
  "s_cbranch_scc1 L_attn_end_%=\n\t" \
  "v_mfma_f32_32x32x16_bf16 %[o], v[188:191], v[164:167], %[o]\n\t" \
  "s_setprio 0\n\t" \
  "L_attn_end_%=:\n\t" \
  "s_nop 15\n\t" \
  ""

typedef short bf16x8 __attribute__((ext_vector_type(8)));
typedef short s16x4 __attribute__((ext_vector_type(4)));
typedef float f32x4 __attribute__((ext_vector_type(4)));
typedef float f32x2 __attribute__((ext_vector_type(2)));
typedef float f32x16 __attribute__((ext_vector_type(16)));
typedef float f32x8 __attribute__((ext_vector_type(8)));
typedef unsigned u32x4 __attribute__((ext_vector_type(4)));
typedef unsigned u32x8 __attribute__((ext_vector_type(8)));
typedef unsigned u32x2 __attribute__((ext_vector_type(2)));
typedef __bf16 bf16x2_t __attribute__((ext_vector_type(2)));
#define LAS __attribute__((address_space(3)))
#define GAS __attribute__((address_space(1)))
typedef const LAS char* lds_cptr;
typedef LAS char* lds_ptr;
typedef GAS unsigned gu32;
typedef GAS unsigned long long gu64;
typedef __amdgpu_buffer_rsrc_t rsrc_t;

constexpr int SEQ = 2048, DM = 512, NH = 32, FF = 1376;
constexpr float EPS = 1e-5f;
constexpr float QSCALE = 0.25f * 1.4426950408889634f;

constexpr unsigned WS_Q = 0, WS_K = 2u << 20, WS_V = 4u << 20, WS_O = 6u << 20, WS_H = 8u << 20, WS_ACT = 10u << 20;
constexpr unsigned WS_KMAX = 16u << 20, WS_HST = (16u << 20) + (64u << 10);
constexpr unsigned WS_WOF = 17u << 20, WS_WDF = WS_WOF + 512 * 1024, WS_WGF = WS_WDF + 32 * 43 * 1024;
constexpr unsigned WS_FLAG = 22u << 20;
constexpr unsigned FL_EPOCH = 0, FL_TMO = 256, FL_1 = 1024, FL_P = 4096;
constexpr unsigned POISON = 0xFFFFFFFFu;
constexpr unsigned WS_USED = 24u << 20;
constexpr size_t WS_DBG = 64u << 20;

#define TS(i) do {} while (0)
#define TS_BEGIN() do {} while (0)
#define TS_END() do {} while (0)
#define TSARG
#define TSPASS

struct Params {
  const char* x; const char* wqkv; const char* wo; const char* wg; const char* wu; const char* wd;
  char* out; char* ws;
};

#define MFMA16(a, b, c) __builtin_amdgcn_mfma_f32_16x16x32_bf16(a, b, c, 0, 0, 0)
#define MFMA32(a, b, c) __builtin_amdgcn_mfma_f32_32x32x16_bf16(a, b, c, 0, 0, 0)
#define PIN(x) asm volatile("" : "+v"(x))
#define SB() __builtin_amdgcn_sched_barrier(0)
#define RLX_AGENT __ATOMIC_RELAXED, __HIP_MEMORY_SCOPE_AGENT

template <int N> __device__ __forceinline__ void wait_bar() {
  asm volatile("s_waitcnt vmcnt(%0) lgkmcnt(0)\n\ts_barrier" ::"n"(N) : "memory");
}
template <int N> __device__ __forceinline__ void wait_vm() { asm volatile("s_waitcnt vmcnt(%0)" ::"n"(N) : "memory"); }
__device__ __forceinline__ void lds_bar() { asm volatile("s_waitcnt lgkmcnt(0)\n\ts_barrier" ::: "memory"); }
__device__ __forceinline__ void raw_bar() { asm volatile("s_barrier" ::: "memory"); }

template <int N, bool SC1> __device__ __forceinline__ void glds(const void* sbase, unsigned voff, unsigned lds_addr) {
#define GL_(O, S) "\n\tglobal_load_lds_dwordx4 %0, %1 offset:" #O S
  if (SC1) {
    if (N == 1) asm volatile("s_mov_b32 m0, %2\n\ts_nop 0" GL_(0, " sc1") :: "v"(voff), "s"(sbase), "s"(lds_addr) : "memory");
    if (N == 2) asm volatile("s_mov_b32 m0, %2\n\ts_nop 0" GL_(0, " sc1") GL_(1024, " sc1") :: "v"(voff), "s"(sbase), "s"(lds_addr) : "memory");
    if (N == 3) asm volatile("s_mov_b32 m0, %2\n\ts_nop 0" GL_(0, " sc1") GL_(1024, " sc1") GL_(2048, " sc1") :: "v"(voff), "s"(sbase), "s"(lds_addr) : "memory");
    if (N == 4) asm volatile("s_mov_b32 m0, %2\n\ts_nop 0" GL_(0, " sc1") GL_(1024, " sc1") GL_(2048, " sc1") GL_(3072, " sc1") :: "v"(voff), "s"(sbase), "s"(lds_addr) : "memory");
  } else {
    if (N == 1) asm volatile("s_mov_b32 m0, %2\n\ts_nop 0" GL_(0, "") :: "v"(voff), "s"(sbase), "s"(lds_addr) : "memory");
    if (N == 2) asm volatile("s_mov_b32 m0, %2\n\ts_nop 0" GL_(0, "") GL_(1024, "") :: "v"(voff), "s"(sbase), "s"(lds_addr) : "memory");
    if (N == 3) asm volatile("s_mov_b32 m0, %2\n\ts_nop 0" GL_(0, "") GL_(1024, "") GL_(2048, "") :: "v"(voff), "s"(sbase), "s"(lds_addr) : "memory");
  }
#undef GL_
}
template <bool SC1> __device__ __forceinline__ void glds16(const void* sbase, unsigned voff, unsigned lds_addr) { glds<1, SC1>(sbase, voff, lds_addr); }
template <bool SC1> __device__ __forceinline__ void glds4(const void* sbase, unsigned voff, unsigned lds_addr) {
  if (SC1) asm volatile("s_mov_b32 m0, %2\n\ts_nop 0\n\tglobal_load_lds_dword %0, %1 sc1" :: "v"(voff), "s"(sbase), "s"(lds_addr) : "memory");
  else     asm volatile("s_mov_b32 m0, %2\n\ts_nop 0\n\tglobal_load_lds_dword %0, %1" :: "v"(voff), "s"(sbase), "s"(lds_addr) : "memory");
}
__device__ __forceinline__ bf16x8 ld128(lds_cptr p) { return *(const LAS bf16x8*)p; }
typedef short v4i16_t __attribute__((ext_vector_type(4)));
__device__ __forceinline__ s16x4 ldtr(lds_cptr p) {
  return __builtin_bit_cast(s16x4, __builtin_amdgcn_ds_read_tr16_b64_v4i16((LAS v4i16_t*)p));
}
__device__ __forceinline__ bf16x8 ldtr2(lds_cptr p, int second) {
  const s16x4 a = ldtr(p), b = ldtr(p + second);
  return (bf16x8){a[0], a[1], a[2], a[3], b[0], b[1], b[2], b[3]};
}
__device__ __forceinline__ unsigned cvtpk(float lo, float hi) {
  f32x2 v = {lo, hi}; bf16x2_t b = __builtin_convertvector(v, bf16x2_t); return __builtin_bit_cast(unsigned, b);
}
__device__ __forceinline__ u32x2 cvt4(f32x4 v) { return (u32x2){cvtpk(v[0], v[1]), cvtpk(v[2], v[3])}; }
__device__ __forceinline__ float bflo(unsigned w) { return __uint_as_float(w << 16); }
__device__ __forceinline__ float bfhi(unsigned w) { return __uint_as_float(w & 0xffff0000u); }
__device__ __forceinline__ f32x4 unpack4(u32x2 w) { return (f32x4){bflo(w[0]), bfhi(w[0]), bflo(w[1]), bfhi(w[1])}; }
__device__ __forceinline__ float bfr(float v) { return bflo(cvtpk(v, 0.f)); }
__device__ __forceinline__ void ln_consts(float sum, float sumsq, float& mean, float& rstd) {
  const float m = sum * (1.f / DM), var = fmaxf(sumsq * (1.f / DM) - m * m, 0.f);
  mean = bfr(m); rstd = __builtin_amdgcn_rcpf(bfr(__builtin_amdgcn_sqrtf(bfr(bfr(var) + EPS))));
}
__device__ __forceinline__ int rfl(int v) { return __builtin_amdgcn_readfirstlane(v); }
__device__ __forceinline__ bf16x8 ones8() { return (bf16x8){0x3F80, 0x3F80, 0x3F80, 0x3F80, 0x3F80, 0x3F80, 0x3F80, 0x3F80}; }
#define Z4 ((f32x4){0.f, 0.f, 0.f, 0.f})
__device__ __forceinline__ void st16(rsrc_t ws, unsigned off, u32x4 v) { __builtin_amdgcn_raw_buffer_store_b128(v, ws, off, 0, 16); }
__device__ __forceinline__ bf16x8 ldw16(rsrc_t ws, unsigned off) { return __builtin_bit_cast(bf16x8, __builtin_amdgcn_raw_buffer_load_b128(ws, off, 0, 16)); }
__device__ __forceinline__ u32x4 pair16(u32x2 X, u32x2 Y) {
  auto a = __builtin_amdgcn_permlane16_swap(X[0], Y[0], false, false);
  auto b = __builtin_amdgcn_permlane16_swap(X[1], Y[1], false, false);
  return (u32x4){a[0], b[0], a[1], b[1]};
}
__device__ __forceinline__ u32x4 pair32(u32x2 X, u32x2 Y) {
  auto a = __builtin_amdgcn_permlane32_swap(X[0], Y[0], false, false);
  auto b = __builtin_amdgcn_permlane32_swap(X[1], Y[1], false, false);
  return (u32x4){a[0], b[0], a[1], b[1]};
}
__device__ __forceinline__ float sum_x16(float v) { auto r = __builtin_amdgcn_permlane16_swap(__float_as_uint(v), __float_as_uint(v), false, false); return __uint_as_float(r[0]) + __uint_as_float(r[1]); }
__device__ __forceinline__ float sum_x32(float v) { auto r = __builtin_amdgcn_permlane32_swap(__float_as_uint(v), __float_as_uint(v), false, false); return __uint_as_float(r[0]) + __uint_as_float(r[1]); }
__device__ __forceinline__ float max_x16(float v) { auto r = __builtin_amdgcn_permlane16_swap(__float_as_uint(v), __float_as_uint(v), false, false); return fmaxf(__uint_as_float(r[0]), __uint_as_float(r[1])); }
__device__ __forceinline__ float lo_x32(float v) { auto r = __builtin_amdgcn_permlane32_swap(__float_as_uint(v), __float_as_uint(v), false, false); return __uint_as_float(r[0]); }
template <int N> __device__ __forceinline__ float ror16(float v) { return __uint_as_float(__builtin_amdgcn_update_dpp(0u, __float_as_uint(v), 0x120 + N, 0xf, 0xf, false)); }
__device__ __forceinline__ float max_row16(float v) { v = fmaxf(v, ror16<8>(v)); v = fmaxf(v, ror16<4>(v)); v = fmaxf(v, ror16<2>(v)); return fmaxf(v, ror16<1>(v)); }
template <int ND, int NM> __device__ __forceinline__ void weave() {
#pragma unroll
  for (int i = 0; i < ND; ++i) { __builtin_amdgcn_sched_group_barrier(0x008, 1, 0); __builtin_amdgcn_sched_group_barrier(0x100, 1, 0); }
  if (NM > ND) __builtin_amdgcn_sched_group_barrier(0x008, NM - ND, 0);
}

__device__ __forceinline__ u32x4 nop4(u32x4 v) { return __builtin_elementwise_min(v, (u32x4){POISON - 1, POISON - 1, POISON - 1, POISON - 1}); }
template <int NP> __device__ __forceinline__ bool pieces_ok(lds_cptr base) {
  const int lane = threadIdx.x & 63;
  unsigned m = 0;
#pragma unroll
  for (int i = 0; i < NP; ++i) { const u32x4 v = *(const LAS u32x4*)(base + i * 1024 + lane * 16); const unsigned a = v[0] > v[1] ? v[0] : v[1], b = v[2] > v[3] ? v[2] : v[3]; m = m > a ? m : a; m = m > b ? m : b; }
  return !__any(m == POISON);
}
template <int NP> struct Chk {
  u32x4 v[NP];
  __device__ __forceinline__ void read(lds_cptr base) {
    const int lane = threadIdx.x & 63;
#pragma unroll
    for (int i = 0; i < NP; ++i) v[i] = *(const LAS u32x4*)(base + i * 1024 + lane * 16);
  }
  __device__ __forceinline__ bool ok() const {
    unsigned m = 0;
#pragma unroll
    for (int i = 0; i < NP; ++i) { const unsigned a = v[i][0] > v[i][1] ? v[i][0] : v[i][1], b = v[i][2] > v[i][3] ? v[i][2] : v[i][3]; m = m > a ? m : a; m = m > b ? m : b; }
    return !__any(m == POISON);
  }
};
struct Spin {
  unsigned n; unsigned long long t0;
  __device__ __forceinline__ Spin() : n(0), t0(__builtin_amdgcn_s_memrealtime()) {}
  __device__ __forceinline__ bool expired(char* ws) {
    if ((++n & 63u) == 0u && __builtin_amdgcn_s_memrealtime() - t0 > 20000000ull) {
      if ((threadIdx.x & 63) == 0) __hip_atomic_store((gu32*)(ws + WS_FLAG + FL_TMO), 1u, RLX_AGENT);
      return true;
    }
    return false;
  }
};

__device__ __forceinline__ unsigned make_tag(unsigned epoch) { return epoch * 0x9E3779B1u + 0x7F4A7C15u; }
__device__ __forceinline__ void publish(char* ws, unsigned flag_off, unsigned tag) {
  if (threadIdx.x == 0) __hip_atomic_store((gu32*)(ws + WS_FLAG + flag_off), tag, RLX_AGENT);
}
template <int NW>
__device__ __forceinline__ void poll(char* ws, unsigned flag_off, unsigned tag) {
  const int lane = threadIdx.x & 63;
  gu32* f = (gu32*)(ws + WS_FLAG + flag_off);
  const unsigned long long t0 = __builtin_amdgcn_s_memrealtime();
  for (unsigned spins = 1;; ++spins) {
    bool ok = true;
    if (NW == 256) {
#pragma unroll
      for (int i = 0; i < 4; ++i) ok &= __hip_atomic_load(f + i * 64 + lane, RLX_AGENT) == tag;
    } else ok = __hip_atomic_load(f + (lane < NW ? lane : 0), RLX_AGENT) == tag;
    if (__all(ok)) break;
    if ((spins & 63u) == 0u && __builtin_amdgcn_s_memrealtime() - t0 > 20000000ull) {
      if (lane == 0) __hip_atomic_store((gu32*)(ws + WS_FLAG + FL_TMO), tag, RLX_AGENT);
      break;
    }
    __builtin_amdgcn_s_sleep(1);
  }
}
template <int NW> __device__ __forceinline__ void await(char* ws, unsigned flag_off, unsigned tag) {
  if (rfl(threadIdx.x >> 6) == 0) poll<NW>(ws, flag_off, tag);
  raw_bar();
}

struct Relayout {
  const char* src; int pitch, dstride, nt; unsigned dst;
  __device__ __forceinline__ void decode(const Params& p, int J) {
    if (J >= 1176) J = 0;
    if (J < 128) { const int ks = J >> 3, cg = J & 7; src = p.wo + ((size_t)ks * 32 * DM + cg * 64) * 2; pitch = DM; dst = WS_WOF + (unsigned)(cg * 4 * 16 + ks) * 1024; dstride = 16 * 1024; nt = 4; }
    else if (J < 472) { const int ks = (J - 128) >> 3, cg = (J - 128) & 7; src = p.wd + ((size_t)ks * 32 * DM + cg * 64) * 2; pitch = DM; dst = WS_WDF + (unsigned)(cg * 4 * 43 + ks) * 1024; dstride = 43 * 1024; nt = 4; }
    else { const int r = J - 472, mat = r >= 352, r2 = r - mat * 352, ks = r2 / 22, cg = r2 - ks * 22;
      src = (mat ? p.wu : p.wg) + ((size_t)ks * 32 * FF + cg * 64) * 2; pitch = FF; dst = WS_WGF + (unsigned)((mat * 86 + cg * 4) * 16 + ks) * 1024; dstride = 16 * 1024; nt = cg == 21 ? 2 : 4; }
  }
  __device__ __forceinline__ void load(unsigned lds, int lane) const {
    const int r = lane >> 3, c = lane & 7;
#pragma unroll
    for (int kg = 0; kg < 4; ++kg) {
      int j = (c >> 1) ^ (((r >> 1) & 1) | ((kg & 1) << 1)); j = j < nt ? j : j - 2;
      glds16<false>(src, (unsigned)((kg * 8 + r) * pitch + j * 16 + (c & 1) * 8) * 2u, lds + kg * 1024);
    }
  }
  __device__ __forceinline__ void store(rsrc_t ws, lds_cptr L, int lane) const {
    const int g = lane >> 4, q = (lane >> 2) & 3, pp = lane & 3, ff = ((q >> 1) & 1) | ((g & 1) << 1);
#pragma unroll
    for (int j = 0; j < 4; ++j)
      if (j < nt) st16(ws, dst + j * dstride + lane * 16, __builtin_bit_cast(u32x4, ldtr2(L + g * 1024 + q * 128 + ((j ^ ff) << 5) + pp * 8, 512)));
  }
};

struct F1 { bf16x8 a[4], w[3]; };
__device__ __forceinline__ void p1_qkv(const Params& p, rsrc_t ws, int b, int rb1, int cb1, char* smem, unsigned epoch TSARG) {
  const int tid = threadIdx.x, lane = tid & 63, wid = rfl(tid >> 6);
  const int r16 = lane & 15, g = lane >> 4;
  const unsigned lds0 = (unsigned)(size_t)(lds_ptr)smem;
  lds_cptr L = (lds_cptr)smem;
  constexpr int STG = 32768, ND = 4, NS = 8, MS = ND * STG;
  LAS f32x2* ms = (LAS f32x2*)(smem + MS);
  const int rb = rb1, cb = cb1;
  if (__builtin_expect(wid >= 4, 1)) {
    const int lw = wid - 4;
    const unsigned voffA = (unsigned)((16 * lw + (lane >> 3)) * DM + (((lane & 7) ^ (lane >> 3)) * 8)) * 2u;
    unsigned voffB[2];
#pragma unroll
    for (int i = 0; i < 2; ++i) { const int r = lane >> 3, c = lane & 7, ff = ((r >> 1) & 1) | (i << 1), j = (c >> 1) ^ ff;
      voffB[i] = (unsigned)((16 * lw + 8 * i + r) * (3 * DM) + j * 16 + (c & 1) * 8) * 2u; }
    const char* xa = p.x + (size_t)rb * 64 * DM * 2;
    const char* wb = p.wqkv + (size_t)cb * 64 * 2;
    const int rot = ((b >> 3) & 7) + 2 * (b >> 6);
    auto issue = [&](int s) {
      const unsigned d = lds0 + (s % ND) * STG;
      const int kc = (s + rot) & 7;
#pragma unroll
      for (int i = 0; i < 2; ++i) glds16<false>(xa + kc * 128 + i * 8 * DM * 2, voffA, d + (2 * lw + i) * 1024);
#pragma unroll
      for (int i = 0; i < 2; ++i) glds<3, false>(wb + (size_t)kc * 64 * (3 * DM) * 2, voffB[i], d + 8192 + (2 * lw + i) * 3072);
    };
    TS(20);
    issue(0); issue(1);
    TS(21);
    wait_vm<8>(); raw_bar();
    TS(23);
    issue(2);
#pragma unroll
    for (int j = 1; j < NS; ++j) {
      if (j + 2 < NS) { issue(j + 2); wait_bar<16>(); } else if (j + 1 < NS) wait_bar<8>(); else wait_bar<0>();
    }
    lds_bar();
  } else {
    f32x4 acc[3][4], cs[3], sx = Z4, sq = Z4;
    int aoff[2][4];
#pragma unroll
    for (int ks = 0; ks < 2; ++ks)
#pragma unroll
      for (int i = 0; i < 4; ++i) aoff[ks][i] = ((wid + i) & 3) * 2048 + (r16 >> 3) * 1024 + (r16 & 7) * 128 + (((4 * ks + g) ^ (r16 & 7)) << 4);
    int boff;
    { const int q = (lane >> 2) & 3, pp = lane & 3, ff = ((q >> 1) & 1) | ((g & 1) << 1);
      boff = 8192 + g * 3072 + q * 128 + ((wid ^ ff) << 5) + pp * 8; }
#pragma unroll
    for (int i = 0; i < 3; ++i) { cs[i] = Z4;
#pragma unroll
      for (int j = 0; j < 4; ++j) acc[i][j] = Z4; }
    const bf16x8 one = ones8();
    F1 fa, fb;
    auto rd = [&](F1& fr, int s, int ks) {
      lds_cptr B = L + (s % ND) * STG;
#pragma unroll
      for (int seg = 0; seg < 3; ++seg) fr.w[seg] = ldtr2(B + boff + ks * 12288 + seg * 1024, 512);
#pragma unroll
      for (int i = 0; i < 4; ++i) fr.a[i] = ld128(B + aoff[ks][i]);
    };
    auto mm = [&](const F1& fr) {
#pragma unroll
      for (int i = 0; i < 4; ++i) {
        acc[0][i] = MFMA16(fr.w[0], fr.a[i], acc[0][i]);
        acc[1][i] = MFMA16(fr.w[1], fr.a[i], acc[1][i]);
        acc[2][i] = MFMA16(fr.a[i], fr.w[2], acc[2][i]);
      }
      cs[0] = MFMA16(fr.w[0], one, cs[0]); cs[1] = MFMA16(fr.w[1], one, cs[1]); cs[2] = MFMA16(one, fr.w[2], cs[2]);
      sx = MFMA16(one, fr.a[0], sx); sq = MFMA16(fr.a[0], fr.a[0], sq);
    };
    const unsigned pz_off = ((unsigned)(cb * 4 + wid) * 64 + rb * 2) * 1024 + lane * 16;
    lds_bar();
    TS(1);
    rd(fa, 0, 0); SB();
#pragma unroll
    for (int s = 0; s < NS; ++s) {
      rd(fb, s, 1); mm(fa); weave<10, 17>(); SB();
      if (s < 6) st16(ws, (s >> 1 == 0 ? WS_Q : s >> 1 == 1 ? WS_K : WS_V) + pz_off + (s & 1) * 1024, (u32x4){POISON, POISON, POISON, POISON});
      if (s == 6 && lane == 0) __hip_atomic_store((gu32*)(p.ws + WS_KMAX) + (cb * 4 + wid) * 32 + rb, POISON, RLX_AGENT);
      if (s + 1 < NS) { lds_bar(); rd(fa, s + 1, 0); mm(fb); weave<10, 17>(); SB(); }
      else { mm(fb); SB(); }
    }
    TS(2);
    if ((r16 >> 2) == g) {
      const int e = r16 & 3; const float s2 = e == 0 ? sq[0] : e == 1 ? sq[1] : e == 2 ? sq[2] : sq[3];
      float mean, rstd; ln_consts(sx[0], s2, mean, rstd);
      ms[wid * 16 + r16] = (f32x2){mean, rstd};
    }
    wait_bar<0>();
    publish(p.ws, FL_1 + (cb * 32 + rb) * 4, make_tag(epoch));
    TS(25);
    const int h = cb * 4 + wid;
    u32x2 qv[4], kv[4], vv[4];
    float kmx = 0.f;
#pragma unroll
    for (int i = 0; i < 4; ++i) {
      const int rt = (wid + i) & 3;
      const f32x2 st = ms[rt * 16 + r16];
      qv[i] = cvt4((acc[0][i] - st.x * cs[0]) * (st.y * QSCALE));
      { const f32x4 v = (acc[1][i] - st.x * cs[1]) * st.y; kv[i] = cvt4(v);
        kmx = fmaxf(kmx, sum_x32(sum_x16((v[0] * v[0] + v[1] * v[1]) + (v[2] * v[2] + v[3] * v[3])))); }
      { const f32x4 m01 = *(LAS f32x4*)(ms + rt * 16 + 4 * g), m23 = *(LAS f32x4*)(ms + rt * 16 + 4 * g + 2); const float cv = cs[2][0];
        vv[i] = (u32x2){cvtpk((acc[2][i][0] - m01[0] * cv) * m01[1], (acc[2][i][1] - m01[2] * cv) * m01[3]),
                        cvtpk((acc[2][i][2] - m23[0] * cv) * m23[1], (acc[2][i][3] - m23[2] * cv) * m23[3])}; }
    }
#pragma unroll
    for (int i = 0; i < 4; i += 2) {
      { const int rt = (wid + i + (g & 1)) & 3, grow = rb * 64 + rt * 16;
        const unsigned off = (((unsigned)h * 64 + (grow >> 5)) * 2 + (g >> 1)) * 512 + ((grow & 31) + r16) * 16;
        st16(ws, WS_K + off, nop4(pair16(kv[i], kv[i + 1]))); st16(ws, WS_Q + off, nop4(pair16(qv[i], qv[i + 1]))); }
      { const int rt = (wid + i + (g >> 1)) & 3, grow = rb * 64 + rt * 16;
        const unsigned off = ((((unsigned)h * 64 + (grow >> 5)) * 2 + (rt & 1)) * 2 + (g & 1)) * 256 + r16 * 16;
        st16(ws, WS_V + off, nop4(pair32(vv[i], vv[i + 1]))); }
    }
    kmx = max_row16(kmx);
    if (lane == 0) __hip_atomic_store((gu32*)(p.ws + WS_KMAX) + h * 32 + rb, min(__float_as_uint(kmx), POISON - 1), RLX_AGENT);
    TS(26);
  }
}

constexpr int A_K = 0, A_V = 65536, A_ONE = 131072, A_Q = A_ONE + 5120, A_KM = A_Q + 8192, A_DUMMY = A_KM + 1024, A_CTR = A_DUMMY + 256, A_RL = A_CTR + 256, A_BYTES = A_RL + 3 * 4096;
constexpr float SAFE_BOUND = 48.f;
#define PRIO_SW 9
#define SP2 5
#define SP3 8
#define SP4 11
constexpr int PRIO_SWITCH = PRIO_SW;

struct P3Pre { bf16x8 wof[16]; u32x2 xv[4]; };
__device__ __forceinline__ void p2_attn(const Params& p, rsrc_t ws, int b, int h, int qr, char* smem, unsigned tag, P3Pre& pre TSARG) {
  const int tid = threadIdx.x, lane = tid & 63, wid = rfl(tid >> 6);
  const int r32 = lane & 31, hh = lane >> 5;
  const unsigned lds0 = (unsigned)(size_t)(lds_ptr)smem;
  lds_cptr L = (lds_cptr)smem;
  const char* kg = p.ws + WS_K + (size_t)h * 65536;
  const char* vg = p.ws + WS_V + (size_t)h * 65536;
  const unsigned vo = lane * 16;
  if (wid < 4) {
    const int t = 4 * wid, gt = (t + 8 * qr) & 63;
    auto first = [&]() {
      glds<2, true>(p.ws + WS_Q + ((size_t)h * 64 + qr * 8 + 2 * wid) * 1024, vo, lds0 + A_Q + 2 * wid * 1024);
      glds4<true>(p.ws + WS_KMAX + h * 128, (lane & 31) * 4, lds0 + A_KM + wid * 256);
      glds<4, true>(kg + gt * 1024, vo, lds0 + A_K + t * 1024);
      glds<4, true>(vg + gt * 1024, vo, lds0 + A_V + t * 1024);
      wait_vm<0>();
      return pieces_ok<2>(L + A_Q + 2 * wid * 1024) && pieces_ok<4>(L + A_K + t * 1024) && pieces_ok<4>(L + A_V + t * 1024) && !__any(*(const LAS unsigned*)(L + A_KM + wid * 256 + lane * 4) == POISON);
    };
    if (!first()) { Spin sp; while (!first() && !sp.expired(p.ws)) {} }
  }
  st16(ws, WS_O + (unsigned)((qr * 16 + 2 * wid + (lane >> 5)) * 16 + (h >> 1)) * 1024 + (h & 1) * 512 + (lane & 31) * 16, (u32x4){POISON, POISON, POISON, POISON});
  if (tid == 0) *(LAS unsigned*)(smem + A_CTR) = 0u;
  { const unsigned o1 = (tid & 31) < 2 ? 0x3F803F80u : 0u;
    *(LAS u32x2*)(smem + A_ONE + tid * 8) = (u32x2){o1, o1};
    if (tid < 128) *(LAS u32x2*)(smem + A_ONE + 4096 + tid * 8) = (u32x2){o1, o1}; }
  if (wid == 0 && blockIdx.x == 0) {
    poll<256>(p.ws, FL_1, tag);
    if (lane == 0) __hip_atomic_store((gu32*)(p.ws + WS_FLAG + FL_EPOCH), tag, RLX_AGENT);
  }
  lds_bar();
  TS(4);
  const bf16x8 qf = ld128(L + A_Q + wid * 1024 + lane * 16);
  float c;
  { float km = *(const LAS float*)(L + A_KM + lane * 4);
    km = max_x16(max_row16(km));
    float q2 = 0.f;
#pragma unroll
    for (int j = 0; j < 8; ++j) { const float v = __uint_as_float((unsigned)(unsigned short)qf[j] << 16); q2 += v * v; }
    q2 = sum_x32(q2);
    c = sqrtf(q2 * km) * 1.02f + 0.01f; }
  const bool fast = __all(c <= SAFE_BOUND);
  const bool vlane = r32 < 16;
  int vaddr = (int)lds0 + (vlane ? A_V : A_ONE) + hh * 256 + (r32 & 15) * 16;
  const int vstep = vlane ? 4096 : 0;
  int kaddr = (int)lds0 + A_K + lane * 16;
  f32x16 o;
#pragma unroll
  for (int i = 0; i < 16; ++i) o[i] = 0.f;
  unsigned go = vo + ((16 + wid + 8 * qr) & 63) * 1024;
  unsigned ld = lds0 + A_K + (16 + wid) * 1024;
  auto settle = [&]() {
    int lane = threadIdx.x & 63; asm volatile("" : "+v"(lane));
    const unsigned go0 = lane * 16 + ((16 + wid + 8 * qr) & 63) * 1024, ld0 = lds0 + A_K + (16 + wid) * 1024;
    auto good = [&]() {
      unsigned m = 0;
#pragma unroll
      for (int j = 0; j < 12; ++j) { const u32x4 v = *(const LAS u32x4*)(L + (j < 6 ? A_K : A_V) + (16 + wid + 8 * (j % 6)) * 1024 + lane * 16); m = max(m, max(max(v[0], v[1]), max(v[2], v[3]))); }
      return !__any(m == POISON);
    };
    if (!good()) {
      Spin sp;
      do {
#pragma unroll
        for (int j = 0; j < 6; ++j) { const unsigned gj = (go0 + j * 8192) & 0xffffu; glds16<true>(kg, gj, ld0 + j * 8192); glds16<true>(vg, gj, ld0 + 65536 + j * 8192); }
        wait_vm<0>();
      } while (!good() && !sp.expired(p.ws));
    }
    raw_bar();
  };
#define FRESH() int lane = threadIdx.x & 63, bq = b; asm volatile("" : "+v"(lane), "+s"(bq)); const int xq = bq & 7, iq = bq >> 3; \
                const int rb = xq * 4 + (iq & 3), cb = iq >> 2, mb = xq * 2 + (iq & 1), fb = iq >> 1, u0 = fb < 11 ? 3 * fb : 33 + 2 * (fb - 11), nu = fb < 11 ? 3 : 2; \
                const int slot = wid - 1, J = bq * 5 + slot, rlo = slot < 2 ? A_Q + slot * 4096 : A_RL + (slot - 2) * 4096; const bool rl = wid >= 1 && wid <= 5 && J < 1176; \
                (void)rb; (void)cb; (void)mb; (void)u0; (void)nu; (void)rlo; (void)rl; (void)lane
  auto split1 = [&]() {
    FRESH();
    if (rl) { Relayout r; r.decode(p, J); r.load(lds0 + rlo, lane); }
  };
  auto split2 = [&]() {
    FRESH();
    if (rl) { wait_vm<0>(); Relayout r; r.decode(p, J); r.store(ws, L + rlo, lane); }
#pragma unroll
    for (int i = 0; i < 5; ++i) {
      const int j = wid + 8 * i;
      if (j < 33) {
        unsigned off;
        if (j < 8) off = WS_H + (unsigned)((rb * 4 + (j >> 1)) * 16 + cb * 2 + (j & 1)) * 1024 + lane * 16;
        else if (j < 32) { const int jj = j - 8, rt = jj / 3, u = jj - rt * 3; off = WS_ACT + (unsigned)((mb * 8 + rt) * 43 + u0 + (u < nu ? u : 0)) * 1024 + lane * 16; }
        else off = WS_HST + (unsigned)(rb * 8 + cb) * 512 + (lane & 31) * 16;
        st16(ws, off, (u32x4){POISON, POISON, POISON, POISON});
      }
    }
  };
  auto split3 = [&]() {
    FRESH();
    wait_vm<0>();
    if (lane == 0 && __hip_atomic_fetch_add((LAS unsigned*)(smem + A_CTR), 1u, __ATOMIC_RELAXED, __HIP_MEMORY_SCOPE_WORKGROUP) == 7u)
      __hip_atomic_store((gu32*)(p.ws + WS_FLAG + FL_P) + bq, tag, RLX_AGENT);
  };
  auto split4 = [&]() {
    if (wid >= 4) {
      FRESH();
      poll<256>(p.ws, FL_P, tag);
      const int r16 = lane & 15, g = lane >> 4, wn = wid & 3;
#pragma unroll
      for (int i = 0; i < 16; ++i) pre.wof[i] = ldw16(ws, WS_WOF + ((unsigned)(cb * 4 + wn) * 16 + ((i + 8 * cb) & 15)) * 1024 + lane * 16);
#pragma unroll
      for (int i = 0; i < 4; ++i) pre.xv[i] = *(const u32x2*)(p.x + ((size_t)(rb * 64 + i * 16 + r16) * DM + cb * 64 + wn * 16 + 4 * g) * 2);
#pragma unroll
      for (int i = 0; i < 3; ++i) {
        const int ln = (iq & 1) * 768 + (wn * 3 + i) * 64 + lane, ch = ln >> 7, jt = ch >= 6 ? ch - 6 : ch;
        glds4<false>(p.ws + WS_WGF, (unsigned)(((ch >= 6) * 86 + u0 * 2 + (jt < 2 * nu ? jt : jt - 2)) * 128 + (ln & 127)) * 128u, lds0 + A_DUMMY);
      }
    }
  };
#undef FRESH
  if (fast) {
    f32x16 negc, sa, sb; f32x8 pp, kk; f32x16 vv;
#pragma unroll
    for (int i = 0; i < 16; ++i) negc[i] = -c;
    int cnt, keep;
    asm volatile(ATTN_ASM_A
                 : [o] "+v"(o), [ka] "+v"(kaddr), [va] "+v"(vaddr), [go] "+v"(go), [ld] "+s"(ld), [keep] "=&s"(keep),
                   "=&{v[128:143]}"(sa), "=&{v[144:159]}"(sb), "=&{v[160:167]}"(pp), "=&{v[168:175]}"(kk), "=&{v[176:191]}"(vv)
                 : [q] "v"(qf), [nc] "v"(negc), [vs] "v"(vstep), [kg] "s"(kg), [vg] "s"(vg)
                 : "memory", "scc");
    settle(); split1();
    const int sw = wid < 4 ? PRIO_SWITCH : 99;
#define ATTN_B(C0, C1) asm volatile(ATTN_ASM_B \
                 : [o] "+v"(o), [ka] "+v"(kaddr), [va] "+v"(vaddr), [cnt] "=&s"(cnt), \
                   "+{v[128:143]}"(sa), "+{v[144:159]}"(sb), "+{v[160:167]}"(pp), "+{v[168:175]}"(kk), "+{v[176:191]}"(vv) \
                 : [q] "v"(qf), [nc] "v"(negc), [vs] "v"(vstep), [sw] "s"(sw), [c0] "s"(C0), [c1] "s"(C1) \
                 : "memory", "scc")
    ATTN_B(2, SP2); split2();
    ATTN_B(SP2, SP3); split3();
    ATTN_B(SP3, SP4); split4();
    ATTN_B(SP4, 16);
#undef ATTN_B
  } else {
#pragma unroll
    for (int j = 0; j < 6; ++j) { const unsigned gj = (go + j * 8192) & 0xffffu; glds16<true>(kg, gj, ld + j * 8192); glds16<true>(vg, gj, ld + 65536 + j * 8192); }
    float m = -INFINITY;
    vaddr -= (int)lds0; kaddr -= (int)lds0;
#pragma unroll 1
    for (int t4 = 0; t4 < 16; ++t4) {
      if (t4 == 2) { wait_vm<0>(); settle(); split1(); }
      if (t4 == SP2) split2();
      if (t4 == SP3) split3();
      if (t4 == SP4) split4();
#pragma unroll 1
      for (int u = 0; u < 4; ++u) {
        const int t = t4 * 4 + u;
        f32x16 z;
#pragma unroll
        for (int i = 0; i < 16; ++i) z[i] = 0.f;
        const f32x16 s = MFMA32(ld128(L + kaddr + t * 1024), qf, z);
        float mx = s[0];
#pragma unroll
        for (int i = 1; i < 16; ++i) mx = fmaxf(mx, s[i]);
        mx = fmaxf(mx, __shfl_xor(mx, 32));
        const float mn = fmaxf(m, mx);
        const float al = __builtin_amdgcn_exp2f(m - mn);
        m = mn;
#pragma unroll
        for (int i = 0; i < 16; ++i) o[i] *= al;
        u32x4 p0, p1;
#pragma unroll
        for (int i = 0; i < 4; ++i) {
          p0[i] = cvtpk(__builtin_amdgcn_exp2f(s[2 * i] - mn), __builtin_amdgcn_exp2f(s[2 * i + 1] - mn));
          p1[i] = cvtpk(__builtin_amdgcn_exp2f(s[8 + 2 * i] - mn), __builtin_amdgcn_exp2f(s[8 + 2 * i + 1] - mn));
        }
        const bf16x8 v0 = ld128(L + vaddr + u * 1024), v1 = ld128(L + vaddr + u * 1024 + 512);
        o = MFMA32(v0, __builtin_bit_cast(bf16x8, p0), o);
        o = MFMA32(v1, __builtin_bit_cast(bf16x8, p1), o);
      }
      vaddr += vstep;
    }
  }
  TS(5);
  const float inv = __builtin_amdgcn_rcpf(lo_x32(o[8]));
  const u32x4 ov = pair32((u32x2){cvtpk(o[0] * inv, o[1] * inv), cvtpk(o[2] * inv, o[3] * inv)}, (u32x2){cvtpk(o[4] * inv, o[5] * inv), cvtpk(o[6] * inv, o[7] * inv)});
  const int row = qr * 256 + wid * 32 + r32;
  st16(ws, WS_O + ((((unsigned)(row >> 4) * 16 + (h >> 1)) * 4 + (h & 1) * 2 + hh) * 16 + (row & 15)) * 16, nop4(ov));
}

constexpr int G64_KPS = 8, G64_STG = 4 * G64_KPS * 1024;
template <int NKS, int ND, int LA, int NPRE, class Pre>
__device__ __forceinline__ void gemm64_load(char* ws, const char* ag  , int rot, char* smem, const Pre& pre) {
  const int lane = threadIdx.x & 63, wid = rfl(threadIdx.x >> 6);
  const unsigned lds0 = (unsigned)(size_t)(lds_ptr)smem;
  lds_cptr L = (lds_cptr)smem + wid * 8192;
  constexpr int NS = (NKS + 7) / 8, LAST = NKS - 8 * (NS - 1), NFULL = NKS / 8;
  static_assert(LAST == 8 || LAST == 3, "last stage");
  auto issue = [&](int s) {
    const unsigned d = lds0 + (s % ND) * G64_STG + wid * 8192;
    if (s < NFULL) { const int q0 = s + rot, q = q0 >= NFULL ? q0 - NFULL : q0; glds<4, true>(ag + q * 8192, lane * 16, d); glds<4, true>(ag + q * 8192 + 4096, lane * 16, d + 4096); }
    else glds<3, true>(ag + s * 8192, lane * 16, d);
  };
  auto okay = [&](int s) { return s < NFULL ? pieces_ok<8>(L + (s % ND) * G64_STG) : pieces_ok<3>(L + (s % ND) * G64_STG); };
  auto ensure = [&](int s) { if (!okay(s)) { Spin sp; do { issue(s); wait_vm<0>(); } while (!okay(s) && !sp.expired(ws)); } };
  auto waitv = [&](int j) {
    const int hi = j + LA - 2 < NS - 1 ? j + LA - 2 : NS - 1;
    const int n = (hi - j) * 8 - ((hi == NS - 1 && hi > j) ? 8 - LAST : 0);
    switch (n) { case 3: wait_vm<3>(); break; case 8: wait_vm<8>(); break; case 11: wait_vm<11>(); break; case 16: wait_vm<16>(); break; default: wait_vm<0>(); break; }
  };
  issue(0); pre(); wait_vm<NPRE>(); ensure(0);
#pragma unroll
  for (int s = 1; s < LA; ++s) if (s < NS) issue(s);
  lds_bar();
#pragma unroll
  for (int j = 1; j < NS; ++j) {
    waitv(j);
    if (j < NFULL) {
      Chk<8> c; c.read(L + (j % ND) * G64_STG);
      if (j + LA - 1 < NS) issue(j + LA - 1);
      if (!c.ok()) ensure(j);
    } else ensure(j);
    lds_bar();
  }
}
template <int NKS, int ND, int TS0>
__device__ __forceinline__ void gemm64_mma(const bf16x8 (&wf)[NKS], char* smem, f32x4 (&acc)[4] TSARG) {
  const int lane = threadIdx.x & 63;
  lds_cptr L = (lds_cptr)smem + lane * 16;
#pragma unroll
  for (int i = 0; i < 4; ++i) acc[i] = Z4;
  bf16x8 f[2][4];
  auto rd = [&](int ks) {
    lds_cptr B = L + ((ks >> 3) % ND) * G64_STG + (ks & 7) * 1024;
#pragma unroll
    for (int rt = 0; rt < 4; ++rt) f[ks & 1][rt] = ld128(B + rt * 8192);
  };
  lds_bar();
  TS(TS0);
  rd(0); SB();
#pragma unroll
  for (int ks = 0; ks < NKS; ++ks) {
    if (ks + 1 < NKS) {
      if (((ks + 1) & 7) == 0) lds_bar();
      rd(ks + 1);
    }
#pragma unroll
    for (int rt = 0; rt < 4; ++rt) acc[rt] = MFMA16(wf[ks], f[ks & 1][rt], acc[rt]);
    if (ks + 1 < NKS) weave<4, 4>();
    SB();
  }
  TS(TS0 + 1);
}

constexpr int G_B = 0, G_BS = 24576, G_A = 3 * G_BS, G_AS = 16384, G_ST = G_A + 4 * G_AS, G_MS = G_ST + 8192, G_CS = G_MS + 1024;
struct WLoad {
  const char* src[3]; unsigned dst; int rot;
  __device__ __forceinline__ void init(const Params& p, int mb, int fb, int lw, unsigned lds0) {
    const int u0 = fb < 11 ? 3 * fb : 33 + 2 * (fb - 11), nu = fb < 11 ? 3 : 2;
    rot = (fb + 4 * (mb & 1)) & 7;
#pragma unroll
    for (int i = 0; i < 3; ++i) { const int jt = 3 * (lw & 1) + i; src[i] = p.ws + WS_WGF + (size_t)((lw >> 1) * 86 + u0 * 2 + (jt < 2 * nu ? jt : jt - 2)) * 16384; }
    dst = lds0 + G_B + lw * 6144;
  }
  __device__ __forceinline__ void issue(int s, int lane) const {
#pragma unroll
    for (int i = 0; i < 3; ++i) glds<2, true>(src[i] + ((s + rot) & 7) * 2048, lane * 16, dst + (s % 3) * G_BS + i * 2048);
  }
};
struct F4 { bf16x8 a[4], w[2][3]; };
__device__ __forceinline__ void p4_gateup(const Params& p, rsrc_t ws, int mb, int fb, char* smem, const WLoad& wl, int wct, int rot5, bf16x8 (&wdf)[43] TSARG) {
  const int tid = threadIdx.x, lane = tid & 63, wid = rfl(tid >> 6);
  const int r16 = lane & 15, g = lane >> 4;
  const unsigned lds0 = (unsigned)(size_t)(lds_ptr)smem;
  lds_cptr L = (lds_cptr)smem;
  constexpr int NS = 8;
  const int u0 = fb < 11 ? 3 * fb : 33 + 2 * (fb - 11), nu = fb < 11 ? 3 : 2;
  if (wid >= 4) {
    const int lw = wid - 4;
    const char* ag = p.ws + WS_H + (size_t)(mb * 8 + 2 * lw) * 16384;
    auto issueA = [&](int s) {
      const unsigned d = lds0 + G_A + (s & 3) * G_AS + lw * 4096;
#pragma unroll
      for (int i = 0; i < 2; ++i) glds<2, true>(ag + i * 16384 + ((s + wl.rot) & 7) * 2048, lane * 16, d + i * 2048);
    };
    auto issueS = [&]() {
      glds<2, true>(p.ws + WS_HST + (size_t)mb * 8192 + lw * 2048, lane * 16, lds0 + G_ST + lw * 2048);
    };
    auto ensure = [&](int s) {
      lds_cptr A = L + G_A + (s & 3) * G_AS + lw * 4096;
      if (!pieces_ok<4>(A)) { Spin sp; do { issueA(s); wait_vm<0>(); } while (!pieces_ok<4>(A) && !sp.expired(p.ws)); }
    };
    float cs[3] = {0.f, 0.f, 0.f};
    const unsigned one_bits = 0x3F803F80u;
    const bf16x2_t one2 = __builtin_bit_cast(bf16x2_t, one_bits);
    auto colsum = [&](int s) {
#pragma unroll
      for (int i = 0; i < 6; ++i) {
        const u32x4 v = *(const LAS u32x4*)(L + G_B + (s % 3) * G_BS + lw * 6144 + i * 1024 + lane * 16);
#pragma unroll
        for (int e = 0; e < 4; ++e) { const unsigned t = v[e]; cs[i >> 1] = __builtin_amdgcn_fdot2_f32_bf16(__builtin_bit_cast(bf16x2_t, t), one2, cs[i >> 1], false); }
      }
    };
    issueA(0);
    colsum(0); colsum(1);
    wait_vm<0>();
    ensure(0);
    issueA(1); issueS(); issueA(2); wl.issue(2, lane);
    lds_bar();
#pragma unroll
    for (int j = 1; j < NS; ++j) {
      if (j == 1) wait_vm<12>(); else if (j + 1 < NS) wait_vm<10>(); else wait_vm<0>();
      Chk<4> c; c.read(L + G_A + (j & 3) * G_AS + lw * 4096);
      if (j + 2 < NS) issueA(j + 2);
      if (!c.ok()) ensure(j);
      if (j == 2) {
        lds_cptr Sg = L + G_ST + lw * 2048;
        if (!pieces_ok<2>(Sg)) { Spin sp; do { issueS(); wait_vm<0>(); } while (!pieces_ok<2>(Sg) && !sp.expired(p.ws)); }
      }
      if (j >= 2) colsum(j);
      lds_bar();
      if (j + 2 < NS) wl.issue(j + 2, lane);
      if (j == 2) {
        const int row = tid - 256;
        if (row < 128) {
          float a = 0.f, bb = 0.f;
#pragma unroll
          for (int c = 0; c < 8; ++c) { const f32x2 v = *(const LAS f32x2*)(L + G_ST + (row >> 6) * 4096 + c * 512 + (row & 63) * 8); a += v.x; bb += v.y; }
          float mean, rstd; ln_consts(a, bb, mean, rstd);
          *(LAS f32x2*)(smem + G_MS + row * 8) = (f32x2){rstd, -mean * rstd};
        }
      }
    }
#pragma unroll
    for (int i = 0; i < 3; ++i) { const float t = sum_x32(sum_x16(cs[i])); if (g == 0) *(LAS float*)(smem + G_CS + ((lw * 3 + i) * 16 + r16) * 4) = t; }
    lds_bar();
  } else {
    const int wm = wid >> 1, wn = wid & 1;
    const int aoff = G_A + wm * 8192 + lane * 16;
    constexpr int m0 = 0, m1 = 12288;
    const int boff = G_B + wn * 6144 + lane * 16;
    f32x4 acc[2][3][4];
#pragma unroll
    for (int i = 0; i < 3; ++i)
#pragma unroll
      for (int j = 0; j < 4; ++j) { acc[0][i][j] = Z4; acc[1][i][j] = Z4; }
    F4 fa, fb2;
    auto rd = [&](F4& f, int s, int ks) {
      lds_cptr B = L + (s % 3) * G_BS, A = L + (s & 3) * G_AS;
#pragma unroll
      for (int i = 0; i < 4; ++i) f.a[i] = ld128(A + aoff + i * 2048 + ks * 1024);
#pragma unroll
      for (int i = 0; i < 3; ++i) { f.w[0][i] = ld128(B + boff + m0 + i * 2048 + ks * 1024); f.w[1][i] = ld128(B + boff + m1 + i * 2048 + ks * 1024); }
    };
    auto mm = [&](const F4& f) {
#pragma unroll
      for (int m = 0; m < 2; ++m)
#pragma unroll
        for (int i = 0; i < 3; ++i)
#pragma unroll
          for (int j = 0; j < 4; ++j) acc[m][i][j] = MFMA16(f.w[m][i], f.a[j], acc[m][i][j]);
    };
    lds_bar();
    TS(11);
    rd(fa, 0, 0); SB();
#pragma unroll
    for (int s = 0; s < NS; ++s) {
      rd(fb2, s, 1); mm(fa); weave<10, 24>(); SB();
      if (s + 1 < NS) {
        lds_bar();
        rd(fa, s + 1, 0); mm(fb2); weave<10, 24>(); SB();
      }
      else { mm(fb2); SB(); }
    }
    TS(12);
    const LAS float* csl = (const LAS float*)(smem + G_CS);
    lds_bar();
    TS(33);
    const LAS f32x2* ms = (const LAS f32x2*)(smem + G_MS);
    f32x2 st[4];
#pragma unroll
    for (int j = 0; j < 4; ++j) st[j] = ms[wm * 64 + j * 16 + r16];
    constexpr float K1 = -2.885390081777927f * 0.7978845608028654f, K2 = -2.885390081777927f * 0.035677408136300125f;
#pragma unroll
    for (int i = 0; i < 3; ++i) {
      const int jt = 3 * wn + i, u = jt >> 1, jj = jt & 1;
      const f32x4 cg = *(const LAS f32x4*)(csl + jt * 16 + 4 * g), cu = *(const LAS f32x4*)(csl + (6 + jt) * 16 + 4 * g);
      u32x2 av[4];
#pragma unroll
      for (int j = 0; j < 4; ++j) {
        const f32x4 ag2 = acc[0][i][j], au = acc[1][i][j];
        const f32x4 ga = ag2 * st[j].x + st[j].y * cg, up = au * st[j].x + st[j].y * cu;
        const f32x4 z = ga * (ga * ga * K2 + K1);
        const f32x4 d = (f32x4){__builtin_amdgcn_exp2f(z[0]), __builtin_amdgcn_exp2f(z[1]), __builtin_amdgcn_exp2f(z[2]), __builtin_amdgcn_exp2f(z[3])} + 1.f;
        const f32x4 rc = (f32x4){__builtin_amdgcn_rcpf(d[0]), __builtin_amdgcn_rcpf(d[1]), __builtin_amdgcn_rcpf(d[2]), __builtin_amdgcn_rcpf(d[3])};
        av[j] = cvt4((ga * rc) * up);
      }
      if (u < nu) {
#pragma unroll
        for (int j = 0; j < 4; j += 2) {
          const unsigned rt = (unsigned)mb * 8 + wm * 4 + j + (g & 1);
          st16(ws, WS_ACT + (((rt * 43 + (u0 + u)) * 4 + jj * 2 + (g >> 1)) * 16 + r16) * 16, nop4(pair16(av[j], av[j + 1])));
        }
      }
    }
    TS(34);
  }
  if (wid >= 4) {
#pragma unroll
    for (int i = 0; i < 43; ++i) { int ks = i; if (i < 40) { ks += rot5; ks = ks >= 40 ? ks - 40 : ks; } wdf[i] = ldw16(ws, WS_WDF + ((unsigned)wct * 43 + ks) * 1024 + lane * 16); }
  }
}

extern __shared__ __attribute__((aligned(16))) char smem_dyn[];

__global__ __launch_bounds__(512, 2) void mega(Params p) {
  char* smem = smem_dyn;
  const int b = blockIdx.x, x = b & 7, i5 = b >> 3;
  const int tid = threadIdx.x, lane = tid & 63, wid = rfl(tid >> 6);
  const int r16 = lane & 15, g = lane >> 4, wn = wid & 3;
  TS_BEGIN();
  const rsrc_t ws = __builtin_amdgcn_make_buffer_rsrc(p.ws, 0, WS_USED, 0x00020000);
  const unsigned epoch = __hip_atomic_load((gu32*)(p.ws + WS_FLAG + FL_EPOCH), RLX_AGENT);

  const int rb1 = (x >> 1) * 8 + (i5 & 7), cb1 = (x & 1) * 4 + (i5 >> 3);
  p1_qkv(p, ws, b, rb1, cb1, smem, epoch TSPASS);
  const unsigned tag = (unsigned)rfl((int)make_tag(epoch));
  TS(3);

  const int h = x * 4 + (i5 & 3), qr = i5 >> 2;
  const int rb = x * 4 + (i5 & 3), cb = i5 >> 2;
  const int mb = x * 2 + (i5 & 1), fb = i5 >> 1;
  await<32>(p.ws, FL_1 + (h >> 2) * 128, tag);
  TS(27);
  P3Pre pre;
  p2_attn(p, ws, b, h, qr, smem, tag, pre TSPASS);
  lds_bar();
  TS(6);

  const int wct = cb * 4 + wn, col = cb * 64 + wn * 16 + 4 * g;
  u32x2 hv[4];
  if (wid >= 4) {
#pragma unroll
    for (int i = 0; i < 16; ++i) PIN(pre.wof[i]);
#pragma unroll
    for (int i = 0; i < 4; ++i) PIN(pre.xv[i]);
    f32x4 acc[4];
    gemm64_mma<16, 2, 8>(pre.wof, smem + G_A, acc TSPASS);
    LAS f32x2* S = (LAS f32x2*)(smem + G_ST);
#pragma unroll
    for (int i = 0; i < 4; ++i) hv[i] = cvt4(unpack4(pre.xv[i]) + acc[i]);
#pragma unroll
    for (int i = 0; i < 4; i += 2) {
      const unsigned rt = (unsigned)rb * 4 + i + (g & 1);
      st16(ws, WS_H + (((rt * 16 + cb * 2 + (wn >> 1)) * 4 + (wn & 1) * 2 + (g >> 1)) * 16 + r16) * 16, nop4(pair16(hv[i], hv[i + 1])));
    }
#pragma unroll
    for (int i = 0; i < 4; ++i) {
      const f32x4 v = unpack4(hv[i]);
      const float s1 = sum_x32(sum_x16((v[0] + v[1]) + (v[2] + v[3]))), s2 = sum_x32(sum_x16((v[0] * v[0] + v[1] * v[1]) + (v[2] * v[2] + v[3] * v[3])));
      if (g == 0) S[(i * 16 + r16) * 4 + wn] = (f32x2){s1, s2};
    }
    lds_bar();
    if (wid == 4) {
      const f32x2 a = S[lane * 4], bb = S[lane * 4 + 1], c = S[lane * 4 + 2], d = S[lane * 4 + 3];
      const unsigned s1 = __float_as_uint((a.x + bb.x) + (c.x + d.x)), s2 = __float_as_uint((a.y + bb.y) + (c.y + d.y));
      __hip_atomic_store((gu64*)(p.ws + WS_HST + (size_t)(rb * 8 + cb) * 512 + lane * 8), ((unsigned long long)(s2 < POISON ? s2 : POISON - 1) << 32) | (s1 < POISON ? s1 : POISON - 1), RLX_AGENT);
    }
    TS(32);
    raw_bar();
  } else {
    auto pre3 = [&]() {
      WLoad w0; w0.init(p, mb, fb, wid, (unsigned)(size_t)(lds_ptr)smem); w0.issue(0, lane); w0.issue(1, lane);
    };
    gemm64_load<16, 2, 2, 12>(p.ws, p.ws + WS_O + (size_t)(rb * 4 + wid) * 16384, cb & 1, smem + G_A, pre3);
    lds_bar();
    raw_bar();
  }
  TS(10);

  bf16x8 wdf[43];
  { WLoad wl;
    if (wid >= 4) wl.init(p, mb, fb, wid - 4, (unsigned)(size_t)(lds_ptr)smem);
    p4_gateup(p, ws, mb, fb, smem, wl, wct, 8 * (cb % 5), wdf TSPASS); }
  lds_bar();
  TS(13);

  if (wid >= 4) {
#pragma unroll
    for (int i = 0; i < 43; ++i) PIN(wdf[i]);
    f32x4 acc[4];
    gemm64_mma<43, 4, 15>(wdf, smem, acc TSPASS);
#pragma unroll
    for (int i = 0; i < 4; ++i) *(u32x2*)(p.out + ((size_t)(rb * 64 + i * 16 + r16) * DM + col) * 2) = cvt4(unpack4(hv[i]) + acc[i]);
  } else {
    gemm64_load<43, 4, 3, 0>(p.ws, p.ws + WS_ACT + (size_t)(rb * 4 + wid) * 43 * 1024, cb % 5, smem, []() {});
  }
  TS_END();
}

extern "C" void kernel_launch(void* const* d_in, const int* in_sizes, int n_in, void* d_out, int out_size, void* d_ws, size_t ws_size, hipStream_t stream) {
  Params p;
  p.x = (const char*)d_in[0]; p.wqkv = (const char*)d_in[1]; p.wo = (const char*)d_in[2];
  p.wg = (const char*)d_in[4]; p.wu = (const char*)d_in[5]; p.wd = (const char*)d_in[6];
  p.out = (char*)d_out; p.ws = (char*)d_ws;
  constexpr int LDS = 160 * 1024;
  (void)hipFuncSetAttribute((const void*)mega, hipFuncAttributeMaxDynamicSharedMemorySize, LDS);
  mega<<<256, 512, LDS, stream>>>(p);
}
```

```cpp
#include <hip/hip_runtime.h>
#include <stdint.h>
#define ATTN_ASM_A \
  "s_mov_b32 %[keep], m0\n\t" \
  "ds_read_b128 v[168:171], %[ka]\n\t" \
  "ds_read_b128 v[172:175], %[ka] offset:1024\n\t" \
  "ds_read_b128 v[176:179], %[va]\n\t" \
  "ds_read_b128 v[184:187], %[va] offset:512\n\t" \
  "v_mov_b32 v164, 0\n\t" \
  "v_mov_b32 v165, 0\n\t" \
  "v_mov_b32 v166, 0\n\t" \
  "v_mov_b32 v167, 0\n\t" \
  "v_mov_b32 v188, 0\n\t" \
  "v_mov_b32 v189, 0\n\t" \
  "v_mov_b32 v190, 0\n\t" \
  "v_mov_b32 v191, 0\n\t" \
  "s_waitcnt lgkmcnt(3)\n\t" \
  "v_mfma_f32_32x32x16_bf16 v[128:143], v[168:171], %[q], %[nc]\n\t" \
  "s_nop 10\n\t" \
  "s_waitcnt lgkmcnt(2)\n\t" \
  "v_mfma_f32_32x32x16_bf16 v[144:159], v[172:175], %[q], %[nc]\n\t" \
  "ds_read_b128 v[168:171], %[ka] offset:2048\n\t" \
  "s_mov_b32 m0, %[ld]\n\t" \
  "v_exp_f32 v128, v128\n\t" \
  "v_exp_f32 v129, v129\n\t" \
  "global_load_lds_dwordx4 %[go], %[kg] sc1\n\t" \
  "v_exp_f32 v130, v130\n\t" \
  "v_exp_f32 v131, v131\n\t" \
  "v_cvt_pk_bf16_f32 v160, v128, v129\n\t" \
  "v_exp_f32 v132, v132\n\t" \
  "v_exp_f32 v133, v133\n\t" \
  "v_cvt_pk_bf16_f32 v161, v130, v131\n\t" \
  "v_mfma_f32_32x32x16_bf16 %[o], v[188:191], v[164:167], %[o]\n\t" \
  "ds_read_b128 v[180:183], %[va] offset:1024\n\t" \
  "ds_read_b128 v[188:191], %[va] offset:1536\n\t" \
  "s_add_u32 m0, %[ld], 0x10000\n\t" \
  "v_exp_f32 v134, v134\n\t" \
  "v_exp_f32 v135, v135\n\t" \
  "global_load_lds_dwordx4 %[go], %[vg] sc1\n\t" \
  "s_add_u32 %[ld], %[ld], 0x2000\n\t" \
  "v_cvt_pk_bf16_f32 v162, v132, v133\n\t" \
  "v_exp_f32 v136, v136\n\t" \
  "v_exp_f32 v137, v137\n\t" \
  "v_cvt_pk_bf16_f32 v163, v134, v135\n\t" \
  "v_add_u32 %[go], 0x2000, %[go]\n\t" \
  "v_and_b32 %[go], 0xffff, %[go]\n\t" \
  "v_exp_f32 v138, v138\n\t" \
  "v_exp_f32 v139, v139\n\t" \
  "s_waitcnt lgkmcnt(4)\n\t" \
  "v_mfma_f32_32x32x16_bf16 %[o], v[176:179], v[160:163], %[o]\n\t" \
  "v_cvt_pk_bf16_f32 v164, v136, v137\n\t" \
  "v_exp_f32 v140, v140\n\t" \
  "v_exp_f32 v141, v141\n\t" \
  "v_cvt_pk_bf16_f32 v165, v138, v139\n\t" \
  "v_exp_f32 v142, v142\n\t" \
  "v_exp_f32 v143, v143\n\t" \
  "v_cvt_pk_bf16_f32 v166, v140, v141\n\t" \
  "v_cvt_pk_bf16_f32 v167, v142, v143\n\t" \
  "s_waitcnt lgkmcnt(2)\n\t" \
  "v_mfma_f32_32x32x16_bf16 v[128:143], v[168:171], %[q], %[nc]\n\t" \
  "ds_read_b128 v[172:175], %[ka] offset:3072\n\t" \
  "s_mov_b32 m0, %[ld]\n\t" \
  "v_exp_f32 v144, v144\n\t" \
  "v_exp_f32 v145, v145\n\t" \
  "global_load_lds_dwordx4 %[go], %[kg] sc1\n\t" \
  "v_exp_f32 v146, v146\n\t" \
  "v_exp_f32 v147, v147\n\t" \
  "v_cvt_pk_bf16_f32 v160, v144, v145\n\t" \
  "v_exp_f32 v148, v148\n\t" \
  "v_exp_f32 v149, v149\n\t" \
  "v_cvt_pk_bf16_f32 v161, v146, v147\n\t" \
  "v_mfma_f32_32x32x16_bf16 %[o], v[184:187], v[164:167], %[o]\n\t" \
  "ds_read_b128 v[176:179], %[va] offset:2048\n\t" \
  "ds_read_b128 v[184:187], %[va] offset:2560\n\t" \
  "s_add_u32 m0, %[ld], 0x10000\n\t" \
  "v_exp_f32 v150, v150\n\t" \
  "v_exp_f32 v151, v151\n\t" \
  "global_load_lds_dwordx4 %[go], %[vg] sc1\n\t" \
  "s_add_u32 %[ld], %[ld], 0x2000\n\t" \
  "v_cvt_pk_bf16_f32 v162, v148, v149\n\t" \
  "v_exp_f32 v152, v152\n\t" \
  "v_exp_f32 v153, v153\n\t" \
  "v_cvt_pk_bf16_f32 v163, v150, v151\n\t" \
  "v_add_u32 %[go], 0x2000, %[go]\n\t" \
  "v_and_b32 %[go], 0xffff, %[go]\n\t" \
  "v_exp_f32 v154, v154\n\t" \
  "v_exp_f32 v155, v155\n\t" \
  "s_waitcnt lgkmcnt(4)\n\t" \
  "v_mfma_f32_32x32x16_bf16 %[o], v[180:183], v[160:163], %[o]\n\t" \
  "v_cvt_pk_bf16_f32 v164, v152, v153\n\t" \
  "v_exp_f32 v156, v156\n\t" \
  "v_exp_f32 v157, v157\n\t" \
  "v_cvt_pk_bf16_f32 v165, v154, v155\n\t" \
  "v_exp_f32 v158, v158\n\t" \
  "v_exp_f32 v159, v159\n\t" \
  "v_cvt_pk_bf16_f32 v166, v156, v157\n\t" \
  "v_cvt_pk_bf16_f32 v167, v158, v159\n\t" \
  "s_waitcnt lgkmcnt(2)\n\t" \
  "v_mfma_f32_32x32x16_bf16 v[144:159], v[172:175], %[q], %[nc]\n\t" \
  "ds_read_b128 v[168:171], %[ka] offset:4096\n\t" \
  "s_mov_b32 m0, %[ld]\n\t" \
  "v_exp_f32 v128, v128\n\t" \
  "v_exp_f32 v129, v129\n\t" \
  "global_load_lds_dwordx4 %[go], %[kg] sc1\n\t" \
  "v_exp_f32 v130, v130\n\t" \
  "v_exp_f32 v131, v131\n\t" \
  "v_cvt_pk_bf16_f32 v160, v128, v129\n\t" \
  "v_exp_f32 v132, v132\n\t" \
  "v_exp_f32 v133, v133\n\t" \
  "v_cvt_pk_bf16_f32 v161, v130, v131\n\t" \
  "v_mfma_f32_32x32x16_bf16 %[o], v[188:191], v[164:167], %[o]\n\t" \
  "ds_read_b128 v[180:183], %[va] offset:3072\n\t" \
  "ds_read_b128 v[188:191], %[va] offset:3584\n\t" \
  "s_add_u32 m0, %[ld], 0x10000\n\t" \
  "v_exp_f32 v134, v134\n\t" \
  "v_exp_f32 v135, v135\n\t" \
  "global_load_lds_dwordx4 %[go], %[vg] sc1\n\t" \
  "s_add_u32 %[ld], %[ld], 0x2000\n\t" \
  "v_cvt_pk_bf16_f32 v162, v132, v133\n\t" \
  "v_exp_f32 v136, v136\n\t" \
  "v_exp_f32 v137, v137\n\t" \
  "v_cvt_pk_bf16_f32 v163, v134, v135\n\t" \
  "v_add_u32 %[go], 0x2000, %[go]\n\t" \
  "v_and_b32 %[go], 0xffff, %[go]\n\t" \
  "v_exp_f32 v138, v138\n\t" \
  "v_exp_f32 v139, v139\n\t" \
  "s_waitcnt lgkmcnt(4)\n\t" \
  "v_mfma_f32_32x32x16_bf16 %[o], v[176:179], v[160:163], %[o]\n\t" \
  "v_cvt_pk_bf16_f32 v164, v136, v137\n\t" \
  "v_exp_f32 v140, v140\n\t" \
  "v_exp_f32 v141, v141\n\t" \
  "v_cvt_pk_bf16_f32 v165, v138, v139\n\t" \
  "v_exp_f32 v142, v142\n\t" \
  "v_exp_f32 v143, v143\n\t" \
  "v_cvt_pk_bf16_f32 v166, v140, v141\n\t" \
  "v_cvt_pk_bf16_f32 v167, v142, v143\n\t" \
  "s_waitcnt lgkmcnt(2)\n\t" \
  "v_mfma_f32_32x32x16_bf16 v[128:143], v[168:171], %[q], %[nc]\n\t" \
  "ds_read_b128 v[172:175], %[ka] offset:5120\n\t" \
  "s_mov_b32 m0, %[ld]\n\t" \
  "v_exp_f32 v144, v144\n\t" \
  "v_exp_f32 v145, v145\n\t" \
  "global_load_lds_dwordx4 %[go], %[kg] sc1\n\t" \
  "v_exp_f32 v146, v146\n\t" \
  "v_exp_f32 v147, v147\n\t" \
  "v_cvt_pk_bf16_f32 v160, v144, v145\n\t" \
  "v_exp_f32 v148, v148\n\t" \
  "v_exp_f32 v149, v149\n\t" \
  "v_cvt_pk_bf16_f32 v161, v146, v147\n\t" \
  "v_mfma_f32_32x32x16_bf16 %[o], v[184:187], v[164:167], %[o]\n\t" \
  "ds_read_b128 v[176:179], %[va] offset:4096\n\t" \
  "ds_read_b128 v[184:187], %[va] offset:4608\n\t" \
  "s_add_u32 m0, %[ld], 0x10000\n\t" \
  "v_exp_f32 v150, v150\n\t" \
  "v_exp_f32 v151, v151\n\t" \
  "global_load_lds_dwordx4 %[go], %[vg] sc1\n\t" \
  "s_add_u32 %[ld], %[ld], 0x2000\n\t" \
  "v_cvt_pk_bf16_f32 v162, v148, v149\n\t" \
  "v_exp_f32 v152, v152\n\t" \
  "v_exp_f32 v153, v153\n\t" \
  "v_cvt_pk_bf16_f32 v163, v150, v151\n\t" \
  "v_add_u32 %[go], 0x2000, %[go]\n\t" \
  "v_and_b32 %[go], 0xffff, %[go]\n\t" \
  "v_exp_f32 v154, v154\n\t" \
  "v_exp_f32 v155, v155\n\t" \
  "s_waitcnt lgkmcnt(4)\n\t" \
  "v_mfma_f32_32x32x16_bf16 %[o], v[180:183], v[160:163], %[o]\n\t" \
  "v_cvt_pk_bf16_f32 v164, v152, v153\n\t" \
  "v_exp_f32 v156, v156\n\t" \
  "v_exp_f32 v157, v157\n\t" \
  "v_cvt_pk_bf16_f32 v165, v154, v155\n\t" \
  "v_exp_f32 v158, v158\n\t" \
  "v_exp_f32 v159, v159\n\t" \
  "v_cvt_pk_bf16_f32 v166, v156, v157\n\t" \
  "v_cvt_pk_bf16_f32 v167, v158, v159\n\t" \
  "v_add_u32 %[ka], 0x1000, %[ka]\n\t" \
  "v_add_u32 %[va], %[vs], %[va]\n\t" \
  "s_waitcnt lgkmcnt(2)\n\t" \
  "v_mfma_f32_32x32x16_bf16 v[144:159], v[172:175], %[q], %[nc]\n\t" \
  "ds_read_b128 v[168:171], %[ka] offset:2048\n\t" \
  "s_mov_b32 m0, %[ld]\n\t" \
  "v_exp_f32 v128, v128\n\t" \
  "v_exp_f32 v129, v129\n\t" \
  "global_load_lds_dwordx4 %[go], %[kg] sc1\n\t" \
  "v_exp_f32 v130, v130\n\t" \
  "v_exp_f32 v131, v131\n\t" \
  "v_cvt_pk_bf16_f32 v160, v128, v129\n\t" \
  "v_exp_f32 v132, v132\n\t" \
  "v_exp_f32 v133, v133\n\t" \
  "v_cvt_pk_bf16_f32 v161, v130, v131\n\t" \
  "v_mfma_f32_32x32x16_bf16 %[o], v[188:191], v[164:167], %[o]\n\t" \
  "ds_read_b128 v[180:183], %[va] offset:1024\n\t" \
  "ds_read_b128 v[188:191], %[va] offset:1536\n\t" \
  "s_add_u32 m0, %[ld], 0x10000\n\t" \
  "v_exp_f32 v134, v134\n\t" \
  "v_exp_f32 v135, v135\n\t" \
  "global_load_lds_dwordx4 %[go], %[vg] sc1\n\t" \
  "s_add_u32 %[ld], %[ld], 0x2000\n\t" \
  "v_cvt_pk_bf16_f32 v162, v132, v133\n\t" \
  "v_exp_f32 v136, v136\n\t" \
  "v_exp_f32 v137, v137\n\t" \
  "v_cvt_pk_bf16_f32 v163, v134, v135\n\t" \
  "v_add_u32 %[go], 0x2000, %[go]\n\t" \
  "v_and_b32 %[go], 0xffff, %[go]\n\t" \
  "v_exp_f32 v138, v138\n\t" \
  "v_exp_f32 v139, v139\n\t" \
  "s_waitcnt lgkmcnt(4)\n\t" \
  "v_mfma_f32_32x32x16_bf16 %[o], v[176:179], v[160:163], %[o]\n\t" \
  "v_cvt_pk_bf16_f32 v164, v136, v137\n\t" \
  "v_exp_f32 v140, v140\n\t" \
  "v_exp_f32 v141, v141\n\t" \
  "v_cvt_pk_bf16_f32 v165, v138, v139\n\t" \
  "v_exp_f32 v142, v142\n\t" \
  "v_exp_f32 v143, v143\n\t" \
  "v_cvt_pk_bf16_f32 v166, v140, v141\n\t" \
  "v_cvt_pk_bf16_f32 v167, v142, v143\n\t" \
  "s_waitcnt lgkmcnt(2)\n\t" \
  "v_mfma_f32_32x32x16_bf16 v[128:143], v[168:171], %[q], %[nc]\n\t" \
  "ds_read_b128 v[172:175], %[ka] offset:3072\n\t" \
  "s_mov_b32 m0, %[ld]\n\t" \
  "v_exp_f32 v144, v144\n\t" \
  "v_exp_f32 v145, v145\n\t" \
  "global_load_lds_dwordx4 %[go], %[kg] sc1\n\t" \
  "v_exp_f32 v146, v146\n\t" \
  "v_exp_f32 v147, v147\n\t" \
  "v_cvt_pk_bf16_f32 v160, v144, v145\n\t" \
  "v_exp_f32 v148, v148\n\t" \
  "v_exp_f32 v149, v149\n\t" \
  "v_cvt_pk_bf16_f32 v161, v146, v147\n\t" \
  "v_mfma_f32_32x32x16_bf16 %[o], v[184:187], v[164:167], %[o]\n\t" \
  "ds_read_b128 v[176:179], %[va] offset:2048\n\t" \
  "ds_read_b128 v[184:187], %[va] offset:2560\n\t" \
  "s_add_u32 m0, %[ld], 0x10000\n\t" \
  "v_exp_f32 v150, v150\n\t" \
  "v_exp_f32 v151, v151\n\t" \
  "global_load_lds_dwordx4 %[go], %[vg] sc1\n\t" \
  "s_add_u32 %[ld], %[ld], 0x2000\n\t" \
  "v_cvt_pk_bf16_f32 v162, v148, v149\n\t" \
  "v_exp_f32 v152, v152\n\t" \
  "v_exp_f32 v153, v153\n\t" \
  "v_cvt_pk_bf16_f32 v163, v150, v151\n\t" \
  "v_add_u32 %[go], 0x2000, %[go]\n\t" \
  "v_and_b32 %[go], 0xffff, %[go]\n\t" \
  "v_exp_f32 v154, v154\n\t" \
  "v_exp_f32 v155, v155\n\t" \
  "s_waitcnt lgkmcnt(4)\n\t" \
  "v_mfma_f32_32x32x16_bf16 %[o], v[180:183], v[160:163], %[o]\n\t" \
  "v_cvt_pk_bf16_f32 v164, v152, v153\n\t" \
  "v_exp_f32 v156, v156\n\t" \
  "v_exp_f32 v157, v157\n\t" \
  "v_cvt_pk_bf16_f32 v165, v154, v155\n\t" \
  "v_exp_f32 v158, v158\n\t" \
  "v_exp_f32 v159, v159\n\t" \
  "v_cvt_pk_bf16_f32 v166, v156, v157\n\t" \
  "v_cvt_pk_bf16_f32 v167, v158, v159\n\t" \
  "s_waitcnt lgkmcnt(2)\n\t" \
  "v_mfma_f32_32x32x16_bf16 v[144:159], v[172:175], %[q], %[nc]\n\t" \
  "ds_read_b128 v[168:171], %[ka] offset:4096\n\t" \
  "v_exp_f32 v128, v128\n\t" \
  "v_exp_f32 v129, v129\n\t" \
  "v_exp_f32 v130, v130\n\t" \
  "v_exp_f32 v131, v131\n\t" \
  "v_cvt_pk_bf16_f32 v160, v128, v129\n\t" \
  "v_exp_f32 v132, v132\n\t" \
  "v_exp_f32 v133, v133\n\t" \
  "v_cvt_pk_bf16_f32 v161, v130, v131\n\t" \
  "v_mfma_f32_32x32x16_bf16 %[o], v[188:191], v[164:167], %[o]\n\t" \
  "ds_read_b128 v[180:183], %[va] offset:3072\n\t" \
  "ds_read_b128 v[188:191], %[va] offset:3584\n\t" \
  "v_exp_f32 v134, v134\n\t" \
  "v_exp_f32 v135, v135\n\t" \
  "v_cvt_pk_bf16_f32 v162, v132, v133\n\t" \
  "v_exp_f32 v136, v136\n\t" \
  "v_exp_f32 v137, v137\n\t" \
  "v_cvt_pk_bf16_f32 v163, v134, v135\n\t" \
  "v_exp_f32 v138, v138\n\t" \
  "v_exp_f32 v139, v139\n\t" \
  "s_waitcnt lgkmcnt(4)\n\t" \
  "v_mfma_f32_32x32x16_bf16 %[o], v[176:179], v[160:163], %[o]\n\t" \
  "v_cvt_pk_bf16_f32 v164, v136, v137\n\t" \
  "v_exp_f32 v140, v140\n\t" \
  "v_exp_f32 v141, v141\n\t" \
  "v_cvt_pk_bf16_f32 v165, v138, v139\n\t" \
  "v_exp_f32 v142, v142\n\t" \
  "v_exp_f32 v143, v143\n\t" \
  "v_cvt_pk_bf16_f32 v166, v140, v141\n\t" \
  "v_cvt_pk_bf16_f32 v167, v142, v143\n\t" \
  "s_waitcnt lgkmcnt(2)\n\t" \
  "v_mfma_f32_32x32x16_bf16 v[128:143], v[168:171], %[q], %[nc]\n\t" \
  "ds_read_b128 v[172:175], %[ka] offset:5120\n\t" \
  "v_exp_f32 v144, v144\n\t" \
  "v_exp_f32 v145, v145\n\t" \
  "v_exp_f32 v146, v146\n\t" \
  "v_exp_f32 v147, v147\n\t" \
  "v_cvt_pk_bf16_f32 v160, v144, v145\n\t" \
  "v_exp_f32 v148, v148\n\t" \
  "v_exp_f32 v149, v149\n\t" \
  "v_cvt_pk_bf16_f32 v161, v146, v147\n\t" \
  "v_mfma_f32_32x32x16_bf16 %[o], v[184:187], v[164:167], %[o]\n\t" \
  "ds_read_b128 v[176:179], %[va] offset:4096\n\t" \
  "ds_read_b128 v[184:187], %[va] offset:4608\n\t" \
  "v_exp_f32 v150, v150\n\t" \
  "v_exp_f32 v151, v151\n\t" \
  "v_cvt_pk_bf16_f32 v162, v148, v149\n\t" \
  "v_exp_f32 v152, v152\n\t" \
  "v_exp_f32 v153, v153\n\t" \
  "v_cvt_pk_bf16_f32 v163, v150, v151\n\t" \
  "v_exp_f32 v154, v154\n\t" \
  "v_exp_f32 v155, v155\n\t" \
  "s_waitcnt lgkmcnt(4)\n\t" \
  "v_mfma_f32_32x32x16_bf16 %[o], v[180:183], v[160:163], %[o]\n\t" \
  "v_cvt_pk_bf16_f32 v164, v152, v153\n\t" \
  "v_exp_f32 v156, v156\n\t" \
  "v_exp_f32 v157, v157\n\t" \
  "v_cvt_pk_bf16_f32 v165, v154, v155\n\t" \
  "v_exp_f32 v158, v158\n\t" \
  "v_exp_f32 v159, v159\n\t" \
  "v_cvt_pk_bf16_f32 v166, v156, v157\n\t" \
  "v_cvt_pk_bf16_f32 v167, v158, v159\n\t" \
  "v_add_u32 %[ka], 0x1000, %[ka]\n\t" \
  "v_add_u32 %[va], %[vs], %[va]\n\t" \
  "s_mov_b32 m0, %[keep]\n\t" \
  "s_waitcnt vmcnt(0) lgkmcnt(0)\n\t" \
  ""
#define ATTN_ASM_B \
  "s_mov_b32 %[cnt], %[c0]\n\t" \
  "s_cmp_lg_u32 %[c0], 2\n\t" \
  "s_cbranch_scc1 L_attn_loop_%=\n\t" \
  "s_cmp_eq_u32 %[sw], 99\n\t" \
  "s_cbranch_scc1 L_attn_y_%=\n\t" \
  "s_setprio 2\n\t" \
  "s_branch L_attn_loop_%=\n\t" \
  "L_attn_y_%=:\n\t" \
  "s_setprio 1\n\t" \
  "L_attn_loop_%=:\n\t" \
  "s_cmp_lg_u32 %[cnt], %[sw]\n\t" \
  "s_cbranch_scc1 L_attn_nosw_%=\n\t" \
  "s_setprio 0\n\t" \
  "L_attn_nosw_%=:\n\t" \
  "s_waitcnt lgkmcnt(2)\n\t" \
  "v_mfma_f32_32x32x16_bf16 v[144:159], v[172:175], %[q], %[nc]\n\t" \
  "ds_read_b128 v[168:171], %[ka] offset:2048\n\t" \
  "v_exp_f32 v128, v128\n\t" \
  "v_exp_f32 v129, v129\n\t" \
  "v_exp_f32 v130, v130\n\t" \
  "v_exp_f32 v131, v131\n\t" \
  "v_cvt_pk_bf16_f32 v160, v128, v129\n\t" \
  "v_exp_f32 v132, v132\n\t" \
  "v_exp_f32 v133, v133\n\t" \
  "v_cvt_pk_bf16_f32 v161, v130, v131\n\t" \
  "v_mfma_f32_32x32x16_bf16 %[o], v[188:191], v[164:167], %[o]\n\t" \
  "ds_read_b128 v[180:183], %[va] offset:1024\n\t" \
  "ds_read_b128 v[188:191], %[va] offset:1536\n\t" \
  "v_exp_f32 v134, v134\n\t" \
  "v_exp_f32 v135, v135\n\t" \
  "v_cvt_pk_bf16_f32 v162, v132, v133\n\t" \
  "v_exp_f32 v136, v136\n\t" \
  "v_exp_f32 v137, v137\n\t" \
  "v_cvt_pk_bf16_f32 v163, v134, v135\n\t" \
  "v_exp_f32 v138, v138\n\t" \
  "v_exp_f32 v139, v139\n\t" \
  "s_waitcnt lgkmcnt(4)\n\t" \
  "v_mfma_f32_32x32x16_bf16 %[o], v[176:179], v[160:163], %[o]\n\t" \
  "v_cvt_pk_bf16_f32 v164, v136, v137\n\t" \
  "v_exp_f32 v140, v140\n\t" \
  "v_exp_f32 v141, v141\n\t" \
  "v_cvt_pk_bf16_f32 v165, v138, v139\n\t" \
  "v_exp_f32 v142, v142\n\t" \
  "v_exp_f32 v143, v143\n\t" \
  "v_cvt_pk_bf16_f32 v166, v140, v141\n\t" \
  "v_cvt_pk_bf16_f32 v167, v142, v143\n\t" \
  "s_waitcnt lgkmcnt(2)\n\t" \
  "v_mfma_f32_32x32x16_bf16 v[128:143], v[168:171], %[q], %[nc]\n\t" \
  "ds_read_b128 v[172:175], %[ka] offset:3072\n\t" \
  "v_exp_f32 v144, v144\n\t" \
  "v_exp_f32 v145, v145\n\t" \
  "v_exp_f32 v146, v146\n\t" \
  "v_exp_f32 v147, v147\n\t" \
  "v_cvt_pk_bf16_f32 v160, v144, v145\n\t" \
  "v_exp_f32 v148, v148\n\t" \
  "v_exp_f32 v149, v149\n\t" \
  "v_cvt_pk_bf16_f32 v161, v146, v147\n\t" \
  "v_mfma_f32_32x32x16_bf16 %[o], v[184:187], v[164:167], %[o]\n\t" \
  "ds_read_b128 v[176:179], %[va] offset:2048\n\t" \
  "ds_read_b128 v[184:187], %[va] offset:2560\n\t" \
  "v_exp_f32 v150, v150\n\t" \
  "v_exp_f32 v151, v151\n\t" \
  "v_cvt_pk_bf16_f32 v162, v148, v149\n\t" \
  "v_exp_f32 v152, v152\n\t" \
  "v_exp_f32 v153, v153\n\t" \
  "v_cvt_pk_bf16_f32 v163, v150, v151\n\t" \
  "v_exp_f32 v154, v154\n\t" \
  "v_exp_f32 v155, v155\n\t" \
  "s_waitcnt lgkmcnt(4)\n\t" \
  "v_mfma_f32_32x32x16_bf16 %[o], v[180:183], v[160:163], %[o]\n\t" \
  "v_cvt_pk_bf16_f32 v164, v152, v153\n\t" \
  "v_exp_f32 v156, v156\n\t" \
  "v_exp_f32 v157, v157\n\t" \
  "v_cvt_pk_bf16_f32 v165, v154, v155\n\t" \
  "v_exp_f32 v158, v158\n\t" \
  "v_exp_f32 v159, v159\n\t" \
  "v_cvt_pk_bf16_f32 v166, v156, v157\n\t" \
  "v_cvt_pk_bf16_f32 v167, v158, v159\n\t" \
  "s_waitcnt lgkmcnt(2)\n\t" \
  "v_mfma_f32_32x32x16_bf16 v[144:159], v[172:175], %[q], %[nc]\n\t" \
  "ds_read_b128 v[168:171], %[ka] offset:4096\n\t" \
  "v_exp_f32 v128, v128\n\t" \
  "v_exp_f32 v129, v129\n\t" \
  "v_exp_f32 v130, v130\n\t" \
  "v_exp_f32 v131, v131\n\t" \
  "v_cvt_pk_bf16_f32 v160, v128, v129\n\t" \
  "v_exp_f32 v132, v132\n\t" \
  "v_exp_f32 v133, v133\n\t" \
  "v_cvt_pk_bf16_f32 v161, v130, v131\n\t" \
  "v_mfma_f32_32x32x16_bf16 %[o], v[188:191], v[164:167], %[o]\n\t" \
  "ds_read_b128 v[180:183], %[va] offset:3072\n\t" \
  "ds_read_b128 v[188:191], %[va] offset:3584\n\t" \
  "v_exp_f32 v134, v134\n\t" \
  "v_exp_f32 v135, v135\n\t" \
  "v_cvt_pk_bf16_f32 v162, v132, v133\n\t" \
  "v_exp_f32 v136, v136\n\t" \
  "v_exp_f32 v137, v137\n\t" \
  "v_cvt_pk_bf16_f32 v163, v134, v135\n\t" \
  "v_exp_f32 v138, v138\n\t" \
  "v_exp_f32 v139, v139\n\t" \
  "s_waitcnt lgkmcnt(4)\n\t" \
  "v_mfma_f32_32x32x16_bf16 %[o], v[176:179], v[160:163], %[o]\n\t" \
  "v_cvt_pk_bf16_f32 v164, v136, v137\n\t" \
  "v_exp_f32 v140, v140\n\t" \
  "v_exp_f32 v141, v141\n\t" \
  "v_cvt_pk_bf16_f32 v165, v138, v139\n\t" \
  "v_exp_f32 v142, v142\n\t" \
  "v_exp_f32 v143, v143\n\t" \
  "v_cvt_pk_bf16_f32 v166, v140, v141\n\t" \
  "v_cvt_pk_bf16_f32 v167, v142, v143\n\t" \
  "s_waitcnt lgkmcnt(2)\n\t" \
  "v_mfma_f32_32x32x16_bf16 v[128:143], v[168:171], %[q], %[nc]\n\t" \
  "ds_read_b128 v[172:175], %[ka] offset:5120\n\t" \
  "v_exp_f32 v144, v144\n\t" \
  "v_exp_f32 v145, v145\n\t" \
  "v_exp_f32 v146, v146\n\t" \
  "v_exp_f32 v147, v147\n\t" \
  "v_cvt_pk_bf16_f32 v160, v144, v145\n\t" \
  "v_exp_f32 v148, v148\n\t" \
  "v_exp_f32 v149, v149\n\t" \
  "v_cvt_pk_bf16_f32 v161, v146, v147\n\t" \
  "v_mfma_f32_32x32x16_bf16 %[o], v[184:187], v[164:167], %[o]\n\t" \
  "ds_read_b128 v[176:179], %[va] offset:4096\n\t" \
  "ds_read_b128 v[184:187], %[va] offset:4608\n\t" \
  "v_exp_f32 v150, v150\n\t" \
  "v_exp_f32 v151, v151\n\t" \
  "v_cvt_pk_bf16_f32 v162, v148, v149\n\t" \
  "v_exp_f32 v152, v152\n\t" \
  "v_exp_f32 v153, v153\n\t" \
  "v_cvt_pk_bf16_f32 v163, v150, v151\n\t" \
  "v_exp_f32 v154, v154\n\t" \
  "v_exp_f32 v155, v155\n\t" \
  "s_waitcnt lgkmcnt(4)\n\t" \
  "v_mfma_f32_32x32x16_bf16 %[o], v[180:183], v[160:163], %[o]\n\t" \
  "v_cvt_pk_bf16_f32 v164, v152, v153\n\t" \
  "v_exp_f32 v156, v156\n\t" \
  "v_exp_f32 v157, v157\n\t" \
  "v_cvt_pk_bf16_f32 v165, v154, v155\n\t" \
  "v_exp_f32 v158, v158\n\t" \
  "v_exp_f32 v159, v159\n\t" \
  "v_cvt_pk_bf16_f32 v166, v156, v157\n\t" \
  "v_cvt_pk_bf16_f32 v167, v158, v159\n\t" \
  "v_add_u32 %[ka], 0x1000, %[ka]\n\t" \
  "v_add_u32 %[va], %[vs], %[va]\n\t" \
  "s_add_u32 %[cnt], %[cnt], 1\n\t" \
  "s_cmp_lt_u32 %[cnt], %[c1]\n\t" \
  "s_cbranch_scc1 L_attn_loop_%=\n\t" \
  "s_waitcnt lgkmcnt(0)\n\t" \
  "s_cmp_lg_u32 %[c1], 16\n\t" \
  "s_cbranch_scc1 L_attn_end_%=\n\t" \
  "v_mfma_f32_32x32x16_bf16 %[o], v[188:191], v[164:167], %[o]\n\t" \
  "s_setprio 0\n\t" \
  "L_attn_end_%=:\n\t" \
  "s_nop 15\n\t" \
  ""

typedef short bf16x8 __attribute__((ext_vector_type(8)));
typedef short s16x4 __attribute__((ext_vector_type(4)));
typedef float f32x4 __attribute__((ext_vector_type(4)));
typedef float f32x2 __attribute__((ext_vector_type(2)));
typedef float f32x16 __attribute__((ext_vector_type(16)));
typedef float f32x8 __attribute__((ext_vector_type(8)));
typedef unsigned u32x4 __attribute__((ext_vector_type(4)));
typedef unsigned u32x8 __attribute__((ext_vector_type(8)));
typedef unsigned u32x2 __attribute__((ext_vector_type(2)));
typedef __bf16 bf16x2_t __attribute__((ext_vector_type(2)));
#define LAS __attribute__((address_space(3)))
#define GAS __attribute__((address_space(1)))
typedef const LAS char* lds_cptr;
typedef LAS char* lds_ptr;
typedef GAS unsigned gu32;
typedef GAS unsigned long long gu64;
typedef __amdgpu_buffer_rsrc_t rsrc_t;

constexpr int SEQ = 2048, DM = 512, NH = 32, FF = 1376;
constexpr float EPS = 1e-5f;
constexpr float QSCALE = 0.25f * 1.4426950408889634f;

constexpr unsigned WS_Q = 0, WS_K = 2u << 20, WS_V = 4u << 20, WS_O = 6u << 20, WS_H = 8u << 20, WS_ACT = 10u << 20;
constexpr unsigned WS_KMAX = 16u << 20, WS_HST = (16u << 20) + (64u << 10);
constexpr unsigned WS_WOF = 17u << 20, WS_WDF = WS_WOF + 512 * 1024, WS_WGF = WS_WDF + 32 * 43 * 1024;
constexpr unsigned WS_FLAG = 22u << 20;
constexpr unsigned FL_EPOCH = 0, FL_TMO = 256, FL_1 = 1024, FL_P = 4096;
constexpr unsigned POISON = 0xFFFFFFFFu;
constexpr unsigned WS_USED = 24u << 20;

struct Params {
  const char* x; const char* wqkv; const char* wo; const char* wg; const char* wu; const char* wd;
  char* out; char* ws;
};

#define MFMA16(a, b, c) __builtin_amdgcn_mfma_f32_16x16x32_bf16(a, b, c, 0, 0, 0)
#define MFMA32(a, b, c) __builtin_amdgcn_mfma_f32_32x32x16_bf16(a, b, c, 0, 0, 0)
#define PIN(x) asm volatile("" : "+v"(x))
#define SB() __builtin_amdgcn_sched_barrier(0)
#define RLX_AGENT __ATOMIC_RELAXED, __HIP_MEMORY_SCOPE_AGENT

template <int N> __device__ __forceinline__ void wait_bar() {
  asm volatile("s_waitcnt vmcnt(%0) lgkmcnt(0)\n\ts_barrier" ::"n"(N) : "memory");
}
template <int N> __device__ __forceinline__ void wait_vm() { asm volatile("s_waitcnt vmcnt(%0)" ::"n"(N) : "memory"); }
__device__ __forceinline__ void lds_bar() { asm volatile("s_waitcnt lgkmcnt(0)\n\ts_barrier" ::: "memory"); }
__device__ __forceinline__ void raw_bar() { asm volatile("s_barrier" ::: "memory"); }

template <int N, bool SC1> __device__ __forceinline__ void glds(const void* sbase, unsigned voff, unsigned lds_addr) {
#define GL_(O, S) "\n\tglobal_load_lds_dwordx4 %0, %1 offset:" #O S
  if (SC1) {
    if (N == 1) asm volatile("s_mov_b32 m0, %2\n\ts_nop 0" GL_(0, " sc1") :: "v"(voff), "s"(sbase), "s"(lds_addr) : "memory");
    if (N == 2) asm volatile("s_mov_b32 m0, %2\n\ts_nop 0" GL_(0, " sc1") GL_(1024, " sc1") :: "v"(voff), "s"(sbase), "s"(lds_addr) : "memory");
    if (N == 3) asm volatile("s_mov_b32 m0, %2\n\ts_nop 0" GL_(0, " sc1") GL_(1024, " sc1") GL_(2048, " sc1") :: "v"(voff), "s"(sbase), "s"(lds_addr) : "memory");
    if (N == 4) asm volatile("s_mov_b32 m0, %2\n\ts_nop 0" GL_(0, " sc1") GL_(1024, " sc1") GL_(2048, " sc1") GL_(3072, " sc1") :: "v"(voff), "s"(sbase), "s"(lds_addr) : "memory");
  } else {
    if (N == 1) asm volatile("s_mov_b32 m0, %2\n\ts_nop 0" GL_(0, "") :: "v"(voff), "s"(sbase), "s"(lds_addr) : "memory");
    if (N == 2) asm volatile("s_mov_b32 m0, %2\n\ts_nop 0" GL_(0, "") GL_(1024, "") :: "v"(voff), "s"(sbase), "s"(lds_addr) : "memory");
    if (N == 3) asm volatile("s_mov_b32 m0, %2\n\ts_nop 0" GL_(0, "") GL_(1024, "") GL_(2048, "") :: "v"(voff), "s"(sbase), "s"(lds_addr) : "memory");
  }
#undef GL_
}
template <bool SC1> __device__ __forceinline__ void glds16(const void* sbase, unsigned voff, unsigned lds_addr) { glds<1, SC1>(sbase, voff, lds_addr); }
template <bool SC1> __device__ __forceinline__ void glds4(const void* sbase, unsigned voff, unsigned lds_addr) {
  if (SC1) asm volatile("s_mov_b32 m0, %2\n\ts_nop 0\n\tglobal_load_lds_dword %0, %1 sc1" :: "v"(voff), "s"(sbase), "s"(lds_addr) : "memory");
  else     asm volatile("s_mov_b32 m0, %2\n\ts_nop 0\n\tglobal_load_lds_dword %0, %1" :: "v"(voff), "s"(sbase), "s"(lds_addr) : "memory");
}
__device__ __forceinline__ bf16x8 ld128(lds_cptr p) { return *(const LAS bf16x8*)p; }
typedef short v4i16_t __attribute__((ext_vector_type(4)));
__device__ __forceinline__ s16x4 ldtr(lds_cptr p) {
  return __builtin_bit_cast(s16x4, __builtin_amdgcn_ds_read_tr16_b64_v4i16((LAS v4i16_t*)p));
}
__device__ __forceinline__ bf16x8 ldtr2(lds_cptr p, int second) {
  const s16x4 a = ldtr(p), b = ldtr(p + second);
  return (bf16x8){a[0], a[1], a[2], a[3], b[0], b[1], b[2], b[3]};
}
__device__ __forceinline__ unsigned cvtpk(float lo, float hi) {
  f32x2 v = {lo, hi}; bf16x2_t b = __builtin_convertvector(v, bf16x2_t); return __builtin_bit_cast(unsigned, b);
}
__device__ __forceinline__ u32x2 cvt4(f32x4 v) { return (u32x2){cvtpk(v[0], v[1]), cvtpk(v[2], v[3])}; }
__device__ __forceinline__ float bflo(unsigned w) { return __uint_as_float(w << 16); }
__device__ __forceinline__ float bfhi(unsigned w) { return __uint_as_float(w & 0xffff0000u); }
__device__ __forceinline__ f32x4 unpack4(u32x2 w) { return (f32x4){bflo(w[0]), bfhi(w[0]), bflo(w[1]), bfhi(w[1])}; }
__device__ __forceinline__ float bfr(float v) { return bflo(cvtpk(v, 0.f)); }
__device__ __forceinline__ void ln_consts(float sum, float sumsq, float& mean, float& rstd) {
  const float m = sum * (1.f / DM), var = fmaxf(sumsq * (1.f / DM) - m * m, 0.f);
  mean = bfr(m); rstd = __builtin_amdgcn_rcpf(bfr(__builtin_amdgcn_sqrtf(bfr(bfr(var) + EPS))));
}
__device__ __forceinline__ int rfl(int v) { return __builtin_amdgcn_readfirstlane(v); }
__device__ __forceinline__ bf16x8 ones8() { return (bf16x8){0x3F80, 0x3F80, 0x3F80, 0x3F80, 0x3F80, 0x3F80, 0x3F80, 0x3F80}; }
#define Z4 ((f32x4){0.f, 0.f, 0.f, 0.f})
__device__ __forceinline__ void st16(rsrc_t ws, unsigned off, u32x4 v) { __builtin_amdgcn_raw_buffer_store_b128(v, ws, off, 0, 16); }
__device__ __forceinline__ bf16x8 ldw16(rsrc_t ws, unsigned off) { return __builtin_bit_cast(bf16x8, __builtin_amdgcn_raw_buffer_load_b128(ws, off, 0, 16)); }
__device__ __forceinline__ u32x4 pair16(u32x2 X, u32x2 Y) {
  auto a = __builtin_amdgcn_permlane16_swap(X[0], Y[0], false, false);
  auto b = __builtin_amdgcn_permlane16_swap(X[1], Y[1], false, false);
  return (u32x4){a[0], b[0], a[1], b[1]};
}
__device__ __forceinline__ u32x4 pair32(u32x2 X, u32x2 Y) {
  auto a = __builtin_amdgcn_permlane32_swap(X[0], Y[0], false, false);
  auto b = __builtin_amdgcn_permlane32_swap(X[1], Y[1], false, false);
  return (u32x4){a[0], b[0], a[1], b[1]};
}
__device__ __forceinline__ float sum_x16(float v) { auto r = __builtin_amdgcn_permlane16_swap(__float_as_uint(v), __float_as_uint(v), false, false); return __uint_as_float(r[0]) + __uint_as_float(r[1]); }
__device__ __forceinline__ float sum_x32(float v) { auto r = __builtin_amdgcn_permlane32_swap(__float_as_uint(v), __float_as_uint(v), false, false); return __uint_as_float(r[0]) + __uint_as_float(r[1]); }
__device__ __forceinline__ float max_x16(float v) { auto r = __builtin_amdgcn_permlane16_swap(__float_as_uint(v), __float_as_uint(v), false, false); return fmaxf(__uint_as_float(r[0]), __uint_as_float(r[1])); }
__device__ __forceinline__ float lo_x32(float v) { auto r = __builtin_amdgcn_permlane32_swap(__float_as_uint(v), __float_as_uint(v), false, false); return __uint_as_float(r[0]); }
template <int N> __device__ __forceinline__ float ror16(float v) { return __uint_as_float(__builtin_amdgcn_update_dpp(0u, __float_as_uint(v), 0x120 + N, 0xf, 0xf, false)); }
__device__ __forceinline__ float max_row16(float v) { v = fmaxf(v, ror16<8>(v)); v = fmaxf(v, ror16<4>(v)); v = fmaxf(v, ror16<2>(v)); return fmaxf(v, ror16<1>(v)); }
template <int ND, int NM> __device__ __forceinline__ void weave() {
#pragma unroll
  for (int i = 0; i < ND; ++i) { __builtin_amdgcn_sched_group_barrier(0x008, 1, 0); __builtin_amdgcn_sched_group_barrier(0x100, 1, 0); }
  if (NM > ND) __builtin_amdgcn_sched_group_barrier(0x008, NM - ND, 0);
}

__device__ __forceinline__ u32x4 nop4(u32x4 v) { return __builtin_elementwise_min(v, (u32x4){POISON - 1, POISON - 1, POISON - 1, POISON - 1}); }
template <int NP> __device__ __forceinline__ bool pieces_ok(lds_cptr base) {
  const int lane = threadIdx.x & 63;
  unsigned m = 0;
#pragma unroll
  for (int i = 0; i < NP; ++i) { const u32x4 v = *(const LAS u32x4*)(base + i * 1024 + lane * 16); const unsigned a = v[0] > v[1] ? v[0] : v[1], b = v[2] > v[3] ? v[2] : v[3]; m = m > a ? m : a; m = m > b ? m : b; }
  return !__any(m == POISON);
}
template <int NP> struct Chk {
  u32x4 v[NP];
  __device__ __forceinline__ void read(lds_cptr base) {
    const int lane = threadIdx.x & 63;
#pragma unroll
    for (int i = 0; i < NP; ++i) v[i] = *(const LAS u32x4*)(base + i * 1024 + lane * 16);
  }
  __device__ __forceinline__ bool ok() const {
    unsigned m = 0;
#pragma unroll
    for (int i = 0; i < NP; ++i) { const unsigned a = v[i][0] > v[i][1] ? v[i][0] : v[i][1], b = v[i][2] > v[i][3] ? v[i][2] : v[i][3]; m = m > a ? m : a; m = m > b ? m : b; }
    return !__any(m == POISON);
  }
};
struct Spin {
  unsigned n; unsigned long long t0;
  __device__ __forceinline__ Spin() : n(0), t0(__builtin_amdgcn_s_memrealtime()) {}
  __device__ __forceinline__ bool expired(char* ws) {
    if ((++n & 63u) == 0u && __builtin_amdgcn_s_memrealtime() - t0 > 20000000ull) {
      if ((threadIdx.x & 63) == 0) __hip_atomic_store((gu32*)(ws + WS_FLAG + FL_TMO), 1u, RLX_AGENT);
      return true;
    }
    return false;
  }
};

__device__ __forceinline__ unsigned make_tag(unsigned epoch) { return epoch * 0x9E3779B1u + 0x7F4A7C15u; }
__device__ __forceinline__ void publish(char* ws, unsigned flag_off, unsigned tag) {
  if (threadIdx.x == 0) __hip_atomic_store((gu32*)(ws + WS_FLAG + flag_off), tag, RLX_AGENT);
}
template <int NW>
__device__ __forceinline__ void poll(char* ws, unsigned flag_off, unsigned tag) {
  const int lane = threadIdx.x & 63;
  gu32* f = (gu32*)(ws + WS_FLAG + flag_off);
  const unsigned long long t0 = __builtin_amdgcn_s_memrealtime();
  for (unsigned spins = 1;; ++spins) {
    bool ok = true;
    if (NW == 256) {
#pragma unroll
      for (int i = 0; i < 4; ++i) ok &= __hip_atomic_load(f + i * 64 + lane, RLX_AGENT) == tag;
    } else ok = __hip_atomic_load(f + (lane < NW ? lane : 0), RLX_AGENT) == tag;
    if (__all(ok)) break;
    if ((spins & 63u) == 0u && __builtin_amdgcn_s_memrealtime() - t0 > 20000000ull) {
      if (lane == 0) __hip_atomic_store((gu32*)(ws + WS_FLAG + FL_TMO), tag, RLX_AGENT);
      break;
    }
    __builtin_amdgcn_s_sleep(1);
  }
}
template <int NW> __device__ __forceinline__ void await(char* ws, unsigned flag_off, unsigned tag) {
  if (rfl(threadIdx.x >> 6) == 0) poll<NW>(ws, flag_off, tag);
  raw_bar();
}

struct Relayout {
  const char* src; int pitch, dstride, nt; unsigned dst;
  __device__ __forceinline__ void decode(const Params& p, int J) {
    if (J >= 1176) J = 0;
    if (J < 128) { const int ks = J >> 3, cg = J & 7; src = p.wo + ((size_t)ks * 32 * DM + cg * 64) * 2; pitch = DM; dst = WS_WOF + (unsigned)(cg * 4 * 16 + ks) * 1024; dstride = 16 * 1024; nt = 4; }
    else if (J < 472) { const int ks = (J - 128) >> 3, cg = (J - 128) & 7; src = p.wd + ((size_t)ks * 32 * DM + cg * 64) * 2; pitch = DM; dst = WS_WDF + (unsigned)(cg * 4 * 43 + ks) * 1024; dstride = 43 * 1024; nt = 4; }
    else { const int r = J - 472, mat = r >= 352, r2 = r - mat * 352, ks = r2 / 22, cg = r2 - ks * 22;
      src = (mat ? p.wu : p.wg) + ((size_t)ks * 32 * FF + cg * 64) * 2; pitch = FF; dst = WS_WGF + (unsigned)((mat * 86 + cg * 4) * 16 + ks) * 1024; dstride = 16 * 1024; nt = cg == 21 ? 2 : 4; }
  }
  __device__ __forceinline__ void load(unsigned lds, int lane) const {
    const int r = lane >> 3, c = lane & 7;
#pragma unroll
    for (int kg = 0; kg < 4; ++kg) {
      int j = (c >> 1) ^ (((r >> 1) & 1) | ((kg & 1) << 1)); j = j < nt ? j : j - 2;
      glds16<false>(src, (unsigned)((kg * 8 + r) * pitch + j * 16 + (c & 1) * 8) * 2u, lds + kg * 1024);
    }
  }
  __device__ __forceinline__ void store(rsrc_t ws, lds_cptr L, int lane) const {
    const int g = lane >> 4, q = (lane >> 2) & 3, pp = lane & 3, ff = ((q >> 1) & 1) | ((g & 1) << 1);
#pragma unroll
    for (int j = 0; j < 4; ++j)
      if (j < nt) st16(ws, dst + j * dstride + lane * 16, __builtin_bit_cast(u32x4, ldtr2(L + g * 1024 + q * 128 + ((j ^ ff) << 5) + pp * 8, 512)));
  }
};

struct F1 { bf16x8 a[4], w[3]; };
__device__ __forceinline__ void p1_qkv(const Params& p, rsrc_t ws, int b, int rb1, int cb1, char* smem, unsigned epoch) {
  const int tid = threadIdx.x, lane = tid & 63, wid = rfl(tid >> 6);
  const int r16 = lane & 15, g = lane >> 4;
  const unsigned lds0 = (unsigned)(size_t)(lds_ptr)smem;
  lds_cptr L = (lds_cptr)smem;
  constexpr int STG = 32768, ND = 4, NS = 8, MS = ND * STG;
  LAS f32x2* ms = (LAS f32x2*)(smem + MS);
  const int rb = rb1, cb = cb1;
  if (__builtin_expect(wid >= 4, 1)) {
    const int lw = wid - 4;
    const unsigned voffA = (unsigned)((16 * lw + (lane >> 3)) * DM + (((lane & 7) ^ (lane >> 3)) * 8)) * 2u;
    unsigned voffB[2];
#pragma unroll
    for (int i = 0; i < 2; ++i) { const int r = lane >> 3, c = lane & 7, ff = ((r >> 1) & 1) | (i << 1), j = (c >> 1) ^ ff;
      voffB[i] = (unsigned)((16 * lw + 8 * i + r) * (3 * DM) + j * 16 + (c & 1) * 8) * 2u; }
    const char* xa = p.x + (size_t)rb * 64 * DM * 2;
    const char* wb = p.wqkv + (size_t)cb * 64 * 2;
    const int rot = ((b >> 3) & 7) + 2 * (b >> 6);
    auto issue = [&](int s) {
      const unsigned d = lds0 + (s % ND) * STG;
      const int kc = (s + rot) & 7;
#pragma unroll
      for (int i = 0; i < 2; ++i) glds16<false>(xa + kc * 128 + i * 8 * DM * 2, voffA, d + (2 * lw + i) * 1024);
#pragma unroll
      for (int i = 0; i < 2; ++i) glds<3, false>(wb + (size_t)kc * 64 * (3 * DM) * 2, voffB[i], d + 8192 + (2 * lw + i) * 3072);
    };
    issue(0); issue(1);
    wait_vm<8>(); raw_bar();
    issue(2);
#pragma unroll
    for (int j = 1; j < NS; ++j) {
      if (j + 2 < NS) { issue(j + 2); wait_bar<16>(); } else if (j + 1 < NS) wait_bar<8>(); else wait_bar<0>();
    }
    lds_bar();
  } else {
    f32x4 acc[3][4], cs[3], sx = Z4, sq = Z4;
    int aoff[2][4];
#pragma unroll
    for (int ks = 0; ks < 2; ++ks)
#pragma unroll
      for (int i = 0; i < 4; ++i) aoff[ks][i] = ((wid + i) & 3) * 2048 + (r16 >> 3) * 1024 + (r16 & 7) * 128 + (((4 * ks + g) ^ (r16 & 7)) << 4);
    int boff;
    { const int q = (lane >> 2) & 3, pp = lane & 3, ff = ((q >> 1) & 1) | ((g & 1) << 1);
      boff = 8192 + g * 3072 + q * 128 + ((wid ^ ff) << 5) + pp * 8; }
#pragma unroll
    for (int i = 0; i < 3; ++i) { cs[i] = Z4;
#pragma unroll
      for (int j = 0; j < 4; ++j) acc[i][j] = Z4; }
    const bf16x8 one = ones8();
    F1 fa, fb;
    auto rd = [&](F1& fr, int s, int ks) {
      lds_cptr B = L + (s % ND) * STG;
#pragma unroll
      for (int seg = 0; seg < 3; ++seg) fr.w[seg] = ldtr2(B + boff + ks * 12288 + seg * 1024, 512);
#pragma unroll
      for (int i = 0; i < 4; ++i) fr.a[i] = ld128(B + aoff[ks][i]);
    };
    auto mm = [&](const F1& fr) {
#pragma unroll
      for (int i = 0; i < 4; ++i) {
        acc[0][i] = MFMA16(fr.w[0], fr.a[i], acc[0][i]);
        acc[1][i] = MFMA16(fr.w[1], fr.a[i], acc[1][i]);
        acc[2][i] = MFMA16(fr.a[i], fr.w[2], acc[2][i]);
      }
      cs[0] = MFMA16(fr.w[0], one, cs[0]); cs[1] = MFMA16(fr.w[1], one, cs[1]); cs[2] = MFMA16(one, fr.w[2], cs[2]);
      sx = MFMA16(one, fr.a[0], sx); sq = MFMA16(fr.a[0], fr.a[0], sq);
    };
    const unsigned pz_off = ((unsigned)(cb * 4 + wid) * 64 + rb * 2) * 1024 + lane * 16;
    lds_bar();
    rd(fa, 0, 0); SB();
#pragma unroll
    for (int s = 0; s < NS; ++s) {
      rd(fb, s, 1); mm(fa); weave<10, 17>(); SB();
      if (s < 6) st16(ws, (s >> 1 == 0 ? WS_Q : s >> 1 == 1 ? WS_K : WS_V) + pz_off + (s & 1) * 1024, (u32x4){POISON, POISON, POISON, POISON});
      if (s == 6 && lane == 0) __hip_atomic_store((gu32*)(p.ws + WS_KMAX) + (cb * 4 + wid) * 32 + rb, POISON, RLX_AGENT);
      if (s + 1 < NS) { lds_bar(); rd(fa, s + 1, 0); mm(fb); weave<10, 17>(); SB(); }
      else { mm(fb); SB(); }
    }
    if ((r16 >> 2) == g) {
      const int e = r16 & 3; const float s2 = e == 0 ? sq[0] : e == 1 ? sq[1] : e == 2 ? sq[2] : sq[3];
      float mean, rstd; ln_consts(sx[0], s2, mean, rstd);
      ms[wid * 16 + r16] = (f32x2){mean, rstd};
    }
    wait_bar<0>();
    publish(p.ws, FL_1 + (cb * 32 + rb) * 4, make_tag(epoch));
    const int h = cb * 4 + wid;
    u32x2 qv[4], kv[4], vv[4];
    float kmx = 0.f;
#pragma unroll
    for (int i = 0; i < 4; ++i) {
      const int rt = (wid + i) & 3;
      const f32x2 st = ms[rt * 16 + r16];
      qv[i] = cvt4((acc[0][i] - st.x * cs[0]) * (st.y * QSCALE));
      { const f32x4 v = (acc[1][i] - st.x * cs[1]) * st.y; kv[i] = cvt4(v);
        kmx = fmaxf(kmx, sum_x32(sum_x16((v[0] * v[0] + v[1] * v[1]) + (v[2] * v[2] + v[3] * v[3])))); }
      { const f32x4 m01 = *(LAS f32x4*)(ms + rt * 16 + 4 * g), m23 = *(LAS f32x4*)(ms + rt * 16 + 4 * g + 2); const float cv = cs[2][0];
        vv[i] = (u32x2){cvtpk((acc[2][i][0] - m01[0] * cv) * m01[1], (acc[2][i][1] - m01[2] * cv) * m01[3]),
                        cvtpk((acc[2][i][2] - m23[0] * cv) * m23[1], (acc[2][i][3] - m23[2] * cv) * m23[3])}; }
    }
#pragma unroll
    for (int i = 0; i < 4; i += 2) {
      { const int rt = (wid + i + (g & 1)) & 3, grow = rb * 64 + rt * 16;
        const unsigned off = (((unsigned)h * 64 + (grow >> 5)) * 2 + (g >> 1)) * 512 + ((grow & 31) + r16) * 16;
        st16(ws, WS_K + off, nop4(pair16(kv[i], kv[i + 1]))); st16(ws, WS_Q + off, nop4(pair16(qv[i], qv[i + 1]))); }
      { const int rt = (wid + i + (g >> 1)) & 3, grow = rb * 64 + rt * 16;
        const unsigned off = ((((unsigned)h * 64 + (grow >> 5)) * 2 + (rt & 1)) * 2 + (g & 1)) * 256 + r16 * 16;
        st16(ws, WS_V + off, nop4(pair32(vv[i], vv[i + 1]))); }
    }
    kmx = max_row16(kmx);
    if (lane == 0) __hip_atomic_store((gu32*)(p.ws + WS_KMAX) + h * 32 + rb, min(__float_as_uint(kmx), POISON - 1), RLX_AGENT);
  }
}

constexpr int A_K = 0, A_V = 65536, A_ONE = 131072, A_Q = A_ONE + 5120, A_KM = A_Q + 8192, A_DUMMY = A_KM + 1024, A_CTR = A_DUMMY + 256, A_RL = A_CTR + 256, A_BYTES = A_RL + 3 * 4096;
constexpr float SAFE_BOUND = 48.f;
#define PRIO_SW 9
#define SP2 5
#define SP3 8
#define SP4 11
constexpr int PRIO_SWITCH = PRIO_SW;

struct P3Pre { bf16x8 wof[16]; u32x2 xv[4]; };
__device__ __forceinline__ void p2_attn(const Params& p, rsrc_t ws, int b, int h, int qr, char* smem, unsigned tag, P3Pre& pre) {
  const int tid = threadIdx.x, lane = tid & 63, wid = rfl(tid >> 6);
  const int r32 = lane & 31, hh = lane >> 5;
  const unsigned lds0 = (unsigned)(size_t)(lds_ptr)smem;
  lds_cptr L = (lds_cptr)smem;
  const char* kg = p.ws + WS_K + (size_t)h * 65536;
  const char* vg = p.ws + WS_V + (size_t)h * 65536;
  const unsigned vo = lane * 16;
  if (wid < 4) {
    const int t = 4 * wid, gt = (t + 8 * qr) & 63;
    auto first = [&]() {
      glds<2, true>(p.ws + WS_Q + ((size_t)h * 64 + qr * 8 + 2 * wid) * 1024, vo, lds0 + A_Q + 2 * wid * 1024);
      glds4<true>(p.ws + WS_KMAX + h * 128, (lane & 31) * 4, lds0 + A_KM + wid * 256);
      glds<4, true>(kg + gt * 1024, vo, lds0 + A_K + t * 1024);
      glds<4, true>(vg + gt * 1024, vo, lds0 + A_V + t * 1024);
      wait_vm<0>();
      return pieces_ok<2>(L + A_Q + 2 * wid * 1024) && pieces_ok<4>(L + A_K + t * 1024) && pieces_ok<4>(L + A_V + t * 1024) && !__any(*(const LAS unsigned*)(L + A_KM + wid * 256 + lane * 4) == POISON);
    };
    if (!first()) { Spin sp; while (!first() && !sp.expired(p.ws)) {} }
  }
  st16(ws, WS_O + (unsigned)((qr * 16 + 2 * wid + (lane >> 5)) * 16 + (h >> 1)) * 1024 + (h & 1) * 512 + (lane & 31) * 16, (u32x4){POISON, POISON, POISON, POISON});
  if (tid == 0) *(LAS unsigned*)(smem + A_CTR) = 0u;
  { const unsigned o1 = (tid & 31) < 2 ? 0x3F803F80u : 0u;
    *(LAS u32x2*)(smem + A_ONE + tid * 8) = (u32x2){o1, o1};
    if (tid < 128) *(LAS u32x2*)(smem + A_ONE + 4096 + tid * 8) = (u32x2){o1, o1}; }
  if (wid == 0 && blockIdx.x == 0) {
    poll<256>(p.ws, FL_1, tag);
    if (lane == 0) __hip_atomic_store((gu32*)(p.ws + WS_FLAG + FL_EPOCH), tag, RLX_AGENT);
  }
  lds_bar();
  const bf16x8 qf = ld128(L + A_Q + wid * 1024 + lane * 16);
  float c;
  { float km = *(const LAS float*)(L + A_KM + lane * 4);
    km = max_x16(max_row16(km));
    float q2 = 0.f;
#pragma unroll
    for (int j = 0; j < 8; ++j) { const float v = __uint_as_float((unsigned)(unsigned short)qf[j] << 16); q2 += v * v; }
    q2 = sum_x32(q2);
    c = sqrtf(q2 * km) * 1.02f + 0.01f; }
  const bool fast = __all(c <= SAFE_BOUND);
  const bool vlane = r32 < 16;
  int vaddr = (int)lds0 + (vlane ? A_V : A_ONE) + hh * 256 + (r32 & 15) * 16;
  const int vstep = vlane ? 4096 : 0;
  int kaddr = (int)lds0 + A_K + lane * 16;
  f32x16 o;
#pragma unroll
  for (int i = 0; i < 16; ++i) o[i] = 0.f;
  unsigned go = vo + ((16 + wid + 8 * qr) & 63) * 1024;
  unsigned ld = lds0 + A_K + (16 + wid) * 1024;
  auto settle = [&]() {
    int lane = threadIdx.x & 63; asm volatile("" : "+v"(lane));
    const unsigned go0 = lane * 16 + ((16 + wid + 8 * qr) & 63) * 1024, ld0 = lds0 + A_K + (16 + wid) * 1024;
    auto good = [&]() {
      unsigned m = 0;
#pragma unroll
      for (int j = 0; j < 12; ++j) { const u32x4 v = *(const LAS u32x4*)(L + (j < 6 ? A_K : A_V) + (16 + wid + 8 * (j % 6)) * 1024 + lane * 16); m = max(m, max(max(v[0], v[1]), max(v[2], v[3]))); }
      return !__any(m == POISON);
    };
    if (!good()) {
      Spin sp;
      do {
#pragma unroll
        for (int j = 0; j < 6; ++j) { const unsigned gj = (go0 + j * 8192) & 0xffffu; glds16<true>(kg, gj, ld0 + j * 8192); glds16<true>(vg, gj, ld0 + 65536 + j * 8192); }
        wait_vm<0>();
      } while (!good() && !sp.expired(p.ws));
    }
    raw_bar();
  };
#define FRESH() int lane = threadIdx.x & 63, bq = b; asm volatile("" : "+v"(lane), "+s"(bq)); const int xq = bq & 7, iq = bq >> 3; \
                const int rb = xq * 4 + (iq & 3), cb = iq >> 2, mb = xq * 2 + (iq & 1), fb = iq >> 1, u0 = fb < 11 ? 3 * fb : 33 + 2 * (fb - 11), nu = fb < 11 ? 3 : 2; \
                const int slot = wid - 1, J = bq * 5 + slot, rlo = slot < 2 ? A_Q + slot * 4096 : A_RL + (slot - 2) * 4096; const bool rl = wid >= 1 && wid <= 5 && J < 1176; \
                (void)rb; (void)cb; (void)mb; (void)u0; (void)nu; (void)rlo; (void)rl; (void)lane
  auto split1 = [&]() {
    FRESH();
    if (rl) { Relayout r; r.decode(p, J); r.load(lds0 + rlo, lane); }
  };
  auto split2 = [&]() {
    FRESH();
    if (rl) { wait_vm<0>(); Relayout r; r.decode(p, J); r.store(ws, L + rlo, lane); }
#pragma unroll
    for (int i = 0; i < 5; ++i) {
      const int j = wid + 8 * i;
      if (j < 33) {
        unsigned off;
        if (j < 8) off = WS_H + (unsigned)((rb * 4 + (j >> 1)) * 16 + cb * 2 + (j & 1)) * 1024 + lane * 16;
        else if (j < 32) { const int jj = j - 8, rt = jj / 3, u = jj - rt * 3; off = WS_ACT + (unsigned)((mb * 8 + rt) * 43 + u0 + (u < nu ? u : 0)) * 1024 + lane * 16; }
        else off = WS_HST + (unsigned)(rb * 8 + cb) * 512 + (lane & 31) * 16;
        st16(ws, off, (u32x4){POISON, POISON, POISON, POISON});
      }
    }
  };
  auto split3 = [&]() {
    FRESH();
    wait_vm<0>();
    if (lane == 0 && __hip_atomic_fetch_add((LAS unsigned*)(smem + A_CTR), 1u, __ATOMIC_RELAXED, __HIP_MEMORY_SCOPE_WORKGROUP) == 7u)
      __hip_atomic_store((gu32*)(p.ws + WS_FLAG + FL_P) + bq, tag, RLX_AGENT);
  };
  auto split4 = [&]() {
    if (wid >= 4) {
      FRESH();
      poll<256>(p.ws, FL_P, tag);
      const int r16 = lane & 15, g = lane >> 4, wn = wid & 3;
#pragma unroll
      for (int i = 0; i < 16; ++i) pre.wof[i] = ldw16(ws, WS_WOF + ((unsigned)(cb * 4 + wn) * 16 + ((i + 8 * cb) & 15)) * 1024 + lane * 16);
#pragma unroll
      for (int i = 0; i < 4; ++i) pre.xv[i] = *(const u32x2*)(p.x + ((size_t)(rb * 64 + i * 16 + r16) * DM + cb * 64 + wn * 16 + 4 * g) * 2);
#pragma unroll
      for (int i = 0; i < 3; ++i) {
        const int ln = (iq & 1) * 768 + (wn * 3 + i) * 64 + lane, ch = ln >> 7, jt = ch >= 6 ? ch - 6 : ch;
        glds4<false>(p.ws + WS_WGF, (unsigned)(((ch >= 6) * 86 + u0 * 2 + (jt < 2 * nu ? jt : jt - 2)) * 128 + (ln & 127)) * 128u, lds0 + A_DUMMY);
      }
    }
  };
#undef FRESH
  if (fast) {
    f32x16 negc, sa, sb; f32x8 pp, kk; f32x16 vv;
#pragma unroll
    for (int i = 0; i < 16; ++i) negc[i] = -c;
    int cnt, keep;
    asm volatile(ATTN_ASM_A
                 : [o] "+v"(o), [ka] "+v"(kaddr), [va] "+v"(vaddr), [go] "+v"(go), [ld] "+s"(ld), [keep] "=&s"(keep),
                   "=&{v[128:143]}"(sa), "=&{v[144:159]}"(sb), "=&{v[160:167]}"(pp), "=&{v[168:175]}"(kk), "=&{v[176:191]}"(vv)
                 : [q] "v"(qf), [nc] "v"(negc), [vs] "v"(vstep), [kg] "s"(kg), [vg] "s"(vg)
                 : "memory", "scc");
    settle(); split1();
    const int sw = wid < 4 ? PRIO_SWITCH : 99;
#define ATTN_B(C0, C1) asm volatile(ATTN_ASM_B \
                 : [o] "+v"(o), [ka] "+v"(kaddr), [va] "+v"(vaddr), [cnt] "=&s"(cnt), \
                   "+{v[128:143]}"(sa), "+{v[144:159]}"(sb), "+{v[160:167]}"(pp), "+{v[168:175]}"(kk), "+{v[176:191]}"(vv) \
                 : [q] "v"(qf), [nc] "v"(negc), [vs] "v"(vstep), [sw] "s"(sw), [c0] "s"(C0), [c1] "s"(C1) \
                 : "memory", "scc")
    ATTN_B(2, SP2); split2();
    ATTN_B(SP2, SP3); split3();
    ATTN_B(SP3, SP4); split4();
    ATTN_B(SP4, 16);
#undef ATTN_B
  } else {
#pragma unroll
    for (int j = 0; j < 6; ++j) { const unsigned gj = (go + j * 8192) & 0xffffu; glds16<true>(kg, gj, ld + j * 8192); glds16<true>(vg, gj, ld + 65536 + j * 8192); }
    float m = -INFINITY;
    vaddr -= (int)lds0; kaddr -= (int)lds0;
#pragma unroll 1
    for (int t4 = 0; t4 < 16; ++t4) {
      if (t4 == 2) { wait_vm<0>(); settle(); split1(); }
      if (t4 == SP2) split2();
      if (t4 == SP3) split3();
      if (t4 == SP4) split4();
#pragma unroll 1
      for (int u = 0; u < 4; ++u) {
        const int t = t4 * 4 + u;
        f32x16 z;
#pragma unroll
        for (int i = 0; i < 16; ++i) z[i] = 0.f;
        const f32x16 s = MFMA32(ld128(L + kaddr + t * 1024), qf, z);
        float mx = s[0];
#pragma unroll
        for (int i = 1; i < 16; ++i) mx = fmaxf(mx, s[i]);
        mx = fmaxf(mx, __shfl_xor(mx, 32));
        const float mn = fmaxf(m, mx);
        const float al = __builtin_amdgcn_exp2f(m - mn);
        m = mn;
#pragma unroll
        for (int i = 0; i < 16; ++i) o[i] *= al;
        u32x4 p0, p1;
#pragma unroll
        for (int i = 0; i < 4; ++i) {
          p0[i] = cvtpk(__builtin_amdgcn_exp2f(s[2 * i] - mn), __builtin_amdgcn_exp2f(s[2 * i + 1] - mn));
          p1[i] = cvtpk(__builtin_amdgcn_exp2f(s[8 + 2 * i] - mn), __builtin_amdgcn_exp2f(s[8 + 2 * i + 1] - mn));
        }
        const bf16x8 v0 = ld128(L + vaddr + u * 1024), v1 = ld128(L + vaddr + u * 1024 + 512);
        o = MFMA32(v0, __builtin_bit_cast(bf16x8, p0), o);
        o = MFMA32(v1, __builtin_bit_cast(bf16x8, p1), o);
      }
      vaddr += vstep;
    }
  }
  const float inv = __builtin_amdgcn_rcpf(lo_x32(o[8]));
  const u32x4 ov = pair32((u32x2){cvtpk(o[0] * inv, o[1] * inv), cvtpk(o[2] * inv, o[3] * inv)}, (u32x2){cvtpk(o[4] * inv, o[5] * inv), cvtpk(o[6] * inv, o[7] * inv)});
  const int row = qr * 256 + wid * 32 + r32;
  st16(ws, WS_O + ((((unsigned)(row >> 4) * 16 + (h >> 1)) * 4 + (h & 1) * 2 + hh) * 16 + (row & 15)) * 16, nop4(ov));
}

constexpr int G64_KPS = 8, G64_STG = 4 * G64_KPS * 1024;
template <int NKS, int ND, int LA, int NPRE, class Pre>
__device__ __forceinline__ void gemm64_load(char* ws, const char* ag  , int rot, char* smem, const Pre& pre) {
  const int lane = threadIdx.x & 63, wid = rfl(threadIdx.x >> 6);
  const unsigned lds0 = (unsigned)(size_t)(lds_ptr)smem;
  lds_cptr L = (lds_cptr)smem + wid * 8192;
  constexpr int NS = (NKS + 7) / 8, LAST = NKS - 8 * (NS - 1), NFULL = NKS / 8;
  static_assert(LAST == 8 || LAST == 3, "last stage");
  auto issue = [&](int s) {
    const unsigned d = lds0 + (s % ND) * G64_STG + wid * 8192;
    if (s < NFULL) { const int q0 = s + rot, q = q0 >= NFULL ? q0 - NFULL : q0; glds<4, true>(ag + q * 8192, lane * 16, d); glds<4, true>(ag + q * 8192 + 4096, lane * 16, d + 4096); }
    else glds<3, true>(ag + s * 8192, lane * 16, d);
  };
  auto okay = [&](int s) { return s < NFULL ? pieces_ok<8>(L + (s % ND) * G64_STG) : pieces_ok<3>(L + (s % ND) * G64_STG); };
  auto ensure = [&](int s) { if (!okay(s)) { Spin sp; do { issue(s); wait_vm<0>(); } while (!okay(s) && !sp.expired(ws)); } };
  auto waitv = [&](int j) {
    const int hi = j + LA - 2 < NS - 1 ? j + LA - 2 : NS - 1;
    const int n = (hi - j) * 8 - ((hi == NS - 1 && hi > j) ? 8 - LAST : 0);
    switch (n) { case 3: wait_vm<3>(); break; case 8: wait_vm<8>(); break; case 11: wait_vm<11>(); break; case 16: wait_vm<16>(); break; default: wait_vm<0>(); break; }
  };
  issue(0); pre(); wait_vm<NPRE>(); ensure(0);
#pragma unroll
  for (int s = 1; s < LA; ++s) if (s < NS) issue(s);
  lds_bar();
#pragma unroll
  for (int j = 1; j < NS; ++j) {
    waitv(j);
    if (j < NFULL) {
      Chk<8> c; c.read(L + (j % ND) * G64_STG);
      if (j + LA - 1 < NS) issue(j + LA - 1);
      if (!c.ok()) ensure(j);
    } else ensure(j);
    lds_bar();
  }
}
template <int NKS, int ND>
__device__ __forceinline__ void gemm64_mma(const bf16x8 (&wf)[NKS], char* smem, f32x4 (&acc)[4]) {
  const int lane = threadIdx.x & 63;
  lds_cptr L = (lds_cptr)smem + lane * 16;
#pragma unroll
  for (int i = 0; i < 4; ++i) acc[i] = Z4;
  bf16x8 f[2][4];
  auto rd = [&](int ks) {
    lds_cptr B = L + ((ks >> 3) % ND) * G64_STG + (ks & 7) * 1024;
#pragma unroll
    for (int rt = 0; rt < 4; ++rt) f[ks & 1][rt] = ld128(B + rt * 8192);
  };
  lds_bar();

  rd(0); SB();
#pragma unroll
  for (int ks = 0; ks < NKS; ++ks) {
    if (ks + 1 < NKS) {
      if (((ks + 1) & 7) == 0) lds_bar();
      rd(ks + 1);
    }
#pragma unroll
    for (int rt = 0; rt < 4; ++rt) acc[rt] = MFMA16(wf[ks], f[ks & 1][rt], acc[rt]);
    if (ks + 1 < NKS) weave<4, 4>();
    SB();
  }

}

constexpr int G_B = 0, G_BS = 24576, G_A = 3 * G_BS, G_AS = 16384, G_ST = G_A + 4 * G_AS, G_MS = G_ST + 8192, G_CS = G_MS + 1024;
struct WLoad {
  const char* src[3]; unsigned dst; int rot;
  __device__ __forceinline__ void init(const Params& p, int mb, int fb, int lw, unsigned lds0) {
    const int u0 = fb < 11 ? 3 * fb : 33 + 2 * (fb - 11), nu = fb < 11 ? 3 : 2;
    rot = (fb + 4 * (mb & 1)) & 7;
#pragma unroll
    for (int i = 0; i < 3; ++i) { const int jt = 3 * (lw & 1) + i; src[i] = p.ws + WS_WGF + (size_t)((lw >> 1) * 86 + u0 * 2 + (jt < 2 * nu ? jt : jt - 2)) * 16384; }
    dst = lds0 + G_B + lw * 6144;
  }
  __device__ __forceinline__ void issue(int s, int lane) const {
#pragma unroll
    for (int i = 0; i < 3; ++i) glds<2, true>(src[i] + ((s + rot) & 7) * 2048, lane * 16, dst + (s % 3) * G_BS + i * 2048);
  }
};
struct F4 { bf16x8 a[4], w[2][3]; };
__device__ __forceinline__ void p4_gateup(const Params& p, rsrc_t ws, int mb, int fb, char* smem, const WLoad& wl, int wct, int rot5, bf16x8 (&wdf)[43]) {
  const int tid = threadIdx.x, lane = tid & 63, wid = rfl(tid >> 6);
  const int r16 = lane & 15, g = lane >> 4;
  const unsigned lds0 = (unsigned)(size_t)(lds_ptr)smem;
  lds_cptr L = (lds_cptr)smem;
  constexpr int NS = 8;
  const int u0 = fb < 11 ? 3 * fb : 33 + 2 * (fb - 11), nu = fb < 11 ? 3 : 2;
  if (wid >= 4) {
    const int lw = wid - 4;
    const char* ag = p.ws + WS_H + (size_t)(mb * 8 + 2 * lw) * 16384;
    auto issueA = [&](int s) {
      const unsigned d = lds0 + G_A + (s & 3) * G_AS + lw * 4096;
#pragma unroll
      for (int i = 0; i < 2; ++i) glds<2, true>(ag + i * 16384 + ((s + wl.rot) & 7) * 2048, lane * 16, d + i * 2048);
    };
    auto issueS = [&]() {
      glds<2, true>(p.ws + WS_HST + (size_t)mb * 8192 + lw * 2048, lane * 16, lds0 + G_ST + lw * 2048);
    };
    auto ensure = [&](int s) {
      lds_cptr A = L + G_A + (s & 3) * G_AS + lw * 4096;
      if (!pieces_ok<4>(A)) { Spin sp; do { issueA(s); wait_vm<0>(); } while (!pieces_ok<4>(A) && !sp.expired(p.ws)); }
    };
    float cs[3] = {0.f, 0.f, 0.f};
    const unsigned one_bits = 0x3F803F80u;
    const bf16x2_t one2 = __builtin_bit_cast(bf16x2_t, one_bits);
    auto colsum = [&](int s) {
#pragma unroll
      for (int i = 0; i < 6; ++i) {
        const u32x4 v = *(const LAS u32x4*)(L + G_B + (s % 3) * G_BS + lw * 6144 + i * 1024 + lane * 16);
#pragma unroll
        for (int e = 0; e < 4; ++e) { const unsigned t = v[e]; cs[i >> 1] = __builtin_amdgcn_fdot2_f32_bf16(__builtin_bit_cast(bf16x2_t, t), one2, cs[i >> 1], false); }
      }
    };
    issueA(0);
    colsum(0); colsum(1);
    wait_vm<0>();
    ensure(0);
    issueA(1); issueS(); issueA(2); wl.issue(2, lane);
    lds_bar();
#pragma unroll
    for (int j = 1; j < NS; ++j) {
      if (j == 1) wait_vm<12>(); else if (j + 1 < NS) wait_vm<10>(); else wait_vm<0>();
      Chk<4> c; c.read(L + G_A + (j & 3) * G_AS + lw * 4096);
      if (j + 2 < NS) issueA(j + 2);
      if (!c.ok()) ensure(j);
      if (j == 2) {
        lds_cptr Sg = L + G_ST + lw * 2048;
        if (!pieces_ok<2>(Sg)) { Spin sp; do { issueS(); wait_vm<0>(); } while (!pieces_ok<2>(Sg) && !sp.expired(p.ws)); }
      }
      if (j >= 2) colsum(j);
      lds_bar();
      if (j + 2 < NS) wl.issue(j + 2, lane);
      if (j == 2) {
        const int row = tid - 256;
        if (row < 128) {
          float a = 0.f, bb = 0.f;
#pragma unroll
          for (int c = 0; c < 8; ++c) { const f32x2 v = *(const LAS f32x2*)(L + G_ST + (row >> 6) * 4096 + c * 512 + (row & 63) * 8); a += v.x; bb += v.y; }
          float mean, rstd; ln_consts(a, bb, mean, rstd);
          *(LAS f32x2*)(smem + G_MS + row * 8) = (f32x2){rstd, -mean * rstd};
        }
      }
    }
#pragma unroll
    for (int i = 0; i < 3; ++i) { const float t = sum_x32(sum_x16(cs[i])); if (g == 0) *(LAS float*)(smem + G_CS + ((lw * 3 + i) * 16 + r16) * 4) = t; }
    lds_bar();
  } else {
    const int wm = wid >> 1, wn = wid & 1;
    const int aoff = G_A + wm * 8192 + lane * 16;
    constexpr int m0 = 0, m1 = 12288;
    const int boff = G_B + wn * 6144 + lane * 16;
    f32x4 acc[2][3][4];
#pragma unroll
    for (int i = 0; i < 3; ++i)
#pragma unroll
      for (int j = 0; j < 4; ++j) { acc[0][i][j] = Z4; acc[1][i][j] = Z4; }
    F4 fa, fb2;
    auto rd = [&](F4& f, int s, int ks) {
      lds_cptr B = L + (s % 3) * G_BS, A = L + (s & 3) * G_AS;
#pragma unroll
      for (int i = 0; i < 4; ++i) f.a[i] = ld128(A + aoff + i * 2048 + ks * 1024);
#pragma unroll
      for (int i = 0; i < 3; ++i) { f.w[0][i] = ld128(B + boff + m0 + i * 2048 + ks * 1024); f.w[1][i] = ld128(B + boff + m1 + i * 2048 + ks * 1024); }
    };
    auto mm = [&](const F4& f) {
#pragma unroll
      for (int m = 0; m < 2; ++m)
#pragma unroll
        for (int i = 0; i < 3; ++i)
#pragma unroll
          for (int j = 0; j < 4; ++j) acc[m][i][j] = MFMA16(f.w[m][i], f.a[j], acc[m][i][j]);
    };
    lds_bar();
    rd(fa, 0, 0); SB();
#pragma unroll
    for (int s = 0; s < NS; ++s) {
      rd(fb2, s, 1); mm(fa); weave<10, 24>(); SB();
      if (s + 1 < NS) {
        lds_bar();
        rd(fa, s + 1, 0); mm(fb2); weave<10, 24>(); SB();
      }
      else { mm(fb2); SB(); }
    }
    const LAS float* csl = (const LAS float*)(smem + G_CS);
    lds_bar();
    const LAS f32x2* ms = (const LAS f32x2*)(smem + G_MS);
    f32x2 st[4];
#pragma unroll
    for (int j = 0; j < 4; ++j) st[j] = ms[wm * 64 + j * 16 + r16];
    constexpr float K1 = -2.885390081777927f * 0.7978845608028654f, K2 = -2.885390081777927f * 0.035677408136300125f;
#pragma unroll
    for (int i = 0; i < 3; ++i) {
      const int jt = 3 * wn + i, u = jt >> 1, jj = jt & 1;
      const f32x4 cg = *(const LAS f32x4*)(csl + jt * 16 + 4 * g), cu = *(const LAS f32x4*)(csl + (6 + jt) * 16 + 4 * g);
      u32x2 av[4];
#pragma unroll
      for (int j = 0; j < 4; ++j) {
        const f32x4 ag2 = acc[0][i][j], au = acc[1][i][j];
        const f32x4 ga = ag2 * st[j].x + st[j].y * cg, up = au * st[j].x + st[j].y * cu;
        const f32x4 z = ga * (ga * ga * K2 + K1);
        const f32x4 d = (f32x4){__builtin_amdgcn_exp2f(z[0]), __builtin_amdgcn_exp2f(z[1]), __builtin_amdgcn_exp2f(z[2]), __builtin_amdgcn_exp2f(z[3])} + 1.f;
        const f32x4 rc = (f32x4){__builtin_amdgcn_rcpf(d[0]), __builtin_amdgcn_rcpf(d[1]), __builtin_amdgcn_rcpf(d[2]), __builtin_amdgcn_rcpf(d[3])};
        av[j] = cvt4((ga * rc) * up);
      }
      if (u < nu) {
#pragma unroll
        for (int j = 0; j < 4; j += 2) {
          const unsigned rt = (unsigned)mb * 8 + wm * 4 + j + (g & 1);
          st16(ws, WS_ACT + (((rt * 43 + (u0 + u)) * 4 + jj * 2 + (g >> 1)) * 16 + r16) * 16, nop4(pair16(av[j], av[j + 1])));
        }
      }
    }
  }
  if (wid >= 4) {
#pragma unroll
    for (int i = 0; i < 43; ++i) { int ks = i; if (i < 40) { ks += rot5; ks = ks >= 40 ? ks - 40 : ks; } wdf[i] = ldw16(ws, WS_WDF + ((unsigned)wct * 43 + ks) * 1024 + lane * 16); }
  }
}

extern __shared__ __attribute__((aligned(16))) char smem_dyn[];

__global__ __launch_bounds__(512, 2) void mega(Params p) {
  char* smem = smem_dyn;
  const int b = blockIdx.x, x = b & 7, i5 = b >> 3;
  const int tid = threadIdx.x, lane = tid & 63, wid = rfl(tid >> 6);
  const int r16 = lane & 15, g = lane >> 4, wn = wid & 3;
  const rsrc_t ws = __builtin_amdgcn_make_buffer_rsrc(p.ws, 0, WS_USED, 0x00020000);
  const unsigned epoch = __hip_atomic_load((gu32*)(p.ws + WS_FLAG + FL_EPOCH), RLX_AGENT);

  const int rb1 = (x >> 1) * 8 + (i5 & 7), cb1 = (x & 1) * 4 + (i5 >> 3);
  p1_qkv(p, ws, b, rb1, cb1, smem, epoch);
  const unsigned tag = (unsigned)rfl((int)make_tag(epoch));

  const int h = x * 4 + (i5 & 3), qr = i5 >> 2;
  const int rb = x * 4 + (i5 & 3), cb = i5 >> 2;
  const int mb = x * 2 + (i5 & 1), fb = i5 >> 1;
  await<32>(p.ws, FL_1 + (h >> 2) * 128, tag);
  P3Pre pre;
  p2_attn(p, ws, b, h, qr, smem, tag, pre);
  lds_bar();

  const int wct = cb * 4 + wn, col = cb * 64 + wn * 16 + 4 * g;
  u32x2 hv[4];
  if (wid >= 4) {
#pragma unroll
    for (int i = 0; i < 16; ++i) PIN(pre.wof[i]);
#pragma unroll
    for (int i = 0; i < 4; ++i) PIN(pre.xv[i]);
    f32x4 acc[4];
    gemm64_mma<16, 2>(pre.wof, smem + G_A, acc);
    LAS f32x2* S = (LAS f32x2*)(smem + G_ST);
#pragma unroll
    for (int i = 0; i < 4; ++i) hv[i] = cvt4(unpack4(pre.xv[i]) + acc[i]);
#pragma unroll
    for (int i = 0; i < 4; i += 2) {
      const unsigned rt = (unsigned)rb * 4 + i + (g & 1);
      st16(ws, WS_H + (((rt * 16 + cb * 2 + (wn >> 1)) * 4 + (wn & 1) * 2 + (g >> 1)) * 16 + r16) * 16, nop4(pair16(hv[i], hv[i + 1])));
    }
#pragma unroll
    for (int i = 0; i < 4; ++i) {
      const f32x4 v = unpack4(hv[i]);
      const float s1 = sum_x32(sum_x16((v[0] + v[1]) + (v[2] + v[3]))), s2 = sum_x32(sum_x16((v[0] * v[0] + v[1] * v[1]) + (v[2] * v[2] + v[3] * v[3])));
      if (g == 0) S[(i * 16 + r16) * 4 + wn] = (f32x2){s1, s2};
    }
    lds_bar();
    if (wid == 4) {
      const f32x2 a = S[lane * 4], bb = S[lane * 4 + 1], c = S[lane * 4 + 2], d = S[lane * 4 + 3];
      const unsigned s1 = __float_as_uint((a.x + bb.x) + (c.x + d.x)), s2 = __float_as_uint((a.y + bb.y) + (c.y + d.y));
      __hip_atomic_store((gu64*)(p.ws + WS_HST + (size_t)(rb * 8 + cb) * 512 + lane * 8), ((unsigned long long)(s2 < POISON ? s2 : POISON - 1) << 32) | (s1 < POISON ? s1 : POISON - 1), RLX_AGENT);
    }
    raw_bar();
  } else {
    auto pre3 = [&]() {
      WLoad w0; w0.init(p, mb, fb, wid, (unsigned)(size_t)(lds_ptr)smem); w0.issue(0, lane); w0.issue(1, lane);
    };
    gemm64_load<16, 2, 2, 12>(p.ws, p.ws + WS_O + (size_t)(rb * 4 + wid) * 16384, cb & 1, smem + G_A, pre3);
    lds_bar();
    raw_bar();
  }

  bf16x8 wdf[43];
  { WLoad wl;
    if (wid >= 4) wl.init(p, mb, fb, wid - 4, (unsigned)(size_t)(lds_ptr)smem);
    p4_gateup(p, ws, mb, fb, smem, wl, wct, 8 * (cb % 5), wdf); }
  lds_bar();

  if (wid >= 4) {
#pragma unroll
    for (int i = 0; i < 43; ++i) PIN(wdf[i]);
    f32x4 acc[4];
    gemm64_mma<43, 4>(wdf, smem, acc);
#pragma unroll
    for (int i = 0; i < 4; i += 2)
      *(u32x4*)(p.out + ((size_t)(rb * 64 + (i + (g & 1)) * 16 + r16) * DM + cb * 64 + wn * 16 + 8 * (g >> 1)) * 2) = pair16(cvt4(unpack4(hv[i]) + acc[i]), cvt4(unpack4(hv[i + 1]) + acc[i + 1]));
  } else {
    gemm64_load<43, 4, 3, 0>(p.ws, p.ws + WS_ACT + (size_t)(rb * 4 + wid) * 43 * 1024, cb % 5, smem, []() {});
  }
}

extern "C" void kernel_launch(void* const* d_in, const int* in_sizes, int n_in, void* d_out, int out_size, void* d_ws, size_t ws_size, hipStream_t stream) {
  Params p;
  p.x = (const char*)d_in[0]; p.wqkv = (const char*)d_in[1]; p.wo = (const char*)d_in[2];
  p.wg = (const char*)d_in[4]; p.wu = (const char*)d_in[5]; p.wd = (const char*)d_in[6];
  p.out = (char*)d_out; p.ws = (char*)d_ws;
  constexpr int LDS = 160 * 1024;
  (void)hipFuncSetAttribute((const void*)mega, hipFuncAttributeMaxDynamicSharedMemorySize, LDS);
  mega<<<256, 512, LDS, stream>>>(p);
}
```

```cpp
#define DIAG 1
#define DIAG_RT 1
#define AMP_I 11
#define AMP_J 12
#define AMP_W 0
#include <hip/hip_runtime.h>
#include <stdint.h>
#define ATTN_ASM_A \
  "s_mov_b32 %[keep], m0\n\t" \
  "ds_read_b128 v[168:171], %[ka]\n\t" \
  "ds_read_b128 v[172:175], %[ka] offset:1024\n\t" \
  "ds_read_b128 v[176:179], %[va]\n\t" \
  "ds_read_b128 v[184:187], %[va] offset:512\n\t" \
  "v_mov_b32 v164, 0\n\t" \
  "v_mov_b32 v165, 0\n\t" \
  "v_mov_b32 v166, 0\n\t" \
  "v_mov_b32 v167, 0\n\t" \
  "v_mov_b32 v188, 0\n\t" \
  "v_mov_b32 v189, 0\n\t" \
  "v_mov_b32 v190, 0\n\t" \
  "v_mov_b32 v191, 0\n\t" \
  "s_waitcnt lgkmcnt(3)\n\t" \
  "v_mfma_f32_32x32x16_bf16 v[128:143], v[168:171], %[q], %[nc]\n\t" \
  "s_nop 10\n\t" \
  "s_waitcnt lgkmcnt(2)\n\t" \
  "v_mfma_f32_32x32x16_bf16 v[144:159], v[172:175], %[q], %[nc]\n\t" \
  "ds_read_b128 v[168:171], %[ka] offset:2048\n\t" \
  "s_mov_b32 m0, %[ld]\n\t" \
  "v_exp_f32 v128, v128\n\t" \
  "v_exp_f32 v129, v129\n\t" \
  "global_load_lds_dwordx4 %[go], %[kg] sc1\n\t" \
  "v_exp_f32 v130, v130\n\t" \
  "v_exp_f32 v131, v131\n\t" \
  "v_cvt_pk_bf16_f32 v160, v128, v129\n\t" \
  "v_exp_f32 v132, v132\n\t" \
  "v_exp_f32 v133, v133\n\t" \
  "v_cvt_pk_bf16_f32 v161, v130, v131\n\t" \
  "v_mfma_f32_32x32x16_bf16 %[o], v[188:191], v[164:167], %[o]\n\t" \
  "ds_read_b128 v[180:183], %[va] offset:1024\n\t" \
  "ds_read_b128 v[188:191], %[va] offset:1536\n\t" \
  "s_add_u32 m0, %[ld], 0x10000\n\t" \
  "v_exp_f32 v134, v134\n\t" \
  "v_exp_f32 v135, v135\n\t" \
  "global_load_lds_dwordx4 %[go], %[vg] sc1\n\t" \
  "s_add_u32 %[ld], %[ld], 0x2000\n\t" \
  "v_cvt_pk_bf16_f32 v162, v132, v133\n\t" \
  "v_exp_f32 v136, v136\n\t" \
  "v_exp_f32 v137, v137\n\t" \
  "v_cvt_pk_bf16_f32 v163, v134, v135\n\t" \
  "v_add_u32 %[go], 0x2000, %[go]\n\t" \
  "v_and_b32 %[go], 0xffff, %[go]\n\t" \
  "v_exp_f32 v138, v138\n\t" \
  "v_exp_f32 v139, v139\n\t" \
  "s_waitcnt lgkmcnt(4)\n\t" \
  "v_mfma_f32_32x32x16_bf16 %[o], v[176:179], v[160:163], %[o]\n\t" \
  "v_cvt_pk_bf16_f32 v164, v136, v137\n\t" \
  "v_exp_f32 v140, v140\n\t" \
  "v_exp_f32 v141, v141\n\t" \
  "v_cvt_pk_bf16_f32 v165, v138, v139\n\t" \
  "v_exp_f32 v142, v142\n\t" \
  "v_exp_f32 v143, v143\n\t" \
  "v_cvt_pk_bf16_f32 v166, v140, v141\n\t" \
  "v_cvt_pk_bf16_f32 v167, v142, v143\n\t" \
  "s_waitcnt lgkmcnt(2)\n\t" \
  "v_mfma_f32_32x32x16_bf16 v[128:143], v[168:171], %[q], %[nc]\n\t" \
  "ds_read_b128 v[172:175], %[ka] offset:3072\n\t" \
  "s_mov_b32 m0, %[ld]\n\t" \
  "v_exp_f32 v144, v144\n\t" \
  "v_exp_f32 v145, v145\n\t" \
  "global_load_lds_dwordx4 %[go], %[kg] sc1\n\t" \
  "v_exp_f32 v146, v146\n\t" \
  "v_exp_f32 v147, v147\n\t" \
  "v_cvt_pk_bf16_f32 v160, v144, v145\n\t" \
  "v_exp_f32 v148, v148\n\t" \
  "v_exp_f32 v149, v149\n\t" \
  "v_cvt_pk_bf16_f32 v161, v146, v147\n\t" \
  "v_mfma_f32_32x32x16_bf16 %[o], v[184:187], v[164:167], %[o]\n\t" \
  "ds_read_b128 v[176:179], %[va] offset:2048\n\t" \
  "ds_read_b128 v[184:187], %[va] offset:2560\n\t" \
  "s_add_u32 m0, %[ld], 0x10000\n\t" \
  "v_exp_f32 v150, v150\n\t" \
  "v_exp_f32 v151, v151\n\t" \
  "global_load_lds_dwordx4 %[go], %[vg] sc1\n\t" \
  "s_add_u32 %[ld], %[ld], 0x2000\n\t" \
  "v_cvt_pk_bf16_f32 v162, v148, v149\n\t" \
  "v_exp_f32 v152, v152\n\t" \
  "v_exp_f32 v153, v153\n\t" \
  "v_cvt_pk_bf16_f32 v163, v150, v151\n\t" \
  "v_add_u32 %[go], 0x2000, %[go]\n\t" \
  "v_and_b32 %[go], 0xffff, %[go]\n\t" \
  "v_exp_f32 v154, v154\n\t" \
  "v_exp_f32 v155, v155\n\t" \
  "s_waitcnt lgkmcnt(4)\n\t" \
  "v_mfma_f32_32x32x16_bf16 %[o], v[180:183], v[160:163], %[o]\n\t" \
  "v_cvt_pk_bf16_f32 v164, v152, v153\n\t" \
  "v_exp_f32 v156, v156\n\t" \
  "v_exp_f32 v157, v157\n\t" \
  "v_cvt_pk_bf16_f32 v165, v154, v155\n\t" \
  "v_exp_f32 v158, v158\n\t" \
  "v_exp_f32 v159, v159\n\t" \
  "v_cvt_pk_bf16_f32 v166, v156, v157\n\t" \
  "v_cvt_pk_bf16_f32 v167, v158, v159\n\t" \
  "s_waitcnt lgkmcnt(2)\n\t" \
  "v_mfma_f32_32x32x16_bf16 v[144:159], v[172:175], %[q], %[nc]\n\t" \
  "ds_read_b128 v[168:171], %[ka] offset:4096\n\t" \
  "s_mov_b32 m0, %[ld]\n\t" \
  "v_exp_f32 v128, v128\n\t" \
  "v_exp_f32 v129, v129\n\t" \
  "global_load_lds_dwordx4 %[go], %[kg] sc1\n\t" \
  "v_exp_f32 v130, v130\n\t" \
  "v_exp_f32 v131, v131\n\t" \
  "v_cvt_pk_bf16_f32 v160, v128, v129\n\t" \
  "v_exp_f32 v132, v132\n\t" \
  "v_exp_f32 v133, v133\n\t" \
  "v_cvt_pk_bf16_f32 v161, v130, v131\n\t" \
  "v_mfma_f32_32x32x16_bf16 %[o], v[188:191], v[164:167], %[o]\n\t" \
  "ds_read_b128 v[180:183], %[va] offset:3072\n\t" \
  "ds_read_b128 v[188:191], %[va] offset:3584\n\t" \
  "s_add_u32 m0, %[ld], 0x10000\n\t" \
  "v_exp_f32 v134, v134\n\t" \
  "v_exp_f32 v135, v135\n\t" \
  "global_load_lds_dwordx4 %[go], %[vg] sc1\n\t" \
  "s_add_u32 %[ld], %[ld], 0x2000\n\t" \
  "v_cvt_pk_bf16_f32 v162, v132, v133\n\t" \
  "v_exp_f32 v136, v136\n\t" \
  "v_exp_f32 v137, v137\n\t" \
  "v_cvt_pk_bf16_f32 v163, v134, v135\n\t" \
  "v_add_u32 %[go], 0x2000, %[go]\n\t" \
  "v_and_b32 %[go], 0xffff, %[go]\n\t" \
  "v_exp_f32 v138, v138\n\t" \
  "v_exp_f32 v139, v139\n\t" \
  "s_waitcnt lgkmcnt(4)\n\t" \
  "v_mfma_f32_32x32x16_bf16 %[o], v[176:179], v[160:163], %[o]\n\t" \
  "v_cvt_pk_bf16_f32 v164, v136, v137\n\t" \
  "v_exp_f32 v140, v140\n\t" \
  "v_exp_f32 v141, v141\n\t" \
  "v_cvt_pk_bf16_f32 v165, v138, v139\n\t" \
  "v_exp_f32 v142, v142\n\t" \
  "v_exp_f32 v143, v143\n\t" \
  "v_cvt_pk_bf16_f32 v166, v140, v141\n\t" \
  "v_cvt_pk_bf16_f32 v167, v142, v143\n\t" \
  "s_waitcnt lgkmcnt(2)\n\t" \
  "v_mfma_f32_32x32x16_bf16 v[128:143], v[168:171], %[q], %[nc]\n\t" \
  "ds_read_b128 v[172:175], %[ka] offset:5120\n\t" \
  "s_mov_b32 m0, %[ld]\n\t" \
  "v_exp_f32 v144, v144\n\t" \
  "v_exp_f32 v145, v145\n\t" \
  "global_load_lds_dwordx4 %[go], %[kg] sc1\n\t" \
  "v_exp_f32 v146, v146\n\t" \
  "v_exp_f32 v147, v147\n\t" \
  "v_cvt_pk_bf16_f32 v160, v144, v145\n\t" \
  "v_exp_f32 v148, v148\n\t" \
  "v_exp_f32 v149, v149\n\t" \
  "v_cvt_pk_bf16_f32 v161, v146, v147\n\t" \
  "v_mfma_f32_32x32x16_bf16 %[o], v[184:187], v[164:167], %[o]\n\t" \
  "ds_read_b128 v[176:179], %[va] offset:4096\n\t" \
  "ds_read_b128 v[184:187], %[va] offset:4608\n\t" \
  "s_add_u32 m0, %[ld], 0x10000\n\t" \
  "v_exp_f32 v150, v150\n\t" \
  "v_exp_f32 v151, v151\n\t" \
  "global_load_lds_dwordx4 %[go], %[vg] sc1\n\t" \
  "s_add_u32 %[ld], %[ld], 0x2000\n\t" \
  "v_cvt_pk_bf16_f32 v162, v148, v149\n\t" \
  "v_exp_f32 v152, v152\n\t" \
  "v_exp_f32 v153, v153\n\t" \
  "v_cvt_pk_bf16_f32 v163, v150, v151\n\t" \
  "v_add_u32 %[go], 0x2000, %[go]\n\t" \
  "v_and_b32 %[go], 0xffff, %[go]\n\t" \
  "v_exp_f32 v154, v154\n\t" \
  "v_exp_f32 v155, v155\n\t" \
  "s_waitcnt lgkmcnt(4)\n\t" \
  "v_mfma_f32_32x32x16_bf16 %[o], v[180:183], v[160:163], %[o]\n\t" \
  "v_cvt_pk_bf16_f32 v164, v152, v153\n\t" \
  "v_exp_f32 v156, v156\n\t" \
  "v_exp_f32 v157, v157\n\t" \
  "v_cvt_pk_bf16_f32 v165, v154, v155\n\t" \
  "v_exp_f32 v158, v158\n\t" \
  "v_exp_f32 v159, v159\n\t" \
  "v_cvt_pk_bf16_f32 v166, v156, v157\n\t" \
  "v_cvt_pk_bf16_f32 v167, v158, v159\n\t" \
  "v_add_u32 %[ka], 0x1000, %[ka]\n\t" \
  "v_add_u32 %[va], %[vs], %[va]\n\t" \
  "s_waitcnt lgkmcnt(2)\n\t" \
  "v_mfma_f32_32x32x16_bf16 v[144:159], v[172:175], %[q], %[nc]\n\t" \
  "ds_read_b128 v[168:171], %[ka] offset:2048\n\t" \
  "s_mov_b32 m0, %[ld]\n\t" \
  "v_exp_f32 v128, v128\n\t" \
  "v_exp_f32 v129, v129\n\t" \
  "global_load_lds_dwordx4 %[go], %[kg] sc1\n\t" \
  "v_exp_f32 v130, v130\n\t" \
  "v_exp_f32 v131, v131\n\t" \
  "v_cvt_pk_bf16_f32 v160, v128, v129\n\t" \
  "v_exp_f32 v132, v132\n\t" \
  "v_exp_f32 v133, v133\n\t" \
  "v_cvt_pk_bf16_f32 v161, v130, v131\n\t" \
  "v_mfma_f32_32x32x16_bf16 %[o], v[188:191], v[164:167], %[o]\n\t" \
  "ds_read_b128 v[180:183], %[va] offset:1024\n\t" \
  "ds_read_b128 v[188:191], %[va] offset:1536\n\t" \
  "s_add_u32 m0, %[ld], 0x10000\n\t" \
  "v_exp_f32 v134, v134\n\t" \
  "v_exp_f32 v135, v135\n\t" \
  "global_load_lds_dwordx4 %[go], %[vg] sc1\n\t" \
  "s_add_u32 %[ld], %[ld], 0x2000\n\t" \
  "v_cvt_pk_bf16_f32 v162, v132, v133\n\t" \
  "v_exp_f32 v136, v136\n\t" \
  "v_exp_f32 v137, v137\n\t" \
  "v_cvt_pk_bf16_f32 v163, v134, v135\n\t" \
  "v_add_u32 %[go], 0x2000, %[go]\n\t" \
  "v_and_b32 %[go], 0xffff, %[go]\n\t" \
  "v_exp_f32 v138, v138\n\t" \
  "v_exp_f32 v139, v139\n\t" \
  "s_waitcnt lgkmcnt(4)\n\t" \
  "v_mfma_f32_32x32x16_bf16 %[o], v[176:179], v[160:163], %[o]\n\t" \
  "v_cvt_pk_bf16_f32 v164, v136, v137\n\t" \
  "v_exp_f32 v140, v140\n\t" \
  "v_exp_f32 v141, v141\n\t" \
  "v_cvt_pk_bf16_f32 v165, v138, v139\n\t" \
  "v_exp_f32 v142, v142\n\t" \
  "v_exp_f32 v143, v143\n\t" \
  "v_cvt_pk_bf16_f32 v166, v140, v141\n\t" \
  "v_cvt_pk_bf16_f32 v167, v142, v143\n\t" \
  "s_waitcnt lgkmcnt(2)\n\t" \
  "v_mfma_f32_32x32x16_bf16 v[128:143], v[168:171], %[q], %[nc]\n\t" \
  "ds_read_b128 v[172:175], %[ka] offset:3072\n\t" \
  "s_mov_b32 m0, %[ld]\n\t" \
  "v_exp_f32 v144, v144\n\t" \
  "v_exp_f32 v145, v145\n\t" \
  "global_load_lds_dwordx4 %[go], %[kg] sc1\n\t" \
  "v_exp_f32 v146, v146\n\t" \
  "v_exp_f32 v147, v147\n\t" \
  "v_cvt_pk_bf16_f32 v160, v144, v145\n\t" \
  "v_exp_f32 v148, v148\n\t" \
  "v_exp_f32 v149, v149\n\t" \
  "v_cvt_pk_bf16_f32 v161, v146, v147\n\t" \
  "v_mfma_f32_32x32x16_bf16 %[o], v[184:187], v[164:167], %[o]\n\t" \
  "ds_read_b128 v[176:179], %[va] offset:2048\n\t" \
  "ds_read_b128 v[184:187], %[va] offset:2560\n\t" \
  "s_add_u32 m0, %[ld], 0x10000\n\t" \
  "v_exp_f32 v150, v150\n\t" \
  "v_exp_f32 v151, v151\n\t" \
  "global_load_lds_dwordx4 %[go], %[vg] sc1\n\t" \
  "s_add_u32 %[ld], %[ld], 0x2000\n\t" \
  "v_cvt_pk_bf16_f32 v162, v148, v149\n\t" \
  "v_exp_f32 v152, v152\n\t" \
  "v_exp_f32 v153, v153\n\t" \
  "v_cvt_pk_bf16_f32 v163, v150, v151\n\t" \
  "v_add_u32 %[go], 0x2000, %[go]\n\t" \
  "v_and_b32 %[go], 0xffff, %[go]\n\t" \
  "v_exp_f32 v154, v154\n\t" \
  "v_exp_f32 v155, v155\n\t" \
  "s_waitcnt lgkmcnt(4)\n\t" \
  "v_mfma_f32_32x32x16_bf16 %[o], v[180:183], v[160:163], %[o]\n\t" \
  "v_cvt_pk_bf16_f32 v164, v152, v153\n\t" \
  "v_exp_f32 v156, v156\n\t" \
  "v_exp_f32 v157, v157\n\t" \
  "v_cvt_pk_bf16_f32 v165, v154, v155\n\t" \
  "v_exp_f32 v158, v158\n\t" \
  "v_exp_f32 v159, v159\n\t" \
  "v_cvt_pk_bf16_f32 v166, v156, v157\n\t" \
  "v_cvt_pk_bf16_f32 v167, v158, v159\n\t" \
  "s_waitcnt lgkmcnt(2)\n\t" \
  "v_mfma_f32_32x32x16_bf16 v[144:159], v[172:175], %[q], %[nc]\n\t" \
  "ds_read_b128 v[168:171], %[ka] offset:4096\n\t" \
  "v_exp_f32 v128, v128\n\t" \
  "v_exp_f32 v129, v129\n\t" \
  "v_exp_f32 v130, v130\n\t" \
  "v_exp_f32 v131, v131\n\t" \
  "v_cvt_pk_bf16_f32 v160, v128, v129\n\t" \
  "v_exp_f32 v132, v132\n\t" \
  "v_exp_f32 v133, v133\n\t" \
  "v_cvt_pk_bf16_f32 v161, v130, v131\n\t" \
  "v_mfma_f32_32x32x16_bf16 %[o], v[188:191], v[164:167], %[o]\n\t" \
  "ds_read_b128 v[180:183], %[va] offset:3072\n\t" \
  "ds_read_b128 v[188:191], %[va] offset:3584\n\t" \
  "v_exp_f32 v134, v134\n\t" \
  "v_exp_f32 v135, v135\n\t" \
  "v_cvt_pk_bf16_f32 v162, v132, v133\n\t" \
  "v_exp_f32 v136, v136\n\t" \
  "v_exp_f32 v137, v137\n\t" \
  "v_cvt_pk_bf16_f32 v163, v134, v135\n\t" \
  "v_exp_f32 v138, v138\n\t" \
  "v_exp_f32 v139, v139\n\t" \
  "s_waitcnt lgkmcnt(4)\n\t" \
  "v_mfma_f32_32x32x16_bf16 %[o], v[176:179], v[160:163], %[o]\n\t" \
  "v_cvt_pk_bf16_f32 v164, v136, v137\n\t" \
  "v_exp_f32 v140, v140\n\t" \
  "v_exp_f32 v141, v141\n\t" \
  "v_cvt_pk_bf16_f32 v165, v138, v139\n\t" \
  "v_exp_f32 v142, v142\n\t" \
  "v_exp_f32 v143, v143\n\t" \
  "v_cvt_pk_bf16_f32 v166, v140, v141\n\t" \
  "v_cvt_pk_bf16_f32 v167, v142, v143\n\t" \
  "s_waitcnt lgkmcnt(2)\n\t" \
  "v_mfma_f32_32x32x16_bf16 v[128:143], v[168:171], %[q], %[nc]\n\t" \
  "ds_read_b128 v[172:175], %[ka] offset:5120\n\t" \
  "v_exp_f32 v144, v144\n\t" \
  "v_exp_f32 v145, v145\n\t" \
  "v_exp_f32 v146, v146\n\t" \
  "v_exp_f32 v147, v147\n\t" \
  "v_cvt_pk_bf16_f32 v160, v144, v145\n\t" \
  "v_exp_f32 v148, v148\n\t" \
  "v_exp_f32 v149, v149\n\t" \
  "v_cvt_pk_bf16_f32 v161, v146, v147\n\t" \
  "v_mfma_f32_32x32x16_bf16 %[o], v[184:187], v[164:167], %[o]\n\t" \
  "ds_read_b128 v[176:179], %[va] offset:4096\n\t" \
  "ds_read_b128 v[184:187], %[va] offset:4608\n\t" \
  "v_exp_f32 v150, v150\n\t" \
  "v_exp_f32 v151, v151\n\t" \
  "v_cvt_pk_bf16_f32 v162, v148, v149\n\t" \
  "v_exp_f32 v152, v152\n\t" \
  "v_exp_f32 v153, v153\n\t" \
  "v_cvt_pk_bf16_f32 v163, v150, v151\n\t" \
  "v_exp_f32 v154, v154\n\t" \
  "v_exp_f32 v155, v155\n\t" \
  "s_waitcnt lgkmcnt(4)\n\t" \
  "v_mfma_f32_32x32x16_bf16 %[o], v[180:183], v[160:163], %[o]\n\t" \
  "v_cvt_pk_bf16_f32 v164, v152, v153\n\t" \
  "v_exp_f32 v156, v156\n\t" \
  "v_exp_f32 v157, v157\n\t" \
  "v_cvt_pk_bf16_f32 v165, v154, v155\n\t" \
  "v_exp_f32 v158, v158\n\t" \
  "v_exp_f32 v159, v159\n\t" \
  "v_cvt_pk_bf16_f32 v166, v156, v157\n\t" \
  "v_cvt_pk_bf16_f32 v167, v158, v159\n\t" \
  "v_add_u32 %[ka], 0x1000, %[ka]\n\t" \
  "v_add_u32 %[va], %[vs], %[va]\n\t" \
  "s_mov_b32 m0, %[keep]\n\t" \
  "s_waitcnt vmcnt(0) lgkmcnt(0)\n\t" \
  ""
#define ATTN_ASM_B \
  "s_mov_b32 %[cnt], %[c0]\n\t" \
  "s_cmp_lg_u32 %[c0], 2\n\t" \
  "s_cbranch_scc1 L_attn_loop_%=\n\t" \
  "s_cmp_eq_u32 %[sw], 99\n\t" \
  "s_cbranch_scc1 L_attn_y_%=\n\t" \
  "s_setprio 2\n\t" \
  "s_branch L_attn_loop_%=\n\t" \
  "L_attn_y_%=:\n\t" \
  "s_setprio 1\n\t" \
  "L_attn_loop_%=:\n\t" \
  "s_cmp_lg_u32 %[cnt], %[sw]\n\t" \
  "s_cbranch_scc1 L_attn_nosw_%=\n\t" \
  "s_setprio 0\n\t" \
  "L_attn_nosw_%=:\n\t" \
  "s_waitcnt lgkmcnt(2)\n\t" \
  "v_mfma_f32_32x32x16_bf16 v[144:159], v[172:175], %[q], %[nc]\n\t" \
  "ds_read_b128 v[168:171], %[ka] offset:2048\n\t" \
  "v_exp_f32 v128, v128\n\t" \
  "v_exp_f32 v129, v129\n\t" \
  "v_exp_f32 v130, v130\n\t" \
  "v_exp_f32 v131, v131\n\t" \
  "v_cvt_pk_bf16_f32 v160, v128, v129\n\t" \
  "v_exp_f32 v132, v132\n\t" \
  "v_exp_f32 v133, v133\n\t" \
  "v_cvt_pk_bf16_f32 v161, v130, v131\n\t" \
  "v_mfma_f32_32x32x16_bf16 %[o], v[188:191], v[164:167], %[o]\n\t" \
  "ds_read_b128 v[180:183], %[va] offset:1024\n\t" \
  "ds_read_b128 v[188:191], %[va] offset:1536\n\t" \
  "v_exp_f32 v134, v134\n\t" \
  "v_exp_f32 v135, v135\n\t" \
  "v_cvt_pk_bf16_f32 v162, v132, v133\n\t" \
  "v_exp_f32 v136, v136\n\t" \
  "v_exp_f32 v137, v137\n\t" \
  "v_cvt_pk_bf16_f32 v163, v134, v135\n\t" \
  "v_exp_f32 v138, v138\n\t" \
  "v_exp_f32 v139, v139\n\t" \
  "s_waitcnt lgkmcnt(4)\n\t" \
  "v_mfma_f32_32x32x16_bf16 %[o], v[176:179], v[160:163], %[o]\n\t" \
  "v_cvt_pk_bf16_f32 v164, v136, v137\n\t" \
  "v_exp_f32 v140, v140\n\t" \
  "v_exp_f32 v141, v141\n\t" \
  "v_cvt_pk_bf16_f32 v165, v138, v139\n\t" \
  "v_exp_f32 v142, v142\n\t" \
  "v_exp_f32 v143, v143\n\t" \
  "v_cvt_pk_bf16_f32 v166, v140, v141\n\t" \
  "v_cvt_pk_bf16_f32 v167, v142, v143\n\t" \
  "s_waitcnt lgkmcnt(2)\n\t" \
  "v_mfma_f32_32x32x16_bf16 v[128:143], v[168:171], %[q], %[nc]\n\t" \
  "ds_read_b128 v[172:175], %[ka] offset:3072\n\t" \
  "v_exp_f32 v144, v144\n\t" \
  "v_exp_f32 v145, v145\n\t" \
  "v_exp_f32 v146, v146\n\t" \
  "v_exp_f32 v147, v147\n\t" \
  "v_cvt_pk_bf16_f32 v160, v144, v145\n\t" \
  "v_exp_f32 v148, v148\n\t" \
  "v_exp_f32 v149, v149\n\t" \
  "v_cvt_pk_bf16_f32 v161, v146, v147\n\t" \
  "v_mfma_f32_32x32x16_bf16 %[o], v[184:187], v[164:167], %[o]\n\t" \
  "ds_read_b128 v[176:179], %[va] offset:2048\n\t" \
  "ds_read_b128 v[184:187], %[va] offset:2560\n\t" \
  "v_exp_f32 v150, v150\n\t" \
  "v_exp_f32 v151, v151\n\t" \
  "v_cvt_pk_bf16_f32 v162, v148, v149\n\t" \
  "v_exp_f32 v152, v152\n\t" \
  "v_exp_f32 v153, v153\n\t" \
  "v_cvt_pk_bf16_f32 v163, v150, v151\n\t" \
  "v_exp_f32 v154, v154\n\t" \
  "v_exp_f32 v155, v155\n\t" \
  "s_waitcnt lgkmcnt(4)\n\t" \
  "v_mfma_f32_32x32x16_bf16 %[o], v[180:183], v[160:163], %[o]\n\t" \
  "v_cvt_pk_bf16_f32 v164, v152, v153\n\t" \
  "v_exp_f32 v156, v156\n\t" \
  "v_exp_f32 v157, v157\n\t" \
  "v_cvt_pk_bf16_f32 v165, v154, v155\n\t" \
  "v_exp_f32 v158, v158\n\t" \
  "v_exp_f32 v159, v159\n\t" \
  "v_cvt_pk_bf16_f32 v166, v156, v157\n\t" \
  "v_cvt_pk_bf16_f32 v167, v158, v159\n\t" \
  "s_waitcnt lgkmcnt(2)\n\t" \
  "v_mfma_f32_32x32x16_bf16 v[144:159], v[172:175], %[q], %[nc]\n\t" \
  "ds_read_b128 v[168:171], %[ka] offset:4096\n\t" \
  "v_exp_f32 v128, v128\n\t" \
  "v_exp_f32 v129, v129\n\t" \
  "v_exp_f32 v130, v130\n\t" \
  "v_exp_f32 v131, v131\n\t" \
  "v_cvt_pk_bf16_f32 v160, v128, v129\n\t" \
  "v_exp_f32 v132, v132\n\t" \
  "v_exp_f32 v133, v133\n\t" \
  "v_cvt_pk_bf16_f32 v161, v130, v131\n\t" \
  "v_mfma_f32_32x32x16_bf16 %[o], v[188:191], v[164:167], %[o]\n\t" \
  "ds_read_b128 v[180:183], %[va] offset:3072\n\t" \
  "ds_read_b128 v[188:191], %[va] offset:3584\n\t" \
  "v_exp_f32 v134, v134\n\t" \
  "v_exp_f32 v135, v135\n\t" \
  "v_cvt_pk_bf16_f32 v162, v132, v133\n\t" \
  "v_exp_f32 v136, v136\n\t" \
  "v_exp_f32 v137, v137\n\t" \
  "v_cvt_pk_bf16_f32 v163, v134, v135\n\t" \
  "v_exp_f32 v138, v138\n\t" \
  "v_exp_f32 v139, v139\n\t" \
  "s_waitcnt lgkmcnt(4)\n\t" \
  "v_mfma_f32_32x32x16_bf16 %[o], v[176:179], v[160:163], %[o]\n\t" \
  "v_cvt_pk_bf16_f32 v164, v136, v137\n\t" \
  "v_exp_f32 v140, v140\n\t" \
  "v_exp_f32 v141, v141\n\t" \
  "v_cvt_pk_bf16_f32 v165, v138, v139\n\t" \
  "v_exp_f32 v142, v142\n\t" \
  "v_exp_f32 v143, v143\n\t" \
  "v_cvt_pk_bf16_f32 v166, v140, v141\n\t" \
  "v_cvt_pk_bf16_f32 v167, v142, v143\n\t" \
  "s_waitcnt lgkmcnt(2)\n\t" \
  "v_mfma_f32_32x32x16_bf16 v[128:143], v[168:171], %[q], %[nc]\n\t" \
  "ds_read_b128 v[172:175], %[ka] offset:5120\n\t" \
  "v_exp_f32 v144, v144\n\t" \
  "v_exp_f32 v145, v145\n\t" \
  "v_exp_f32 v146, v146\n\t" \
  "v_exp_f32 v147, v147\n\t" \
  "v_cvt_pk_bf16_f32 v160, v144, v145\n\t" \
  "v_exp_f32 v148, v148\n\t" \
  "v_exp_f32 v149, v149\n\t" \
  "v_cvt_pk_bf16_f32 v161, v146, v147\n\t" \
  "v_mfma_f32_32x32x16_bf16 %[o], v[184:187], v[164:167], %[o]\n\t" \
  "ds_read_b128 v[176:179], %[va] offset:4096\n\t" \
  "ds_read_b128 v[184:187], %[va] offset:4608\n\t" \
  "v_exp_f32 v150, v150\n\t" \
  "v_exp_f32 v151, v151\n\t" \
  "v_cvt_pk_bf16_f32 v162, v148, v149\n\t" \
  "v_exp_f32 v152, v152\n\t" \
  "v_exp_f32 v153, v153\n\t" \
  "v_cvt_pk_bf16_f32 v163, v150, v151\n\t" \
  "v_exp_f32 v154, v154\n\t" \
  "v_exp_f32 v155, v155\n\t" \
  "s_waitcnt lgkmcnt(4)\n\t" \
  "v_mfma_f32_32x32x16_bf16 %[o], v[180:183], v[160:163], %[o]\n\t" \
  "v_cvt_pk_bf16_f32 v164, v152, v153\n\t" \
  "v_exp_f32 v156, v156\n\t" \
  "v_exp_f32 v157, v157\n\t" \
  "v_cvt_pk_bf16_f32 v165, v154, v155\n\t" \
  "v_exp_f32 v158, v158\n\t" \
  "v_exp_f32 v159, v159\n\t" \
  "v_cvt_pk_bf16_f32 v166, v156, v157\n\t" \
  "v_cvt_pk_bf16_f32 v167, v158, v159\n\t" \
  "v_add_u32 %[ka], 0x1000, %[ka]\n\t" \
  "v_add_u32 %[va], %[vs], %[va]\n\t" \
  "s_add_u32 %[cnt], %[cnt], 1\n\t" \
  "s_cmp_lt_u32 %[cnt], %[c1]\n\t" \
  "s_cbranch_scc1 L_attn_loop_%=\n\t" \
  "s_waitcnt lgkmcnt(0)\n\t" \
  "s_cmp_lg_u32 %[c1], 16\n\t" \
  "s_cbranch_scc1 L_attn_end_%=\n\t" \
  "v_mfma_f32_32x32x16_bf16 %[o], v[188:191], v[164:167], %[o]\n\t" \
  "s_setprio 0\n\t" \
  "L_attn_end_%=:\n\t" \
  "s_nop 15\n\t" \
  ""

typedef short bf16x8 __attribute__((ext_vector_type(8)));
typedef short s16x4 __attribute__((ext_vector_type(4)));
typedef float f32x4 __attribute__((ext_vector_type(4)));
typedef float f32x2 __attribute__((ext_vector_type(2)));
typedef float f32x16 __attribute__((ext_vector_type(16)));
typedef float f32x8 __attribute__((ext_vector_type(8)));
typedef unsigned u32x4 __attribute__((ext_vector_type(4)));
typedef unsigned u32x8 __attribute__((ext_vector_type(8)));
typedef unsigned u32x2 __attribute__((ext_vector_type(2)));
typedef __bf16 bf16x2_t __attribute__((ext_vector_type(2)));
#define LAS __attribute__((address_space(3)))
#define GAS __attribute__((address_space(1)))
typedef const LAS char* lds_cptr;
typedef LAS char* lds_ptr;
typedef GAS unsigned gu32;
typedef GAS unsigned long long gu64;
typedef __amdgpu_buffer_rsrc_t rsrc_t;

constexpr int SEQ = 2048, DM = 512, NH = 32, FF = 1376;
constexpr float EPS = 1e-5f;
constexpr float QSCALE = 0.25f * 1.4426950408889634f;

constexpr unsigned WS_Q = 0, WS_K = 2u << 20, WS_V = 4u << 20, WS_O = 6u << 20, WS_H = 8u << 20, WS_ACT = 10u << 20;
constexpr unsigned WS_KMAX = 16u << 20, WS_HST = (16u << 20) + (64u << 10);
constexpr unsigned WS_WOF = 17u << 20, WS_WDF = WS_WOF + 512 * 1024, WS_WGF = WS_WDF + 32 * 43 * 1024;
constexpr unsigned WS_FLAG = 22u << 20;
constexpr unsigned FL_EPOCH = 0, FL_TMO = 256, FL_1 = 1024, FL_P = 4096;
constexpr unsigned POISON = 0xFFFFFFFFu;
constexpr unsigned WS_USED = 24u << 20;
constexpr size_t WS_DBG = 64u << 20;

constexpr int NTS = 40;
struct Stamps { unsigned long long t[NTS]; unsigned long long r0; };
#define TS(i) do { if ((i) == AMP_I || (i) == AMP_J) { asm volatile("s_memrealtime %0\n\ts_waitcnt lgkmcnt(0)" : "=s"(st_.t[i]) :: "memory"); } } while (0)
#define TS_BEGIN() Stamps st_; for (int i_ = 0; i_ < NTS; ++i_) st_.t[i_] = 0; st_.r0 = __builtin_amdgcn_s_memrealtime(); TS(0)
#define TS_END() do { asm volatile("s_waitcnt vmcnt(0)" ::: "memory"); TS(NTS - 1); if ((threadIdx.x >> 6) == AMP_W) { const unsigned long long d_ = (st_.t[AMP_J] - st_.t[AMP_I]) * 10ull; \
   const unsigned long long t0_ = __builtin_amdgcn_s_memrealtime(); while (__builtin_amdgcn_s_memrealtime() - t0_ < d_) __builtin_amdgcn_s_sleep(4); } } while (0)
#define TSARG , Stamps& st_
#define TSPASS , st_

struct Params {
  const char* x; const char* wqkv; const char* wo; const char* wg; const char* wu; const char* wd;
  char* out; char* ws;
};

#define MFMA16(a, b, c) __builtin_amdgcn_mfma_f32_16x16x32_bf16(a, b, c, 0, 0, 0)
#define MFMA32(a, b, c) __builtin_amdgcn_mfma_f32_32x32x16_bf16(a, b, c, 0, 0, 0)
#define PIN(x) asm volatile("" : "+v"(x))
#define SB() __builtin_amdgcn_sched_barrier(0)
#define RLX_AGENT __ATOMIC_RELAXED, __HIP_MEMORY_SCOPE_AGENT

template <int N> __device__ __forceinline__ void wait_bar() {
  asm volatile("s_waitcnt vmcnt(%0) lgkmcnt(0)\n\ts_barrier" ::"n"(N) : "memory");
}
template <int N> __device__ __forceinline__ void wait_vm() { asm volatile("s_waitcnt vmcnt(%0)" ::"n"(N) : "memory"); }
__device__ __forceinline__ void lds_bar() { asm volatile("s_waitcnt lgkmcnt(0)\n\ts_barrier" ::: "memory"); }
__device__ __forceinline__ void raw_bar() { asm volatile("s_barrier" ::: "memory"); }

template <int N, bool SC1> __device__ __forceinline__ void glds(const void* sbase, unsigned voff, unsigned lds_addr) {
#define GL_(O, S) "\n\tglobal_load_lds_dwordx4 %0, %1 offset:" #O S
  if (SC1) {
    if (N == 1) asm volatile("s_mov_b32 m0, %2\n\ts_nop 0" GL_(0, " sc1") :: "v"(voff), "s"(sbase), "s"(lds_addr) : "memory");
    if (N == 2) asm volatile("s_mov_b32 m0, %2\n\ts_nop 0" GL_(0, " sc1") GL_(1024, " sc1") :: "v"(voff), "s"(sbase), "s"(lds_addr) : "memory");
    if (N == 3) asm volatile("s_mov_b32 m0, %2\n\ts_nop 0" GL_(0, " sc1") GL_(1024, " sc1") GL_(2048, " sc1") :: "v"(voff), "s"(sbase), "s"(lds_addr) : "memory");
    if (N == 4) asm volatile("s_mov_b32 m0, %2\n\ts_nop 0" GL_(0, " sc1") GL_(1024, " sc1") GL_(2048, " sc1") GL_(3072, " sc1") :: "v"(voff), "s"(sbase), "s"(lds_addr) : "memory");
  } else {
    if (N == 1) asm volatile("s_mov_b32 m0, %2\n\ts_nop 0" GL_(0, "") :: "v"(voff), "s"(sbase), "s"(lds_addr) : "memory");
    if (N == 2) asm volatile("s_mov_b32 m0, %2\n\ts_nop 0" GL_(0, "") GL_(1024, "") :: "v"(voff), "s"(sbase), "s"(lds_addr) : "memory");
    if (N == 3) asm volatile("s_mov_b32 m0, %2\n\ts_nop 0" GL_(0, "") GL_(1024, "") GL_(2048, "") :: "v"(voff), "s"(sbase), "s"(lds_addr) : "memory");
  }
#undef GL_
}
template <bool SC1> __device__ __forceinline__ void glds16(const void* sbase, unsigned voff, unsigned lds_addr) { glds<1, SC1>(sbase, voff, lds_addr); }
template <bool SC1> __device__ __forceinline__ void glds4(const void* sbase, unsigned voff, unsigned lds_addr) {
  if (SC1) asm volatile("s_mov_b32 m0, %2\n\ts_nop 0\n\tglobal_load_lds_dword %0, %1 sc1" :: "v"(voff), "s"(sbase), "s"(lds_addr) : "memory");
  else     asm volatile("s_mov_b32 m0, %2\n\ts_nop 0\n\tglobal_load_lds_dword %0, %1" :: "v"(voff), "s"(sbase), "s"(lds_addr) : "memory");
}
__device__ __forceinline__ bf16x8 ld128(lds_cptr p) { return *(const LAS bf16x8*)p; }
typedef short v4i16_t __attribute__((ext_vector_type(4)));
__device__ __forceinline__ s16x4 ldtr(lds_cptr p) {
  return __builtin_bit_cast(s16x4, __builtin_amdgcn_ds_read_tr16_b64_v4i16((LAS v4i16_t*)p));
}
__device__ __forceinline__ bf16x8 ldtr2(lds_cptr p, int second) {
  const s16x4 a = ldtr(p), b = ldtr(p + second);
  return (bf16x8){a[0], a[1], a[2], a[3], b[0], b[1], b[2], b[3]};
}
__device__ __forceinline__ unsigned cvtpk(float lo, float hi) {
  f32x2 v = {lo, hi}; bf16x2_t b = __builtin_convertvector(v, bf16x2_t); return __builtin_bit_cast(unsigned, b);
}
__device__ __forceinline__ u32x2 cvt4(f32x4 v) { return (u32x2){cvtpk(v[0], v[1]), cvtpk(v[2], v[3])}; }
__device__ __forceinline__ float bflo(unsigned w) { return __uint_as_float(w << 16); }
__device__ __forceinline__ float bfhi(unsigned w) { return __uint_as_float(w & 0xffff0000u); }
__device__ __forceinline__ f32x4 unpack4(u32x2 w) { return (f32x4){bflo(w[0]), bfhi(w[0]), bflo(w[1]), bfhi(w[1])}; }
__device__ __forceinline__ float bfr(float v) { return bflo(cvtpk(v, 0.f)); }
__device__ __forceinline__ void ln_consts(float sum, float sumsq, float& mean, float& rstd) {
  const float m = sum * (1.f / DM), var = fmaxf(sumsq * (1.f / DM) - m * m, 0.f);
  mean = bfr(m); rstd = __builtin_amdgcn_rcpf(bfr(__builtin_amdgcn_sqrtf(bfr(bfr(var) + EPS))));
}
__device__ __forceinline__ int rfl(int v) { return __builtin_amdgcn_readfirstlane(v); }
__device__ __forceinline__ bf16x8 ones8() { return (bf16x8){0x3F80, 0x3F80, 0x3F80, 0x3F80, 0x3F80, 0x3F80, 0x3F80, 0x3F80}; }
#define Z4 ((f32x4){0.f, 0.f, 0.f, 0.f})
__device__ __forceinline__ void st16(rsrc_t ws, unsigned off, u32x4 v) { __builtin_amdgcn_raw_buffer_store_b128(v, ws, off, 0, 16); }
__device__ __forceinline__ bf16x8 ldw16(rsrc_t ws, unsigned off) { return __builtin_bit_cast(bf16x8, __builtin_amdgcn_raw_buffer_load_b128(ws, off, 0, 16)); }
__device__ __forceinline__ u32x4 pair16(u32x2 X, u32x2 Y) {
  auto a = __builtin_amdgcn_permlane16_swap(X[0], Y[0], false, false);
  auto b = __builtin_amdgcn_permlane16_swap(X[1], Y[1], false, false);
  return (u32x4){a[0], b[0], a[1], b[1]};
}
__device__ __forceinline__ u32x4 pair32(u32x2 X, u32x2 Y) {
  auto a = __builtin_amdgcn_permlane32_swap(X[0], Y[0], false, false);
  auto b = __builtin_amdgcn_permlane32_swap(X[1], Y[1], false, false);
  return (u32x4){a[0], b[0], a[1], b[1]};
}
__device__ __forceinline__ float sum_x16(float v) { auto r = __builtin_amdgcn_permlane16_swap(__float_as_uint(v), __float_as_uint(v), false, false); return __uint_as_float(r[0]) + __uint_as_float(r[1]); }
__device__ __forceinline__ float sum_x32(float v) { auto r = __builtin_amdgcn_permlane32_swap(__float_as_uint(v), __float_as_uint(v), false, false); return __uint_as_float(r[0]) + __uint_as_float(r[1]); }
__device__ __forceinline__ float max_x16(float v) { auto r = __builtin_amdgcn_permlane16_swap(__float_as_uint(v), __float_as_uint(v), false, false); return fmaxf(__uint_as_float(r[0]), __uint_as_float(r[1])); }
__device__ __forceinline__ float lo_x32(float v) { auto r = __builtin_amdgcn_permlane32_swap(__float_as_uint(v), __float_as_uint(v), false, false); return __uint_as_float(r[0]); }
template <int N> __device__ __forceinline__ float ror16(float v) { return __uint_as_float(__builtin_amdgcn_update_dpp(0u, __float_as_uint(v), 0x120 + N, 0xf, 0xf, false)); }
__device__ __forceinline__ float max_row16(float v) { v = fmaxf(v, ror16<8>(v)); v = fmaxf(v, ror16<4>(v)); v = fmaxf(v, ror16<2>(v)); return fmaxf(v, ror16<1>(v)); }
template <int ND, int NM> __device__ __forceinline__ void weave() {
#pragma unroll
  for (int i = 0; i < ND; ++i) { __builtin_amdgcn_sched_group_barrier(0x008, 1, 0); __builtin_amdgcn_sched_group_barrier(0x100, 1, 0); }
  if (NM > ND) __builtin_amdgcn_sched_group_barrier(0x008, NM - ND, 0);
}

__device__ __forceinline__ u32x4 nop4(u32x4 v) { return __builtin_elementwise_min(v, (u32x4){POISON - 1, POISON - 1, POISON - 1, POISON - 1}); }
template <int NP> __device__ __forceinline__ bool pieces_ok(lds_cptr base) {
  const int lane = threadIdx.x & 63;
  unsigned m = 0;
#pragma unroll
  for (int i = 0; i < NP; ++i) { const u32x4 v = *(const LAS u32x4*)(base + i * 1024 + lane * 16); const unsigned a = v[0] > v[1] ? v[0] : v[1], b = v[2] > v[3] ? v[2] : v[3]; m = m > a ? m : a; m = m > b ? m : b; }
  return !__any(m == POISON);
}
template <int NP> struct Chk {
  u32x4 v[NP];
  __device__ __forceinline__ void read(lds_cptr base) {
    const int lane = threadIdx.x & 63;
#pragma unroll
    for (int i = 0; i < NP; ++i) v[i] = *(const LAS u32x4*)(base + i * 1024 + lane * 16);
  }
  __device__ __forceinline__ bool ok() const {
    unsigned m = 0;
#pragma unroll
    for (int i = 0; i < NP; ++i) { const unsigned a = v[i][0] > v[i][1] ? v[i][0] : v[i][1], b = v[i][2] > v[i][3] ? v[i][2] : v[i][3]; m = m > a ? m : a; m = m > b ? m : b; }
    return !__any(m == POISON);
  }
};
struct Spin {
  unsigned n; unsigned long long t0;
  __device__ __forceinline__ Spin() : n(0), t0(__builtin_amdgcn_s_memrealtime()) {}
  __device__ __forceinline__ bool expired(char* ws) {
    if ((++n & 63u) == 0u && __builtin_amdgcn_s_memrealtime() - t0 > 20000000ull) {
      if ((threadIdx.x & 63) == 0) __hip_atomic_store((gu32*)(ws + WS_FLAG + FL_TMO), 1u, RLX_AGENT);
      return true;
    }
    return false;
  }
};

__device__ __forceinline__ unsigned make_tag(unsigned epoch) { return epoch * 0x9E3779B1u + 0x7F4A7C15u; }
__device__ __forceinline__ void publish(char* ws, unsigned flag_off, unsigned tag) {
  if (threadIdx.x == 0) __hip_atomic_store((gu32*)(ws + WS_FLAG + flag_off), tag, RLX_AGENT);
}
template <int NW>
__device__ __forceinline__ void poll(char* ws, unsigned flag_off, unsigned tag) {
  const int lane = threadIdx.x & 63;
  gu32* f = (gu32*)(ws + WS_FLAG + flag_off);
  const unsigned long long t0 = __builtin_amdgcn_s_memrealtime();
  for (unsigned spins = 1;; ++spins) {
    bool ok = true;
    if (NW == 256) {
#pragma unroll
      for (int i = 0; i < 4; ++i) ok &= __hip_atomic_load(f + i * 64 + lane, RLX_AGENT) == tag;
    } else ok = __hip_atomic_load(f + (lane < NW ? lane : 0), RLX_AGENT) == tag;
    if (__all(ok)) break;
    if ((spins & 63u) == 0u && __builtin_amdgcn_s_memrealtime() - t0 > 20000000ull) {
      if (lane == 0) __hip_atomic_store((gu32*)(ws + WS_FLAG + FL_TMO), tag, RLX_AGENT);
      break;
    }
    __builtin_amdgcn_s_sleep(1);
  }
}
template <int NW> __device__ __forceinline__ void await(char* ws, unsigned flag_off, unsigned tag) {
  if (rfl(threadIdx.x >> 6) == 0) poll<NW>(ws, flag_off, tag);
  raw_bar();
}

struct Relayout {
  const char* src; int pitch, dstride, nt; unsigned dst;
  __device__ __forceinline__ void decode(const Params& p, int J) {
    if (J >= 1176) J = 0;
    if (J < 128) { const int ks = J >> 3, cg = J & 7; src = p.wo + ((size_t)ks * 32 * DM + cg * 64) * 2; pitch = DM; dst = WS_WOF + (unsigned)(cg * 4 * 16 + ks) * 1024; dstride = 16 * 1024; nt = 4; }
    else if (J < 472) { const int ks = (J - 128) >> 3, cg = (J - 128) & 7; src = p.wd + ((size_t)ks * 32 * DM + cg * 64) * 2; pitch = DM; dst = WS_WDF + (unsigned)(cg * 4 * 43 + ks) * 1024; dstride = 43 * 1024; nt = 4; }
    else { const int r = J - 472, mat = r >= 352, r2 = r - mat * 352, ks = r2 / 22, cg = r2 - ks * 22;
      src = (mat ? p.wu : p.wg) + ((size_t)ks * 32 * FF + cg * 64) * 2; pitch = FF; dst = WS_WGF + (unsigned)((mat * 86 + cg * 4) * 16 + ks) * 1024; dstride = 16 * 1024; nt = cg == 21 ? 2 : 4; }
  }
  __device__ __forceinline__ void load(unsigned lds, int lane) const {
    const int r = lane >> 3, c = lane & 7;
#pragma unroll
    for (int kg = 0; kg < 4; ++kg) {
      int j = (c >> 1) ^ (((r >> 1) & 1) | ((kg & 1) << 1)); j = j < nt ? j : j - 2;
      glds16<false>(src, (unsigned)((kg * 8 + r) * pitch + j * 16 + (c & 1) * 8) * 2u, lds + kg * 1024);
    }
  }
  __device__ __forceinline__ void store(rsrc_t ws, lds_cptr L, int lane) const {
    const int g = lane >> 4, q = (lane >> 2) & 3, pp = lane & 3, ff = ((q >> 1) & 1) | ((g & 1) << 1);
#pragma unroll
    for (int j = 0; j < 4; ++j)
      if (j < nt) st16(ws, dst + j * dstride + lane * 16, __builtin_bit_cast(u32x4, ldtr2(L + g * 1024 + q * 128 + ((j ^ ff) << 5) + pp * 8, 512)));
  }
};

struct F1 { bf16x8 a[4], w[3]; };
__device__ __forceinline__ void p1_qkv(const Params& p, rsrc_t ws, int b, int rb1, int cb1, char* smem, unsigned epoch TSARG) {
  const int tid = threadIdx.x, lane = tid & 63, wid = rfl(tid >> 6);
  const int r16 = lane & 15, g = lane >> 4;
  const unsigned lds0 = (unsigned)(size_t)(lds_ptr)smem;
  lds_cptr L = (lds_cptr)smem;
  constexpr int STG = 32768, ND = 4, NS = 8, MS = ND * STG;
  LAS f32x2* ms = (LAS f32x2*)(smem + MS);
  const int rb = rb1, cb = cb1;
  if (__builtin_expect(wid >= 4, 1)) {
    const int lw = wid - 4;
    const unsigned voffA = (unsigned)((16 * lw + (lane >> 3)) * DM + (((lane & 7) ^ (lane >> 3)) * 8)) * 2u;
    unsigned voffB[2];
#pragma unroll
    for (int i = 0; i < 2; ++i) { const int r = lane >> 3, c = lane & 7, ff = ((r >> 1) & 1) | (i << 1), j = (c >> 1) ^ ff;
      voffB[i] = (unsigned)((16 * lw + 8 * i + r) * (3 * DM) + j * 16 + (c & 1) * 8) * 2u; }
    const char* xa = p.x + (size_t)rb * 64 * DM * 2;
    const char* wb = p.wqkv + (size_t)cb * 64 * 2;
    const int rot = ((b >> 3) & 7) + 2 * (b >> 6);
    auto issue = [&](int s) {
      const unsigned d = lds0 + (s % ND) * STG;
      const int kc = (s + rot) & 7;
#pragma unroll
      for (int i = 0; i < 2; ++i) glds16<false>(xa + kc * 128 + i * 8 * DM * 2, voffA, d + (2 * lw + i) * 1024);
#pragma unroll
      for (int i = 0; i < 2; ++i) glds<3, false>(wb + (size_t)kc * 64 * (3 * DM) * 2, voffB[i], d + 8192 + (2 * lw + i) * 3072);
    };
    TS(20);
    issue(0); issue(1);
    TS(21);
    wait_vm<8>(); raw_bar();
    TS(23);
    issue(2);
#pragma unroll
    for (int j = 1; j < NS; ++j) {
      if (j + 2 < NS) { issue(j + 2); wait_bar<16>(); } else if (j + 1 < NS) wait_bar<8>(); else wait_bar<0>();
    }
    lds_bar();
  } else {
    f32x4 acc[3][4], cs[3], sx = Z4, sq = Z4;
    int aoff[2][4];
#pragma unroll
    for (int ks = 0; ks < 2; ++ks)
#pragma unroll
      for (int i = 0; i < 4; ++i) aoff[ks][i] = ((wid + i) & 3) * 2048 + (r16 >> 3) * 1024 + (r16 & 7) * 128 + (((4 * ks + g) ^ (r16 & 7)) << 4);
    int boff;
    { const int q = (lane >> 2) & 3, pp = lane & 3, ff = ((q >> 1) & 1) | ((g & 1) << 1);
      boff = 8192 + g * 3072 + q * 128 + ((wid ^ ff) << 5) + pp * 8; }
#pragma unroll
    for (int i = 0; i < 3; ++i) { cs[i] = Z4;
#pragma unroll
      for (int j = 0; j < 4; ++j) acc[i][j] = Z4; }
    const bf16x8 one = ones8();
    F1 fa, fb;
    auto rd = [&](F1& fr, int s, int ks) {
      lds_cptr B = L + (s % ND) * STG;
#pragma unroll
      for (int seg = 0; seg < 3; ++seg) fr.w[seg] = ldtr2(B + boff + ks * 12288 + seg * 1024, 512);
#pragma unroll
      for (int i = 0; i < 4; ++i) fr.a[i] = ld128(B + aoff[ks][i]);
    };
    auto mm = [&](const F1& fr) {
#pragma unroll
      for (int i = 0; i < 4; ++i) {
        acc[0][i] = MFMA16(fr.w[0], fr.a[i], acc[0][i]);
        acc[1][i] = MFMA16(fr.w[1], fr.a[i], acc[1][i]);
        acc[2][i] = MFMA16(fr.a[i], fr.w[2], acc[2][i]);
      }
      cs[0] = MFMA16(fr.w[0], one, cs[0]); cs[1] = MFMA16(fr.w[1], one, cs[1]); cs[2] = MFMA16(one, fr.w[2], cs[2]);
      sx = MFMA16(one, fr.a[0], sx); sq = MFMA16(fr.a[0], fr.a[0], sq);
    };
    const unsigned pz_off = ((unsigned)(cb * 4 + wid) * 64 + rb * 2) * 1024 + lane * 16;
    lds_bar();
    TS(1);
    rd(fa, 0, 0); SB();
#pragma unroll
    for (int s = 0; s < NS; ++s) {
      rd(fb, s, 1); mm(fa); weave<10, 17>(); SB();
      if (s < 6) st16(ws, (s >> 1 == 0 ? WS_Q : s >> 1 == 1 ? WS_K : WS_V) + pz_off + (s & 1) * 1024, (u32x4){POISON, POISON, POISON, POISON});
      if (s == 6 && lane == 0) __hip_atomic_store((gu32*)(p.ws + WS_KMAX) + (cb * 4 + wid) * 32 + rb, POISON, RLX_AGENT);
      if (s + 1 < NS) { lds_bar(); rd(fa, s + 1, 0); mm(fb); weave<10, 17>(); SB(); }
      else { mm(fb); SB(); }
    }
    TS(2);
    if ((r16 >> 2) == g) {
      const int e = r16 & 3; const float s2 = e == 0 ? sq[0] : e == 1 ? sq[1] : e == 2 ? sq[2] : sq[3];
      float mean, rstd; ln_consts(sx[0], s2, mean, rstd);
      ms[wid * 16 + r16] = (f32x2){mean, rstd};
    }
    wait_bar<0>();
    publish(p.ws, FL_1 + (cb * 32 + rb) * 4, make_tag(epoch));
    TS(25);
    const int h = cb * 4 + wid;
    u32x2 qv[4], kv[4], vv[4];
    float kmx = 0.f;
#pragma unroll
    for (int i = 0; i < 4; ++i) {
      const int rt = (wid + i) & 3;
      const f32x2 st = ms[rt * 16 + r16];
      qv[i] = cvt4((acc[0][i] - st.x * cs[0]) * (st.y * QSCALE));
      { const f32x4 v = (acc[1][i] - st.x * cs[1]) * st.y; kv[i] = cvt4(v);
        kmx = fmaxf(kmx, sum_x32(sum_x16((v[0] * v[0] + v[1] * v[1]) + (v[2] * v[2] + v[3] * v[3])))); }
      { const f32x4 m01 = *(LAS f32x4*)(ms + rt * 16 + 4 * g), m23 = *(LAS f32x4*)(ms + rt * 16 + 4 * g + 2); const float cv = cs[2][0];
        vv[i] = (u32x2){cvtpk((acc[2][i][0] - m01[0] * cv) * m01[1], (acc[2][i][1] - m01[2] * cv) * m01[3]),
                        cvtpk((acc[2][i][2] - m23[0] * cv) * m23[1], (acc[2][i][3] - m23[2] * cv) * m23[3])}; }
    }
#pragma unroll
    for (int i = 0; i < 4; i += 2) {
      { const int rt = (wid + i + (g & 1)) & 3, grow = rb * 64 + rt * 16;
        const unsigned off = (((unsigned)h * 64 + (grow >> 5)) * 2 + (g >> 1)) * 512 + ((grow & 31) + r16) * 16;
        st16(ws, WS_K + off, nop4(pair16(kv[i], kv[i + 1]))); st16(ws, WS_Q + off, nop4(pair16(qv[i], qv[i + 1]))); }
      { const int rt = (wid + i + (g >> 1)) & 3, grow = rb * 64 + rt * 16;
        const unsigned off = ((((unsigned)h * 64 + (grow >> 5)) * 2 + (rt & 1)) * 2 + (g & 1)) * 256 + r16 * 16;
        st16(ws, WS_V + off, nop4(pair32(vv[i], vv[i + 1]))); }
    }
    kmx = max_row16(kmx);
    if (lane == 0) __hip_atomic_store((gu32*)(p.ws + WS_KMAX) + h * 32 + rb, min(__float_as_uint(kmx), POISON - 1), RLX_AGENT);
    TS(26);
  }
}

constexpr int A_K = 0, A_V = 65536, A_ONE = 131072, A_Q = A_ONE + 5120, A_KM = A_Q + 8192, A_DUMMY = A_KM + 1024, A_CTR = A_DUMMY + 256, A_RL = A_CTR + 256, A_BYTES = A_RL + 3 * 4096;
constexpr float SAFE_BOUND = 48.f;
#define PRIO_SW 9
#define SP2 5
#define SP3 8
#define SP4 11
constexpr int PRIO_SWITCH = PRIO_SW;

struct P3Pre { bf16x8 wof[16]; u32x2 xv[4]; };
__device__ __forceinline__ void p2_attn(const Params& p, rsrc_t ws, int b, int h, int qr, char* smem, unsigned tag, P3Pre& pre TSARG) {
  const int tid = threadIdx.x, lane = tid & 63, wid = rfl(tid >> 6);
  const int r32 = lane & 31, hh = lane >> 5;
  const unsigned lds0 = (unsigned)(size_t)(lds_ptr)smem;
  lds_cptr L = (lds_cptr)smem;
  const char* kg = p.ws + WS_K + (size_t)h * 65536;
  const char* vg = p.ws + WS_V + (size_t)h * 65536;
  const unsigned vo = lane * 16;
  if (wid < 4) {
    const int t = 4 * wid, gt = (t + 8 * qr) & 63;
    auto first = [&]() {
      glds<2, true>(p.ws + WS_Q + ((size_t)h * 64 + qr * 8 + 2 * wid) * 1024, vo, lds0 + A_Q + 2 * wid * 1024);
      glds4<true>(p.ws + WS_KMAX + h * 128, (lane & 31) * 4, lds0 + A_KM + wid * 256);
      glds<4, true>(kg + gt * 1024, vo, lds0 + A_K + t * 1024);
      glds<4, true>(vg + gt * 1024, vo, lds0 + A_V + t * 1024);
      wait_vm<0>();
      return pieces_ok<2>(L + A_Q + 2 * wid * 1024) && pieces_ok<4>(L + A_K + t * 1024) && pieces_ok<4>(L + A_V + t * 1024) && !__any(*(const LAS unsigned*)(L + A_KM + wid * 256 + lane * 4) == POISON);
    };
    if (!first()) { Spin sp; while (!first() && !sp.expired(p.ws)) {} }
  }
  st16(ws, WS_O + (unsigned)((qr * 16 + 2 * wid + (lane >> 5)) * 16 + (h >> 1)) * 1024 + (h & 1) * 512 + (lane & 31) * 16, (u32x4){POISON, POISON, POISON, POISON});
  if (tid == 0) *(LAS unsigned*)(smem + A_CTR) = 0u;
  { const unsigned o1 = (tid & 31) < 2 ? 0x3F803F80u : 0u;
    *(LAS u32x2*)(smem + A_ONE + tid * 8) = (u32x2){o1, o1};
    if (tid < 128) *(LAS u32x2*)(smem + A_ONE + 4096 + tid * 8) = (u32x2){o1, o1}; }
  if (wid == 0 && blockIdx.x == 0) {
    poll<256>(p.ws, FL_1, tag);
    if (lane == 0) __hip_atomic_store((gu32*)(p.ws + WS_FLAG + FL_EPOCH), tag, RLX_AGENT);
  }
  lds_bar();
  TS(4);
  const bf16x8 qf = ld128(L + A_Q + wid * 1024 + lane * 16);
  float c;
  { float km = *(const LAS float*)(L + A_KM + lane * 4);
    km = max_x16(max_row16(km));
    float q2 = 0.f;
#pragma unroll
    for (int j = 0; j < 8; ++j) { const float v = __uint_as_float((unsigned)(unsigned short)qf[j] << 16); q2 += v * v; }
    q2 = sum_x32(q2);
    c = sqrtf(q2 * km) * 1.02f + 0.01f; }
  const bool fast = __all(c <= SAFE_BOUND);
  const bool vlane = r32 < 16;
  int vaddr = (int)lds0 + (vlane ? A_V : A_ONE) + hh * 256 + (r32 & 15) * 16;
  const int vstep = vlane ? 4096 : 0;
  int kaddr = (int)lds0 + A_K + lane * 16;
  f32x16 o;
#pragma unroll
  for (int i = 0; i < 16; ++i) o[i] = 0.f;
  unsigned go = vo + ((16 + wid + 8 * qr) & 63) * 1024;
  unsigned ld = lds0 + A_K + (16 + wid) * 1024;
  auto settle = [&]() {
    int lane = threadIdx.x & 63; asm volatile("" : "+v"(lane));
    const unsigned go0 = lane * 16 + ((16 + wid + 8 * qr) & 63) * 1024, ld0 = lds0 + A_K + (16 + wid) * 1024;
    auto good = [&]() {
      unsigned m = 0;
#pragma unroll
      for (int j = 0; j < 12; ++j) { const u32x4 v = *(const LAS u32x4*)(L + (j < 6 ? A_K : A_V) + (16 + wid + 8 * (j % 6)) * 1024 + lane * 16); m = max(m, max(max(v[0], v[1]), max(v[2], v[3]))); }
      return !__any(m == POISON);
    };
    if (!good()) {
      Spin sp;
      do {
#pragma unroll
        for (int j = 0; j < 6; ++j) { const unsigned gj = (go0 + j * 8192) & 0xffffu; glds16<true>(kg, gj, ld0 + j * 8192); glds16<true>(vg, gj, ld0 + 65536 + j * 8192); }
        wait_vm<0>();
      } while (!good() && !sp.expired(p.ws));
    }
    raw_bar();
  };
#define FRESH() int lane = threadIdx.x & 63, bq = b; asm volatile("" : "+v"(lane), "+s"(bq)); const int xq = bq & 7, iq = bq >> 3; \
                const int rb = xq * 4 + (iq & 3), cb = iq >> 2, mb = xq * 2 + (iq & 1), fb = iq >> 1, u0 = fb < 11 ? 3 * fb : 33 + 2 * (fb - 11), nu = fb < 11 ? 3 : 2; \
                const int slot = wid - 1, J = bq * 5 + slot, rlo = slot < 2 ? A_Q + slot * 4096 : A_RL + (slot - 2) * 4096; const bool rl = wid >= 1 && wid <= 5 && J < 1176; \
                (void)rb; (void)cb; (void)mb; (void)u0; (void)nu; (void)rlo; (void)rl; (void)lane
  auto split1 = [&]() {
    FRESH();
    if (rl) { Relayout r; r.decode(p, J); r.load(lds0 + rlo, lane); }
  };
  auto split2 = [&]() {
    FRESH();
    if (rl) { wait_vm<0>(); Relayout r; r.decode(p, J); r.store(ws, L + rlo, lane); }
#pragma unroll
    for (int i = 0; i < 5; ++i) {
      const int j = wid + 8 * i;
      if (j < 33) {
        unsigned off;
        if (j < 8) off = WS_H + (unsigned)((rb * 4 + (j >> 1)) * 16 + cb * 2 + (j & 1)) * 1024 + lane * 16;
        else if (j < 32) { const int jj = j - 8, rt = jj / 3, u = jj - rt * 3; off = WS_ACT + (unsigned)((mb * 8 + rt) * 43 + u0 + (u < nu ? u : 0)) * 1024 + lane * 16; }
        else off = WS_HST + (unsigned)(rb * 8 + cb) * 512 + (lane & 31) * 16;
        st16(ws, off, (u32x4){POISON, POISON, POISON, POISON});
      }
    }
  };
  auto split3 = [&]() {
    FRESH();
    wait_vm<0>();
    if (lane == 0 && __hip_atomic_fetch_add((LAS unsigned*)(smem + A_CTR), 1u, __ATOMIC_RELAXED, __HIP_MEMORY_SCOPE_WORKGROUP) == 7u)
      __hip_atomic_store((gu32*)(p.ws + WS_FLAG + FL_P) + bq, tag, RLX_AGENT);
  };
  auto split4 = [&]() {
    if (wid >= 4) {
      FRESH();
      poll<256>(p.ws, FL_P, tag);
      const int r16 = lane & 15, g = lane >> 4, wn = wid & 3;
#pragma unroll
      for (int i = 0; i < 16; ++i) pre.wof[i] = ldw16(ws, WS_WOF + ((unsigned)(cb * 4 + wn) * 16 + ((i + 8 * cb) & 15)) * 1024 + lane * 16);
#pragma unroll
      for (int i = 0; i < 4; ++i) pre.xv[i] = *(const u32x2*)(p.x + ((size_t)(rb * 64 + i * 16 + r16) * DM + cb * 64 + wn * 16 + 4 * g) * 2);
#pragma unroll
      for (int i = 0; i < 3; ++i) {
        const int ln = (iq & 1) * 768 + (wn * 3 + i) * 64 + lane, ch = ln >> 7, jt = ch >= 6 ? ch - 6 : ch;
        glds4<false>(p.ws + WS_WGF, (unsigned)(((ch >= 6) * 86 + u0 * 2 + (jt < 2 * nu ? jt : jt - 2)) * 128 + (ln & 127)) * 128u, lds0 + A_DUMMY);
      }
    }
  };
#undef FRESH
  if (fast) {
    f32x16 negc, sa, sb; f32x8 pp, kk; f32x16 vv;
#pragma unroll
    for (int i = 0; i < 16; ++i) negc[i] = -c;
    int cnt, keep;
    asm volatile(ATTN_ASM_A
                 : [o] "+v"(o), [ka] "+v"(kaddr), [va] "+v"(vaddr), [go] "+v"(go), [ld] "+s"(ld), [keep] "=&s"(keep),
                   "=&{v[128:143]}"(sa), "=&{v[144:159]}"(sb), "=&{v[160:167]}"(pp), "=&{v[168:175]}"(kk), "=&{v[176:191]}"(vv)
                 : [q] "v"(qf), [nc] "v"(negc), [vs] "v"(vstep), [kg] "s"(kg), [vg] "s"(vg)
                 : "memory", "scc");
    settle(); split1();
    const int sw = wid < 4 ? PRIO_SWITCH : 99;
#define ATTN_B(C0, C1) asm volatile(ATTN_ASM_B \
                 : [o] "+v"(o), [ka] "+v"(kaddr), [va] "+v"(vaddr), [cnt] "=&s"(cnt), \
                   "+{v[128:143]}"(sa), "+{v[144:159]}"(sb), "+{v[160:167]}"(pp), "+{v[168:175]}"(kk), "+{v[176:191]}"(vv) \
                 : [q] "v"(qf), [nc] "v"(negc), [vs] "v"(vstep), [sw] "s"(sw), [c0] "s"(C0), [c1] "s"(C1) \
                 : "memory", "scc")
    ATTN_B(2, SP2); split2();
    ATTN_B(SP2, SP3); split3();
    ATTN_B(SP3, SP4); split4();
    ATTN_B(SP4, 16);
#undef ATTN_B
  } else {
#pragma unroll
    for (int j = 0; j < 6; ++j) { const unsigned gj = (go + j * 8192) & 0xffffu; glds16<true>(kg, gj, ld + j * 8192); glds16<true>(vg, gj, ld + 65536 + j * 8192); }
    float m = -INFINITY;
    vaddr -= (int)lds0; kaddr -= (int)lds0;
#pragma unroll 1
    for (int t4 = 0; t4 < 16; ++t4) {
      if (t4 == 2) { wait_vm<0>(); settle(); split1(); }
      if (t4 == SP2) split2();
      if (t4 == SP3) split3();
      if (t4 == SP4) split4();
#pragma unroll 1
      for (int u = 0; u < 4; ++u) {
        const int t = t4 * 4 + u;
        f32x16 z;
#pragma unroll
        for (int i = 0; i < 16; ++i) z[i] = 0.f;
        const f32x16 s = MFMA32(ld128(L + kaddr + t * 1024), qf, z);
        float mx = s[0];
#pragma unroll
        for (int i = 1; i < 16; ++i) mx = fmaxf(mx, s[i]);
        mx = fmaxf(mx, __shfl_xor(mx, 32));
        const float mn = fmaxf(m, mx);
        const float al = __builtin_amdgcn_exp2f(m - mn);
        m = mn;
#pragma unroll
        for (int i = 0; i < 16; ++i) o[i] *= al;
        u32x4 p0, p1;
#pragma unroll
        for (int i = 0; i < 4; ++i) {
          p0[i] = cvtpk(__builtin_amdgcn_exp2f(s[2 * i] - mn), __builtin_amdgcn_exp2f(s[2 * i + 1] - mn));
          p1[i] = cvtpk(__builtin_amdgcn_exp2f(s[8 + 2 * i] - mn), __builtin_amdgcn_exp2f(s[8 + 2 * i + 1] - mn));
        }
        const bf16x8 v0 = ld128(L + vaddr + u * 1024), v1 = ld128(L + vaddr + u * 1024 + 512);
        o = MFMA32(v0, __builtin_bit_cast(bf16x8, p0), o);
        o = MFMA32(v1, __builtin_bit_cast(bf16x8, p1), o);
      }
      vaddr += vstep;
    }
  }
  TS(5);
  const float inv = __builtin_amdgcn_rcpf(lo_x32(o[8]));
  const u32x4 ov = pair32((u32x2){cvtpk(o[0] * inv, o[1] * inv), cvtpk(o[2] * inv, o[3] * inv)}, (u32x2){cvtpk(o[4] * inv, o[5] * inv), cvtpk(o[6] * inv, o[7] * inv)});
  const int row = qr * 256 + wid * 32 + r32;
  st16(ws, WS_O + ((((unsigned)(row >> 4) * 16 + (h >> 1)) * 4 + (h & 1) * 2 + hh) * 16 + (row & 15)) * 16, nop4(ov));
}

constexpr int G64_KPS = 8, G64_STG = 4 * G64_KPS * 1024;
template <int NKS, int ND, int LA, int NPRE, class Pre>
__device__ __forceinline__ void gemm64_load(char* ws, const char* ag  , int rot, char* smem, const Pre& pre) {
  const int lane = threadIdx.x & 63, wid = rfl(threadIdx.x >> 6);
  const unsigned lds0 = (unsigned)(size_t)(lds_ptr)smem;
  lds_cptr L = (lds_cptr)smem + wid * 8192;
  constexpr int NS = (NKS + 7) / 8, LAST = NKS - 8 * (NS - 1), NFULL = NKS / 8;
  static_assert(LAST == 8 || LAST == 3, "last stage");
  auto issue = [&](int s) {
    const unsigned d = lds0 + (s % ND) * G64_STG + wid * 8192;
    if (s < NFULL) { const int q0 = s + rot, q = q0 >= NFULL ? q0 - NFULL : q0; glds<4, true>(ag + q * 8192, lane * 16, d); glds<4, true>(ag + q * 8192 + 4096, lane * 16, d + 4096); }
    else glds<3, true>(ag + s * 8192, lane * 16, d);
  };
  auto okay = [&](int s) { return s < NFULL ? pieces_ok<8>(L + (s % ND) * G64_STG) : pieces_ok<3>(L + (s % ND) * G64_STG); };
  auto ensure = [&](int s) { if (!okay(s)) { Spin sp; do { issue(s); wait_vm<0>(); } while (!okay(s) && !sp.expired(ws)); } };
  auto waitv = [&](int j) {
    const int hi = j + LA - 2 < NS - 1 ? j + LA - 2 : NS - 1;
    const int n = (hi - j) * 8 - ((hi == NS - 1 && hi > j) ? 8 - LAST : 0);
    switch (n) { case 3: wait_vm<3>(); break; case 8: wait_vm<8>(); break; case 11: wait_vm<11>(); break; case 16: wait_vm<16>(); break; default: wait_vm<0>(); break; }
  };
  issue(0); pre(); wait_vm<NPRE>(); ensure(0);
#pragma unroll
  for (int s = 1; s < LA; ++s) if (s < NS) issue(s);
  lds_bar();
#pragma unroll
  for (int j = 1; j < NS; ++j) {
    waitv(j);
    if (j < NFULL) {
      Chk<8> c; c.read(L + (j % ND) * G64_STG);
      if (j + LA - 1 < NS) issue(j + LA - 1);
      if (!c.ok()) ensure(j);
    } else ensure(j);
    lds_bar();
  }
}
template <int NKS, int ND, int TS0>
__device__ __forceinline__ void gemm64_mma(const bf16x8 (&wf)[NKS], char* smem, f32x4 (&acc)[4] TSARG) {
  const int lane = threadIdx.x & 63;
  lds_cptr L = (lds_cptr)smem + lane * 16;
#pragma unroll
  for (int i = 0; i < 4; ++i) acc[i] = Z4;
  bf16x8 f[2][4];
  auto rd = [&](int ks) {
    lds_cptr B = L + ((ks >> 3) % ND) * G64_STG + (ks & 7) * 1024;
#pragma unroll
    for (int rt = 0; rt < 4; ++rt) f[ks & 1][rt] = ld128(B + rt * 8192);
  };
  lds_bar();
  TS(TS0);
  rd(0); SB();
#pragma unroll
  for (int ks = 0; ks < NKS; ++ks) {
    if (ks + 1 < NKS) {
      if (((ks + 1) & 7) == 0) lds_bar();
      rd(ks + 1);
    }
#pragma unroll
    for (int rt = 0; rt < 4; ++rt) acc[rt] = MFMA16(wf[ks], f[ks & 1][rt], acc[rt]);
    if (ks + 1 < NKS) weave<4, 4>();
    SB();
  }
  TS(TS0 + 1);
}

constexpr int G_B = 0, G_BS = 24576, G_A = 3 * G_BS, G_AS = 16384, G_ST = G_A + 4 * G_AS, G_MS = G_ST + 8192, G_CS = G_MS + 1024;
struct WLoad {
  const char* src[3]; unsigned dst; int rot;
  __device__ __forceinline__ void init(const Params& p, int mb, int fb, int lw, unsigned lds0) {
    const int u0 = fb < 11 ? 3 * fb : 33 + 2 * (fb - 11), nu = fb < 11 ? 3 : 2;
    rot = (fb + 4 * (mb & 1)) & 7;
#pragma unroll
    for (int i = 0; i < 3; ++i) { const int jt = 3 * (lw & 1) + i; src[i] = p.ws + WS_WGF + (size_t)((lw >> 1) * 86 + u0 * 2 + (jt < 2 * nu ? jt : jt - 2)) * 16384; }
    dst = lds0 + G_B + lw * 6144;
  }
  __device__ __forceinline__ void issue(int s, int lane) const {
#pragma unroll
    for (int i = 0; i < 3; ++i) glds<2, true>(src[i] + ((s + rot) & 7) * 2048, lane * 16, dst + (s % 3) * G_BS + i * 2048);
  }
};
struct F4 { bf16x8 a[4], w[2][3]; };
__device__ __forceinline__ void p4_gateup(const Params& p, rsrc_t ws, int mb, int fb, char* smem, const WLoad& wl, int wct, int rot5, bf16x8 (&wdf)[43] TSARG) {
  const int tid = threadIdx.x, lane = tid & 63, wid = rfl(tid >> 6);
  const int r16 = lane & 15, g = lane >> 4;
  const unsigned lds0 = (unsigned)(size_t)(lds_ptr)smem;
  lds_cptr L = (lds_cptr)smem;
  constexpr int NS = 8;
  const int u0 = fb < 11 ? 3 * fb : 33 + 2 * (fb - 11), nu = fb < 11 ? 3 : 2;
  if (wid >= 4) {
    const int lw = wid - 4;
    const char* ag = p.ws + WS_H + (size_t)(mb * 8 + 2 * lw) * 16384;
    auto issueA = [&](int s) {
      const unsigned d = lds0 + G_A + (s & 3) * G_AS + lw * 4096;
#pragma unroll
      for (int i = 0; i < 2; ++i) glds<2, true>(ag + i * 16384 + ((s + wl.rot) & 7) * 2048, lane * 16, d + i * 2048);
    };
    auto issueS = [&]() {
      glds<2, true>(p.ws + WS_HST + (size_t)mb * 8192 + lw * 2048, lane * 16, lds0 + G_ST + lw * 2048);
    };
    auto ensure = [&](int s) {
      lds_cptr A = L + G_A + (s & 3) * G_AS + lw * 4096;
      if (!pieces_ok<4>(A)) { Spin sp; do { issueA(s); wait_vm<0>(); } while (!pieces_ok<4>(A) && !sp.expired(p.ws)); }
    };
    float cs[3] = {0.f, 0.f, 0.f};
    const unsigned one_bits = 0x3F803F80u;
    const bf16x2_t one2 = __builtin_bit_cast(bf16x2_t, one_bits);
    auto colsum = [&](int s) {
#pragma unroll
      for (int i = 0; i < 6; ++i) {
        const u32x4 v = *(const LAS u32x4*)(L + G_B + (s % 3) * G_BS + lw * 6144 + i * 1024 + lane * 16);
#pragma unroll
        for (int e = 0; e < 4; ++e) { const unsigned t = v[e]; cs[i >> 1] = __builtin_amdgcn_fdot2_f32_bf16(__builtin_bit_cast(bf16x2_t, t), one2, cs[i >> 1], false); }
      }
    };
    issueA(0);
    colsum(0); colsum(1);
    wait_vm<0>();
    ensure(0);
    issueA(1); issueS(); issueA(2); wl.issue(2, lane);
    lds_bar();
#pragma unroll
    for (int j = 1; j < NS; ++j) {
      if (j == 1) wait_vm<12>(); else if (j + 1 < NS) wait_vm<10>(); else wait_vm<0>();
      Chk<4> c; c.read(L + G_A + (j & 3) * G_AS + lw * 4096);
      if (j + 2 < NS) issueA(j + 2);
      if (!c.ok()) ensure(j);
      if (j == 2) {
        lds_cptr Sg = L + G_ST + lw * 2048;
        if (!pieces_ok<2>(Sg)) { Spin sp; do { issueS(); wait_vm<0>(); } while (!pieces_ok<2>(Sg) && !sp.expired(p.ws)); }
      }
      if (j >= 2) colsum(j);
      lds_bar();
      if (j + 2 < NS) wl.issue(j + 2, lane);
      if (j == 2) {
        const int row = tid - 256;
        if (row < 128) {
          float a = 0.f, bb = 0.f;
#pragma unroll
          for (int c = 0; c < 8; ++c) { const f32x2 v = *(const LAS f32x2*)(L + G_ST + (row >> 6) * 4096 + c * 512 + (row & 63) * 8); a += v.x; bb += v.y; }
          float mean, rstd; ln_consts(a, bb, mean, rstd);
          *(LAS f32x2*)(smem + G_MS + row * 8) = (f32x2){rstd, -mean * rstd};
        }
      }
    }
#pragma unroll
    for (int i = 0; i < 3; ++i) { const float t = sum_x32(sum_x16(cs[i])); if (g == 0) *(LAS float*)(smem + G_CS + ((lw * 3 + i) * 16 + r16) * 4) = t; }
    lds_bar();
  } else {
    const int wm = wid >> 1, wn = wid & 1;
    const int aoff = G_A + wm * 8192 + lane * 16;
    constexpr int m0 = 0, m1 = 12288;
    const int boff = G_B + wn * 6144 + lane * 16;
    f32x4 acc[2][3][4];
#pragma unroll
    for (int i = 0; i < 3; ++i)
#pragma unroll
      for (int j = 0; j < 4; ++j) { acc[0][i][j] = Z4; acc[1][i][j] = Z4; }
    F4 fa, fb2;
    auto rd = [&](F4& f, int s, int ks) {
      lds_cptr B = L + (s % 3) * G_BS, A = L + (s & 3) * G_AS;
#pragma unroll
      for (int i = 0; i < 4; ++i) f.a[i] = ld128(A + aoff + i * 2048 + ks * 1024);
#pragma unroll
      for (int i = 0; i < 3; ++i) { f.w[0][i] = ld128(B + boff + m0 + i * 2048 + ks * 1024); f.w[1][i] = ld128(B + boff + m1 + i * 2048 + ks * 1024); }
    };
    auto mm = [&](const F4& f) {
#pragma unroll
      for (int m = 0; m < 2; ++m)
#pragma unroll
        for (int i = 0; i < 3; ++i)
#pragma unroll
          for (int j = 0; j < 4; ++j) acc[m][i][j] = MFMA16(f.w[m][i], f.a[j], acc[m][i][j]);
    };
    lds_bar();
    TS(11);
    rd(fa, 0, 0); SB();
#pragma unroll
    for (int s = 0; s < NS; ++s) {
      rd(fb2, s, 1); mm(fa); weave<10, 24>(); SB();
      if (s + 1 < NS) {
        lds_bar();
        rd(fa, s + 1, 0); mm(fb2); weave<10, 24>(); SB();
      }
      else { mm(fb2); SB(); }
    }
    TS(12);
    const LAS float* csl = (const LAS float*)(smem + G_CS);
    lds_bar();
    TS(33);
    const LAS f32x2* ms = (const LAS f32x2*)(smem + G_MS);
    f32x2 st[4];
#pragma unroll
    for (int j = 0; j < 4; ++j) st[j] = ms[wm * 64 + j * 16 + r16];
    constexpr float K1 = -2.885390081777927f * 0.7978845608028654f, K2 = -2.885390081777927f * 0.035677408136300125f;
#pragma unroll
    for (int i = 0; i < 3; ++i) {
      const int jt = 3 * wn + i, u = jt >> 1, jj = jt & 1;
      const f32x4 cg = *(const LAS f32x4*)(csl + jt * 16 + 4 * g), cu = *(const LAS f32x4*)(csl + (6 + jt) * 16 + 4 * g);
      u32x2 av[4];
#pragma unroll
      for (int j = 0; j < 4; ++j) {
        const f32x4 ag2 = acc[0][i][j], au = acc[1][i][j];
        const f32x4 ga = ag2 * st[j].x + st[j].y * cg, up = au * st[j].x + st[j].y * cu;
        const f32x4 z = ga * (ga * ga * K2 + K1);
        const f32x4 d = (f32x4){__builtin_amdgcn_exp2f(z[0]), __builtin_amdgcn_exp2f(z[1]), __builtin_amdgcn_exp2f(z[2]), __builtin_amdgcn_exp2f(z[3])} + 1.f;
        const f32x4 rc = (f32x4){__builtin_amdgcn_rcpf(d[0]), __builtin_amdgcn_rcpf(d[1]), __builtin_amdgcn_rcpf(d[2]), __builtin_amdgcn_rcpf(d[3])};
        av[j] = cvt4((ga * rc) * up);
      }
      if (u < nu) {
#pragma unroll
        for (int j = 0; j < 4; j += 2) {
          const unsigned rt = (unsigned)mb * 8 + wm * 4 + j + (g & 1);
          st16(ws, WS_ACT + (((rt * 43 + (u0 + u)) * 4 + jj * 2 + (g >> 1)) * 16 + r16) * 16, nop4(pair16(av[j], av[j + 1])));
        }
      }
    }
    TS(34);
  }
  if (wid >= 4) {
#pragma unroll
    for (int i = 0; i < 43; ++i) { int ks = i; if (i < 40) { ks += rot5; ks = ks >= 40 ? ks - 40 : ks; } wdf[i] = ldw16(ws, WS_WDF + ((unsigned)wct * 43 + ks) * 1024 + lane * 16); }
  }
}

extern __shared__ __attribute__((aligned(16))) char smem_dyn[];

__global__ __launch_bounds__(512, 2) void mega(Params p) {
  char* smem = smem_dyn;
  const int b = blockIdx.x, x = b & 7, i5 = b >> 3;
  const int tid = threadIdx.x, lane = tid & 63, wid = rfl(tid >> 6);
  const int r16 = lane & 15, g = lane >> 4, wn = wid & 3;
  TS_BEGIN();
  const rsrc_t ws = __builtin_amdgcn_make_buffer_rsrc(p.ws, 0, WS_USED, 0x00020000);
  const unsigned epoch = __hip_atomic_load((gu32*)(p.ws + WS_FLAG + FL_EPOCH), RLX_AGENT);

  const int rb1 = (x >> 1) * 8 + (i5 & 7), cb1 = (x & 1) * 4 + (i5 >> 3);
  p1_qkv(p, ws, b, rb1, cb1, smem, epoch TSPASS);
  const unsigned tag = (unsigned)rfl((int)make_tag(epoch));
  TS(3);

  const int h = x * 4 + (i5 & 3), qr = i5 >> 2;
  const int rb = x * 4 + (i5 & 3), cb = i5 >> 2;
  const int mb = x * 2 + (i5 & 1), fb = i5 >> 1;
  await<32>(p.ws, FL_1 + (h >> 2) * 128, tag);
  TS(27);
  P3Pre pre;
  p2_attn(p, ws, b, h, qr, smem, tag, pre TSPASS);
  lds_bar();
  TS(6);

  const int wct = cb * 4 + wn, col = cb * 64 + wn * 16 + 4 * g;
  u32x2 hv[4];
  if (wid >= 4) {
#pragma unroll
    for (int i = 0; i < 16; ++i) PIN(pre.wof[i]);
#pragma unroll
    for (int i = 0; i < 4; ++i) PIN(pre.xv[i]);
    f32x4 acc[4];
    gemm64_mma<16, 2, 8>(pre.wof, smem + G_A, acc TSPASS);
    LAS f32x2* S = (LAS f32x2*)(smem + G_ST);
#pragma unroll
    for (int i = 0; i < 4; ++i) hv[i] = cvt4(unpack4(pre.xv[i]) + acc[i]);
#pragma unroll
    for (int i = 0; i < 4; i += 2) {
      const unsigned rt = (unsigned)rb * 4 + i + (g & 1);
      st16(ws, WS_H + (((rt * 16 + cb * 2 + (wn >> 1)) * 4 + (wn & 1) * 2 + (g >> 1)) * 16 + r16) * 16, nop4(pair16(hv[i], hv[i + 1])));
    }
#pragma unroll
    for (int i = 0; i < 4; ++i) {
      const f32x4 v = unpack4(hv[i]);
      const float s1 = sum_x32(sum_x16((v[0] + v[1]) + (v[2] + v[3]))), s2 = sum_x32(sum_x16((v[0] * v[0] + v[1] * v[1]) + (v[2] * v[2] + v[3] * v[3])));
      if (g == 0) S[(i * 16 + r16) * 4 + wn] = (f32x2){s1, s2};
    }
    lds_bar();
    if (wid == 4) {
      const f32x2 a = S[lane * 4], bb = S[lane * 4 + 1], c = S[lane * 4 + 2], d = S[lane * 4 + 3];
      const unsigned s1 = __float_as_uint((a.x + bb.x) + (c.x + d.x)), s2 = __float_as_uint((a.y + bb.y) + (c.y + d.y));
      __hip_atomic_store((gu64*)(p.ws + WS_HST + (size_t)(rb * 8 + cb) * 512 + lane * 8), ((unsigned long long)(s2 < POISON ? s2 : POISON - 1) << 32) | (s1 < POISON ? s1 : POISON - 1), RLX_AGENT);
    }
    TS(32);
    raw_bar();
  } else {
    auto pre3 = [&]() {
      WLoad w0; w0.init(p, mb, fb, wid, (unsigned)(size_t)(lds_ptr)smem); w0.issue(0, lane); w0.issue(1, lane);
    };
    gemm64_load<16, 2, 2, 12>(p.ws, p.ws + WS_O + (size_t)(rb * 4 + wid) * 16384, cb & 1, smem + G_A, pre3);
    lds_bar();
    raw_bar();
  }
  TS(10);

  bf16x8 wdf[43];
  { WLoad wl;
    if (wid >= 4) wl.init(p, mb, fb, wid - 4, (unsigned)(size_t)(lds_ptr)smem);
    p4_gateup(p, ws, mb, fb, smem, wl, wct, 8 * (cb % 5), wdf TSPASS); }
  lds_bar();
  TS(13);

  if (wid >= 4) {
#pragma unroll
    for (int i = 0; i < 43; ++i) PIN(wdf[i]);
    f32x4 acc[4];
    gemm64_mma<43, 4, 15>(wdf, smem, acc TSPASS);
#pragma unroll
    for (int i = 0; i < 4; i += 2)
      *(u32x4*)(p.out + ((size_t)(rb * 64 + (i + (g & 1)) * 16 + r16) * DM + cb * 64 + wn * 16 + 8 * (g >> 1)) * 2) = pair16(cvt4(unpack4(hv[i]) + acc[i]), cvt4(unpack4(hv[i + 1]) + acc[i + 1]));
  } else {
    gemm64_load<43, 4, 3, 0>(p.ws, p.ws + WS_ACT + (size_t)(rb * 4 + wid) * 43 * 1024, cb % 5, smem, []() {});
  }
  TS_END();
}

extern "C" void kernel_launch(void* const* d_in, const int* in_sizes, int n_in, void* d_out, int out_size, void* d_ws, size_t ws_size, hipStream_t stream) {
  Params p;
  p.x = (const char*)d_in[0]; p.wqkv = (const char*)d_in[1]; p.wo = (const char*)d_in[2];
  p.wg = (const char*)d_in[4]; p.wu = (const char*)d_in[5]; p.wd = (const char*)d_in[6];
  p.out = (char*)d_out; p.ws = (char*)d_ws;
  constexpr int LDS = 160 * 1024;
  (void)hipFuncSetAttribute((const void*)mega, hipFuncAttributeMaxDynamicSharedMemorySize, LDS);
  mega<<<256, 512, LDS, stream>>>(p);
}
```

```cpp
#include <hip/hip_runtime.h>
#include <stdint.h>
#define ATTN_ASM_A \
  "s_mov_b32 %[keep], m0\n\t" \
  "ds_read_b128 v[168:171], %[ka]\n\t" \
  "ds_read_b128 v[172:175], %[ka] offset:1024\n\t" \
  "ds_read_b128 v[176:179], %[va]\n\t" \
  "ds_read_b128 v[184:187], %[va] offset:512\n\t" \
  "v_mov_b32 v164, 0\n\t" \
  "v_mov_b32 v165, 0\n\t" \
  "v_mov_b32 v166, 0\n\t" \
  "v_mov_b32 v167, 0\n\t" \
  "v_mov_b32 v188, 0\n\t" \
  "v_mov_b32 v189, 0\n\t" \
  "v_mov_b32 v190, 0\n\t" \
  "v_mov_b32 v191, 0\n\t" \
  "s_waitcnt lgkmcnt(3)\n\t" \
  "v_mfma_f32_32x32x16_bf16 v[128:143], v[168:171], %[q], %[nc]\n\t" \
  "s_nop 10\n\t" \
  "s_waitcnt lgkmcnt(2)\n\t" \
  "v_mfma_f32_32x32x16_bf16 v[144:159], v[172:175], %[q], %[nc]\n\t" \
  "ds_read_b128 v[168:171], %[ka] offset:2048\n\t" \
  "s_mov_b32 m0, %[ld]\n\t" \
  "v_exp_f32 v128, v128\n\t" \
  "v_exp_f32 v129, v129\n\t" \
  "global_load_lds_dwordx4 %[go], %[kg] sc1\n\t" \
  "v_exp_f32 v130, v130\n\t" \
  "v_exp_f32 v131, v131\n\t" \
  "v_cvt_pk_bf16_f32 v160, v128, v129\n\t" \
  "v_exp_f32 v132, v132\n\t" \
  "v_exp_f32 v133, v133\n\t" \
  "v_cvt_pk_bf16_f32 v161, v130, v131\n\t" \
  "v_mfma_f32_32x32x16_bf16 %[o], v[188:191], v[164:167], %[o]\n\t" \
  "ds_read_b128 v[180:183], %[va] offset:1024\n\t" \
  "ds_read_b128 v[188:191], %[va] offset:1536\n\t" \
  "s_add_u32 m0, %[ld], 0x10000\n\t" \
  "v_exp_f32 v134, v134\n\t" \
  "v_exp_f32 v135, v135\n\t" \
  "global_load_lds_dwordx4 %[go], %[vg] sc1\n\t" \
  "s_add_u32 %[ld], %[ld], 0x2000\n\t" \
  "v_cvt_pk_bf16_f32 v162, v132, v133\n\t" \
  "v_exp_f32 v136, v136\n\t" \
  "v_exp_f32 v137, v137\n\t" \
  "v_cvt_pk_bf16_f32 v163, v134, v135\n\t" \
  "v_add_u32 %[go], 0x2000, %[go]\n\t" \
  "v_and_b32 %[go], 0xffff, %[go]\n\t" \
  "v_exp_f32 v138, v138\n\t" \
  "v_exp_f32 v139, v139\n\t" \
  "s_waitcnt lgkmcnt(4)\n\t" \
  "v_mfma_f32_32x32x16_bf16 %[o], v[176:179], v[160:163], %[o]\n\t" \
  "v_cvt_pk_bf16_f32 v164, v136, v137\n\t" \
  "v_exp_f32 v140, v140\n\t" \
  "v_exp_f32 v141, v141\n\t" \
  "v_cvt_pk_bf16_f32 v165, v138, v139\n\t" \
  "v_exp_f32 v142, v142\n\t" \
  "v_exp_f32 v143, v143\n\t" \
  "v_cvt_pk_bf16_f32 v166, v140, v141\n\t" \
  "v_cvt_pk_bf16_f32 v167, v142, v143\n\t" \
  "s_waitcnt lgkmcnt(2)\n\t" \
  "v_mfma_f32_32x32x16_bf16 v[128:143], v[168:171], %[q], %[nc]\n\t" \
  "ds_read_b128 v[172:175], %[ka] offset:3072\n\t" \
  "s_mov_b32 m0, %[ld]\n\t" \
  "v_exp_f32 v144, v144\n\t" \
  "v_exp_f32 v145, v145\n\t" \
  "global_load_lds_dwordx4 %[go], %[kg] sc1\n\t" \
  "v_exp_f32 v146, v146\n\t" \
  "v_exp_f32 v147, v147\n\t" \
  "v_cvt_pk_bf16_f32 v160, v144, v145\n\t" \
  "v_exp_f32 v148, v148\n\t" \
  "v_exp_f32 v149, v149\n\t" \
  "v_cvt_pk_bf16_f32 v161, v146, v147\n\t" \
  "v_mfma_f32_32x32x16_bf16 %[o], v[184:187], v[164:167], %[o]\n\t" \
  "ds_read_b128 v[176:179], %[va] offset:2048\n\t" \
  "ds_read_b128 v[184:187], %[va] offset:2560\n\t" \
  "s_add_u32 m0, %[ld], 0x10000\n\t" \
  "v_exp_f32 v150, v150\n\t" \
  "v_exp_f32 v151, v151\n\t" \
  "global_load_lds_dwordx4 %[go], %[vg] sc1\n\t" \
  "s_add_u32 %[ld], %[ld], 0x2000\n\t" \
  "v_cvt_pk_bf16_f32 v162, v148, v149\n\t" \
  "v_exp_f32 v152, v152\n\t" \
  "v_exp_f32 v153, v153\n\t" \
  "v_cvt_pk_bf16_f32 v163, v150, v151\n\t" \
  "v_add_u32 %[go], 0x2000, %[go]\n\t" \
  "v_and_b32 %[go], 0xffff, %[go]\n\t" \
  "v_exp_f32 v154, v154\n\t" \
  "v_exp_f32 v155, v155\n\t" \
  "s_waitcnt lgkmcnt(4)\n\t" \
  "v_mfma_f32_32x32x16_bf16 %[o], v[180:183], v[160:163], %[o]\n\t" \
  "v_cvt_pk_bf16_f32 v164, v152, v153\n\t" \
  "v_exp_f32 v156, v156\n\t" \
  "v_exp_f32 v157, v157\n\t" \
  "v_cvt_pk_bf16_f32 v165, v154, v155\n\t" \
  "v_exp_f32 v158, v158\n\t" \
  "v_exp_f32 v159, v159\n\t" \
  "v_cvt_pk_bf16_f32 v166, v156, v157\n\t" \
  "v_cvt_pk_bf16_f32 v167, v158, v159\n\t" \
  "s_waitcnt lgkmcnt(2)\n\t" \
  "v_mfma_f32_32x32x16_bf16 v[144:159], v[172:175], %[q], %[nc]\n\t" \
  "ds_read_b128 v[168:171], %[ka] offset:4096\n\t" \
  "s_mov_b32 m0, %[ld]\n\t" \
  "v_exp_f32 v128, v128\n\t" \
  "v_exp_f32 v129, v129\n\t" \
  "global_load_lds_dwordx4 %[go], %[kg] sc1\n\t" \
  "v_exp_f32 v130, v130\n\t" \
  "v_exp_f32 v131, v131\n\t" \
  "v_cvt_pk_bf16_f32 v160, v128, v129\n\t" \
  "v_exp_f32 v132, v132\n\t" \
  "v_exp_f32 v133, v133\n\t" \
  "v_cvt_pk_bf16_f32 v161, v130, v131\n\t" \
  "v_mfma_f32_32x32x16_bf16 %[o], v[188:191], v[164:167], %[o]\n\t" \
  "ds_read_b128 v[180:183], %[va] offset:3072\n\t" \
  "ds_read_b128 v[188:191], %[va] offset:3584\n\t" \
  "s_add_u32 m0, %[ld], 0x10000\n\t" \
  "v_exp_f32 v134, v134\n\t" \
  "v_exp_f32 v135, v135\n\t" \
  "global_load_lds_dwordx4 %[go], %[vg] sc1\n\t" \
  "s_add_u32 %[ld], %[ld], 0x2000\n\t" \
  "v_cvt_pk_bf16_f32 v162, v132, v133\n\t" \
  "v_exp_f32 v136, v136\n\t" \
  "v_exp_f32 v137, v137\n\t" \
  "v_cvt_pk_bf16_f32 v163, v134, v135\n\t" \
  "v_add_u32 %[go], 0x2000, %[go]\n\t" \
  "v_and_b32 %[go], 0xffff, %[go]\n\t" \
  "v_exp_f32 v138, v138\n\t" \
  "v_exp_f32 v139, v139\n\t" \
  "s_waitcnt lgkmcnt(4)\n\t" \
  "v_mfma_f32_32x32x16_bf16 %[o], v[176:179], v[160:163], %[o]\n\t" \
  "v_cvt_pk_bf16_f32 v164, v136, v137\n\t" \
  "v_exp_f32 v140, v140\n\t" \
  "v_exp_f32 v141, v141\n\t" \
  "v_cvt_pk_bf16_f32 v165, v138, v139\n\t" \
  "v_exp_f32 v142, v142\n\t" \
  "v_exp_f32 v143, v143\n\t" \
  "v_cvt_pk_bf16_f32 v166, v140, v141\n\t" \
  "v_cvt_pk_bf16_f32 v167, v142, v143\n\t" \
  "s_waitcnt lgkmcnt(2)\n\t" \
  "v_mfma_f32_32x32x16_bf16 v[128:143], v[168:171], %[q], %[nc]\n\t" \
  "ds_read_b128 v[172:175], %[ka] offset:5120\n\t" \
  "s_mov_b32 m0, %[ld]\n\t" \
  "v_exp_f32 v144, v144\n\t" \
  "v_exp_f32 v145, v145\n\t" \
  "global_load_lds_dwordx4 %[go], %[kg] sc1\n\t" \
  "v_exp_f32 v146, v146\n\t" \
  "v_exp_f32 v147, v147\n\t" \
  "v_cvt_pk_bf16_f32 v160, v144, v145\n\t" \
  "v_exp_f32 v148, v148\n\t" \
  "v_exp_f32 v149, v149\n\t" \
  "v_cvt_pk_bf16_f32 v161, v146, v147\n\t" \
  "v_mfma_f32_32x32x16_bf16 %[o], v[184:187], v[164:167], %[o]\n\t" \
  "ds_read_b128 v[176:179], %[va] offset:4096\n\t" \
  "ds_read_b128 v[184:187], %[va] offset:4608\n\t" \
  "s_add_u32 m0, %[ld], 0x10000\n\t" \
  "v_exp_f32 v150, v150\n\t" \
  "v_exp_f32 v151, v151\n\t" \
  "global_load_lds_dwordx4 %[go], %[vg] sc1\n\t" \
  "s_add_u32 %[ld], %[ld], 0x2000\n\t" \
  "v_cvt_pk_bf16_f32 v162, v148, v149\n\t" \
  "v_exp_f32 v152, v152\n\t" \
  "v_exp_f32 v153, v153\n\t" \
  "v_cvt_pk_bf16_f32 v163, v150, v151\n\t" \
  "v_add_u32 %[go], 0x2000, %[go]\n\t" \
  "v_and_b32 %[go], 0xffff, %[go]\n\t" \
  "v_exp_f32 v154, v154\n\t" \
  "v_exp_f32 v155, v155\n\t" \
  "s_waitcnt lgkmcnt(4)\n\t" \
  "v_mfma_f32_32x32x16_bf16 %[o], v[180:183], v[160:163], %[o]\n\t" \
  "v_cvt_pk_bf16_f32 v164, v152, v153\n\t" \
  "v_exp_f32 v156, v156\n\t" \
  "v_exp_f32 v157, v157\n\t" \
  "v_cvt_pk_bf16_f32 v165, v154, v155\n\t" \
  "v_exp_f32 v158, v158\n\t" \
  "v_exp_f32 v159, v159\n\t" \
  "v_cvt_pk_bf16_f32 v166, v156, v157\n\t" \
  "v_cvt_pk_bf16_f32 v167, v158, v159\n\t" \
  "v_add_u32 %[ka], 0x1000, %[ka]\n\t" \
  "v_add_u32 %[va], %[vs], %[va]\n\t" \
  "s_waitcnt lgkmcnt(2)\n\t" \
  "v_mfma_f32_32x32x16_bf16 v[144:159], v[172:175], %[q], %[nc]\n\t" \
  "ds_read_b128 v[168:171], %[ka] offset:2048\n\t" \
  "s_mov_b32 m0, %[ld]\n\t" \
  "v_exp_f32 v128, v128\n\t" \
  "v_exp_f32 v129, v129\n\t" \
  "global_load_lds_dwordx4 %[go], %[kg] sc1\n\t" \
  "v_exp_f32 v130, v130\n\t" \
  "v_exp_f32 v131, v131\n\t" \
  "v_cvt_pk_bf16_f32 v160, v128, v129\n\t" \
  "v_exp_f32 v132, v132\n\t" \
  "v_exp_f32 v133, v133\n\t" \
  "v_cvt_pk_bf16_f32 v161, v130, v131\n\t" \
  "v_mfma_f32_32x32x16_bf16 %[o], v[188:191], v[164:167], %[o]\n\t" \
  "ds_read_b128 v[180:183], %[va] offset:1024\n\t" \
  "ds_read_b128 v[188:191], %[va] offset:1536\n\t" \
  "s_add_u32 m0, %[ld], 0x10000\n\t" \
  "v_exp_f32 v134, v134\n\t" \
  "v_exp_f32 v135, v135\n\t" \
  "global_load_lds_dwordx4 %[go], %[vg] sc1\n\t" \
  "s_add_u32 %[ld], %[ld], 0x2000\n\t" \
  "v_cvt_pk_bf16_f32 v162, v132, v133\n\t" \
  "v_exp_f32 v136, v136\n\t" \
  "v_exp_f32 v137, v137\n\t" \
  "v_cvt_pk_bf16_f32 v163, v134, v135\n\t" \
  "v_add_u32 %[go], 0x2000, %[go]\n\t" \
  "v_and_b32 %[go], 0xffff, %[go]\n\t" \
  "v_exp_f32 v138, v138\n\t" \
  "v_exp_f32 v139, v139\n\t" \
  "s_waitcnt lgkmcnt(4)\n\t" \
  "v_mfma_f32_32x32x16_bf16 %[o], v[176:179], v[160:163], %[o]\n\t" \
  "v_cvt_pk_bf16_f32 v164, v136, v137\n\t" \
  "v_exp_f32 v140, v140\n\t" \
  "v_exp_f32 v141, v141\n\t" \
  "v_cvt_pk_bf16_f32 v165, v138, v139\n\t" \
  "v_exp_f32 v142, v142\n\t" \
  "v_exp_f32 v143, v143\n\t" \
  "v_cvt_pk_bf16_f32 v166, v140, v141\n\t" \
  "v_cvt_pk_bf16_f32 v167, v142, v143\n\t" \
  "s_waitcnt lgkmcnt(2)\n\t" \
  "v_mfma_f32_32x32x16_bf16 v[128:143], v[168:171], %[q], %[nc]\n\t" \
  "ds_read_b128 v[172:175], %[ka] offset:3072\n\t" \
  "s_mov_b32 m0, %[ld]\n\t" \
  "v_exp_f32 v144, v144\n\t" \
  "v_exp_f32 v145, v145\n\t" \
  "global_load_lds_dwordx4 %[go], %[kg] sc1\n\t" \
  "v_exp_f32 v146, v146\n\t" \
  "v_exp_f32 v147, v147\n\t" \
  "v_cvt_pk_bf16_f32 v160, v144, v145\n\t" \
  "v_exp_f32 v148, v148\n\t" \
  "v_exp_f32 v149, v149\n\t" \
  "v_cvt_pk_bf16_f32 v161, v146, v147\n\t" \
  "v_mfma_f32_32x32x16_bf16 %[o], v[184:187], v[164:167], %[o]\n\t" \
  "ds_read_b128 v[176:179], %[va] offset:2048\n\t" \
  "ds_read_b128 v[184:187], %[va] offset:2560\n\t" \
  "s_add_u32 m0, %[ld], 0x10000\n\t" \
  "v_exp_f32 v150, v150\n\t" \
  "v_exp_f32 v151, v151\n\t" \
  "global_load_lds_dwordx4 %[go], %[vg] sc1\n\t" \
  "s_add_u32 %[ld], %[ld], 0x2000\n\t" \
  "v_cvt_pk_bf16_f32 v162, v148, v149\n\t" \
  "v_exp_f32 v152, v152\n\t" \
  "v_exp_f32 v153, v153\n\t" \
  "v_cvt_pk_bf16_f32 v163, v150, v151\n\t" \
  "v_add_u32 %[go], 0x2000, %[go]\n\t" \
  "v_and_b32 %[go], 0xffff, %[go]\n\t" \
  "v_exp_f32 v154, v154\n\t" \
  "v_exp_f32 v155, v155\n\t" \
  "s_waitcnt lgkmcnt(4)\n\t" \
  "v_mfma_f32_32x32x16_bf16 %[o], v[180:183], v[160:163], %[o]\n\t" \
  "v_cvt_pk_bf16_f32 v164, v152, v153\n\t" \
  "v_exp_f32 v156, v156\n\t" \
  "v_exp_f32 v157, v157\n\t" \
  "v_cvt_pk_bf16_f32 v165, v154, v155\n\t" \
  "v_exp_f32 v158, v158\n\t" \
  "v_exp_f32 v159, v159\n\t" \
  "v_cvt_pk_bf16_f32 v166, v156, v157\n\t" \
  "v_cvt_pk_bf16_f32 v167, v158, v159\n\t" \
  "s_waitcnt lgkmcnt(2)\n\t" \
  "v_mfma_f32_32x32x16_bf16 v[144:159], v[172:175], %[q], %[nc]\n\t" \
  "ds_read_b128 v[168:171], %[ka] offset:4096\n\t" \
  "v_exp_f32 v128, v128\n\t" \
  "v_exp_f32 v129, v129\n\t" \
  "v_exp_f32 v130, v130\n\t" \
  "v_exp_f32 v131, v131\n\t" \
  "v_cvt_pk_bf16_f32 v160, v128, v129\n\t" \
  "v_exp_f32 v132, v132\n\t" \
  "v_exp_f32 v133, v133\n\t" \
  "v_cvt_pk_bf16_f32 v161, v130, v131\n\t" \
  "v_mfma_f32_32x32x16_bf16 %[o], v[188:191], v[164:167], %[o]\n\t" \
  "ds_read_b128 v[180:183], %[va] offset:3072\n\t" \
  "ds_read_b128 v[188:191], %[va] offset:3584\n\t" \
  "v_exp_f32 v134, v134\n\t" \
  "v_exp_f32 v135, v135\n\t" \
  "v_cvt_pk_bf16_f32 v162, v132, v133\n\t" \
  "v_exp_f32 v136, v136\n\t" \
  "v_exp_f32 v137, v137\n\t" \
  "v_cvt_pk_bf16_f32 v163, v134, v135\n\t" \
  "v_exp_f32 v138, v138\n\t" \
  "v_exp_f32 v139, v139\n\t" \
  "s_waitcnt lgkmcnt(4)\n\t" \
  "v_mfma_f32_32x32x16_bf16 %[o], v[176:179], v[160:163], %[o]\n\t" \
  "v_cvt_pk_bf16_f32 v164, v136, v137\n\t" \
  "v_exp_f32 v140, v140\n\t" \
  "v_exp_f32 v141, v141\n\t" \
  "v_cvt_pk_bf16_f32 v165, v138, v139\n\t" \
  "v_exp_f32 v142, v142\n\t" \
  "v_exp_f32 v143, v143\n\t" \
  "v_cvt_pk_bf16_f32 v166, v140, v141\n\t" \
  "v_cvt_pk_bf16_f32 v167, v142, v143\n\t" \
  "s_waitcnt lgkmcnt(2)\n\t" \
  "v_mfma_f32_32x32x16_bf16 v[128:143], v[168:171], %[q], %[nc]\n\t" \
  "ds_read_b128 v[172:175], %[ka] offset:5120\n\t" \
  "v_exp_f32 v144, v144\n\t" \
  "v_exp_f32 v145, v145\n\t" \
  "v_exp_f32 v146, v146\n\t" \
  "v_exp_f32 v147, v147\n\t" \
  "v_cvt_pk_bf16_f32 v160, v144, v145\n\t" \
  "v_exp_f32 v148, v148\n\t" \
  "v_exp_f32 v149, v149\n\t" \
  "v_cvt_pk_bf16_f32 v161, v146, v147\n\t" \
  "v_mfma_f32_32x32x16_bf16 %[o], v[184:187], v[164:167], %[o]\n\t" \
  "ds_read_b128 v[176:179], %[va] offset:4096\n\t" \
  "ds_read_b128 v[184:187], %[va] offset:4608\n\t" \
  "v_exp_f32 v150, v150\n\t" \
  "v_exp_f32 v151, v151\n\t" \
  "v_cvt_pk_bf16_f32 v162, v148, v149\n\t" \
  "v_exp_f32 v152, v152\n\t" \
  "v_exp_f32 v153, v153\n\t" \
  "v_cvt_pk_bf16_f32 v163, v150, v151\n\t" \
  "v_exp_f32 v154, v154\n\t" \
  "v_exp_f32 v155, v155\n\t" \
  "s_waitcnt lgkmcnt(4)\n\t" \
  "v_mfma_f32_32x32x16_bf16 %[o], v[180:183], v[160:163], %[o]\n\t" \
  "v_cvt_pk_bf16_f32 v164, v152, v153\n\t" \
  "v_exp_f32 v156, v156\n\t" \
  "v_exp_f32 v157, v157\n\t" \
  "v_cvt_pk_bf16_f32 v165, v154, v155\n\t" \
  "v_exp_f32 v158, v158\n\t" \
  "v_exp_f32 v159, v159\n\t" \
  "v_cvt_pk_bf16_f32 v166, v156, v157\n\t" \
  "v_cvt_pk_bf16_f32 v167, v158, v159\n\t" \
  "v_add_u32 %[ka], 0x1000, %[ka]\n\t" \
  "v_add_u32 %[va], %[vs], %[va]\n\t" \
  "s_mov_b32 m0, %[keep]\n\t" \
  "s_waitcnt vmcnt(0) lgkmcnt(0)\n\t" \
  ""
#define ATTN_ASM_B \
  "s_mov_b32 %[cnt], %[c0]\n\t" \
  "s_cmp_lg_u32 %[c0], 2\n\t" \
  "s_cbranch_scc1 L_attn_loop_%=\n\t" \
  "s_cmp_eq_u32 %[sw], 99\n\t" \
  "s_cbranch_scc1 L_attn_y_%=\n\t" \
  "s_setprio 2\n\t" \
  "s_branch L_attn_loop_%=\n\t" \
  "L_attn_y_%=:\n\t" \
  "s_setprio 1\n\t" \
  "L_attn_loop_%=:\n\t" \
  "s_cmp_lg_u32 %[cnt], %[sw]\n\t" \
  "s_cbranch_scc1 L_attn_nosw_%=\n\t" \
  "s_setprio 0\n\t" \
  "L_attn_nosw_%=:\n\t" \
  "s_waitcnt lgkmcnt(2)\n\t" \
  "v_mfma_f32_32x32x16_bf16 v[144:159], v[172:175], %[q], %[nc]\n\t" \
  "ds_read_b128 v[168:171], %[ka] offset:2048\n\t" \
  "v_exp_f32 v128, v128\n\t" \
  "v_exp_f32 v129, v129\n\t" \
  "v_exp_f32 v130, v130\n\t" \
  "v_exp_f32 v131, v131\n\t" \
  "v_cvt_pk_bf16_f32 v160, v128, v129\n\t" \
  "v_exp_f32 v132, v132\n\t" \
  "v_exp_f32 v133, v133\n\t" \
  "v_cvt_pk_bf16_f32 v161, v130, v131\n\t" \
  "v_mfma_f32_32x32x16_bf16 %[o], v[188:191], v[164:167], %[o]\n\t" \
  "ds_read_b128 v[180:183], %[va] offset:1024\n\t" \
  "ds_read_b128 v[188:191], %[va] offset:1536\n\t" \
  "v_exp_f32 v134, v134\n\t" \
  "v_exp_f32 v135, v135\n\t" \
  "v_cvt_pk_bf16_f32 v162, v132, v133\n\t" \
  "v_exp_f32 v136, v136\n\t" \
  "v_exp_f32 v137, v137\n\t" \
  "v_cvt_pk_bf16_f32 v163, v134, v135\n\t" \
  "v_exp_f32 v138, v138\n\t" \
  "v_exp_f32 v139, v139\n\t" \
  "s_waitcnt lgkmcnt(4)\n\t" \
  "v_mfma_f32_32x32x16_bf16 %[o], v[176:179], v[160:163], %[o]\n\t" \
  "v_cvt_pk_bf16_f32 v164, v136, v137\n\t" \
  "v_exp_f32 v140, v140\n\t" \
  "v_exp_f32 v141, v141\n\t" \
  "v_cvt_pk_bf16_f32 v165, v138, v139\n\t" \
  "v_exp_f32 v142, v142\n\t" \
  "v_exp_f32 v143, v143\n\t" \
  "v_cvt_pk_bf16_f32 v166, v140, v141\n\t" \
  "v_cvt_pk_bf16_f32 v167, v142, v143\n\t" \
  "s_waitcnt lgkmcnt(2)\n\t" \
  "v_mfma_f32_32x32x16_bf16 v[128:143], v[168:171], %[q], %[nc]\n\t" \
  "ds_read_b128 v[172:175], %[ka] offset:3072\n\t" \
  "v_exp_f32 v144, v144\n\t" \
  "v_exp_f32 v145, v145\n\t" \
  "v_exp_f32 v146, v146\n\t" \
  "v_exp_f32 v147, v147\n\t" \
  "v_cvt_pk_bf16_f32 v160, v144, v145\n\t" \
  "v_exp_f32 v148, v148\n\t" \
  "v_exp_f32 v149, v149\n\t" \
  "v_cvt_pk_bf16_f32 v161, v146, v147\n\t" \
  "v_mfma_f32_32x32x16_bf16 %[o], v[184:187], v[164:167], %[o]\n\t" \
  "ds_read_b128 v[176:179], %[va] offset:2048\n\t" \
  "ds_read_b128 v[184:187], %[va] offset:2560\n\t" \
  "v_exp_f32 v150, v150\n\t" \
  "v_exp_f32 v151, v151\n\t" \
  "v_cvt_pk_bf16_f32 v162, v148, v149\n\t" \
  "v_exp_f32 v152, v152\n\t" \
  "v_exp_f32 v153, v153\n\t" \
  "v_cvt_pk_bf16_f32 v163, v150, v151\n\t" \
  "v_exp_f32 v154, v154\n\t" \
  "v_exp_f32 v155, v155\n\t" \
  "s_waitcnt lgkmcnt(4)\n\t" \
  "v_mfma_f32_32x32x16_bf16 %[o], v[180:183], v[160:163], %[o]\n\t" \
  "v_cvt_pk_bf16_f32 v164, v152, v153\n\t" \
  "v_exp_f32 v156, v156\n\t" \
  "v_exp_f32 v157, v157\n\t" \
  "v_cvt_pk_bf16_f32 v165, v154, v155\n\t" \
  "v_exp_f32 v158, v158\n\t" \
  "v_exp_f32 v159, v159\n\t" \
  "v_cvt_pk_bf16_f32 v166, v156, v157\n\t" \
  "v_cvt_pk_bf16_f32 v167, v158, v159\n\t" \
  "s_waitcnt lgkmcnt(2)\n\t" \
  "v_mfma_f32_32x32x16_bf16 v[144:159], v[172:175], %[q], %[nc]\n\t" \
  "ds_read_b128 v[168:171], %[ka] offset:4096\n\t" \
  "v_exp_f32 v128, v128\n\t" \
  "v_exp_f32 v129, v129\n\t" \
  "v_exp_f32 v130, v130\n\t" \
  "v_exp_f32 v131, v131\n\t" \
  "v_cvt_pk_bf16_f32 v160, v128, v129\n\t" \
  "v_exp_f32 v132, v132\n\t" \
  "v_exp_f32 v133, v133\n\t" \
  "v_cvt_pk_bf16_f32 v161, v130, v131\n\t" \
  "v_mfma_f32_32x32x16_bf16 %[o], v[188:191], v[164:167], %[o]\n\t" \
  "ds_read_b128 v[180:183], %[va] offset:3072\n\t" \
  "ds_read_b128 v[188:191], %[va] offset:3584\n\t" \
  "v_exp_f32 v134, v134\n\t" \
  "v_exp_f32 v135, v135\n\t" \
  "v_cvt_pk_bf16_f32 v162, v132, v133\n\t" \
  "v_exp_f32 v136, v136\n\t" \
  "v_exp_f32 v137, v137\n\t" \
  "v_cvt_pk_bf16_f32 v163, v134, v135\n\t" \
  "v_exp_f32 v138, v138\n\t" \
  "v_exp_f32 v139, v139\n\t" \
  "s_waitcnt lgkmcnt(4)\n\t" \
  "v_mfma_f32_32x32x16_bf16 %[o], v[176:179], v[160:163], %[o]\n\t" \
  "v_cvt_pk_bf16_f32 v164, v136, v137\n\t" \
  "v_exp_f32 v140, v140\n\t" \
  "v_exp_f32 v141, v141\n\t" \
  "v_cvt_pk_bf16_f32 v165, v138, v139\n\t" \
  "v_exp_f32 v142, v142\n\t" \
  "v_exp_f32 v143, v143\n\t" \
  "v_cvt_pk_bf16_f32 v166, v140, v141\n\t" \
  "v_cvt_pk_bf16_f32 v167, v142, v143\n\t" \
  "s_waitcnt lgkmcnt(2)\n\t" \
  "v_mfma_f32_32x32x16_bf16 v[128:143], v[168:171], %[q], %[nc]\n\t" \
  "ds_read_b128 v[172:175], %[ka] offset:5120\n\t" \
  "v_exp_f32 v144, v144\n\t" \
  "v_exp_f32 v145, v145\n\t" \
  "v_exp_f32 v146, v146\n\t" \
  "v_exp_f32 v147, v147\n\t" \
  "v_cvt_pk_bf16_f32 v160, v144, v145\n\t" \
  "v_exp_f32 v148, v148\n\t" \
  "v_exp_f32 v149, v149\n\t" \
  "v_cvt_pk_bf16_f32 v161, v146, v147\n\t" \
  "v_mfma_f32_32x32x16_bf16 %[o], v[184:187], v[164:167], %[o]\n\t" \
  "ds_read_b128 v[176:179], %[va] offset:4096\n\t" \
  "ds_read_b128 v[184:187], %[va] offset:4608\n\t" \
  "v_exp_f32 v150, v150\n\t" \
  "v_exp_f32 v151, v151\n\t" \
  "v_cvt_pk_bf16_f32 v162, v148, v149\n\t" \
  "v_exp_f32 v152, v152\n\t" \
  "v_exp_f32 v153, v153\n\t" \
  "v_cvt_pk_bf16_f32 v163, v150, v151\n\t" \
  "v_exp_f32 v154, v154\n\t" \
  "v_exp_f32 v155, v155\n\t" \
  "s_waitcnt lgkmcnt(4)\n\t" \
  "v_mfma_f32_32x32x16_bf16 %[o], v[180:183], v[160:163], %[o]\n\t" \
  "v_cvt_pk_bf16_f32 v164, v152, v153\n\t" \
  "v_exp_f32 v156, v156\n\t" \
  "v_exp_f32 v157, v157\n\t" \
  "v_cvt_pk_bf16_f32 v165, v154, v155\n\t" \
  "v_exp_f32 v158, v158\n\t" \
  "v_exp_f32 v159, v159\n\t" \
  "v_cvt_pk_bf16_f32 v166, v156, v157\n\t" \
  "v_cvt_pk_bf16_f32 v167, v158, v159\n\t" \
  "v_add_u32 %[ka], 0x1000, %[ka]\n\t" \
  "v_add_u32 %[va], %[vs], %[va]\n\t" \
  "s_add_u32 %[cnt], %[cnt], 1\n\t" \
  "s_cmp_lt_u32 %[cnt], %[c1]\n\t" \
  "s_cbranch_scc1 L_attn_loop_%=\n\t" \
  "s_waitcnt lgkmcnt(0)\n\t" \
  "s_cmp_lg_u32 %[c1], 16\n\t" \
  "s_cbranch_scc1 L_attn_end_%=\n\t" \
  "v_mfma_f32_32x32x16_bf16 %[o], v[188:191], v[164:167], %[o]\n\t" \
  "s_setprio 0\n\t" \
  "L_attn_end_%=:\n\t" \
  "s_nop 15\n\t" \
  ""

typedef short bf16x8 __attribute__((ext_vector_type(8)));
typedef short s16x4 __attribute__((ext_vector_type(4)));
typedef float f32x4 __attribute__((ext_vector_type(4)));
typedef float f32x2 __attribute__((ext_vector_type(2)));
typedef float f32x16 __attribute__((ext_vector_type(16)));
typedef float f32x8 __attribute__((ext_vector_type(8)));
typedef unsigned u32x4 __attribute__((ext_vector_type(4)));
typedef unsigned u32x2 __attribute__((ext_vector_type(2)));
typedef __bf16 bf16x2_t __attribute__((ext_vector_type(2)));
#define LAS __attribute__((address_space(3)))
#define GAS __attribute__((address_space(1)))
typedef const LAS char* lds_cptr;
typedef LAS char* lds_ptr;
typedef GAS unsigned gu32;
typedef GAS unsigned long long gu64;
typedef __amdgpu_buffer_rsrc_t rsrc_t;

constexpr int DM = 512, FF = 1376;
constexpr float EPS = 1e-5f;
constexpr float QSCALE = 0.25f * 1.4426950408889634f;

constexpr unsigned WS_Q = 0, WS_K = 2u << 20, WS_V = 4u << 20, WS_O = 6u << 20, WS_H = 8u << 20, WS_ACT = 10u << 20;
constexpr unsigned WS_KMAX = 16u << 20, WS_HST = (16u << 20) + (64u << 10);
constexpr unsigned WS_WOF = 17u << 20, WS_WDF = WS_WOF + 512 * 1024, WS_WGF = WS_WDF + 32 * 43 * 1024;
constexpr unsigned WS_FLAG = 22u << 20;
constexpr unsigned FL_EPOCH = 0, FL_TMO = 256, FL_1 = 1024, FL_P = 4096;
constexpr unsigned POISON = 0xFFFFFFFFu;
constexpr unsigned WS_USED = 24u << 20;

struct Params {
  const char* x; const char* wqkv; const char* wo; const char* wg; const char* wu; const char* wd;
  char* out; char* ws;
};

#define MFMA16(a, b, c) __builtin_amdgcn_mfma_f32_16x16x32_bf16(a, b, c, 0, 0, 0)
#define MFMA32(a, b, c) __builtin_amdgcn_mfma_f32_32x32x16_bf16(a, b, c, 0, 0, 0)
#define PIN(x) asm volatile("" : "+v"(x))
#define SB() __builtin_amdgcn_sched_barrier(0)
#define RLX_AGENT __ATOMIC_RELAXED, __HIP_MEMORY_SCOPE_AGENT

template <int N> __device__ __forceinline__ void wait_bar() {
  asm volatile("s_waitcnt vmcnt(%0) lgkmcnt(0)\n\ts_barrier" ::"n"(N) : "memory");
}
template <int N> __device__ __forceinline__ void wait_vm() { asm volatile("s_waitcnt vmcnt(%0)" ::"n"(N) : "memory"); }
__device__ __forceinline__ void lds_bar() { asm volatile("s_waitcnt lgkmcnt(0)\n\ts_barrier" ::: "memory"); }
__device__ __forceinline__ void raw_bar() { asm volatile("s_barrier" ::: "memory"); }

template <int N, bool SC1> __device__ __forceinline__ void glds(const void* sbase, unsigned voff, unsigned lds_addr) {
#define GL_(O, S) "\n\tglobal_load_lds_dwordx4 %0, %1 offset:" #O S
  if (SC1) {
    if (N == 1) asm volatile("s_mov_b32 m0, %2\n\ts_nop 0" GL_(0, " sc1") :: "v"(voff), "s"(sbase), "s"(lds_addr) : "memory");
    if (N == 2) asm volatile("s_mov_b32 m0, %2\n\ts_nop 0" GL_(0, " sc1") GL_(1024, " sc1") :: "v"(voff), "s"(sbase), "s"(lds_addr) : "memory");
    if (N == 3) asm volatile("s_mov_b32 m0, %2\n\ts_nop 0" GL_(0, " sc1") GL_(1024, " sc1") GL_(2048, " sc1") :: "v"(voff), "s"(sbase), "s"(lds_addr) : "memory");
    if (N == 4) asm volatile("s_mov_b32 m0, %2\n\ts_nop 0" GL_(0, " sc1") GL_(1024, " sc1") GL_(2048, " sc1") GL_(3072, " sc1") :: "v"(voff), "s"(sbase), "s"(lds_addr) : "memory");
  } else {
    if (N == 1) asm volatile("s_mov_b32 m0, %2\n\ts_nop 0" GL_(0, "") :: "v"(voff), "s"(sbase), "s"(lds_addr) : "memory");
    if (N == 2) asm volatile("s_mov_b32 m0, %2\n\ts_nop 0" GL_(0, "") GL_(1024, "") :: "v"(voff), "s"(sbase), "s"(lds_addr) : "memory");
    if (N == 3) asm volatile("s_mov_b32 m0, %2\n\ts_nop 0" GL_(0, "") GL_(1024, "") GL_(2048, "") :: "v"(voff), "s"(sbase), "s"(lds_addr) : "memory");
  }
#undef GL_
}
template <bool SC1> __device__ __forceinline__ void glds16(const void* sbase, unsigned voff, unsigned lds_addr) { glds<1, SC1>(sbase, voff, lds_addr); }
template <bool SC1> __device__ __forceinline__ void glds4(const void* sbase, unsigned voff, unsigned lds_addr) {
  if (SC1) asm volatile("s_mov_b32 m0, %2\n\ts_nop 0\n\tglobal_load_lds_dword %0, %1 sc1" :: "v"(voff), "s"(sbase), "s"(lds_addr) : "memory");
  else     asm volatile("s_mov_b32 m0, %2\n\ts_nop 0\n\tglobal_load_lds_dword %0, %1" :: "v"(voff), "s"(sbase), "s"(lds_addr) : "memory");
}
__device__ __forceinline__ bf16x8 ld128(lds_cptr p) { return *(const LAS bf16x8*)p; }
typedef short v4i16_t __attribute__((ext_vector_type(4)));
__device__ __forceinline__ s16x4 ldtr(lds_cptr p) {
  return __builtin_bit_cast(s16x4, __builtin_amdgcn_ds_read_tr16_b64_v4i16((LAS v4i16_t*)p));
}
__device__ __forceinline__ bf16x8 ldtr2(lds_cptr p, int second) {
  const s16x4 a = ldtr(p), b = ldtr(p + second);
  return (bf16x8){a[0], a[1], a[2], a[3], b[0], b[1], b[2], b[3]};
}
__device__ __forceinline__ unsigned cvtpk(float lo, float hi) {
  f32x2 v = {lo, hi}; bf16x2_t b = __builtin_convertvector(v, bf16x2_t); return __builtin_bit_cast(unsigned, b);
}
__device__ __forceinline__ u32x2 cvt4(f32x4 v) { return (u32x2){cvtpk(v[0], v[1]), cvtpk(v[2], v[3])}; }
__device__ __forceinline__ float bflo(unsigned w) { return __uint_as_float(w << 16); }
__device__ __forceinline__ float bfhi(unsigned w) { return __uint_as_float(w & 0xffff0000u); }
__device__ __forceinline__ f32x4 unpack4(u32x2 w) { return (f32x4){bflo(w[0]), bfhi(w[0]), bflo(w[1]), bfhi(w[1])}; }
__device__ __forceinline__ float bfr(float v) { return bflo(cvtpk(v, 0.f)); }
__device__ __forceinline__ void ln_consts(float sum, float sumsq, float& mean, float& rstd) {
  const float m = sum * (1.f / DM), var = fmaxf(sumsq * (1.f / DM) - m * m, 0.f);
  mean = bfr(m); rstd = __builtin_amdgcn_rcpf(bfr(__builtin_amdgcn_sqrtf(bfr(bfr(var) + EPS))));
}
__device__ __forceinline__ int rfl(int v) { return __builtin_amdgcn_readfirstlane(v); }
__device__ __forceinline__ bf16x8 ones8() { return (bf16x8){0x3F80, 0x3F80, 0x3F80, 0x3F80, 0x3F80, 0x3F80, 0x3F80, 0x3F80}; }
#define Z4 ((f32x4){0.f, 0.f, 0.f, 0.f})
__device__ __forceinline__ void st16(rsrc_t ws, unsigned off, u32x4 v) { __builtin_amdgcn_raw_buffer_store_b128(v, ws, off, 0, 16); }
__device__ __forceinline__ bf16x8 ldw16(rsrc_t ws, unsigned off) { return __builtin_bit_cast(bf16x8, __builtin_amdgcn_raw_buffer_load_b128(ws, off, 0, 16)); }
__device__ __forceinline__ u32x4 pair16(u32x2 X, u32x2 Y) {
  auto a = __builtin_amdgcn_permlane16_swap(X[0], Y[0], false, false);
  auto b = __builtin_amdgcn_permlane16_swap(X[1], Y[1], false, false);
  return (u32x4){a[0], b[0], a[1], b[1]};
}
__device__ __forceinline__ u32x4 pair32(u32x2 X, u32x2 Y) {
  auto a = __builtin_amdgcn_permlane32_swap(X[0], Y[0], false, false);
  auto b = __builtin_amdgcn_permlane32_swap(X[1], Y[1], false, false);
  return (u32x4){a[0], b[0], a[1], b[1]};
}
__device__ __forceinline__ float sum_x16(float v) { auto r = __builtin_amdgcn_permlane16_swap(__float_as_uint(v), __float_as_uint(v), false, false); return __uint_as_float(r[0]) + __uint_as_float(r[1]); }
__device__ __forceinline__ float sum_x32(float v) { auto r = __builtin_amdgcn_permlane32_swap(__float_as_uint(v), __float_as_uint(v), false, false); return __uint_as_float(r[0]) + __uint_as_float(r[1]); }
__device__ __forceinline__ float max_x16(float v) { auto r = __builtin_amdgcn_permlane16_swap(__float_as_uint(v), __float_as_uint(v), false, false); return fmaxf(__uint_as_float(r[0]), __uint_as_float(r[1])); }
__device__ __forceinline__ float lo_x32(float v) { auto r = __builtin_amdgcn_permlane32_swap(__float_as_uint(v), __float_as_uint(v), false, false); return __uint_as_float(r[0]); }
template <int N> __device__ __forceinline__ float ror16(float v) { return __uint_as_float(__builtin_amdgcn_update_dpp(0u, __float_as_uint(v), 0x120 + N, 0xf, 0xf, false)); }
__device__ __forceinline__ float max_row16(float v) { v = fmaxf(v, ror16<8>(v)); v = fmaxf(v, ror16<4>(v)); v = fmaxf(v, ror16<2>(v)); return fmaxf(v, ror16<1>(v)); }
template <int ND, int NM> __device__ __forceinline__ void weave() {
#pragma unroll
  for (int i = 0; i < ND; ++i) { __builtin_amdgcn_sched_group_barrier(0x008, 1, 0); __builtin_amdgcn_sched_group_barrier(0x100, 1, 0); }
  if (NM > ND) __builtin_amdgcn_sched_group_barrier(0x008, NM - ND, 0);
}

__device__ __forceinline__ u32x4 nop4(u32x4 v) { return __builtin_elementwise_min(v, (u32x4){POISON - 1, POISON - 1, POISON - 1, POISON - 1}); }
template <int NP> __device__ __forceinline__ bool pieces_ok(lds_cptr base) {
  const int lane = threadIdx.x & 63;
  unsigned m = 0;
#pragma unroll
  for (int i = 0; i < NP; ++i) { const u32x4 v = *(const LAS u32x4*)(base + i * 1024 + lane * 16); const unsigned a = v[0] > v[1] ? v[0] : v[1], b = v[2] > v[3] ? v[2] : v[3]; m = m > a ? m : a; m = m > b ? m : b; }
  return !__any(m == POISON);
}
template <int NP> struct Chk {
  u32x4 v[NP];
  __device__ __forceinline__ void read(lds_cptr base) {
    const int lane = threadIdx.x & 63;
#pragma unroll
    for (int i = 0; i < NP; ++i) v[i] = *(const LAS u32x4*)(base + i * 1024 + lane * 16);
  }
  __device__ __forceinline__ bool ok() const {
    unsigned m = 0;
#pragma unroll
    for (int i = 0; i < NP; ++i) { const unsigned a = v[i][0] > v[i][1] ? v[i][0] : v[i][1], b = v[i][2] > v[i][3] ? v[i][2] : v[i][3]; m = m > a ? m : a; m = m > b ? m : b; }
    return !__any(m == POISON);
  }
};
struct Spin {
  unsigned n; unsigned long long t0;
  __device__ __forceinline__ Spin() : n(0), t0(__builtin_amdgcn_s_memrealtime()) {}
  __device__ __forceinline__ bool expired(char* ws) {
    if ((++n & 63u) == 0u && __builtin_amdgcn_s_memrealtime() - t0 > 20000000ull) {
      if ((threadIdx.x & 63) == 0) __hip_atomic_store((gu32*)(ws + WS_FLAG + FL_TMO), 1u, RLX_AGENT);
      return true;
    }
    return false;
  }
};

__device__ __forceinline__ unsigned make_tag(unsigned epoch) { return epoch * 0x9E3779B1u + 0x7F4A7C15u; }
__device__ __forceinline__ void publish(char* ws, unsigned flag_off, unsigned tag) {
  if (threadIdx.x == 0) __hip_atomic_store((gu32*)(ws + WS_FLAG + flag_off), tag, RLX_AGENT);
}
template <int NW>
__device__ __forceinline__ void poll(char* ws, unsigned flag_off, unsigned tag) {
  const int lane = threadIdx.x & 63;
  gu32* f = (gu32*)(ws + WS_FLAG + flag_off);
  const unsigned long long t0 = __builtin_amdgcn_s_memrealtime();
  for (unsigned spins = 1;; ++spins) {
    bool ok = true;
    if (NW == 256) {
#pragma unroll
      for (int i = 0; i < 4; ++i) ok &= __hip_atomic_load(f + i * 64 + lane, RLX_AGENT) == tag;
    } else ok = __hip_atomic_load(f + (lane < NW ? lane : 0), RLX_AGENT) == tag;
    if (__all(ok)) break;
    if ((spins & 63u) == 0u && __builtin_amdgcn_s_memrealtime() - t0 > 20000000ull) {
      if (lane == 0) __hip_atomic_store((gu32*)(ws + WS_FLAG + FL_TMO), tag, RLX_AGENT);
      break;
    }
    __builtin_amdgcn_s_sleep(1);
  }
}
template <int NW> __device__ __forceinline__ void await(char* ws, unsigned flag_off, unsigned tag) {
  if (rfl(threadIdx.x >> 6) == 0) poll<NW>(ws, flag_off, tag);
  raw_bar();
}

struct Relayout {
  const char* src; int pitch, dstride, nt; unsigned dst;
  __device__ __forceinline__ void decode(const Params& p, int J) {
    if (J >= 1176) J = 0;
    if (J < 128) { const int ks = J >> 3, cg = J & 7; src = p.wo + ((size_t)ks * 32 * DM + cg * 64) * 2; pitch = DM; dst = WS_WOF + (unsigned)(cg * 4 * 16 + ks) * 1024; dstride = 16 * 1024; nt = 4; }
    else if (J < 472) { const int ks = (J - 128) >> 3, cg = (J - 128) & 7; src = p.wd + ((size_t)ks * 32 * DM + cg * 64) * 2; pitch = DM; dst = WS_WDF + (unsigned)(cg * 4 * 43 + ks) * 1024; dstride = 43 * 1024; nt = 4; }
    else { const int r = J - 472, mat = r >= 352, r2 = r - mat * 352, ks = r2 / 22, cg = r2 - ks * 22;
      src = (mat ? p.wu : p.wg) + ((size_t)ks * 32 * FF + cg * 64) * 2; pitch = FF; dst = WS_WGF + (unsigned)((mat * 86 + cg * 4) * 16 + ks) * 1024; dstride = 16 * 1024; nt = cg == 21 ? 2 : 4; }
  }
  __device__ __forceinline__ void load(unsigned lds, int lane) const {
    const int r = lane >> 3, c = lane & 7;
#pragma unroll
    for (int kg = 0; kg < 4; ++kg) {
      int j = (c >> 1) ^ (((r >> 1) & 1) | ((kg & 1) << 1)); j = j < nt ? j : j - 2;
      glds16<false>(src, (unsigned)((kg * 8 + r) * pitch + j * 16 + (c & 1) * 8) * 2u, lds + kg * 1024);
    }
  }
  __device__ __forceinline__ void store(rsrc_t ws, lds_cptr L, int lane) const {
    const int g = lane >> 4, q = (lane >> 2) & 3, pp = lane & 3, ff = ((q >> 1) & 1) | ((g & 1) << 1);
#pragma unroll
    for (int j = 0; j < 4; ++j)
      if (j < nt) st16(ws, dst + j * dstride + lane * 16, __builtin_bit_cast(u32x4, ldtr2(L + g * 1024 + q * 128 + ((j ^ ff) << 5) + pp * 8, 512)));
  }
};

struct F1 { bf16x8 a[4], w[3]; };
__device__ __forceinline__ void p1_qkv(const Params& p, rsrc_t ws, int b, int rb1, int cb1, char* smem, unsigned epoch) {
  const int tid = threadIdx.x, lane = tid & 63, wid = rfl(tid >> 6);
  const int r16 = lane & 15, g = lane >> 4;
  const unsigned lds0 = (unsigned)(size_t)(lds_ptr)smem;
  lds_cptr L = (lds_cptr)smem;
  constexpr int STG = 32768, ND = 4, NS = 8, MS = ND * STG;
  LAS f32x2* ms = (LAS f32x2*)(smem + MS);
  const int rb = rb1, cb = cb1;
  if (__builtin_expect(wid >= 4, 1)) {
    const int lw = wid - 4;
    const unsigned voffA = (unsigned)((16 * lw + (lane >> 3)) * DM + (((lane & 7) ^ (lane >> 3)) * 8)) * 2u;
    unsigned voffB[2];
#pragma unroll
    for (int i = 0; i < 2; ++i) { const int r = lane >> 3, c = lane & 7, ff = ((r >> 1) & 1) | (i << 1), j = (c >> 1) ^ ff;
      voffB[i] = (unsigned)((16 * lw + 8 * i + r) * (3 * DM) + j * 16 + (c & 1) * 8) * 2u; }
    const char* xa = p.x + (size_t)rb * 64 * DM * 2;
    const char* wb = p.wqkv + (size_t)cb * 64 * 2;
    const int rot = ((b >> 3) & 7) + 2 * (b >> 6);
    auto issue = [&](int s) {
      const unsigned d = lds0 + (s % ND) * STG;
      const int kc = (s + rot) & 7;
#pragma unroll
      for (int i = 0; i < 2; ++i) glds16<false>(xa + kc * 128 + i * 8 * DM * 2, voffA, d + (2 * lw + i) * 1024);
#pragma unroll
      for (int i = 0; i < 2; ++i) glds<3, false>(wb + (size_t)kc * 64 * (3 * DM) * 2, voffB[i], d + 8192 + (2 * lw + i) * 3072);
    };
    issue(0); issue(1);
    wait_vm<8>(); raw_bar();
    issue(2);
#pragma unroll
    for (int j = 1; j < NS; ++j) {
      if (j + 2 < NS) { issue(j + 2); wait_bar<16>(); } else if (j + 1 < NS) wait_bar<8>(); else wait_bar<0>();
    }
    lds_bar();
  } else {
    f32x4 acc[3][4], cs[3], sx = Z4, sq = Z4;
    int aoff[2][4];
#pragma unroll
    for (int ks = 0; ks < 2; ++ks)
#pragma unroll
      for (int i = 0; i < 4; ++i) aoff[ks][i] = ((wid + i) & 3) * 2048 + (r16 >> 3) * 1024 + (r16 & 7) * 128 + (((4 * ks + g) ^ (r16 & 7)) << 4);
    int boff;
    { const int q = (lane >> 2) & 3, pp = lane & 3, ff = ((q >> 1) & 1) | ((g & 1) << 1);
      boff = 8192 + g * 3072 + q * 128 + ((wid ^ ff) << 5) + pp * 8; }
#pragma unroll
    for (int i = 0; i < 3; ++i) { cs[i] = Z4;
#pragma unroll
      for (int j = 0; j < 4; ++j) acc[i][j] = Z4; }
    const bf16x8 one = ones8();
    F1 fa, fb;
    auto rd = [&](F1& fr, int s, int ks) {
      lds_cptr B = L + (s % ND) * STG;
#pragma unroll
      for (int seg = 0; seg < 3; ++seg) fr.w[seg] = ldtr2(B + boff + ks * 12288 + seg * 1024, 512);
#pragma unroll
      for (int i = 0; i < 4; ++i) fr.a[i] = ld128(B + aoff[ks][i]);
    };
    auto mm = [&](const F1& fr) {
#pragma unroll
      for (int i = 0; i < 4; ++i) {
        acc[0][i] = MFMA16(fr.w[0], fr.a[i], acc[0][i]);
        acc[1][i] = MFMA16(fr.w[1], fr.a[i], acc[1][i]);
        acc[2][i] = MFMA16(fr.a[i], fr.w[2], acc[2][i]);
      }
      cs[0] = MFMA16(fr.w[0], one, cs[0]); cs[1] = MFMA16(fr.w[1], one, cs[1]); cs[2] = MFMA16(one, fr.w[2], cs[2]);
      sx = MFMA16(one, fr.a[0], sx); sq = MFMA16(fr.a[0], fr.a[0], sq);
    };
    const unsigned pz_off = ((unsigned)(cb * 4 + wid) * 64 + rb * 2) * 1024 + lane * 16;
    lds_bar();
    rd(fa, 0, 0); SB();
#pragma unroll
    for (int s = 0; s < NS; ++s) {
      rd(fb, s, 1); mm(fa); weave<10, 17>(); SB();
      if (s < 6) st16(ws, (s >> 1 == 0 ? WS_Q : s >> 1 == 1 ? WS_K : WS_V) + pz_off + (s & 1) * 1024, (u32x4){POISON, POISON, POISON, POISON});
      if (s == 6 && lane == 0) __hip_atomic_store((gu32*)(p.ws + WS_KMAX) + (cb * 4 + wid) * 32 + rb, POISON, RLX_AGENT);
      if (s + 1 < NS) { lds_bar(); rd(fa, s + 1, 0); mm(fb); weave<10, 17>(); SB(); }
      else { mm(fb); SB(); }
    }
    if ((r16 >> 2) == g) {
      const int e = r16 & 3; const float s2 = e == 0 ? sq[0] : e == 1 ? sq[1] : e == 2 ? sq[2] : sq[3];
      float mean, rstd; ln_consts(sx[0], s2, mean, rstd);
      ms[wid * 16 + r16] = (f32x2){mean, rstd};
    }
    wait_bar<0>();
    publish(p.ws, FL_1 + (cb * 32 + rb) * 4, make_tag(epoch));
    const int h = cb * 4 + wid;
    u32x2 qv[4], kv[4], vv[4];
    float kmx = 0.f;
#pragma unroll
    for (int i = 0; i < 4; ++i) {
      const int rt = (wid + i) & 3;
      const f32x2 st = ms[rt * 16 + r16];
      qv[i] = cvt4((acc[0][i] - st.x * cs[0]) * (st.y * QSCALE));
      { const f32x4 v = (acc[1][i] - st.x * cs[1]) * st.y; kv[i] = cvt4(v);
        kmx = fmaxf(kmx, sum_x32(sum_x16((v[0] * v[0] + v[1] * v[1]) + (v[2] * v[2] + v[3] * v[3])))); }
      { const f32x4 m01 = *(LAS f32x4*)(ms + rt * 16 + 4 * g), m23 = *(LAS f32x4*)(ms + rt * 16 + 4 * g + 2); const float cv = cs[2][0];
        vv[i] = (u32x2){cvtpk((acc[2][i][0] - m01[0] * cv) * m01[1], (acc[2][i][1] - m01[2] * cv) * m01[3]),
                        cvtpk((acc[2][i][2] - m23[0] * cv) * m23[1], (acc[2][i][3] - m23[2] * cv) * m23[3])}; }
    }
#pragma unroll
    for (int i = 0; i < 4; i += 2) {
      { const int rt = (wid + i + (g & 1)) & 3, grow = rb * 64 + rt * 16;
        const unsigned off = (((unsigned)h * 64 + (grow >> 5)) * 2 + (g >> 1)) * 512 + ((grow & 31) + r16) * 16;
        st16(ws, WS_K + off, nop4(pair16(kv[i], kv[i + 1]))); st16(ws, WS_Q + off, nop4(pair16(qv[i], qv[i + 1]))); }
      { const int rt = (wid + i + (g >> 1)) & 3, grow = rb * 64 + rt * 16;
        const unsigned off = ((((unsigned)h * 64 + (grow >> 5)) * 2 + (rt & 1)) * 2 + (g & 1)) * 256 + r16 * 16;
        st16(ws, WS_V + off, nop4(pair32(vv[i], vv[i + 1]))); }
    }
    kmx = max_row16(kmx);
    if (lane == 0) __hip_atomic_store((gu32*)(p.ws + WS_KMAX) + h * 32 + rb, min(__float_as_uint(kmx), POISON - 1), RLX_AGENT);
  }
}

constexpr int A_K = 0, A_V = 65536, A_ONE = 131072, A_Q = A_ONE + 5120, A_KM = A_Q + 8192, A_DUMMY = A_KM + 1024, A_CTR = A_DUMMY + 256, A_RL = A_CTR + 256, A_BYTES = A_RL + 3 * 4096;
constexpr float SAFE_BOUND = 48.f;
#define PRIO_SW 9
#define SP2 5
#define SP3 8
#define SP4 11
constexpr int PRIO_SWITCH = PRIO_SW;

struct P3Pre { bf16x8 wof[16]; u32x2 xv[4]; };
__device__ __forceinline__ void p2_attn(const Params& p, rsrc_t ws, int b, int h, int qr, char* smem, unsigned tag, P3Pre& pre) {
  const int tid = threadIdx.x, lane = tid & 63, wid = rfl(tid >> 6);
  const int r32 = lane & 31, hh = lane >> 5;
  const unsigned lds0 = (unsigned)(size_t)(lds_ptr)smem;
  lds_cptr L = (lds_cptr)smem;
  const char* kg = p.ws + WS_K + (size_t)h * 65536;
  const char* vg = p.ws + WS_V + (size_t)h * 65536;
  const unsigned vo = lane * 16;
  if (wid < 4) {
    const int t = 4 * wid, gt = (t + 8 * qr) & 63;
    auto first = [&]() {
      glds<2, true>(p.ws + WS_Q + ((size_t)h * 64 + qr * 8 + 2 * wid) * 1024, vo, lds0 + A_Q + 2 * wid * 1024);
      glds4<true>(p.ws + WS_KMAX + h * 128, (lane & 31) * 4, lds0 + A_KM + wid * 256);
      glds<4, true>(kg + gt * 1024, vo, lds0 + A_K + t * 1024);
      glds<4, true>(vg + gt * 1024, vo, lds0 + A_V + t * 1024);
      wait_vm<0>();
      return pieces_ok<2>(L + A_Q + 2 * wid * 1024) && pieces_ok<4>(L + A_K + t * 1024) && pieces_ok<4>(L + A_V + t * 1024) && !__any(*(const LAS unsigned*)(L + A_KM + wid * 256 + lane * 4) == POISON);
    };
    if (!first()) { Spin sp; while (!first() && !sp.expired(p.ws)) {} }
  }
  st16(ws, WS_O + (unsigned)((qr * 16 + 2 * wid + (lane >> 5)) * 16 + (h >> 1)) * 1024 + (h & 1) * 512 + (lane & 31) * 16, (u32x4){POISON, POISON, POISON, POISON});
  if (tid == 0) *(LAS unsigned*)(smem + A_CTR) = 0u;
  { const unsigned o1 = (tid & 31) < 2 ? 0x3F803F80u : 0u;
    *(LAS u32x2*)(smem + A_ONE + tid * 8) = (u32x2){o1, o1};
    if (tid < 128) *(LAS u32x2*)(smem + A_ONE + 4096 + tid * 8) = (u32x2){o1, o1}; }
  if (wid == 0 && blockIdx.x == 0) {
    poll<256>(p.ws, FL_1, tag);
    if (lane == 0) __hip_atomic_store((gu32*)(p.ws + WS_FLAG + FL_EPOCH), tag, RLX_AGENT);
  }
  lds_bar();
  const bf16x8 qf = ld128(L + A_Q + wid * 1024 + lane * 16);
  float c;
  { float km = *(const LAS float*)(L + A_KM + lane * 4);
    km = max_x16(max_row16(km));
    float q2 = 0.f;
#pragma unroll
    for (int j = 0; j < 8; ++j) { const float v = __uint_as_float((unsigned)(unsigned short)qf[j] << 16); q2 += v * v; }
    q2 = sum_x32(q2);
    c = sqrtf(q2 * km) * 1.02f + 0.01f; }
  const bool fast = __all(c <= SAFE_BOUND);
  const bool vlane = r32 < 16;
  int vaddr = (int)lds0 + (vlane ? A_V : A_ONE) + hh * 256 + (r32 & 15) * 16;
  const int vstep = vlane ? 4096 : 0;
  int kaddr = (int)lds0 + A_K + lane * 16;
  f32x16 o;
#pragma unroll
  for (int i = 0; i < 16; ++i) o[i] = 0.f;
  unsigned go = vo + ((16 + wid + 8 * qr) & 63) * 1024;
  unsigned ld = lds0 + A_K + (16 + wid) * 1024;
  auto settle = [&]() {
    int lane = threadIdx.x & 63; asm volatile("" : "+v"(lane));
    const unsigned go0 = lane * 16 + ((16 + wid + 8 * qr) & 63) * 1024, ld0 = lds0 + A_K + (16 + wid) * 1024;
    auto good = [&]() {
      unsigned m = 0;
#pragma unroll
      for (int j = 0; j < 12; ++j) { const u32x4 v = *(const LAS u32x4*)(L + (j < 6 ? A_K : A_V) + (16 + wid + 8 * (j % 6)) * 1024 + lane * 16); m = max(m, max(max(v[0], v[1]), max(v[2], v[3]))); }
      return !__any(m == POISON);
    };
    if (!good()) {
      Spin sp;
      do {
#pragma unroll
        for (int j = 0; j < 6; ++j) { const unsigned gj = (go0 + j * 8192) & 0xffffu; glds16<true>(kg, gj, ld0 + j * 8192); glds16<true>(vg, gj, ld0 + 65536 + j * 8192); }
        wait_vm<0>();
      } while (!good() && !sp.expired(p.ws));
    }
    raw_bar();
  };
#define FRESH() int lane = threadIdx.x & 63, bq = b; asm volatile("" : "+v"(lane), "+s"(bq)); const int xq = bq & 7, iq = bq >> 3; \
                const int rb = xq * 4 + (iq & 3), cb = iq >> 2, mb = xq * 2 + (iq & 1), fb = iq >> 1, u0 = fb < 11 ? 3 * fb : 33 + 2 * (fb - 11), nu = fb < 11 ? 3 : 2; \
                const int slot = wid - 1, J = bq * 5 + slot, rlo = slot < 2 ? A_Q + slot * 4096 : A_RL + (slot - 2) * 4096; const bool rl = wid >= 1 && wid <= 5 && J < 1176; \
                (void)rb; (void)cb; (void)mb; (void)u0; (void)nu; (void)rlo; (void)rl; (void)lane
  auto split1 = [&]() {
    FRESH();
    if (rl) { Relayout r; r.decode(p, J); r.load(lds0 + rlo, lane); }
  };
  auto split2 = [&]() {
    FRESH();
    if (rl) { wait_vm<0>(); Relayout r; r.decode(p, J); r.store(ws, L + rlo, lane); }
#pragma unroll
    for (int i = 0; i < 5; ++i) {
      const int j = wid + 8 * i;
      if (j < 33) {
        unsigned off;
        if (j < 8) off = WS_H + (unsigned)((rb * 4 + (j >> 1)) * 16 + cb * 2 + (j & 1)) * 1024 + lane * 16;
        else if (j < 32) { const int jj = j - 8, rt = jj / 3, u = jj - rt * 3; off = WS_ACT + (unsigned)((mb * 8 + rt) * 43 + u0 + (u < nu ? u : 0)) * 1024 + lane * 16; }
        else off = WS_HST + (unsigned)(rb * 8 + cb) * 512 + (lane & 31) * 16;
        st16(ws, off, (u32x4){POISON, POISON, POISON, POISON});
      }
    }
  };
  auto split3 = [&]() {
    FRESH();
    wait_vm<0>();
    if (lane == 0 && __hip_atomic_fetch_add((LAS unsigned*)(smem + A_CTR), 1u, __ATOMIC_RELAXED, __HIP_MEMORY_SCOPE_WORKGROUP) == 7u)
      __hip_atomic_store((gu32*)(p.ws + WS_FLAG + FL_P) + bq, tag, RLX_AGENT);
  };
  auto split4 = [&]() {
    if (wid >= 4) {
      FRESH();
      poll<256>(p.ws, FL_P, tag);
      const int r16 = lane & 15, g = lane >> 4, wn = wid & 3;
#pragma unroll
      for (int i = 0; i < 16; ++i) pre.wof[i] = ldw16(ws, WS_WOF + ((unsigned)(cb * 4 + wn) * 16 + ((i + 8 * cb) & 15)) * 1024 + lane * 16);
#pragma unroll
      for (int i = 0; i < 4; ++i) pre.xv[i] = *(const u32x2*)(p.x + ((size_t)(rb * 64 + i * 16 + r16) * DM + cb * 64 + wn * 16 + 4 * g) * 2);
#pragma unroll
      for (int i = 0; i < 3; ++i) {
        const int ln = (iq & 1) * 768 + (wn * 3 + i) * 64 + lane, ch = ln >> 7, jt = ch >= 6 ? ch - 6 : ch;
        glds4<false>(p.ws + WS_WGF, (unsigned)(((ch >= 6) * 86 + u0 * 2 + (jt < 2 * nu ? jt : jt - 2)) * 128 + (ln & 127)) * 128u, lds0 + A_DUMMY);
      }
    }
  };
#undef FRESH
  if (fast) {
    f32x16 negc, sa, sb; f32x8 pp, kk; f32x16 vv;
#pragma unroll
    for (int i = 0; i < 16; ++i) negc[i] = -c;
    int cnt, keep;
    asm volatile(ATTN_ASM_A
                 : [o] "+v"(o), [ka] "+v"(kaddr), [va] "+v"(vaddr), [go] "+v"(go), [ld] "+s"(ld), [keep] "=&s"(keep),
                   "=&{v[128:143]}"(sa), "=&{v[144:159]}"(sb), "=&{v[160:167]}"(pp), "=&{v[168:175]}"(kk), "=&{v[176:191]}"(vv)
                 : [q] "v"(qf), [nc] "v"(negc), [vs] "v"(vstep), [kg] "s"(kg), [vg] "s"(vg)
                 : "memory", "scc");
    settle(); split1();
    const int sw = wid < 4 ? PRIO_SWITCH : 99;
#define ATTN_B(C0, C1) asm volatile(ATTN_ASM_B \
                 : [o] "+v"(o), [ka] "+v"(kaddr), [va] "+v"(vaddr), [cnt] "=&s"(cnt), \
                   "+{v[128:143]}"(sa), "+{v[144:159]}"(sb), "+{v[160:167]}"(pp), "+{v[168:175]}"(kk), "+{v[176:191]}"(vv) \
                 : [q] "v"(qf), [nc] "v"(negc), [vs] "v"(vstep), [sw] "s"(sw), [c0] "s"(C0), [c1] "s"(C1) \
                 : "memory", "scc")
    ATTN_B(2, SP2); split2();
    ATTN_B(SP2, SP3); split3();
    ATTN_B(SP3, SP4); split4();
    ATTN_B(SP4, 16);
#undef ATTN_B
  } else {
#pragma unroll
    for (int j = 0; j < 6; ++j) { const unsigned gj = (go + j * 8192) & 0xffffu; glds16<true>(kg, gj, ld + j * 8192); glds16<true>(vg, gj, ld + 65536 + j * 8192); }
    float m = -INFINITY;
    vaddr -= (int)lds0; kaddr -= (int)lds0;
#pragma unroll 1
    for (int t4 = 0; t4 < 16; ++t4) {
      if (t4 == 2) { wait_vm<0>(); settle(); split1(); }
      if (t4 == SP2) split2();
      if (t4 == SP3) split3();
      if (t4 == SP4) split4();
#pragma unroll 1
      for (int u = 0; u < 4; ++u) {
        const int t = t4 * 4 + u;
        f32x16 z;
#pragma unroll
        for (int i = 0; i < 16; ++i) z[i] = 0.f;
        const f32x16 s = MFMA32(ld128(L + kaddr + t * 1024), qf, z);
        float mx = s[0];
#pragma unroll
        for (int i = 1; i < 16; ++i) mx = fmaxf(mx, s[i]);
        mx = fmaxf(mx, __shfl_xor(mx, 32));
        const float mn = fmaxf(m, mx);
        const float al = __builtin_amdgcn_exp2f(m - mn);
        m = mn;
#pragma unroll
        for (int i = 0; i < 16; ++i) o[i] *= al;
        u32x4 p0, p1;
#pragma unroll
        for (int i = 0; i < 4; ++i) {
          p0[i] = cvtpk(__builtin_amdgcn_exp2f(s[2 * i] - mn), __builtin_amdgcn_exp2f(s[2 * i + 1] - mn));
          p1[i] = cvtpk(__builtin_amdgcn_exp2f(s[8 + 2 * i] - mn), __builtin_amdgcn_exp2f(s[8 + 2 * i + 1] - mn));
        }
        const bf16x8 v0 = ld128(L + vaddr + u * 1024), v1 = ld128(L + vaddr + u * 1024 + 512);
        o = MFMA32(v0, __builtin_bit_cast(bf16x8, p0), o);
        o = MFMA32(v1, __builtin_bit_cast(bf16x8, p1), o);
      }
      vaddr += vstep;
    }
  }
  const float inv = __builtin_amdgcn_rcpf(lo_x32(o[8]));
  const u32x4 ov = pair32((u32x2){cvtpk(o[0] * inv, o[1] * inv), cvtpk(o[2] * inv, o[3] * inv)}, (u32x2){cvtpk(o[4] * inv, o[5] * inv), cvtpk(o[6] * inv, o[7] * inv)});
  const int row = qr * 256 + wid * 32 + r32;
  st16(ws, WS_O + ((((unsigned)(row >> 4) * 16 + (h >> 1)) * 4 + (h & 1) * 2 + hh) * 16 + (row & 15)) * 16, nop4(ov));
}

constexpr int G64_KPS = 8, G64_STG = 4 * G64_KPS * 1024;
template <int NKS, int ND, int LA, int NPRE, class Pre>
__device__ __forceinline__ void gemm64_load(char* ws, const char* ag  , int rot, char* smem, const Pre& pre) {
  const int lane = threadIdx.x & 63, wid = rfl(threadIdx.x >> 6);
  const unsigned lds0 = (unsigned)(size_t)(lds_ptr)smem;
  lds_cptr L = (lds_cptr)smem + wid * 8192;
  constexpr int NS = (NKS + 7) / 8, LAST = NKS - 8 * (NS - 1), NFULL = NKS / 8;
  auto issue = [&](int s) {
    const unsigned d = lds0 + (s % ND) * G64_STG + wid * 8192;
    if (s < NFULL) { const int q0 = s + rot, q = q0 >= NFULL ? q0 - NFULL : q0; glds<4, true>(ag + q * 8192, lane * 16, d); glds<4, true>(ag + q * 8192 + 4096, lane * 16, d + 4096); }
    else glds<3, true>(ag + s * 8192, lane * 16, d);
  };
  auto okay = [&](int s) { return s < NFULL ? pieces_ok<8>(L + (s % ND) * G64_STG) : pieces_ok<3>(L + (s % ND) * G64_STG); };
  auto ensure = [&](int s) { if (!okay(s)) { Spin sp; do { issue(s); wait_vm<0>(); } while (!okay(s) && !sp.expired(ws)); } };
  auto waitv = [&](int j) {
    const int hi = j + LA - 2 < NS - 1 ? j + LA - 2 : NS - 1;
    const int n = (hi - j) * 8 - ((hi == NS - 1 && hi > j) ? 8 - LAST : 0);
    switch (n) { case 3: wait_vm<3>(); break; case 8: wait_vm<8>(); break; case 11: wait_vm<11>(); break; case 16: wait_vm<16>(); break; default: wait_vm<0>(); break; }
  };
  issue(0); pre(); wait_vm<NPRE>(); ensure(0);
#pragma unroll
  for (int s = 1; s < LA; ++s) if (s < NS) issue(s);
  lds_bar();
#pragma unroll
  for (int j = 1; j < NS; ++j) {
    waitv(j);
    if (j < NFULL) {
      Chk<8> c; c.read(L + (j % ND) * G64_STG);
      if (j + LA - 1 < NS) issue(j + LA - 1);
      if (!c.ok()) ensure(j);
    } else ensure(j);
    lds_bar();
  }
}
template <int NKS, int ND>
__device__ __forceinline__ void gemm64_mma(const bf16x8 (&wf)[NKS], char* smem, f32x4 (&acc)[4]) {
  const int lane = threadIdx.x & 63;
  lds_cptr L = (lds_cptr)smem + lane * 16;
#pragma unroll
  for (int i = 0; i < 4; ++i) acc[i] = Z4;
  bf16x8 f[2][4];
  auto rd = [&](int ks) {
    lds_cptr B = L + ((ks >> 3) % ND) * G64_STG + (ks & 7) * 1024;
#pragma unroll
    for (int rt = 0; rt < 4; ++rt) f[ks & 1][rt] = ld128(B + rt * 8192);
  };
  lds_bar();

  rd(0); SB();
#pragma unroll
  for (int ks = 0; ks < NKS; ++ks) {
    if (ks + 1 < NKS) {
      if (((ks + 1) & 7) == 0) lds_bar();
      rd(ks + 1);
    }
#pragma unroll
    for (int rt = 0; rt < 4; ++rt) acc[rt] = MFMA16(wf[ks], f[ks & 1][rt], acc[rt]);
    if (ks + 1 < NKS) weave<4, 4>();
    SB();
  }

}

constexpr int G_B = 0, G_BS = 24576, G_A = 3 * G_BS, G_AS = 16384, G_ST = G_A + 4 * G_AS, G_MS = G_ST + 8192, G_CS = G_MS + 1024;
struct WLoad {
  const char* src[3]; unsigned dst; int rot;
  __device__ __forceinline__ void init(const Params& p, int mb, int fb, int lw, unsigned lds0) {
    const int u0 = fb < 11 ? 3 * fb : 33 + 2 * (fb - 11), nu = fb < 11 ? 3 : 2;
    rot = (fb + 4 * (mb & 1)) & 7;
#pragma unroll
    for (int i = 0; i < 3; ++i) { const int jt = 3 * (lw & 1) + i; src[i] = p.ws + WS_WGF + (size_t)((lw >> 1) * 86 + u0 * 2 + (jt < 2 * nu ? jt : jt - 2)) * 16384; }
    dst = lds0 + G_B + lw * 6144;
  }
  __device__ __forceinline__ void issue(int s, int lane) const {
#pragma unroll
    for (int i = 0; i < 3; ++i) glds<2, true>(src[i] + ((s + rot) & 7) * 2048, lane * 16, dst + (s % 3) * G_BS + i * 2048);
  }
};
struct F4 { bf16x8 a[4], w[2][3]; };
__device__ __forceinline__ void p4_gateup(const Params& p, rsrc_t ws, int mb, int fb, char* smem, const WLoad& wl, int wct, int rot5, bf16x8 (&wdf)[43]) {
  const int tid = threadIdx.x, lane = tid & 63, wid = rfl(tid >> 6);
  const int r16 = lane & 15, g = lane >> 4;
  const unsigned lds0 = (unsigned)(size_t)(lds_ptr)smem;
  lds_cptr L = (lds_cptr)smem;
  constexpr int NS = 8;
  const int u0 = fb < 11 ? 3 * fb : 33 + 2 * (fb - 11), nu = fb < 11 ? 3 : 2;
  if (wid >= 4) {
    const int lw = wid - 4;
    const char* ag = p.ws + WS_H + (size_t)(mb * 8 + 2 * lw) * 16384;
    auto issueA = [&](int s) {
      const unsigned d = lds0 + G_A + (s & 3) * G_AS + lw * 4096;
#pragma unroll
      for (int i = 0; i < 2; ++i) glds<2, true>(ag + i * 16384 + ((s + wl.rot) & 7) * 2048, lane * 16, d + i * 2048);
    };
    auto issueS = [&]() {
      glds<2, true>(p.ws + WS_HST + (size_t)mb * 8192 + lw * 2048, lane * 16, lds0 + G_ST + lw * 2048);
    };
    auto ensure = [&](int s) {
      lds_cptr A = L + G_A + (s & 3) * G_AS + lw * 4096;
      if (!pieces_ok<4>(A)) { Spin sp; do { issueA(s); wait_vm<0>(); } while (!pieces_ok<4>(A) && !sp.expired(p.ws)); }
    };
    float cs[3] = {0.f, 0.f, 0.f};
    const unsigned one_bits = 0x3F803F80u;
    const bf16x2_t one2 = __builtin_bit_cast(bf16x2_t, one_bits);
    auto colsum = [&](int s) {
#pragma unroll
      for (int i = 0; i < 6; ++i) {
        const u32x4 v = *(const LAS u32x4*)(L + G_B + (s % 3) * G_BS + lw * 6144 + i * 1024 + lane * 16);
#pragma unroll
        for (int e = 0; e < 4; ++e) { const unsigned t = v[e]; cs[i >> 1] = __builtin_amdgcn_fdot2_f32_bf16(__builtin_bit_cast(bf16x2_t, t), one2, cs[i >> 1], false); }
      }
    };
    issueA(0);
    colsum(0); colsum(1);
    wait_vm<0>();
    ensure(0);
    issueA(1); issueS(); issueA(2); wl.issue(2, lane);
    lds_bar();
#pragma unroll
    for (int j = 1; j < NS; ++j) {
      if (j == 1) wait_vm<12>(); else if (j + 1 < NS) wait_vm<10>(); else wait_vm<0>();
      Chk<4> c; c.read(L + G_A + (j & 3) * G_AS + lw * 4096);
      if (j + 2 < NS) issueA(j + 2);
      if (!c.ok()) ensure(j);
      if (j == 2) {
        lds_cptr Sg = L + G_ST + lw * 2048;
        if (!pieces_ok<2>(Sg)) { Spin sp; do { issueS(); wait_vm<0>(); } while (!pieces_ok<2>(Sg) && !sp.expired(p.ws)); }
      }
      if (j >= 2) colsum(j);
      lds_bar();
      if (j + 2 < NS) wl.issue(j + 2, lane);
      if (j == 2) {
        const int row = tid - 256;
        if (row < 128) {
          float a = 0.f, bb = 0.f;
#pragma unroll
          for (int c = 0; c < 8; ++c) { const f32x2 v = *(const LAS f32x2*)(L + G_ST + (row >> 6) * 4096 + c * 512 + (row & 63) * 8); a += v.x; bb += v.y; }
          float mean, rstd; ln_consts(a, bb, mean, rstd);
          *(LAS f32x2*)(smem + G_MS + row * 8) = (f32x2){rstd, -mean * rstd};
        }
      }
    }
#pragma unroll
    for (int i = 0; i < 3; ++i) { const float t = sum_x32(sum_x16(cs[i])); if (g == 0) *(LAS float*)(smem + G_CS + ((lw * 3 + i) * 16 + r16) * 4) = t; }
    lds_bar();
  } else {
    const int wm = wid >> 1, wn = wid & 1;
    const int aoff = G_A + wm * 8192 + lane * 16;
    constexpr int m0 = 0, m1 = 12288;
    const int boff = G_B + wn * 6144 + lane * 16;
    f32x4 acc[2][3][4];
#pragma unroll
    for (int i = 0; i < 3; ++i)
#pragma unroll
      for (int j = 0; j < 4; ++j) { acc[0][i][j] = Z4; acc[1][i][j] = Z4; }
    F4 fa, fb2;
    auto rd = [&](F4& f, int s, int ks) {
      lds_cptr B = L + (s % 3) * G_BS, A = L + (s & 3) * G_AS;
#pragma unroll
      for (int i = 0; i < 4; ++i) f.a[i] = ld128(A + aoff + i * 2048 + ks * 1024);
#pragma unroll
      for (int i = 0; i < 3; ++i) { f.w[0][i] = ld128(B + boff + m0 + i * 2048 + ks * 1024); f.w[1][i] = ld128(B + boff + m1 + i * 2048 + ks * 1024); }
    };
    auto mm = [&](const F4& f) {
#pragma unroll
      for (int m = 0; m < 2; ++m)
#pragma unroll
        for (int i = 0; i < 3; ++i)
#pragma unroll
          for (int j = 0; j < 4; ++j) acc[m][i][j] = MFMA16(f.w[m][i], f.a[j], acc[m][i][j]);
    };
    lds_bar();
    rd(fa, 0, 0); SB();
#pragma unroll
    for (int s = 0; s < NS; ++s) {
      rd(fb2, s, 1); mm(fa); weave<10, 24>(); SB();
      if (s + 1 < NS) {
        lds_bar();
        rd(fa, s + 1, 0); mm(fb2); weave<10, 24>(); SB();
      }
      else { mm(fb2); SB(); }
    }
    const LAS float* csl = (const LAS float*)(smem + G_CS);
    lds_bar();
    const LAS f32x2* ms = (const LAS f32x2*)(smem + G_MS);
    f32x2 st[4];
#pragma unroll
    for (int j = 0; j < 4; ++j) st[j] = ms[wm * 64 + j * 16 + r16];
    constexpr float K1 = -2.885390081777927f * 0.7978845608028654f, K2 = -2.885390081777927f * 0.035677408136300125f;
#pragma unroll
    for (int i = 0; i < 3; ++i) {
      const int jt = 3 * wn + i, u = jt >> 1, jj = jt & 1;
      const f32x4 cg = *(const LAS f32x4*)(csl + jt * 16 + 4 * g), cu = *(const LAS f32x4*)(csl + (6 + jt) * 16 + 4 * g);
      u32x2 av[4];
#pragma unroll
      for (int j = 0; j < 4; ++j) {
        const f32x4 ag2 = acc[0][i][j], au = acc[1][i][j];
        const f32x4 ga = ag2 * st[j].x + st[j].y * cg, up = au * st[j].x + st[j].y * cu;
        const f32x4 z = ga * (ga * ga * K2 + K1);
        const f32x4 d = (f32x4){__builtin_amdgcn_exp2f(z[0]), __builtin_amdgcn_exp2f(z[1]), __builtin_amdgcn_exp2f(z[2]), __builtin_amdgcn_exp2f(z[3])} + 1.f;
        const f32x4 rc = (f32x4){__builtin_amdgcn_rcpf(d[0]), __builtin_amdgcn_rcpf(d[1]), __builtin_amdgcn_rcpf(d[2]), __builtin_amdgcn_rcpf(d[3])};
        av[j] = cvt4((ga * rc) * up);
      }
      if (u < nu) {
#pragma unroll
        for (int j = 0; j < 4; j += 2) {
          const unsigned rt = (unsigned)mb * 8 + wm * 4 + j + (g & 1);
          st16(ws, WS_ACT + (((rt * 43 + (u0 + u)) * 4 + jj * 2 + (g >> 1)) * 16 + r16) * 16, nop4(pair16(av[j], av[j + 1])));
        }
      }
    }
  }
  if (wid >= 4) {
#pragma unroll
    for (int i = 0; i < 43; ++i) { int ks = i; if (i < 40) { ks += rot5; ks = ks >= 40 ? ks - 40 : ks; } wdf[i] = ldw16(ws, WS_WDF + ((unsigned)wct * 43 + ks) * 1024 + lane * 16); }
  }
}

extern __shared__ __attribute__((aligned(16))) char smem_dyn[];

__global__ __launch_bounds__(512, 2) void mega(Params p) {
  char* smem = smem_dyn;
  const int b = blockIdx.x, x = b & 7, i5 = b >> 3;
  const int tid = threadIdx.x, lane = tid & 63, wid = rfl(tid >> 6);
  const int r16 = lane & 15, g = lane >> 4, wn = wid & 3;
  const rsrc_t ws = __builtin_amdgcn_make_buffer_rsrc(p.ws, 0, WS_USED, 0x00020000);
  const unsigned epoch = __hip_atomic_load((gu32*)(p.ws + WS_FLAG + FL_EPOCH), RLX_AGENT);

  const int rb1 = (x >> 1) * 8 + (i5 & 7), cb1 = (x & 1) * 4 + (i5 >> 3);
  p1_qkv(p, ws, b, rb1, cb1, smem, epoch);
  const unsigned tag = (unsigned)rfl((int)make_tag(epoch));

  const int h = x * 4 + (i5 & 3), qr = i5 >> 2;
  const int rb = x * 4 + (i5 & 3), cb = i5 >> 2;
  const int mb = x * 2 + (i5 & 1), fb = i5 >> 1;
  await<32>(p.ws, FL_1 + (h >> 2) * 128, tag);
  P3Pre pre;
  p2_attn(p, ws, b, h, qr, smem, tag, pre);
  lds_bar();

  const int wct = cb * 4 + wn, col = cb * 64 + wn * 16 + 4 * g;
  u32x2 hv[4];
  if (wid >= 4) {
#pragma unroll
    for (int i = 0; i < 16; ++i) PIN(pre.wof[i]);
#pragma unroll
    for (int i = 0; i < 4; ++i) PIN(pre.xv[i]);
    f32x4 acc[4];
    gemm64_mma<16, 2>(pre.wof, smem + G_A, acc);
    LAS f32x2* S = (LAS f32x2*)(smem + G_ST);
#pragma unroll
    for (int i = 0; i < 4; ++i) hv[i] = cvt4(unpack4(pre.xv[i]) + acc[i]);
#pragma unroll
    for (int i = 0; i < 4; i += 2) {
      const unsigned rt = (unsigned)rb * 4 + i + (g & 1);
      st16(ws, WS_H + (((rt * 16 + cb * 2 + (wn >> 1)) * 4 + (wn & 1) * 2 + (g >> 1)) * 16 + r16) * 16, nop4(pair16(hv[i], hv[i + 1])));
    }
#pragma unroll
    for (int i = 0; i < 4; ++i) {
      const f32x4 v = unpack4(hv[i]);
      const float s1 = sum_x32(sum_x16((v[0] + v[1]) + (v[2] + v[3]))), s2 = sum_x32(sum_x16((v[0] * v[0] + v[1] * v[1]) + (v[2] * v[2] + v[3] * v[3])));
      if (g == 0) S[(i * 16 + r16) * 4 + wn] = (f32x2){s1, s2};
    }
    lds_bar();
    if (wid == 4) {
      const f32x2 a = S[lane * 4], bb = S[lane * 4 + 1], c = S[lane * 4 + 2], d = S[lane * 4 + 3];
      const unsigned s1 = __float_as_uint((a.x + bb.x) + (c.x + d.x)), s2 = __float_as_uint((a.y + bb.y) + (c.y + d.y));
      __hip_atomic_store((gu64*)(p.ws + WS_HST + (size_t)(rb * 8 + cb) * 512 + lane * 8), ((unsigned long long)(s2 < POISON ? s2 : POISON - 1) << 32) | (s1 < POISON ? s1 : POISON - 1), RLX_AGENT);
    }
    raw_bar();
  } else {
    auto pre3 = [&]() {
      WLoad w0; w0.init(p, mb, fb, wid, (unsigned)(size_t)(lds_ptr)smem); w0.issue(0, lane); w0.issue(1, lane);
    };
    gemm64_load<16, 2, 2, 12>(p.ws, p.ws + WS_O + (size_t)(rb * 4 + wid) * 16384, cb & 1, smem + G_A, pre3);
    lds_bar();
    raw_bar();
  }

  bf16x8 wdf[43];
  { WLoad wl;
    if (wid >= 4) wl.init(p, mb, fb, wid - 4, (unsigned)(size_t)(lds_ptr)smem);
    p4_gateup(p, ws, mb, fb, smem, wl, wct, 8 * (cb % 5), wdf); }
  lds_bar();

  if (wid >= 4) {
#pragma unroll
    for (int i = 0; i < 43; ++i) PIN(wdf[i]);
    f32x4 acc[4];
    gemm64_mma<43, 4>(wdf, smem, acc);
#pragma unroll
    for (int i = 0; i < 4; i += 2)
      *(u32x4*)(p.out + ((size_t)(rb * 64 + (i + (g & 1)) * 16 + r16) * DM + cb * 64 + wn * 16 + 8 * (g >> 1)) * 2) = pair16(cvt4(unpack4(hv[i]) + acc[i]), cvt4(unpack4(hv[i + 1]) + acc[i + 1]));
  } else {
    gemm64_load<43, 4, 3, 0>(p.ws, p.ws + WS_ACT + (size_t)(rb * 4 + wid) * 43 * 1024, cb % 5, smem, []() {});
  }
}

extern "C" void kernel_launch(void* const* d_in, const int* in_sizes, int n_in, void* d_out, int out_size, void* d_ws, size_t ws_size, hipStream_t stream) {
  Params p;
  p.x = (const char*)d_in[0]; p.wqkv = (const char*)d_in[1]; p.wo = (const char*)d_in[2];
  p.wg = (const char*)d_in[4]; p.wu = (const char*)d_in[5]; p.wd = (const char*)d_in[6];
  p.out = (char*)d_out; p.ws = (char*)d_ws;
  constexpr int LDS = 160 * 1024;
  (void)hipFuncSetAttribute((const void*)mega, hipFuncAttributeMaxDynamicSharedMemorySize, LDS);
  mega<<<256, 512, LDS, stream>>>(p);
}
```
